```python
import torch

def _softmax(x, axis=-1):
    return torch.softmax(x, dim=axis)

def _gelu(x):
    return torch.nn.functional.gelu(x, approximate='tanh')

def _silu(x):
    return torch.nn.functional.silu(x)

def _sigmoid(x):
    return torch.sigmoid(x)

def _layernorm(x, axis=-1, eps=1e-05):
    m = x.mean(dim=axis, keepdim=True)
    v = x.var(dim=axis, keepdim=True, unbiased=False)
    return (x - m) / torch.sqrt(v + eps)

def _scatter_add(tgt, idx, src, axis=0):
    return tgt.clone().index_add_(axis, idx, src)

def _segment_sum(src, seg, n):
    out = torch.zeros((n,) + src.shape[1:], dtype=src.dtype)
    return out.index_add_(0, seg, src)

def _pad(x, pw):
    flat = tuple((v for pair in reversed(pw) for v in pair))
    return torch.nn.functional.pad(x, flat)
kernel_name = 'sequential_preLN__ssm_conv_s4__mixer_gmlp__prompt'

def setup_inputs():

    _w = lambda a: (lambda t: t.to(torch.bfloat16) if t.is_floating_point() else t)(torch.as_tensor(a))

    def _A_setup():
        B, L, D, N = (1, 2048, 576, 128)
        A_re = -(lambda _x: torch.exp(torch.as_tensor(_x)))((lambda *s: torch.randn(*s, dtype=torch.bfloat16) * 6.4)(D, N))
        A_im = torch.pi * (lambda *s: torch.randn(*s, dtype=torch.bfloat16) * 0.05)(D, N)
        B_re = (lambda *s: torch.randn(*s, dtype=torch.bfloat16) * 0.05)(D, N)
        B_im = (lambda *s: torch.randn(*s, dtype=torch.bfloat16) * 0.05)(D, N)
        C_re = (lambda *s: torch.randn(*s, dtype=torch.bfloat16) * 0.05)(D, N)
        C_im = (lambda *s: torch.randn(*s, dtype=torch.bfloat16) * 0.05)(D, N)
        log_dt = (lambda *s: torch.randn(*s, dtype=torch.bfloat16) * 0.05)(D) * 0.5 - 4.0
        Dskip = (lambda *s: torch.randn(*s, dtype=torch.bfloat16) * 0.05)(D)
        return {'u': _w((lambda *s: torch.randn(*s, dtype=torch.bfloat16) * 0.05)(B, L, D)), 'A_re': _w(A_re), 'A_im': _w(A_im), 'B_re': _w(B_re), 'B_im': _w(B_im), 'C_re': _w(C_re), 'C_im': _w(C_im), 'log_dt': _w(log_dt), 'Dskip': _w(Dskip)}
    _A_in = _A_setup()

    def _B_setup():
        B, S, D, F = (1, 2048, 576, 4608)
        Fh = F // 2
        return {'x': _w((lambda *s: torch.randn(*s, dtype=torch.bfloat16) * 0.05)(B, S, D)), 'w_in': _w((lambda *s: torch.randn(*s, dtype=torch.bfloat16) * 0.4)(D, 2 * Fh)), 'w_sp': _w(0.01 * (lambda *s: torch.randn(*s, dtype=torch.bfloat16) * 0.4)(S, S)), 'b_sp': _w(1.0 + 0.01 * (lambda *s: torch.randn(*s, dtype=torch.bfloat16) * 0.05)(S)), 'w_out': _w((lambda *s: torch.randn(*s, dtype=torch.bfloat16) * 0.05)(Fh, D))}
    _B_in = _B_setup()
    return {'A_u': _A_in['u'], 'A_A_re': _A_in['A_re'], 'A_A_im': _A_in['A_im'], 'A_B_re': _A_in['B_re'], 'A_B_im': _A_in['B_im'], 'A_C_re': _A_in['C_re'], 'A_C_im': _A_in['C_im'], 'A_log_dt': _A_in['log_dt'], 'A_Dskip': _A_in['Dskip'], 'B_x': _B_in['x'], 'B_w_in': _B_in['w_in'], 'B_w_sp': _B_in['w_sp'], 'B_b_sp': _B_in['b_sp'], 'B_w_out': _B_in['w_out']}

def reference(A_u, A_A_re, A_A_im, A_B_re, A_B_im, A_C_re, A_C_im, A_log_dt, A_Dskip, B_x, B_w_in, B_w_sp, B_b_sp, B_w_out):

    def _A_ref(u, A_re, A_im, B_re, B_im, C_re, C_im, log_dt, Dskip):
        Bsz, L, D = u.shape
        dt = torch.exp(log_dt)[:, None]
        A = A_re + 1j * A_im
        Bc = B_re + 1j * B_im
        Cc = C_re + 1j * C_im
        logdA = dt * A
        dB = dt * Bc
        l = (lambda x, dt: x.to(dt))(torch.arange(L), A_re.dtype)
        P = torch.exp(logdA[:, :, None] * l[None, None, :])
        K = torch.einsum('dn,dnl,dn->dl', Cc, P, dB).real
        L2 = 2 * L
        Kp = _pad(K, ((0, 0), (0, L)))
        up = (lambda x, *d: x.permute(*d))(u, 0, 2, 1)
        up = _pad(up, ((0, 0), (0, 0), (0, L)))
        Kf = (lambda x, axes=(-2, -1): torch.fft.rfft2(x.float(), dim=axes))(Kp[:, None, :], axes=(-2, -1))
        Uf = (lambda x, axes=(-2, -1): torch.fft.rfft2(x.float(), dim=axes))(up[:, :, None, :], axes=(-2, -1))
        Yf = Uf * Kf[None, :, :, :]
        y = (lambda x, s, axes=(-2, -1): torch.fft.irfft2(x, s=s, dim=axes).to(torch.bfloat16))(Yf, (1, L2), axes=(-2, -1))[:, :, 0, :L]
        y = (lambda x, *d: x.permute(*d))(y, 0, 2, 1) + u * Dskip[None, None, :]
        return y

    def _B_ref(x, w_in, w_sp, b_sp, w_out):
        B, S, D = x.shape
        F = w_in.shape[-1]
        Fh = F // 2
        z = _gelu(torch.einsum('bsd,df->bsf', x, w_in))
        z1 = z[..., :Fh].clone()
        z2 = _layernorm(z[..., Fh:], axis=-1)
        g = torch.einsum('ts,bsf->btf', w_sp, z2) + b_sp[None, :, None]
        return torch.einsum('bsf,fd->bsd', z1 * g, w_out)
    x = A_u
    h = x + _A_ref(_layernorm(x, axis=-1), A_A_re, A_A_im, A_B_re, A_B_im, A_C_re, A_C_im, A_log_dt, A_Dskip)
    h2 = h + _B_ref(_layernorm(h, axis=-1), B_w_in, B_w_sp, B_b_sp, B_w_out)
    return h2
```

```cpp
#include <hip/hip_runtime.h>
#include <hip/hip_bf16.h>
#include <stdint.h>

typedef __attribute__((ext_vector_type(8))) __bf16 bf16x8;
typedef __attribute__((ext_vector_type(2))) __bf16 bf16x2;
typedef __attribute__((ext_vector_type(8))) _Float16 f16x8;
typedef __attribute__((ext_vector_type(2))) _Float16 f16x2;
typedef __attribute__((ext_vector_type(4))) float f32x4;
typedef __attribute__((ext_vector_type(2))) float f32x2;
typedef __attribute__((ext_vector_type(4))) unsigned u32x4;
typedef __attribute__((ext_vector_type(2))) unsigned u32x2;
typedef __attribute__((ext_vector_type(4))) short s16x4;
#define LDSP __attribute__((address_space(3)))
#define DEVI __device__ __forceinline__

constexpr int L = 2048, D = 576, NM = 128, F = 4608, FH = 2304;
constexpr float EPS = 1e-5f;
constexpr size_t KR_PITCH = 2064 * 2;
constexpr size_t O_UT = 0;
constexpr size_t O_XT = O_UT + (size_t)D * L * 2;
constexpr size_t O_KR = O_XT + (size_t)D * L * 2;
constexpr size_t O_KS = O_KR + (size_t)D * KR_PITCH;
constexpr size_t O_CS = O_KS + 4096;
constexpr size_t O_WI = O_CS + (size_t)18 * F * 4;
constexpr size_t O_WO = O_WI + (size_t)D * F * 2;
constexpr size_t O_HF = O_WO + (size_t)FH * D * 2;
constexpr size_t O_ZF = O_HF + (size_t)L * D * 2;
constexpr size_t O_PF = O_ZF + (size_t)L * FH * 2;
constexpr size_t O_ST = O_PF + (size_t)L * FH * 2;
constexpr unsigned SENT = 0xFFFFFFFFu;

DEVI unsigned pk_bf16(float a, float b) { f32x2 v = {a, b}; return __builtin_bit_cast(unsigned, __builtin_convertvector(v, bf16x2)); }
DEVI unsigned pk_f16(float a, float b) { f32x2 v = {a, b}; return __builtin_bit_cast(unsigned, __builtin_convertvector(v, f16x2)); }
DEVI float bf_lo(unsigned u) { return __builtin_bit_cast(float, u << 16); }
DEVI float bf_hi(unsigned u) { return __builtin_bit_cast(float, u & 0xFFFF0000u); }
DEVI float asf(unsigned u) { return __builtin_bit_cast(float, u); }
DEVI unsigned asu(float f) { return __builtin_bit_cast(unsigned, f); }
DEVI float rbf(float a) { return bf_lo(pk_bf16(a, a)); }
DEVI float h_lo(unsigned u) { return (float)__builtin_bit_cast(f16x2, u)[0]; }
DEVI float h_hi(unsigned u) { return (float)__builtin_bit_cast(f16x2, u)[1]; }
template <int CTRL> DEVI float dpp_add(float v) {
  return v + __builtin_bit_cast(float, __builtin_amdgcn_update_dpp(0, __builtin_bit_cast(int, v), CTRL, 0xF, 0xF, true));
}
DEVI float row16_sum(float v) { v = dpp_add<0x128>(v); v = dpp_add<0x124>(v); v = dpp_add<0x122>(v); v = dpp_add<0x121>(v); return v; }
DEVI float q_sum(float v) { v += __shfl_xor(v, 16); v += __shfl_xor(v, 32); return v; }
DEVI u32x2 tr_read(const char* p) {
  return __builtin_bit_cast(u32x2, __builtin_amdgcn_ds_read_tr16_b64_v4i16((LDSP s16x4*)p));
}
DEVI f32x4 mfma_bf(u32x4 a, u32x4 b, f32x4 c) {
  return __builtin_amdgcn_mfma_f32_16x16x32_bf16(__builtin_bit_cast(bf16x8, a), __builtin_bit_cast(bf16x8, b), c, 0, 0, 0);
}
DEVI f32x4 mfma_h(u32x4 a, u32x4 b, f32x4 c) {
  return __builtin_amdgcn_mfma_f32_16x16x32_f16(__builtin_bit_cast(f16x8, a), __builtin_bit_cast(f16x8, b), c, 0, 0, 0);
}
DEVI void ln_scalars(float mean, float var, float& mb, float& rinv) {
  mb = rbf(mean);
  float d = rbf(__builtin_sqrtf(rbf(rbf(var) + EPS)));
  rinv = 1.0f / d;
}
DEVI float gelu(float x) {
  float w = x * __builtin_fmaf(x * x, -0.10294324f, -2.3022082f);
  return x * __builtin_amdgcn_rcpf(1.0f + __builtin_amdgcn_exp2f(w));
}

constexpr int TP = 1168;
constexpr int NB_LN = 64, NB_WI = 144, NB_WO = 72;

__global__ __launch_bounds__(256) void k_prep(const char* __restrict__ x, const char* __restrict__ a_re, const char* __restrict__ a_im,
    const char* __restrict__ b_re, const char* __restrict__ b_im, const char* __restrict__ c_re, const char* __restrict__ c_im,
    const char* __restrict__ log_dt, const char* __restrict__ w_in, const char* __restrict__ w_out, char* __restrict__ ws) {
  __shared__ __attribute__((aligned(16))) char sm[32 * TP];
  const int tid = threadIdx.x, lane = tid & 63, w = tid >> 6, li = lane & 15, q = lane >> 4;
  int b = blockIdx.x;
  if (b < NB_LN + NB_WI + NB_WO) {
    const int row = tid >> 3, sub = tid & 7;
    const char* src;
    if (b < NB_LN) src = x + (size_t)(32 * b + row) * 1152;
    else if (b < NB_LN + NB_WI) { int u = b - NB_LN; src = w_in + (size_t)(32 * (u >> 3) + row) * 9216 + (u & 7) * 1152; }
    else {
      int G = b - NB_LN - NB_WI, uu = G / 9, idx = G % 9, hh = row >> 4;
      int panel = idx < 4 ? 2 * uu : (idx < 8 ? 2 * uu + 1 : 2 * uu + hh);
      int tile = idx < 8 ? 2 * (idx & 3) + hh : 8;
      src = w_out + (size_t)(144 * panel + 16 * tile + (row & 15)) * 1152;
    }
    u32x4 v[9];
#pragma unroll
    for (int k = 0; k < 9; ++k) v[k] = *(const u32x4*)(src + (sub + 8 * k) * 16);
#pragma unroll
    for (int k = 0; k < 9; ++k) *(u32x4*)(sm + row * TP + (sub + 8 * k) * 16) = v[k];
    __syncthreads();
    const char* trb = sm + (li >> 2) * TP + (li & 3) * 8;
    if (b < NB_LN) {
      char* xt = ws + O_XT + 64 * b + 16 * q;
#pragma unroll
      for (int k = 0; k < 9; ++k) {
        int ct = 9 * w + k;
        u32x2 lo = tr_read(trb + (8 * q) * TP + ct * 32), hi = tr_read(trb + (8 * q + 4) * TP + ct * 32);
        *(u32x4*)(xt + (size_t)(16 * ct + li) * 4096) = u32x4{lo[0], lo[1], hi[0], hi[1]};
      }
      float s = 0.f;
#pragma unroll
      for (int k = 0; k < 9; ++k)
#pragma unroll
        for (int e = 0; e < 4; ++e) s += bf_lo(v[k][e]) + bf_hi(v[k][e]);
      s += __shfl_xor(s, 1); s += __shfl_xor(s, 2); s += __shfl_xor(s, 4);
      float mean = s * (1.0f / D), ss = 0.f;
#pragma unroll
      for (int k = 0; k < 9; ++k)
#pragma unroll
        for (int e = 0; e < 4; ++e) { float a = bf_lo(v[k][e]) - mean, c = bf_hi(v[k][e]) - mean; ss += a * a + c * c; }
      ss += __shfl_xor(ss, 1); ss += __shfl_xor(ss, 2); ss += __shfl_xor(ss, 4);
      float mb, rinv; ln_scalars(mean, ss * (1.0f / D), mb, rinv);
      __syncthreads();
#pragma unroll
      for (int k = 0; k < 9; ++k) {
        u32x4 o;
#pragma unroll
        for (int e = 0; e < 4; ++e) {
          unsigned c = pk_bf16(bf_lo(v[k][e]) - mb, bf_hi(v[k][e]) - mb);
          unsigned u = pk_bf16(bf_lo(c) * rinv, bf_hi(c) * rinv);
          o[e] = pk_f16(bf_lo(u), bf_hi(u));
        }
        *(u32x4*)(sm + row * TP + (sub + 8 * k) * 16) = o;
      }
      __syncthreads();
      char* ut = ws + O_UT + 64 * b + 16 * q;
#pragma unroll
      for (int k = 0; k < 9; ++k) {
        int ct = 9 * w + k;
        u32x2 lo = tr_read(trb + (8 * q) * TP + ct * 32), hi = tr_read(trb + (8 * q + 4) * TP + ct * 32);
        *(u32x4*)(ut + (size_t)(16 * ct + li) * 4096) = u32x4{lo[0], lo[1], hi[0], hi[1]};
      }
    } else if (b < NB_LN + NB_WI) {
      int u = b - NB_LN, g = u >> 3, cb = u & 7;
      int fp = (cb & 3) * 4 + w, hf = cb < 4 ? 1 : 0;
      char* dst = ws + O_WI + ((size_t)((fp * 2 + hf) * 18 + g) * 9) * 1024 + lane * 16;
      float* cs = (float*)(ws + O_CS) + g * F + cb * 576 + w * 144 + li;
#pragma unroll
      for (int k = 0; k < 9; ++k) {
        int ct = 9 * w + k;
        u32x2 lo = tr_read(trb + (8 * q) * TP + ct * 32), hi = tr_read(trb + (8 * q + 4) * TP + ct * 32);
        *(u32x4*)(dst + k * 1024) = u32x4{lo[0], lo[1], hi[0], hi[1]};
        float s = (bf_lo(lo[0]) + bf_hi(lo[0])) + (bf_lo(lo[1]) + bf_hi(lo[1])) + (bf_lo(hi[0]) + bf_hi(hi[0])) + (bf_lo(hi[1]) + bf_hi(hi[1]));
        s = q_sum(s);
        if (q == 0) cs[16 * k] = s;
      }
    } else {
      int G = b - NB_LN - NB_WI, par = G & 1;
#pragma unroll
      for (int jj = 0; jj < 2; ++jj) {
        int j = 2 * w + jj;
        char* dst = ws + O_WO + ((size_t)(j * 36 + (G >> 1)) * 9) * 1024 + lane * 16;
#pragma unroll
        for (int n = 0; n < 5; ++n) {
          int c0 = 72 * j + 16 * n - (n == 4 ? 8 * par : 0);
          u32x2 lo = tr_read(trb + (4 * q) * TP + c0 * 2), hi = tr_read(trb + (16 + 4 * q) * TP + c0 * 2);
          if (n < 4) *(u32x4*)(dst + (4 * par + n) * 1024) = u32x4{lo[0], lo[1], hi[0], hi[1]};
          else if ((li >> 3) == par) *(u32x4*)(dst + 8 * 1024) = u32x4{lo[0], lo[1], hi[0], hi[1]};
        }
      }
    }
    return;
  }
  const int d = b - (NB_LN + NB_WI + NB_WO);
  if (d < 256 && tid < 64) *(u32x4*)(ws + O_ST + d * 1024 + tid * 16) = u32x4{SENT, SENT, SENT, SENT};
  const float dt = rbf(__expf(bf_lo(*(const unsigned short*)(log_dt + 2 * d))));
  float ar[8], ai[8], cr[8], ci[8];
  float mx = 0.f;
#pragma unroll
  for (int s = 0; s < 2; ++s) {
    size_t off = ((size_t)d * NM + 16 * (2 * w + s) + 4 * q) * 2;
    u32x2 var = *(const u32x2*)(a_re + off), vai = *(const u32x2*)(a_im + off), vbr = *(const u32x2*)(b_re + off),
          vbi = *(const u32x2*)(b_im + off), vcr = *(const u32x2*)(c_re + off), vci = *(const u32x2*)(c_im + off);
#pragma unroll
    for (int e = 0; e < 4; ++e) {
      auto g = [&](u32x2 v) { return (e & 1) ? bf_hi(v[e >> 1]) : bf_lo(v[e >> 1]); };
      ar[4 * s + e] = (dt * g(var)) * 1.44269504f;
      ai[4 * s + e] = (dt * g(vai)) * 0.15915494f;
      float br = dt * g(vbr), bi = dt * g(vbi), c0 = g(vcr), c1 = g(vci);
      cr[4 * s + e] = c0 * br - c1 * bi; ci[4 * s + e] = c0 * bi + c1 * br;
      mx = fmaxf(mx, fmaxf(fabsf(cr[4 * s + e]), fabsf(ci[4 * s + e])));
    }
  }
  mx = fmaxf(mx, __shfl_xor(mx, 16)); mx = fmaxf(mx, __shfl_xor(mx, 32));
  const float inv = mx > 0.f ? 1.0f / mx : 0.f;
  float lh[3];
  lh[0] = (float)(128 * (li >> 3) + (li & 7));
  lh[1] = li < 8 ? (float)(256 + 128 * (li >> 2) + 2 * (li & 3)) : (float)(512 + 128 * ((li - 8) >> 1) + 4 * (li & 1));
  lh[2] = li < 9 ? (float)(1024 + 128 * li) : 0.f;
  const float ll = (float)(8 * li);
  f32x4 acc[3] = {};
#pragma unroll
  for (int s = 0; s < 2; ++s) {
    u32x4 fb, fa[3];
#pragma unroll
    for (int e = 0; e < 4; ++e) {
      float a_r = ar[4 * s + e], a_i = ai[4 * s + e];
      {
        float m = __builtin_amdgcn_exp2f(a_r * ll) * inv, ph = __builtin_amdgcn_fractf(a_i * ll);
        float er = m * __builtin_amdgcn_cosf(ph), ei = m * __builtin_amdgcn_sinf(ph);
        fb[e] = pk_f16(cr[4 * s + e] * er - ci[4 * s + e] * ei, -(cr[4 * s + e] * ei + ci[4 * s + e] * er));
      }
#pragma unroll
      for (int t = 0; t < 3; ++t) {
        float m = __builtin_amdgcn_exp2f(a_r * lh[t]), ph = __builtin_amdgcn_fractf(a_i * lh[t]);
        fa[t][e] = pk_f16(m * __builtin_amdgcn_cosf(ph), m * __builtin_amdgcn_sinf(ph));
      }
    }
#pragma unroll
    for (int t = 0; t < 3; ++t) acc[t] = mfma_h(fa[t], fb, acc[t]);
  }
  float* kp = (float*)sm;
  float* wm = kp + 4 * 816;
#pragma unroll
  for (int t = 0; t < 3; ++t)
#pragma unroll
    for (int r = 0; r < 4; ++r) kp[w * 816 + (16 * t + 4 * q + r) * 17 + li] = acc[t][r] * mx;
  if (lane == 0) wm[w] = mx;
  __syncthreads();
  const float sc = fmaxf(fmaxf(wm[0], wm[1]), fmaxf(wm[2], wm[3]));
  const float isc = sc > 0.f ? 1.0f / sc : 0.f;
  float kv[8];
#pragma unroll
  for (int j = 0; j < 8; ++j) {
    int l = 2047 - 8 * tid - j;
    int lr = (int)rbf((float)l);
    int a = lr >> 7, e = lr & 7, lo = (lr >> 3) & 15;
    int row = a < 2 ? 8 * a + e : (a < 4 ? 8 + 4 * a + (e >> 1) : (a < 8 ? 16 + 2 * a + (e >> 2) : 24 + a));
    int idx = row * 17 + lo;
    kv[j] = ((kp[idx] + kp[816 + idx]) + (kp[1632 + idx] + kp[2448 + idx])) * isc;
  }
  char* kr = ws + O_KR + (size_t)d * KR_PITCH;
  *(u32x4*)(kr + tid * 16) = u32x4{pk_f16(kv[0], kv[1]), pk_f16(kv[2], kv[3]), pk_f16(kv[4], kv[5]), pk_f16(kv[6], kv[7])};
  if (tid < 2) *(u32x4*)(kr + 4096 + tid * 16) = u32x4{0, 0, 0, 0};
  if (tid == 0) ((float*)(ws + O_KS))[d] = sc;
}

constexpr int CV_K = 8 * 260 * 16, CV_U = 4576, CV_CH = CV_K + CV_U, CV_LDS = 4 * CV_CH;
constexpr int NB_CV = 144, NB_TOUCH = 112;

__global__ __launch_bounds__(256) void k_conv(const char* __restrict__ dskip, const char* __restrict__ w_sp, char* __restrict__ ws) {
  extern __shared__ __attribute__((aligned(16))) char sm[];
  const int tid = threadIdx.x, lane = tid & 63, w = tid >> 6, li = lane & 15, q = lane >> 4;
  const int b = blockIdx.x;
  if (b >= NB_CV) {
    u32x4 a = {0, 0, 0, 0};
    for (int i = (b - NB_CV) * 256 + tid; i < L * L * 2 / 16; i += NB_TOUCH * 256) a ^= *(const u32x4*)(w_sp + (size_t)i * 16);
    asm volatile("" ::"v"(a));
    return;
  }
  const int d = 4 * b + w;
  char* sk = sm + w * CV_CH; char* su = sk + CV_K;
  const char* kr = ws + O_KR + (size_t)d * KR_PITCH;
  const char* ut = ws + O_UT + (size_t)d * 4096;
  const char* xt = ws + O_XT + (size_t)d * 4096;
  u32x4 k0[4], k1[4], uu[4]; u32x2 xv[8];
#pragma unroll
  for (int r = 0; r < 4; ++r) { k0[r] = *(const u32x4*)(kr + (lane + 64 * r) * 16); k1[r] = *(const u32x4*)(kr + (lane + 64 * r) * 16 + 16); }
#pragma unroll
  for (int r = 0; r < 4; ++r) uu[r] = *(const u32x4*)(ut + (lane + 64 * r) * 16);
#pragma unroll
  for (int t = 0; t < 8; ++t) xv[t] = *(const u32x2*)(xt + t * 512 + li * 32 + q * 8);
  const float sc = ((const float*)(ws + O_KS))[d];
  const float dsk = bf_lo(*(const unsigned short*)(dskip + 2 * d));
  if (lane < 30) *(u32x4*)(su + lane * 16) = u32x4{0, 0, 0, 0};
  if (lane < 8) *(u32x4*)(sk + (lane * 260 + 256) * 16) = u32x4{0, 0, 0, 0};
#pragma unroll
  for (int r = 0; r < 4; ++r) {
    unsigned e[8] = {k0[r][0], k0[r][1], k0[r][2], k0[r][3], k1[r][0], k1[r][1], k1[r][2], k1[r][3]}, o[7];
#pragma unroll
    for (int i = 0; i < 7; ++i) o[i] = __builtin_amdgcn_alignbit(e[i + 1], e[i], 16);
    char* p = sk + (lane + 64 * r) * 16;
#pragma unroll
    for (int s = 0; s < 4; ++s) {
      *(u32x4*)(p + (2 * s) * 260 * 16) = u32x4{e[s], e[s + 1], e[s + 2], e[s + 3]};
      *(u32x4*)(p + (2 * s + 1) * 260 * 16) = u32x4{o[s], o[s + 1], o[s + 2], o[s + 3]};
    }
    *(u32x4*)(su + 480 + (lane + 64 * r) * 16) = uu[r];
  }
  f32x4 acc[8] = {};
  const char* pa = sk + ((7 - (li & 7)) * 260 + 253 - (li >> 3) + q) * 16;
  const char* pb = su + 448 + li * 32 + q * 16;
#pragma unroll 2
  for (int a0 = 0; a0 < 8; ++a0) {
    u32x4 fa[8], fb[8];
#pragma unroll
    for (int m = 0; m < 8; ++m) { fa[m] = *(const u32x4*)(pa - 64 * a0 - 512 * m); fb[m] = *(const u32x4*)(pb - 64 * a0 + 512 * m); }
#pragma unroll
    for (int t = 0; t < 8; ++t)
#pragma unroll
      for (int m = 0; m <= t; ++m) acc[t] = mfma_h(fa[m], fb[t - m], acc[t]);
  }
#pragma unroll
  for (int t = 0; t < 8; ++t) {
    u32x2 uv = *(const u32x2*)(su + 480 + t * 512 + li * 32 + q * 8);
    float uf[4] = {h_lo(uv[0]), h_hi(uv[0]), h_lo(uv[1]), h_hi(uv[1])};
    float xf[4] = {bf_lo(xv[t][0]), bf_hi(xv[t][0]), bf_lo(xv[t][1]), bf_hi(xv[t][1])};
    float h[4];
#pragma unroll
    for (int r = 0; r < 4; ++r) h[r] = xf[r] + rbf(rbf(acc[t][r] * sc) + rbf(uf[r] * dsk));
    *(u32x2*)(sk + t * 512 + li * 32 + q * 8) = u32x2{pk_bf16(h[0], h[1]), pk_bf16(h[2], h[3])};
  }
  __syncthreads();
  char* hf = ws + O_HF + (size_t)(b >> 3) * 8192 + (b & 1) * 512 + ((b >> 1) & 3) * 128 + li * 8;
#pragma unroll
  for (int r = 0; r < 8; ++r) {
    int T = 16 * r + (tid >> 4);
    int t = 32 * (T >> 1) + 8 * (li >> 2) + 4 * (T & 1) + (li & 3);
    unsigned c0 = *(const unsigned short*)(sm + t * 2), c1 = *(const unsigned short*)(sm + CV_CH + t * 2),
             c2 = *(const unsigned short*)(sm + 2 * CV_CH + t * 2), c3 = *(const unsigned short*)(sm + 3 * CV_CH + t * 2);
    *(u32x2*)(hf + (size_t)(T >> 3) * 18 * 8192 + (T & 7) * 1024) = u32x2{c0 | (c1 << 16), c2 | (c3 << 16)};
  }
}

constexpr int STG = 34816, NSLOT = 4, RING = NSLOT * STG;
constexpr int X_ST = RING, X_CS = RING + 1024, G_LDS = RING + 1024 + 576;

DEVI void glds4(unsigned m0v, const char* base, unsigned v0, unsigned v1, unsigned v2, unsigned v3) {
  unsigned keep;
  asm volatile("s_mov_b32 %0, m0\n\ts_mov_b32 m0, %1\n\ts_nop 0\n\t"
               "global_load_lds_dwordx4 %3, %2\n\tglobal_load_lds_dwordx4 %4, %2 offset:1024\n\t"
               "global_load_lds_dwordx4 %5, %2 offset:2048\n\tglobal_load_lds_dwordx4 %6, %2 offset:3072\n\ts_mov_b32 m0, %0"
               : "=&s"(keep) : "s"(m0v), "s"(base), "v"(v0), "v"(v1), "v"(v2), "v"(v3) : "memory");
}
DEVI void glds1(unsigned m0v, const char* base, unsigned v0) {
  unsigned keep;
  asm volatile("s_mov_b32 %0, m0\n\ts_mov_b32 m0, %1\n\ts_nop 0\n\tglobal_load_lds_dwordx4 %3, %2\n\ts_mov_b32 m0, %0"
               : "=&s"(keep) : "s"(m0v), "s"(base), "v"(v0) : "memory");
}
template <int NP> DEVI void ld_lin(unsigned dst, const char* src, unsigned l16) {
  glds4(dst, src, l16, l16, l16, l16);
  glds4(dst + 4096, src + 4096, l16, l16, l16, l16);
  if (NP == 9) glds1(dst + 8192, src + 8192, l16);
}
template <int N> DEVI void wait_vm() { asm volatile("s_waitcnt vmcnt(%0)" ::"i"(N) : "memory"); }
DEVI void bar() { __builtin_amdgcn_sched_barrier(0); asm volatile("s_waitcnt lgkmcnt(0)\n\ts_barrier" ::: "memory"); __builtin_amdgcn_sched_barrier(0); }
DEVI unsigned lds_addr(const char* p) { return (unsigned)(uintptr_t)(LDSP const char*)p; }

struct Frags { u32x4 m[2], n[9]; };
template <int MK> DEVI void ld_frags(Frags& f, const char* slot, int kk, int w, int lane) {
  if (MK == 0) {
#pragma unroll
    for (int m = 0; m < 2; ++m) {
      const char* p = slot + (kk * 8 + 2 * w + m) * 1024 + lane * 8;
      u32x2 a = *(const u32x2*)p, c = *(const u32x2*)(p + 512);
      f.m[m] = u32x4{a[0], a[1], c[0], c[1]};
    }
  } else if (MK == 1) {
#pragma unroll
    for (int m = 0; m < 2; ++m)
      f.m[m] = *(const u32x4*)(slot + (2 * w + m) * 2048 + (lane & 15) * 128 + (((4 * kk + (lane >> 4)) ^ (lane & 7)) * 16));
  } else {
#pragma unroll
    for (int m = 0; m < 2; ++m) f.m[m] = *(const u32x4*)(slot + ((2 * kk + m) * 4 + w) * 1024 + lane * 16);
  }
#pragma unroll
  for (int n = 0; n < 9; ++n) f.n[n] = *(const u32x4*)(slot + 16384 + (kk * 9 + n) * 1024 + lane * 16);
}
template <int ORI> DEVI void mma(f32x4 (&acc)[2][9], const Frags& f) {
#pragma unroll
  for (int n = 0; n < 9; ++n)
#pragma unroll
    for (int m = 0; m < 2; ++m) acc[m][n] = ORI == 0 ? mfma_bf(f.m[m], f.n[n], acc[m][n]) : mfma_bf(f.n[n], f.m[m], acc[m][n]);
}
DEVI void interleave() {
#pragma unroll
  for (int i = 0; i < 9; ++i) { __builtin_amdgcn_sched_group_barrier(0x008, 2, 0); __builtin_amdgcn_sched_group_barrier(0x100, 2, 0); }
}
DEVI void h_stats(const char* slot, int lw, int lane, float (&s)[2], float (&ss)[2]) {
#pragma unroll
  for (int kk = 0; kk < 2; ++kk)
#pragma unroll
    for (int m = 0; m < 2; ++m) {
      const char* p = slot + (kk * 8 + 2 * lw + m) * 1024 + lane * 8;
      u32x2 a = *(const u32x2*)p, c = *(const u32x2*)(p + 512);
      unsigned v[4] = {a[0], a[1], c[0], c[1]};
#pragma unroll
      for (int e = 0; e < 4; ++e) { float x0 = bf_lo(v[e]), x1 = bf_hi(v[e]); s[m] += x0 + x1; ss[m] += x0 * x0 + x1 * x1; }
    }
}
DEVI void h_stats_fin(char* sm, int lw, int lane, float (&s)[2], float (&ss)[2]) {
#pragma unroll
  for (int m = 0; m < 2; ++m) {
    float mean = q_sum(s[m]) * (1.0f / D), var = fmaxf(q_sum(ss[m]) * (1.0f / D) - mean * mean, 0.f), mb, rinv;
    ln_scalars(mean, var, mb, rinv);
    int li = lane & 15, ls = 32 * lw + 8 * (li >> 2) + 4 * m + (li & 3);
    if (lane < 16) *(f32x2*)(sm + X_ST + ls * 8) = f32x2{rinv, rinv * mb};
  }
}
DEVI void colsum_to_lds(char* sm, const char* ws, int col0, int t) {
  if (t < 144) {
    const float* cs = (const float*)(ws + O_CS) + col0 + t;
    float s = 0.f;
#pragma unroll
    for (int g = 0; g < 18; ++g) s += cs[g * F];
    *(float*)(sm + X_CS + t * 4) = s;
  }
}

__global__ __launch_bounds__(512, 2) void k_z2(char* __restrict__ ws) {
  extern __shared__ __attribute__((aligned(16))) char sm[];
  const int tid = threadIdx.x, lane = tid & 63, w = __builtin_amdgcn_readfirstlane(tid >> 6), li = lane & 15, q = lane >> 4;
  const int xc = blockIdx.x & 7, ii = blockIdx.x >> 3;
  const int tp = (xc & 1) * 8 + (ii >> 2), fp = (xc >> 1) * 4 + (ii & 3);
  constexpr int S = 9;
  if (w >= 4) {
    const int lw = w - 4;
    const unsigned l16 = lane * 16, base = lds_addr(sm);
    float s[2] = {0, 0}, ss[2] = {0, 0};
    auto run = [&](auto NPc, const char* src, unsigned dst, int stride) {
      constexpr int NP = decltype(NPc)::value;
#pragma unroll
      for (int t = 0; t < 4; ++t) ld_lin<NP>(dst + t * STG, src + t * stride, l16);
      wait_vm<3 * NP>(); bar();
      h_stats(sm, lw, lane, s, ss);
#pragma unroll
      for (int t = 0; t < S - 1; ++t) {
        if (t + 3 < S) wait_vm<2 * NP>(); else if (t + 2 < S) wait_vm<NP>(); else wait_vm<0>();
        bar();
        if (t + 4 < S) ld_lin<NP>(dst + (t & 3) * STG, src + (t + 4) * stride, l16);
        h_stats(sm + ((t + 1) & 3) * STG, lw, lane, s, ss);
      }
    };
    if (lw < 2) run(std::integral_constant<int, 8>{}, ws + O_HF + (size_t)tp * 18 * 8192 + lw * 8192, base + lw * 8192, 16384);
    else run(std::integral_constant<int, 9>{}, ws + O_WI + (size_t)(fp * 2) * 18 * 9216 + (lw - 2) * 9216, base + 16384 + (lw - 2) * 9216, 18432);
    h_stats_fin(sm, lw, lane, s, ss);
    bar();
    return;
  }
  f32x4 acc[2][9] = {};
  Frags X, Y;
  colsum_to_lds(sm, ws, FH + 144 * fp, tid);
  bar();
  ld_frags<0>(X, sm, 0, w, lane);
#pragma unroll 1
  for (int t = 0; t < S - 1; ++t) {
    ld_frags<0>(Y, sm + (t & 3) * STG, 1, w, lane); mma<0>(acc, X); interleave();
    bar();
    ld_frags<0>(X, sm + ((t + 1) & 3) * STG, 0, w, lane); mma<0>(acc, Y); interleave();
    __builtin_amdgcn_sched_barrier(0);
  }
  ld_frags<0>(Y, sm + ((S - 1) & 3) * STG, 1, w, lane); mma<0>(acc, X); interleave();
  mma<0>(acc, Y);
  bar();
  float rs[8], rm[8];
#pragma unroll
  for (int j = 0; j < 4; ++j) { f32x4 v = *(const f32x4*)(sm + X_ST + (32 * w + 8 * q + 2 * j) * 8); rs[2 * j] = v[0]; rm[2 * j] = v[1]; rs[2 * j + 1] = v[2]; rm[2 * j + 1] = v[3]; }
  u32x4 zb[9];
  float S1[8] = {}, S2[8] = {};
#pragma unroll
  for (int n = 0; n < 9; ++n) {
    float cs = *(const float*)(sm + X_CS + (16 * n + li) * 4);
#pragma unroll
    for (int jp = 0; jp < 4; ++jp) {
      int j0 = 2 * jp, j1 = j0 + 1;
      unsigned pre = pk_bf16(rs[j0] * acc[j0 >> 2][n][j0 & 3] - rm[j0] * cs, rs[j1] * acc[j1 >> 2][n][j1 & 3] - rm[j1] * cs);
      unsigned z = pk_bf16(gelu(bf_lo(pre)), gelu(bf_hi(pre)));
      zb[n][jp] = z;
      float z0 = bf_lo(z), z1 = bf_hi(z);
      S1[j0] += z0; S2[j0] += z0 * z0; S1[j1] += z1; S2[j1] += z1 * z1;
    }
  }
  auto rsrc = __builtin_amdgcn_make_buffer_rsrc(ws + O_ST + (size_t)tp * 16384, 0, 16384, 0x00020000);
  {
#pragma unroll
    for (int j = 0; j < 8; ++j) { S1[j] = row16_sum(S1[j]); S2[j] = row16_sum(S2[j]); }
    if (li == 0) {
#pragma unroll
      for (int jp = 0; jp < 4; ++jp) {
        u32x4 v = {asu(S1[2 * jp]), asu(S2[2 * jp]), asu(S1[2 * jp + 1]), asu(S2[2 * jp + 1])};
#pragma unroll
        for (int e = 0; e < 4; ++e) v[e] = v[e] == SENT ? 0x7FC00000u : v[e];
        __builtin_amdgcn_raw_buffer_store_b128(v, rsrc, fp * 1024 + (32 * w + 8 * q) * 8 + jp * 16, 0, 16);
      }
    }
  }
  u32x4 pv[4];
  for (int spin = 0; spin < (1 << 20); ++spin) {
    bool ok = true;
#pragma unroll
    for (int jp = 0; jp < 4; ++jp) {
      pv[jp] = __builtin_amdgcn_raw_buffer_load_b128(rsrc, li * 1024 + (32 * w + 8 * q) * 8 + jp * 16, 0, 16);
      ok = ok && pv[jp][0] != SENT && pv[jp][1] != SENT && pv[jp][2] != SENT && pv[jp][3] != SENT;
    }
    if (__all(ok)) break;
    __builtin_amdgcn_s_sleep(2);
    asm volatile("" ::: "memory");
  }
  float mb[8], ri[8];
#pragma unroll
  for (int j = 0; j < 8; ++j) {
    float a = row16_sum(asf(pv[j >> 1][2 * (j & 1)])), c = row16_sum(asf(pv[j >> 1][2 * (j & 1) + 1]));
    float mean = a * (1.0f / FH), var = fmaxf(c * (1.0f / FH) - mean * mean, 0.f);
    ln_scalars(mean, var, mb[j], ri[j]);
  }
  char* zf = ws + O_ZF + ((size_t)(fp * 64 + 4 * tp + w) * 9) * 1024 + lane * 16;
#pragma unroll
  for (int n = 0; n < 9; ++n) {
    u32x4 o;
#pragma unroll
    for (int jp = 0; jp < 4; ++jp) {
      unsigned c = pk_bf16(bf_lo(zb[n][jp]) - mb[2 * jp], bf_hi(zb[n][jp]) - mb[2 * jp + 1]);
      o[jp] = pk_bf16(bf_lo(c) * ri[2 * jp], bf_hi(c) * ri[2 * jp + 1]);
    }
    *(u32x4*)(zf + n * 1024) = o;
  }
}

__global__ __launch_bounds__(512, 2) void k_sp(const char* __restrict__ w_sp, const char* __restrict__ b_sp, char* __restrict__ ws) {
  extern __shared__ __attribute__((aligned(16))) char sm[];
  const int tid = threadIdx.x, lane = tid & 63, w = __builtin_amdgcn_readfirstlane(tid >> 6), li = lane & 15, q = lane >> 4;
  const int xc = blockIdx.x & 7, ii = blockIdx.x >> 3;
  const int tp = (xc & 1) * 8 + (ii >> 2), fp = (xc >> 1) * 4 + (ii & 3);
  constexpr int S1 = 9, S2 = 32, S = S1 + S2;
  if (w >= 4) {
    const int lw = w - 4;
    const unsigned l16 = lane * 16, base = lds_addr(sm);
    const int ph = ((ii >> 2) + ii) & 3;
    float s[2] = {0, 0}, ss[2] = {0, 0};
    unsigned vo[8];
#pragma unroll
    for (int j = 0; j < 8; ++j) {
      int t = 64 * (lw & 1) + 32 * (j >> 2) + 4 * ((j >> 1) & 1) + 16 * (j & 1) + 8 * (lane >> 5) + ((lane >> 3) & 3);
      vo[j] = t * 4096 + (((lane & 7) ^ ((lane >> 3) & 7)) * 16) + 3072 - (j & 3) * 1024;
    }
    const char* wsrc = w_sp + (size_t)tp * 128 * 4096 - 3072;
    auto run = [&](auto NPc, const char* src1, const char* src2, unsigned dst, int stride) {
      constexpr int NP = decltype(NPc)::value;
      auto issue = [&](int t) {
        unsigned dd = dst + (t & 3) * STG;
        if (t < S1) ld_lin<NP>(dd, src1 + t * stride, l16);
        else {
          int k = t - S1; k = (k & 3) == ph ? ((k + 4) & 31) : k;
          if (NP == 9) ld_lin<NP>(dd, src2 + k * stride, l16);
          else { glds4(dd, wsrc + k * 128, vo[0], vo[1], vo[2], vo[3]); glds4(dd + 4096, wsrc + k * 128, vo[4], vo[5], vo[6], vo[7]); }
        }
      };
#pragma unroll
      for (int t = 0; t < 4; ++t) issue(t);
      wait_vm<3 * NP>(); bar();
      h_stats(sm, lw, lane, s, ss);
#pragma unroll
      for (int t = 0; t < S1 - 1; ++t) {
        wait_vm<2 * NP>(); bar();
        issue(t + 4);
        h_stats(sm + ((t + 1) & 3) * STG, lw, lane, s, ss);
      }
      h_stats_fin(sm, lw, lane, s, ss);
#pragma unroll 1
      for (int t = S1 - 1; t < S - 4; ++t) { wait_vm<2 * NP>(); bar(); issue(t + 4); }
      wait_vm<2 * NP>(); bar();
      wait_vm<NP>(); bar();
      wait_vm<0>(); bar();
    };
    if (lw < 2) run(std::integral_constant<int, 8>{}, ws + O_HF + (size_t)tp * 18 * 8192 + lw * 8192, nullptr, base + lw * 8192, 16384);
    else run(std::integral_constant<int, 9>{}, ws + O_WI + (size_t)(fp * 2 + 1) * 18 * 9216 + (lw - 2) * 9216,
             ws + O_ZF + (size_t)fp * 64 * 9216 + (lw - 2) * 9216, base + 16384 + (lw - 2) * 9216, 18432);
    return;
  }
  f32x4 acc[2][9] = {};
  Frags X, Y;
  float bs[2];
  colsum_to_lds(sm, ws, 144 * fp, tid);
#pragma unroll
  for (int m = 0; m < 2; ++m) bs[m] = bf_lo(*(const unsigned short*)(b_sp + (128 * tp + 32 * w + 8 * (li >> 2) + 4 * m + (li & 3)) * 2));
  bar();
  ld_frags<0>(X, sm, 0, w, lane);
#pragma unroll 1
  for (int t = 0; t < S1 - 1; ++t) {
    ld_frags<0>(Y, sm + (t & 3) * STG, 1, w, lane); mma<1>(acc, X); interleave();
    bar();
    ld_frags<0>(X, sm + ((t + 1) & 3) * STG, 0, w, lane); mma<1>(acc, Y); interleave();
    __builtin_amdgcn_sched_barrier(0);
  }
  ld_frags<0>(Y, sm + ((S1 - 1) & 3) * STG, 1, w, lane); mma<1>(acc, X); interleave();
  bar();
  ld_frags<1>(X, sm + (S1 & 3) * STG, 0, w, lane); mma<1>(acc, Y); interleave();
  __builtin_amdgcn_sched_barrier(0);
  u32x2 z1[2][9];
  {
    f32x2 st[2];
#pragma unroll
    for (int m = 0; m < 2; ++m) st[m] = *(const f32x2*)(sm + X_ST + (32 * w + 8 * (li >> 2) + 4 * m + (li & 3)) * 8);
#pragma unroll
    for (int n = 0; n < 9; ++n) {
      f32x4 cs = *(const f32x4*)(sm + X_CS + (16 * n + 4 * q) * 4);
#pragma unroll
      for (int m = 0; m < 2; ++m) {
#pragma unroll
        for (int rp = 0; rp < 2; ++rp) {
          unsigned pre = pk_bf16(st[m][0] * acc[m][n][2 * rp] - st[m][1] * cs[2 * rp], st[m][0] * acc[m][n][2 * rp + 1] - st[m][1] * cs[2 * rp + 1]);
          z1[m][n][rp] = pk_bf16(gelu(bf_lo(pre)), gelu(bf_hi(pre)));
        }
        asm volatile("" : "+v"(z1[m][n]));
        acc[m][n] = f32x4{0, 0, 0, 0};
      }
    }
  }
  __builtin_amdgcn_sched_barrier(0);
#pragma unroll 1
  for (int t = S1; t < S - 1; ++t) {
    ld_frags<1>(Y, sm + (t & 3) * STG, 1, w, lane); mma<1>(acc, X); interleave();
    bar();
    ld_frags<1>(X, sm + ((t + 1) & 3) * STG, 0, w, lane); mma<1>(acc, Y); interleave();
    __builtin_amdgcn_sched_barrier(0);
  }
  ld_frags<1>(Y, sm + ((S - 1) & 3) * STG, 1, w, lane); mma<1>(acc, X); interleave();
  mma<1>(acc, Y);
  char* pf = ws + O_PF + ((size_t)((2 * tp + (w >> 1)) * 72 + 9 * (fp >> 1)) * 4 + 2 * (w & 1)) * 1024 + lane * 16;
#pragma unroll
  for (int m = 0; m < 2; ++m) {
    u32x2 p[9];
#pragma unroll
    for (int n = 0; n < 9; ++n)
#pragma unroll
      for (int rp = 0; rp < 2; ++rp) {
        unsigned g0 = pk_bf16(acc[m][n][2 * rp], acc[m][n][2 * rp + 1]);
        unsigned g1 = pk_bf16(bf_lo(g0) + bs[m], bf_hi(g0) + bs[m]);
        p[n][rp] = pk_bf16(bf_lo(g1) * bf_lo(z1[m][n][rp]), bf_hi(g1) * bf_hi(z1[m][n][rp]));
      }
#pragma unroll
    for (int a = 0; a < 4; ++a)
      *(u32x4*)(pf + (4 * (fp & 1) + a) * 4096 + m * 1024) = u32x4{p[2 * a][0], p[2 * a][1], p[2 * a + 1][0], p[2 * a + 1][1]};
    *(u32x2*)(pf + 8 * 4096 + m * 1024 + (fp & 1) * 8) = p[8];
  }
}

DEVI void mma3(f32x4 (&acc)[6], const Frags& f) {
#pragma unroll
  for (int n = 0; n < 4; ++n) acc[n] = mfma_bf(f.n[n], f.m[0], acc[n]);
  acc[4] = mfma_bf(f.n[8], f.m[0], acc[4]);
#pragma unroll
  for (int n = 0; n < 4; ++n) acc[n] = mfma_bf(f.n[4 + n], f.m[1], acc[n]);
  acc[5] = mfma_bf(f.n[8], f.m[1], acc[5]);
}
__global__ __launch_bounds__(512, 2) void k_out(char* __restrict__ ws, char* __restrict__ out) {
  extern __shared__ __attribute__((aligned(16))) char sm[];
  const int tid = threadIdx.x, lane = tid & 63, w = __builtin_amdgcn_readfirstlane(tid >> 6), li = lane & 15, q = lane >> 4;
  const int xc = blockIdx.x & 7, ii = blockIdx.x >> 3;
  const int t64 = 4 * xc + (ii >> 3), j = ii & 7;
  constexpr int S = 18;
  if (w >= 4) {
    const int lw = w - 4;
    const unsigned l16 = lane * 16, base = lds_addr(sm);
    const int ph = ((ii >> 3) + ii) & 3;
    auto run = [&](auto NPc, const char* src, unsigned dst, int stride) {
      constexpr int NP = decltype(NPc)::value;
      auto issue = [&](int t) {
        int k = t;
        if ((t & 3) == ph) k = t + 4 < S ? t + 4 : ph;
        ld_lin<NP>(dst + (t & 3) * STG, src + k * stride, l16);
      };
#pragma unroll
      for (int t = 0; t < 4; ++t) issue(t);
      wait_vm<3 * NP>(); bar();
#pragma unroll 1
      for (int t = 0; t < S - 4; ++t) { wait_vm<2 * NP>(); bar(); issue(t + 4); }
      wait_vm<2 * NP>(); bar();
      wait_vm<NP>(); bar();
      wait_vm<0>(); bar();
    };
    if (lw < 2) run(std::integral_constant<int, 8>{}, ws + O_PF + (size_t)t64 * 72 * 4096 + lw * 8192, base + lw * 8192, 16384);
    else run(std::integral_constant<int, 9>{}, ws + O_WO + (size_t)j * 36 * 9216 + (lw - 2) * 9216, base + 16384 + (lw - 2) * 9216, 18432);
    return;
  }
  const int T = 4 * t64 + w;
  u32x2 hv[5];
#pragma unroll
  for (int n = 0; n < 5; ++n) {
    int dd = 72 * j + 16 * n + 4 * (n == 4 ? (q & 1) : q);
    hv[n] = *(const u32x2*)(ws + O_HF + ((size_t)((T >> 3) * 18 + (dd >> 5)) * 8 + (T & 7)) * 1024 + ((dd >> 2) & 1) * 512 + (((dd >> 3) & 3) * 16 + li) * 8);
  }
  f32x4 acc[6] = {};
  Frags X, Y;
  bar();
  ld_frags<2>(X, sm, 0, w, lane);
#pragma unroll 1
  for (int t = 0; t < S - 1; ++t) {
    ld_frags<2>(Y, sm + (t & 3) * STG, 1, w, lane); mma3(acc, X);
    bar();
    ld_frags<2>(X, sm + ((t + 1) & 3) * STG, 0, w, lane); mma3(acc, Y);
    __builtin_amdgcn_sched_barrier(0);
  }
  ld_frags<2>(Y, sm + ((S - 1) & 3) * STG, 1, w, lane); mma3(acc, X);
  mma3(acc, Y);
#pragma unroll
  for (int r = 0; r < 4; ++r) acc[4][r] += __shfl_xor(acc[5][r], 32);
  const int t = 32 * (T >> 1) + 8 * (li >> 2) + 4 * (T & 1) + (li & 3);
  char* o = out + (size_t)t * 1152 + (72 * j + 4 * q) * 2;
#pragma unroll
  for (int n = 0; n < 5; ++n) {
    unsigned a = pk_bf16(acc[n][0], acc[n][1]), c = pk_bf16(acc[n][2], acc[n][3]);
    u32x2 v = {pk_bf16(bf_lo(hv[n][0]) + bf_lo(a), bf_hi(hv[n][0]) + bf_hi(a)), pk_bf16(bf_lo(hv[n][1]) + bf_lo(c), bf_hi(hv[n][1]) + bf_hi(c))};
    if (n < 4 || q < 2) *(u32x2*)(o + n * 32) = v;
  }
}

extern "C" void kernel_launch(void* const* d_in, const int* in_sizes, int n_in, void* d_out, int out_size, void* d_ws, size_t ws_size,
                              hipStream_t stream) {
  char* ws = (char*)d_ws;
  (void)hipFuncSetAttribute((const void*)k_conv, hipFuncAttributeMaxDynamicSharedMemorySize, CV_LDS);
  (void)hipFuncSetAttribute((const void*)k_z2, hipFuncAttributeMaxDynamicSharedMemorySize, G_LDS);
  (void)hipFuncSetAttribute((const void*)k_sp, hipFuncAttributeMaxDynamicSharedMemorySize, G_LDS);
  (void)hipFuncSetAttribute((const void*)k_out, hipFuncAttributeMaxDynamicSharedMemorySize, G_LDS);
  k_prep<<<NB_LN + NB_WI + NB_WO + D, 256, 0, stream>>>((const char*)d_in[0], (const char*)d_in[1], (const char*)d_in[2], (const char*)d_in[3],
      (const char*)d_in[4], (const char*)d_in[5], (const char*)d_in[6], (const char*)d_in[7], (const char*)d_in[10], (const char*)d_in[13], ws);
  k_conv<<<NB_CV + NB_TOUCH, 256, CV_LDS, stream>>>((const char*)d_in[8], (const char*)d_in[11], ws);
  k_z2<<<256, 512, G_LDS, stream>>>(ws);
  k_sp<<<256, 512, G_LDS, stream>>>((const char*)d_in[11], (const char*)d_in[12], ws);
  k_out<<<256, 512, G_LDS, stream>>>(ws, (char*)d_out);
}
```

```cpp
#include <hip/hip_runtime.h>
#include <hip/hip_bf16.h>
#include <stdint.h>

typedef __attribute__((ext_vector_type(8))) __bf16 bf16x8;
typedef __attribute__((ext_vector_type(2))) __bf16 bf16x2;
typedef __attribute__((ext_vector_type(8))) _Float16 f16x8;
typedef __attribute__((ext_vector_type(2))) _Float16 f16x2;
typedef __attribute__((ext_vector_type(4))) float f32x4;
typedef __attribute__((ext_vector_type(2))) float f32x2;
typedef __attribute__((ext_vector_type(4))) unsigned u32x4;
typedef __attribute__((ext_vector_type(2))) unsigned u32x2;
typedef __attribute__((ext_vector_type(4))) short s16x4;
#define LDSP __attribute__((address_space(3)))
#define DEVI __device__ __forceinline__

constexpr int L = 2048, D = 576, NM = 128, F = 4608, FH = 2304;
constexpr float EPS = 1e-5f;
constexpr size_t KR_PITCH = 2064 * 2;
constexpr size_t O_UT = 0;
constexpr size_t O_XT = O_UT + (size_t)D * L * 2;
constexpr size_t O_KR = O_XT + (size_t)D * L * 2;
constexpr size_t O_KS = O_KR + (size_t)D * KR_PITCH;
constexpr size_t O_CS = O_KS + 4096;
constexpr size_t O_WI = O_CS + (size_t)18 * F * 4;
constexpr size_t O_WO = O_WI + (size_t)D * F * 2;
constexpr size_t O_HF = O_WO + (size_t)FH * D * 2;
constexpr size_t O_ZF = O_HF + (size_t)L * D * 2;
constexpr size_t O_PF = O_ZF + (size_t)L * FH * 2;
constexpr size_t O_ST = O_PF + (size_t)L * FH * 2;
constexpr size_t O_ZFL = O_ST + 262144;
constexpr size_t O_STAMP = 128u << 20;
#define STAMP(k,i)
#define RSTAMP(k,i)
constexpr unsigned SENT = 0xFFFFFFFFu;

DEVI unsigned pk_bf16(float a, float b) { f32x2 v = {a, b}; return __builtin_bit_cast(unsigned, __builtin_convertvector(v, bf16x2)); }
DEVI unsigned pk_f16(float a, float b) { f32x2 v = {a, b}; return __builtin_bit_cast(unsigned, __builtin_convertvector(v, f16x2)); }
DEVI float bf_lo(unsigned u) { return __builtin_bit_cast(float, u << 16); }
DEVI float bf_hi(unsigned u) { return __builtin_bit_cast(float, u & 0xFFFF0000u); }
DEVI float asf(unsigned u) { return __builtin_bit_cast(float, u); }
DEVI unsigned asu(float f) { return __builtin_bit_cast(unsigned, f); }
DEVI float rbf(float a) { return bf_lo(pk_bf16(a, a)); }
DEVI float h_lo(unsigned u) { return (float)__builtin_bit_cast(f16x2, u)[0]; }
DEVI float h_hi(unsigned u) { return (float)__builtin_bit_cast(f16x2, u)[1]; }
template <int CTRL> DEVI float dpp_add(float v) {
  return v + __builtin_bit_cast(float, __builtin_amdgcn_update_dpp(0, __builtin_bit_cast(int, v), CTRL, 0xF, 0xF, true));
}
DEVI float row16_sum(float v) { v = dpp_add<0x128>(v); v = dpp_add<0x124>(v); v = dpp_add<0x122>(v); v = dpp_add<0x121>(v); return v; }
DEVI float q_sum(float v) { v += __shfl_xor(v, 16); v += __shfl_xor(v, 32); return v; }
DEVI u32x2 tr_read(const char* p) {
  return __builtin_bit_cast(u32x2, __builtin_amdgcn_ds_read_tr16_b64_v4i16((LDSP s16x4*)p));
}
DEVI f32x4 mfma_bf(u32x4 a, u32x4 b, f32x4 c) {
  return __builtin_amdgcn_mfma_f32_16x16x32_bf16(__builtin_bit_cast(bf16x8, a), __builtin_bit_cast(bf16x8, b), c, 0, 0, 0);
}
DEVI f32x4 mfma_h(u32x4 a, u32x4 b, f32x4 c) {
  return __builtin_amdgcn_mfma_f32_16x16x32_f16(__builtin_bit_cast(f16x8, a), __builtin_bit_cast(f16x8, b), c, 0, 0, 0);
}
DEVI void ln_scalars(float mean, float var, float& mb, float& rinv) {
  mb = rbf(mean);
  float d = rbf(__builtin_amdgcn_sqrtf(rbf(rbf(var) + EPS)));
  rinv = __builtin_amdgcn_rcpf(d);
}
DEVI float gelu(float x) {
  float w = x * __builtin_fmaf(x * x, -0.10294324f, -2.3022082f);
  return x * __builtin_amdgcn_rcpf(1.0f + __builtin_amdgcn_exp2f(w));
}

constexpr int TP = 1168;
constexpr int NB_LN = 64, NB_WI = 144, NB_WO = 72;

__global__ __launch_bounds__(256, 4) void k_prep(const char* __restrict__ x, const char* __restrict__ a_re, const char* __restrict__ a_im,
    const char* __restrict__ b_re, const char* __restrict__ b_im, const char* __restrict__ c_re, const char* __restrict__ c_im,
    const char* __restrict__ log_dt, const char* __restrict__ w_in, const char* __restrict__ w_out, char* __restrict__ ws) {
  __shared__ __attribute__((aligned(16))) char sm[32 * TP];
  const int tid = threadIdx.x, lane = tid & 63, w = tid >> 6, li = lane & 15, q = lane >> 4;
  int b = blockIdx.x;
  if (b < NB_LN + NB_WI + NB_WO) {
    STAMP(4,0);
    const int row = tid >> 3, sub = tid & 7;
    const char* src;
    if (b < NB_LN) src = x + (size_t)(32 * b + row) * 1152;
    else if (b < NB_LN + NB_WI) { int u = b - NB_LN; src = w_in + (size_t)(32 * (u >> 3) + row) * 9216 + (u & 7) * 1152; }
    else {
      int G = b - NB_LN - NB_WI, uu = G / 9, idx = G % 9, hh = row >> 4;
      int panel = idx < 4 ? 2 * uu : (idx < 8 ? 2 * uu + 1 : 2 * uu + hh);
      int tile = idx < 8 ? 2 * (idx & 3) + hh : 8;
      src = w_out + (size_t)(144 * panel + 16 * tile + (row & 15)) * 1152;
    }
    u32x4 v[9];
#pragma unroll
    for (int k = 0; k < 9; ++k) v[k] = *(const u32x4*)(src + (sub + 8 * k) * 16);
#pragma unroll
    for (int k = 0; k < 9; ++k) *(u32x4*)(sm + row * TP + (sub + 8 * k) * 16) = v[k];
    __syncthreads();
    STAMP(4,1);
    const char* trb = sm + (li >> 2) * TP + (li & 3) * 8;
    if (b < NB_LN) {
      char* xt = ws + O_XT + 64 * b + 16 * q;
#pragma unroll
      for (int k = 0; k < 9; ++k) {
        int ct = 9 * w + k;
        u32x2 lo = tr_read(trb + (8 * q) * TP + ct * 32), hi = tr_read(trb + (8 * q + 4) * TP + ct * 32);
        *(u32x4*)(xt + (size_t)(16 * ct + li) * 4096) = u32x4{lo[0], lo[1], hi[0], hi[1]};
      }
      float s = 0.f;
#pragma unroll
      for (int k = 0; k < 9; ++k)
#pragma unroll
        for (int e = 0; e < 4; ++e) s += bf_lo(v[k][e]) + bf_hi(v[k][e]);
      s += __shfl_xor(s, 1); s += __shfl_xor(s, 2); s += __shfl_xor(s, 4);
      float mean = s * (1.0f / D), ss = 0.f;
#pragma unroll
      for (int k = 0; k < 9; ++k)
#pragma unroll
        for (int e = 0; e < 4; ++e) { float a = bf_lo(v[k][e]) - mean, c = bf_hi(v[k][e]) - mean; ss += a * a + c * c; }
      ss += __shfl_xor(ss, 1); ss += __shfl_xor(ss, 2); ss += __shfl_xor(ss, 4);
      float mb, rinv; ln_scalars(mean, ss * (1.0f / D), mb, rinv);
      __syncthreads();
#pragma unroll
      for (int k = 0; k < 9; ++k) {
        u32x4 o;
#pragma unroll
        for (int e = 0; e < 4; ++e) {
          unsigned c = pk_bf16(bf_lo(v[k][e]) - mb, bf_hi(v[k][e]) - mb);
          unsigned u = pk_bf16(bf_lo(c) * rinv, bf_hi(c) * rinv);
          o[e] = pk_f16(bf_lo(u), bf_hi(u));
        }
        *(u32x4*)(sm + row * TP + (sub + 8 * k) * 16) = o;
      }
      __syncthreads();
      char* ut = ws + O_UT + 64 * b + 16 * q;
#pragma unroll
      for (int k = 0; k < 9; ++k) {
        int ct = 9 * w + k;
        u32x2 lo = tr_read(trb + (8 * q) * TP + ct * 32), hi = tr_read(trb + (8 * q + 4) * TP + ct * 32);
        *(u32x4*)(ut + (size_t)(16 * ct + li) * 4096) = u32x4{lo[0], lo[1], hi[0], hi[1]};
      }
    } else if (b < NB_LN + NB_WI) {
      int u = b - NB_LN, g = u >> 3, cb = u & 7;
      int fp = (cb & 3) * 4 + w, hf = cb < 4 ? 1 : 0;
      char* dst = ws + O_WI + ((size_t)((fp * 2 + hf) * 18 + g) * 9) * 1024 + lane * 16;
      float* cs = (float*)(ws + O_CS) + g * F + cb * 576 + w * 144 + li;
#pragma unroll
      for (int k = 0; k < 9; ++k) {
        int ct = 9 * w + k;
        u32x2 lo = tr_read(trb + (8 * q) * TP + ct * 32), hi = tr_read(trb + (8 * q + 4) * TP + ct * 32);
        *(u32x4*)(dst + k * 1024) = u32x4{lo[0], lo[1], hi[0], hi[1]};
        float s = (bf_lo(lo[0]) + bf_hi(lo[0])) + (bf_lo(lo[1]) + bf_hi(lo[1])) + (bf_lo(hi[0]) + bf_hi(hi[0])) + (bf_lo(hi[1]) + bf_hi(hi[1]));
        s = q_sum(s);
        if (q == 0) cs[16 * k] = s;
      }
    } else {
      int G = b - NB_LN - NB_WI, par = G & 1;
#pragma unroll
      for (int jj = 0; jj < 2; ++jj) {
        int j = 2 * w + jj;
        char* dst = ws + O_WO + ((size_t)(j * 36 + (G >> 1)) * 9) * 1024 + lane * 16;
#pragma unroll
        for (int n = 0; n < 5; ++n) {
          int c0 = 72 * j + 16 * n - (n == 4 ? 8 * par : 0);
          u32x2 lo = tr_read(trb + (4 * q) * TP + c0 * 2), hi = tr_read(trb + (16 + 4 * q) * TP + c0 * 2);
          if (n < 4) *(u32x4*)(dst + (4 * par + n) * 1024) = u32x4{lo[0], lo[1], hi[0], hi[1]};
          else if ((li >> 3) == par) *(u32x4*)(dst + 8 * 1024) = u32x4{lo[0], lo[1], hi[0], hi[1]};
        }
      }
    }
    STAMP(4,2);
    return;
  }
  STAMP(5,0); STAMP(6,0);
  const int d = b - (NB_LN + NB_WI + NB_WO);
  if (d < 256 && tid < 64) *(u32x4*)(ws + O_ST + d * 1024 + tid * 16) = u32x4{SENT, SENT, SENT, SENT};
  if (d == 256 && tid < 128) *(u32x4*)(ws + O_ZFL + tid * 16) = u32x4{0, 0, 0, 0};
  const float dt = rbf(__expf(bf_lo(*(const unsigned short*)(log_dt + 2 * d))));
  float ar[8], ai[8], cr[8], ci[8];
  float mx = 0.f;
#pragma unroll
  for (int s = 0; s < 2; ++s) {
    size_t off = ((size_t)d * NM + 16 * (2 * w + s) + 4 * q) * 2;
    u32x2 var = *(const u32x2*)(a_re + off), vai = *(const u32x2*)(a_im + off), vbr = *(const u32x2*)(b_re + off),
          vbi = *(const u32x2*)(b_im + off), vcr = *(const u32x2*)(c_re + off), vci = *(const u32x2*)(c_im + off);
#pragma unroll
    for (int e = 0; e < 4; ++e) {
      auto g = [&](u32x2 v) { return (e & 1) ? bf_hi(v[e >> 1]) : bf_lo(v[e >> 1]); };
      ar[4 * s + e] = (dt * g(var)) * 1.44269504f;
      ai[4 * s + e] = (dt * g(vai)) * 0.15915494f;
      float br = dt * g(vbr), bi = dt * g(vbi), c0 = g(vcr), c1 = g(vci);
      cr[4 * s + e] = c0 * br - c1 * bi; ci[4 * s + e] = c0 * bi + c1 * br;
      mx = fmaxf(mx, fmaxf(fabsf(cr[4 * s + e]), fabsf(ci[4 * s + e])));
    }
  }
  mx = fmaxf(mx, __shfl_xor(mx, 16)); mx = fmaxf(mx, __shfl_xor(mx, 32));
  STAMP(5,1); STAMP(6,1);
  const float inv = mx > 0.f ? 1.0f / mx : 0.f;
  float lh[3];
  lh[0] = (float)(128 * (li >> 3) + (li & 7));
  lh[1] = li < 8 ? (float)(256 + 128 * (li >> 2) + 2 * (li & 3)) : (float)(512 + 128 * ((li - 8) >> 1) + 4 * (li & 1));
  lh[2] = li < 9 ? (float)(1024 + 128 * li) : 0.f;
  const float ll = (float)(8 * li);
  f32x4 acc[3] = {};
#pragma unroll
  for (int s = 0; s < 2; ++s) {
    u32x4 fb, fa[3];
#pragma unroll
    for (int e = 0; e < 4; ++e) {
      float a_r = ar[4 * s + e], a_i = ai[4 * s + e];
      {
        float m = __builtin_amdgcn_exp2f(a_r * ll) * inv, ph = __builtin_amdgcn_fractf(a_i * ll);
        float er = m * __builtin_amdgcn_cosf(ph), ei = m * __builtin_amdgcn_sinf(ph);
        fb[e] = pk_f16(cr[4 * s + e] * er - ci[4 * s + e] * ei, -(cr[4 * s + e] * ei + ci[4 * s + e] * er));
      }
#pragma unroll
      for (int t = 0; t < 3; ++t) {
        float m = __builtin_amdgcn_exp2f(a_r * lh[t]), ph = __builtin_amdgcn_fractf(a_i * lh[t]);
        fa[t][e] = pk_f16(m * __builtin_amdgcn_cosf(ph), m * __builtin_amdgcn_sinf(ph));
      }
    }
#pragma unroll
    for (int t = 0; t < 3; ++t) acc[t] = mfma_h(fa[t], fb, acc[t]);
  }
  STAMP(5,2); STAMP(6,2);
  float* kp = (float*)sm;
  float* wm = kp + 4 * 816;
#pragma unroll
  for (int t = 0; t < 3; ++t)
#pragma unroll
    for (int r = 0; r < 4; ++r) kp[w * 816 + (16 * t + 4 * q + r) * 17 + li] = acc[t][r] * mx;
  if (lane == 0) wm[w] = mx;
  __syncthreads();
  const float sc = fmaxf(fmaxf(wm[0], wm[1]), fmaxf(wm[2], wm[3]));
  const float isc = sc > 0.f ? 1.0f / sc : 0.f;
  float kv[8];
#pragma unroll
  for (int j = 0; j < 8; ++j) {
    int l = 2047 - 8 * tid - j;
    int lr = (int)rbf((float)l);
    int a = lr >> 7, e = lr & 7, lo = (lr >> 3) & 15;
    int row = a < 2 ? 8 * a + e : (a < 4 ? 8 + 4 * a + (e >> 1) : (a < 8 ? 16 + 2 * a + (e >> 2) : 24 + a));
    int idx = row * 17 + lo;
    kv[j] = ((kp[idx] + kp[816 + idx]) + (kp[1632 + idx] + kp[2448 + idx])) * isc;
  }
  char* kr = ws + O_KR + (size_t)d * KR_PITCH;
  *(u32x4*)(kr + tid * 16) = u32x4{pk_f16(kv[0], kv[1]), pk_f16(kv[2], kv[3]), pk_f16(kv[4], kv[5]), pk_f16(kv[6], kv[7])};
  if (tid < 2) *(u32x4*)(kr + 4096 + tid * 16) = u32x4{0, 0, 0, 0};
  if (tid == 0) ((float*)(ws + O_KS))[d] = sc;
  STAMP(5,3); STAMP(6,3);
}

constexpr int CV_K = 8 * 260 * 16, CV_U = 4576, CV_CH = CV_K + CV_U, CV_LDS = 4 * CV_CH;
constexpr int NB_CV = 144, NB_TOUCH = 112;

__global__ __launch_bounds__(256) void k_conv(const char* __restrict__ dskip, const char* __restrict__ w_sp, char* __restrict__ ws) {
  extern __shared__ __attribute__((aligned(16))) char sm[];
  const int tid = threadIdx.x, lane = tid & 63, w = tid >> 6, li = lane & 15, q = lane >> 4;
  const int b = blockIdx.x;
  if (b >= NB_CV) {
    u32x4 a = {0, 0, 0, 0};
    for (int i = (b - NB_CV) * 256 + tid; i < L * L * 2 / 16; i += NB_TOUCH * 256) a ^= *(const u32x4*)(w_sp + (size_t)i * 16);
    asm volatile("" ::"v"(a));
    return;
  }
  RSTAMP(1,14); STAMP(1,0);
  const int d = 4 * b + w;
  char* sk = sm + w * CV_CH; char* su = sk + CV_K;
  const char* kr = ws + O_KR + (size_t)d * KR_PITCH;
  const char* ut = ws + O_UT + (size_t)d * 4096;
  const char* xt = ws + O_XT + (size_t)d * 4096;
  u32x4 k0[4], k1[4], uu[4]; u32x2 xv[8];
#pragma unroll
  for (int r = 0; r < 4; ++r) { k0[r] = *(const u32x4*)(kr + (lane + 64 * r) * 16); k1[r] = *(const u32x4*)(kr + (lane + 64 * r) * 16 + 16); }
#pragma unroll
  for (int r = 0; r < 4; ++r) uu[r] = *(const u32x4*)(ut + (lane + 64 * r) * 16);
#pragma unroll
  for (int t = 0; t < 8; ++t) xv[t] = *(const u32x2*)(xt + t * 512 + li * 32 + q * 8);
  const float sc = ((const float*)(ws + O_KS))[d];
  const float dsk = bf_lo(*(const unsigned short*)(dskip + 2 * d));
  if (lane < 30) *(u32x4*)(su + lane * 16) = u32x4{0, 0, 0, 0};
  if (lane < 8) *(u32x4*)(sk + (lane * 260 + 256) * 16) = u32x4{0, 0, 0, 0};
#pragma unroll
  for (int r = 0; r < 4; ++r) {
    unsigned e[8] = {k0[r][0], k0[r][1], k0[r][2], k0[r][3], k1[r][0], k1[r][1], k1[r][2], k1[r][3]}, o[7];
#pragma unroll
    for (int i = 0; i < 7; ++i) o[i] = __builtin_amdgcn_alignbit(e[i + 1], e[i], 16);
    char* p = sk + (lane + 64 * r) * 16;
#pragma unroll
    for (int s = 0; s < 4; ++s) {
      *(u32x4*)(p + (2 * s) * 260 * 16) = u32x4{e[s], e[s + 1], e[s + 2], e[s + 3]};
      *(u32x4*)(p + (2 * s + 1) * 260 * 16) = u32x4{o[s], o[s + 1], o[s + 2], o[s + 3]};
    }
    *(u32x4*)(su + 480 + (lane + 64 * r) * 16) = uu[r];
  }
  STAMP(1,1);
  f32x4 acc[8] = {};
  const char* pa = sk + ((7 - (li & 7)) * 260 + 253 - (li >> 3) + q) * 16;
  const char* pb = su + 448 + li * 32 + q * 16;
#pragma unroll 2
  for (int a0 = 0; a0 < 8; ++a0) {
    u32x4 fa[8], fb[8];
#pragma unroll
    for (int m = 0; m < 8; ++m) { fa[m] = *(const u32x4*)(pa - 64 * a0 - 512 * m); fb[m] = *(const u32x4*)(pb - 64 * a0 + 512 * m); }
#pragma unroll
    for (int t = 0; t < 8; ++t)
#pragma unroll
      for (int m = 0; m <= t; ++m) acc[t] = mfma_h(fa[m], fb[t - m], acc[t]);
  }
  STAMP(1,2);
#pragma unroll
  for (int t = 0; t < 8; ++t) {
    u32x2 uv = *(const u32x2*)(su + 480 + t * 512 + li * 32 + q * 8);
    float uf[4] = {h_lo(uv[0]), h_hi(uv[0]), h_lo(uv[1]), h_hi(uv[1])};
    float xf[4] = {bf_lo(xv[t][0]), bf_hi(xv[t][0]), bf_lo(xv[t][1]), bf_hi(xv[t][1])};
    float h[4];
#pragma unroll
    for (int r = 0; r < 4; ++r) h[r] = xf[r] + rbf(rbf(acc[t][r] * sc) + rbf(uf[r] * dsk));
    *(u32x2*)(sk + t * 512 + li * 32 + q * 8) = u32x2{pk_bf16(h[0], h[1]), pk_bf16(h[2], h[3])};
  }
  __syncthreads();
  STAMP(1,3);
  char* hf = ws + O_HF + (size_t)(b >> 3) * 8192 + (b & 1) * 512 + ((b >> 1) & 3) * 128 + li * 8;
#pragma unroll
  for (int r = 0; r < 8; ++r) {
    int T = 16 * r + (tid >> 4);
    int t = 32 * (T >> 1) + 8 * (li >> 2) + 4 * (T & 1) + (li & 3);
    unsigned c0 = *(const unsigned short*)(sm + t * 2), c1 = *(const unsigned short*)(sm + CV_CH + t * 2),
             c2 = *(const unsigned short*)(sm + 2 * CV_CH + t * 2), c3 = *(const unsigned short*)(sm + 3 * CV_CH + t * 2);
    *(u32x2*)(hf + (size_t)(T >> 3) * 18 * 8192 + (T & 7) * 1024) = u32x2{c0 | (c1 << 16), c2 | (c3 << 16)};
  }
  STAMP(1,4); asm volatile("s_waitcnt vmcnt(0)"); STAMP(1,5); RSTAMP(1,15);
}

constexpr int STG = 34816, NSLOT = 4, RING = NSLOT * STG;
constexpr int X_ST = RING, X_CS = RING + 1024, G_LDS = RING + 1024 + 576;

DEVI void glds4(unsigned m0v, const char* base, unsigned v0, unsigned v1, unsigned v2, unsigned v3) {
  unsigned keep;
  asm volatile("s_mov_b32 %0, m0\n\ts_mov_b32 m0, %1\n\ts_nop 0\n\t"
               "global_load_lds_dwordx4 %3, %2\n\tglobal_load_lds_dwordx4 %4, %2 offset:1024\n\t"
               "global_load_lds_dwordx4 %5, %2 offset:2048\n\tglobal_load_lds_dwordx4 %6, %2 offset:3072\n\ts_mov_b32 m0, %0"
               : "=&s"(keep) : "s"(m0v), "s"(base), "v"(v0), "v"(v1), "v"(v2), "v"(v3) : "memory");
}
DEVI void glds1(unsigned m0v, const char* base, unsigned v0) {
  unsigned keep;
  asm volatile("s_mov_b32 %0, m0\n\ts_mov_b32 m0, %1\n\ts_nop 0\n\tglobal_load_lds_dwordx4 %3, %2\n\ts_mov_b32 m0, %0"
               : "=&s"(keep) : "s"(m0v), "s"(base), "v"(v0) : "memory");
}
template <int NP> DEVI void ld_lin(unsigned dst, const char* src, unsigned l16) {
  glds4(dst, src, l16, l16, l16, l16);
  glds4(dst + 4096, src + 4096, l16, l16, l16, l16);
  if (NP == 9) glds1(dst + 8192, src + 8192, l16);
}
template <int N> DEVI void wait_vm() { asm volatile("s_waitcnt vmcnt(%0)" ::"i"(N) : "memory"); }
DEVI void bar() { __builtin_amdgcn_sched_barrier(0); asm volatile("s_waitcnt lgkmcnt(0)\n\ts_barrier" ::: "memory"); __builtin_amdgcn_sched_barrier(0); }
DEVI unsigned lds_addr(const char* p) { return (unsigned)(uintptr_t)(LDSP const char*)p; }

struct Frags { u32x4 m[2], n[9]; };
DEVI void ld_frags3(Frags& f, const char* slot, int kk, int w, int lane) {
#pragma unroll
  for (int m = 0; m < 2; ++m) f.m[m] = *(const u32x4*)(slot + ((2 * kk + m) * 4 + w) * 1024 + lane * 16);
#pragma unroll
  for (int n = 0; n < 9; ++n) f.n[n] = *(const u32x4*)(slot + 16384 + (kk * 9 + n) * 1024 + lane * 16);
}

constexpr int X_PS = X_CS + 1152, X_CNT = X_PS + 1024, X_LN = X_CNT + 16, M_LDS = X_LN + 8 * 256;
#define TPOLL 12
#define TFLAG 15
struct Fr { u32x4 m[2], n[5]; };
struct Ctx { const char* sm; int am0, am1, an, an8; bool ta; };
template <int MK> DEVI void ldf(Fr& f, const Ctx& c, int t, int kk) {
  const char* slot = c.sm + (t & (NSLOT - 1)) * STG;
  if (MK == 0) {
#pragma unroll
    for (int m = 0; m < 2; ++m) {
      const char* p = slot + c.am0 + (kk * 8 + m) * 1024;
      u32x2 a = *(const u32x2*)p, b = *(const u32x2*)(p + 512);
      f.m[m] = u32x4{a[0], a[1], b[0], b[1]};
    }
  } else {
#pragma unroll
    for (int m = 0; m < 2; ++m) f.m[m] = *(const u32x4*)(slot + (c.am1 ^ (kk * 64)) + m * 2048);
  }
#pragma unroll
  for (int n = 0; n < 4; ++n) f.n[n] = *(const u32x4*)(slot + c.an + (kk * 9 + n) * 1024);
  if (c.ta) f.n[4] = *(const u32x4*)(slot + c.an8 + kk * 9216);
}
DEVI float dot2(unsigned a, unsigned b, float c) {
  return __builtin_amdgcn_fdot2_f32_bf16(__builtin_bit_cast(bf16x2, a), __builtin_bit_cast(bf16x2, b), c, false);
}
DEVI void hstat(const Fr& f, float (&s)[2], float (&ss)[2]) {
#pragma unroll
  for (int m = 0; m < 2; ++m)
#pragma unroll
    for (int e = 0; e < 4; ++e) { unsigned v = f.m[m][e]; s[m] = dot2(v, 0x3F803F80u, s[m]); ss[m] = dot2(v, v, ss[m]); }
}
template <int ORI> DEVI f32x4 mm(u32x4 m, u32x4 n, f32x4 c) {
  return ORI == 0 ? mfma_bf(m, n, c) : mfma_bf(n, m, c); }
template <int MK, int ORI, bool ST, bool LD> DEVI void seg(f32x4 (&acc)[2][5], const Fr& cur, Fr& nxt, const Ctx& c, int t, int kk, float (&s)[2], float (&ss)[2]) {
  const char* slot = c.sm + (t & (NSLOT - 1)) * STG;
  constexpr bool LDX = LD;
  if (LDX) {
    if (MK == 0) {
#pragma unroll
      for (int m = 0; m < 2; ++m) {
        const char* p = slot + c.am0 + (kk * 8 + m) * 1024;
        u32x2 a = *(const u32x2*)p, b = *(const u32x2*)(p + 512);
        nxt.m[m] = u32x4{a[0], a[1], b[0], b[1]};
      }
    } else {
#pragma unroll
      for (int m = 0; m < 2; ++m) nxt.m[m] = *(const u32x4*)(slot + (c.am1 ^ (kk * 64)) + m * 2048);
    }
#pragma unroll
    for (int n = 0; n < 4; ++n) nxt.n[n] = *(const u32x4*)(slot + c.an + (kk * 9 + n) * 1024);
  }
#pragma unroll
  for (int n = 0; n < 4; ++n)
#pragma unroll
    for (int m = 0; m < 2; ++m) acc[m][n] = mm<ORI>(cur.m[m], cur.n[n], acc[m][n]);
#pragma unroll
  for (int i = 0; i < 8; ++i) { __builtin_amdgcn_sched_group_barrier(0x008, 1, 0); __builtin_amdgcn_sched_group_barrier(0x100, 1, 0); }
  __builtin_amdgcn_sched_barrier(0);
  if (c.ta) {
    if (LDX) nxt.n[4] = *(const u32x4*)(slot + c.an8 + kk * 9216);
#pragma unroll
    for (int m = 0; m < 2; ++m) acc[m][4] = mm<ORI>(cur.m[m], cur.n[4], acc[m][4]);
  }
  else if (ST) hstat(cur, s, ss);
  __builtin_amdgcn_sched_barrier(0);
}
template <int CTRL> DEVI float dpp_get(float v) {
  return __builtin_bit_cast(float, __builtin_amdgcn_update_dpp(0, __builtin_bit_cast(int, v), CTRL, 0xF, 0xF, true));
}
DEVI void glds4s(unsigned m0v, const char* base, unsigned v0) {
  unsigned keep;
  asm volatile("s_mov_b32 %0, m0\n\ts_mov_b32 m0, %1\n\ts_nop 0\n\t"
               "global_load_lds_dwordx4 %3, %2 sc1\n\tglobal_load_lds_dwordx4 %3, %2 offset:1024 sc1\n\t"
               "global_load_lds_dwordx4 %3, %2 offset:2048 sc1\n\tglobal_load_lds_dwordx4 %3, %2 offset:3072 sc1\n\ts_mov_b32 m0, %0"
               : "=&s"(keep) : "s"(m0v), "s"(base), "v"(v0) : "memory");
}
DEVI void glds1s(unsigned m0v, const char* base, unsigned v0) {
  unsigned keep;
  asm volatile("s_mov_b32 %0, m0\n\ts_mov_b32 m0, %1\n\ts_nop 0\n\tglobal_load_lds_dwordx4 %3, %2 sc1\n\ts_mov_b32 m0, %0"
               : "=&s"(keep) : "s"(m0v), "s"(base), "v"(v0) : "memory");
}
typedef __attribute__((address_space(1))) unsigned gu32;
constexpr int SPIN = 1 << 16;

__global__ __launch_bounds__(768, 3) void k_mix(const char* __restrict__ w_sp, const char* __restrict__ b_sp, char* __restrict__ ws) {
  extern __shared__ __attribute__((aligned(16))) char sm[];
  const int tid = threadIdx.x, lane = tid & 63, w = __builtin_amdgcn_readfirstlane(tid >> 6), li = lane & 15, q = lane >> 4;
  const int xc = blockIdx.x & 7, ii = blockIdx.x >> 3;
  const int tp = (xc & 1) * 8 + (ii >> 2), fp = (xc >> 1) * 4 + (ii & 3);
  constexpr int S = 50;
  RSTAMP(3,14); STAMP(3,0);
  gu32* zflag = (gu32*)(ws + O_ZFL) + fp * 32;
  if (w >= 8) {
    const int lw = w - 8;
    const unsigned l16 = lane * 16, base = lds_addr(sm);
    const int ph = ((ii >> 2) + ii) & 3;
    auto k9 = [&](int t) { return (t & 3) == ph ? (t + 4 < 9 ? t + 4 : ph) : t; };
    auto k32 = [&](int t) { return (t & 3) == ph ? ((t + 4) & 31) : t; };
    if (lw < 2) {
      constexpr int NP = 8;
      unsigned vo[8];
#pragma unroll
      for (int j = 0; j < 8; ++j) {
        int t = 64 * lw + 32 * (j >> 2) + 4 * ((j >> 1) & 1) + 16 * (j & 1) + 8 * (lane >> 5) + ((lane >> 3) & 3);
        vo[j] = t * 4096 + (((lane & 7) ^ ((lane >> 3) & 7)) * 16) + 3072 - (j & 3) * 1024;
      }
      const char* wsrc = w_sp + (size_t)tp * 128 * 4096 - 3072;
      const char* hsrc = ws + O_HF + (size_t)tp * 18 * 8192 + lw * 8192;
      const unsigned dst = base + lw * 8192;
#pragma unroll 1
      for (int t = 0; t < NSLOT; ++t) ld_lin<NP>(dst + t * STG, hsrc + k9(t) * 16384, l16);
      wait_vm<(NSLOT - 1) * NP>(); bar();
#pragma unroll 1
      for (int t = NSLOT; t < 18; ++t) {
        wait_vm<(NSLOT - 2) * NP>(); bar();
        ld_lin<NP>(dst + (t & (NSLOT - 1)) * STG, hsrc + k9(t < 9 ? t : t - 9) * 16384, l16);
      }
#pragma unroll 1
      for (int t = 18; t < S; ++t) {
        wait_vm<(NSLOT - 2) * NP>(); bar();
        const unsigned dd = dst + (t & (NSLOT - 1)) * STG; const char* sp = wsrc + k32(t - 18) * 128;
        glds4(dd, sp, vo[0], vo[1], vo[2], vo[3]); glds4(dd + 4096, sp, vo[4], vo[5], vo[6], vo[7]);
      }
      wait_vm<2 * NP>(); bar();
      wait_vm<NP>(); bar();
      wait_vm<0>(); bar();
    } else {
      constexpr int NP = 9;
      const char* wi = ws + O_WI + (size_t)(fp * 2) * 18 * 9216 + (lw - 2) * 9216;
      const char* zs = ws + O_ZF + (size_t)fp * 64 * 9216 + (lw - 2) * 9216;
      const unsigned dst = base + 16384 + (lw - 2) * 9216;
      auto zld = [&](int t) {
        const unsigned dd = dst + (t & (NSLOT - 1)) * STG; const char* sp = zs + k32(t - 18) * 18432;
        glds4s(dd, sp, l16); glds4s(dd + 4096, sp + 4096, l16); glds1s(dd + 8192, sp + 8192, l16);
      };
#pragma unroll 1
      for (int t = 0; t < NSLOT; ++t) ld_lin<NP>(dst + t * STG, wi + k9(t) * 18432, l16);
      wait_vm<(NSLOT - 1) * NP>(); bar();
#pragma unroll 1
      for (int t = NSLOT; t < 18; ++t) {
        wait_vm<(NSLOT - 2) * NP>(); bar();
        ld_lin<NP>(dst + (t & (NSLOT - 1)) * STG, wi + (t < 9 ? k9(t) : 9 + k9(t - 9)) * 18432, l16);
      }
      wait_vm<2 * NP>(); bar();
      wait_vm<NP>(); bar();
      wait_vm<0>(); bar();
      for (int spin = 0; spin < SPIN; ++spin) {
        unsigned v = __hip_atomic_load(zflag + li, __ATOMIC_RELAXED, __HIP_MEMORY_SCOPE_AGENT);
        if (__all(v == 1u)) break;
        __builtin_amdgcn_s_sleep(1);
      }
      asm volatile("" ::: "memory");
#pragma unroll 1
      for (int t = 18; t < 21; ++t) zld(t);
#pragma unroll 1
      for (int t = 21; t < S; ++t) { wait_vm<2 * NP>(); bar(); zld(t); }
      wait_vm<2 * NP>(); bar();
      wait_vm<NP>(); bar();
      wait_vm<0>(); bar();
    }
    return;
  }
  const int rg = w & 3, ty = w >> 2;
  Ctx c;
  c.sm = sm; c.ta = ty == 0;
  c.am0 = 2 * rg * 1024 + lane * 8;
  c.am1 = 2 * rg * 2048 + li * 128 + ((q ^ (lane & 7)) * 16);
  c.an = 16384 + ty * 4096 + lane * 16; c.an8 = 16384 + 8192 + lane * 16;
  float bs[2];
#pragma unroll
  for (int m = 0; m < 2; ++m) bs[m] = bf_lo(*(const unsigned short*)(b_sp + (128 * tp + 32 * rg + 8 * (li >> 2) + 4 * m + (li & 3)) * 2));
  if (tid < 288) {
    const float* cs = (const float*)(ws + O_CS) + (tid < 144 ? FH + tid : tid - 144) + 144 * fp;
    float s = 0.f;
#pragma unroll
    for (int g = 0; g < 18; ++g) s += cs[g * F];
    *(float*)(sm + X_CS + tid * 4) = s;
  } else if (tid < 288 + 65) *(f32x4*)(sm + X_PS + (tid - 288) * 16) = f32x4{0, 0, 0, 0};
  f32x4 acc[2][5] = {};
  Fr X, Y;
  float hs[2] = {0, 0}, hq[2] = {0, 0};
  bar();
  STAMP(3,1);
  ldf<0>(X, c, 0, 0);
#pragma unroll 1
  for (int t = 0; t < 8; ++t) {
    seg<0, 0, true, true>(acc, X, Y, c, t, 1, hs, hq);
    bar();
    seg<0, 0, true, true>(acc, Y, X, c, t + 1, 0, hs, hq);
  }
  seg<0, 0, true, true>(acc, X, Y, c, 8, 1, hs, hq);
  if (!c.ta) {
    hstat(Y, hs, hq);
#pragma unroll
    for (int m = 0; m < 2; ++m) {
      float mean = q_sum(hs[m]) * (1.0f / D), var = fmaxf(q_sum(hq[m]) * (1.0f / D) - mean * mean, 0.f), mb, rinv;
      ln_scalars(mean, var, mb, rinv);
      if (lane < 16) *(f32x2*)(sm + X_ST + (32 * rg + 8 * (li >> 2) + 4 * m + (li & 3)) * 8) = f32x2{rinv, rinv * mb};
    }
  }
  bar();
  seg<0, 0, false, true>(acc, Y, X, c, 9, 0, hs, hq);
  STAMP(3,2);
  u32x4 zb[5];
  {
    float rs[8], rm[8], S1[8] = {}, S2[8] = {};
#pragma unroll
    for (int j = 0; j < 4; ++j) { f32x4 v = *(const f32x4*)(sm + X_ST + (32 * rg + 8 * q + 2 * j) * 8); rs[2 * j] = v[0]; rm[2 * j] = v[1]; rs[2 * j + 1] = v[2]; rm[2 * j + 1] = v[3]; }
    auto tile = [&](int n, int col) {
      float cs = *(const float*)(sm + X_CS + (col + li) * 4);
#pragma unroll
      for (int jp = 0; jp < 4; ++jp) {
        int j0 = 2 * jp, j1 = j0 + 1;
        float z0 = gelu(rs[j0] * acc[j0 >> 2][n][j0 & 3] - rm[j0] * cs), z1 = gelu(rs[j1] * acc[j1 >> 2][n][j1 & 3] - rm[j1] * cs);
        zb[n][jp] = pk_bf16(z0, z1);
        S1[j0] += z0; S2[j0] += z0 * z0; S1[j1] += z1; S2[j1] += z1 * z1;
      }
      asm volatile("" : "+v"(zb[n]));
    };
#pragma unroll
    for (int n = 0; n < 4; ++n) tile(n, 64 * ty + 16 * n);
    if (c.ta) tile(4, 128);
    float v[16];
#pragma unroll
    for (int j = 0; j < 8; ++j) { v[2 * j] = S1[j]; v[2 * j + 1] = S2[j]; }
#pragma unroll
    for (int i = 0; i < 8; ++i) { bool h = li & 8; v[i] = (h ? v[i + 8] : v[i]) + dpp_get<0x128>(h ? v[i] : v[i + 8]); }
#pragma unroll
    for (int i = 0; i < 4; ++i) { bool h = li & 4; v[i] = (h ? v[i + 4] : v[i]) + dpp_get<0x141>(h ? v[i] : v[i + 4]); }
#pragma unroll
    for (int i = 0; i < 2; ++i) { bool h = li & 2; v[i] = (h ? v[i + 2] : v[i]) + dpp_get<0x4E>(h ? v[i] : v[i + 2]); }
    { bool h = li & 1; v[0] = (h ? v[1] : v[0]) + dpp_get<0xB1>(h ? v[0] : v[1]); }
    float* ps = (float*)(sm + X_PS) + rg * 64 + lane;
    __hip_atomic_fetch_add(ps, v[0], __ATOMIC_RELAXED, __HIP_MEMORY_SCOPE_WORKGROUP);
    asm volatile("" ::: "memory");
    unsigned old = 0;
    if (lane == 0) old = __hip_atomic_fetch_add((unsigned*)(sm + X_CNT) + rg, 1u, __ATOMIC_RELAXED, __HIP_MEMORY_SCOPE_WORKGROUP);
    if (__builtin_amdgcn_readfirstlane(old) == 1u) {
      asm volatile("" ::: "memory");
      unsigned tot = asu(__hip_atomic_load(ps, __ATOMIC_RELAXED, __HIP_MEMORY_SCOPE_WORKGROUP));
      tot = tot == SENT ? 0x7FC00000u : tot;
      __hip_atomic_store((gu32*)(ws + O_ST) + ((tp * 4 + rg) * 16 + fp) * 64 + lane, tot, __ATOMIC_RELAXED, __HIP_MEMORY_SCOPE_AGENT);
    }
#pragma unroll
    for (int m = 0; m < 2; ++m)
#pragma unroll
      for (int n = 0; n < 5; ++n) acc[m][n] = f32x4{0, 0, 0, 0};
  }
  STAMP(3,3);
  auto it2 = [&](int t) {
    seg<0, 1, false, true>(acc, X, Y, c, t, 1, hs, hq);
    bar();
    seg<0, 1, false, true>(acc, Y, X, c, t + 1, 0, hs, hq);
  };
#pragma unroll 1
  for (int t = 9; t < TPOLL; ++t) it2(t);
  auto rsrc = __builtin_amdgcn_make_buffer_rsrc(ws + O_ST + (size_t)(tp * 4 + rg) * 4096, 0, 4096, 0x00020000);
  u32x4 pv[4];
#pragma unroll
  for (int k = 0; k < 4; ++k) pv[k] = __builtin_amdgcn_raw_buffer_load_b128(rsrc, lane * 16 + k * 1024, 0, 16);
  __builtin_amdgcn_sched_barrier(0);
  it2(TPOLL);
  STAMP(3,4);
  for (int spin = 0; spin < SPIN; ++spin) {
    bool ok = true;
#pragma unroll
    for (int k = 0; k < 4; ++k) ok = ok && pv[k][0] != SENT && pv[k][1] != SENT && pv[k][2] != SENT && pv[k][3] != SENT;
    if (__all(ok)) break;
    __builtin_amdgcn_s_sleep(1);
    asm volatile("" ::: "memory");
#pragma unroll
    for (int k = 0; k < 4; ++k) pv[k] = __builtin_amdgcn_raw_buffer_load_b128(rsrc, lane * 16 + k * 1024, 0, 16);
  }
  STAMP(3,5);
  {
    float t4[4];
#pragma unroll
    for (int e = 0; e < 4; ++e) t4[e] = q_sum((asf(pv[0][e]) + asf(pv[1][e])) + (asf(pv[2][e]) + asf(pv[3][e])));
    f32x4 o;
#pragma unroll
    for (int r = 0; r < 2; ++r) {
      float mean = t4[2 * r] * (1.0f / FH), var = fmaxf(t4[2 * r + 1] * (1.0f / FH) - mean * mean, 0.f), mb, ri;
      ln_scalars(mean, var, mb, ri);
      o[2 * r] = ri; o[2 * r + 1] = -mb * ri;
    }
    char* ln = sm + X_LN + w * 256;
    if (lane < 16) *(f32x4*)(ln + li * 16) = o;
    float ri[8], rb[8];
#pragma unroll
    for (int j = 0; j < 4; ++j) { f32x4 v = *(volatile const f32x4*)(ln + (4 * q + j) * 16); ri[2 * j] = v[0]; rb[2 * j] = v[1]; ri[2 * j + 1] = v[2]; rb[2 * j + 1] = v[3]; }
    auto zr = __builtin_amdgcn_make_buffer_rsrc(ws + O_ZF + ((size_t)(fp * 64 + 4 * tp + rg) * 9) * 1024, 0, 9216, 0x00020000);
    auto put = [&](int n, int off) {
      u32x4 o4;
#pragma unroll
      for (int jp = 0; jp < 4; ++jp) o4[jp] = pk_bf16(bf_lo(zb[n][jp]) * ri[2 * jp] + rb[2 * jp], bf_hi(zb[n][jp]) * ri[2 * jp + 1] + rb[2 * jp + 1]);
      __builtin_amdgcn_raw_buffer_store_b128(o4, zr, lane * 16 + off, 0, 16);
    };
#pragma unroll
    for (int n = 0; n < 4; ++n) put(n, ty * 4096 + n * 1024);
    if (c.ta) put(4, 8192);
  }
  STAMP(3,6);
#pragma unroll 1
  for (int t = TPOLL + 1; t < 17; ++t) {
    seg<0, 1, false, true>(acc, X, Y, c, t, 1, hs, hq);
    if (t == TFLAG) wait_vm<0>();
    bar();
    if (t == TFLAG && tid == 0) __hip_atomic_store(zflag + tp, 1u, __ATOMIC_RELAXED, __HIP_MEMORY_SCOPE_AGENT);
    seg<0, 1, false, true>(acc, Y, X, c, t + 1, 0, hs, hq);
  }
  seg<0, 1, false, true>(acc, X, Y, c, 17, 1, hs, hq);
  seg<0, 1, false, false>(acc, Y, X, c, 17, 1, hs, hq);
  STAMP(3,7);
  u32x2 z1[2][5];
  {
    f32x2 st[2];
#pragma unroll
    for (int m = 0; m < 2; ++m) st[m] = *(const f32x2*)(sm + X_ST + (32 * rg + 8 * (li >> 2) + 4 * m + (li & 3)) * 8);
    auto tile = [&](int n, int col) {
      f32x4 cs = *(const f32x4*)(sm + X_CS + (144 + col + 4 * q) * 4);
#pragma unroll
      for (int m = 0; m < 2; ++m) {
#pragma unroll
        for (int rp = 0; rp < 2; ++rp)
          z1[m][n][rp] = pk_bf16(gelu(st[m][0] * acc[m][n][2 * rp] - st[m][1] * cs[2 * rp]), gelu(st[m][0] * acc[m][n][2 * rp + 1] - st[m][1] * cs[2 * rp + 1]));
        asm volatile("" : "+v"(z1[m][n]));
      }
    };
#pragma unroll
    for (int n = 0; n < 4; ++n) tile(n, 64 * ty + 16 * n);
    if (c.ta) tile(4, 128);
#pragma unroll
    for (int m = 0; m < 2; ++m)
#pragma unroll
      for (int n = 0; n < 5; ++n) acc[m][n] = f32x4{0, 0, 0, 0};
  }
  STAMP(3,8);
  bar();
  STAMP(3,9);
  ldf<1>(X, c, 18, 0);
#pragma unroll 1
  for (int t = 18; t < S - 1; ++t) {
    seg<1, 1, false, true>(acc, X, Y, c, t, 1, hs, hq);
    bar();
    seg<1, 1, false, true>(acc, Y, X, c, t + 1, 0, hs, hq);
  }
  seg<1, 1, false, true>(acc, X, Y, c, S - 1, 1, hs, hq);
  seg<1, 1, false, false>(acc, Y, X, c, S - 1, 1, hs, hq);
  STAMP(3,10);
  char* pf = ws + O_PF + ((size_t)((2 * tp + (rg >> 1)) * 72 + 9 * (fp >> 1)) * 4 + 2 * (rg & 1)) * 1024 + lane * 16;
#pragma unroll
  for (int m = 0; m < 2; ++m) {
    auto pp = [&](int n) {
      return u32x2{pk_bf16((acc[m][n][0] + bs[m]) * bf_lo(z1[m][n][0]), (acc[m][n][1] + bs[m]) * bf_hi(z1[m][n][0])),
                   pk_bf16((acc[m][n][2] + bs[m]) * bf_lo(z1[m][n][1]), (acc[m][n][3] + bs[m]) * bf_hi(z1[m][n][1]))};
    };
#pragma unroll
    for (int a = 0; a < 2; ++a) {
      u32x2 p0 = pp(2 * a), p1 = pp(2 * a + 1);
      *(u32x4*)(pf + (4 * (fp & 1) + 2 * ty + a) * 4096 + m * 1024) = u32x4{p0[0], p0[1], p1[0], p1[1]};
    }
    if (c.ta) *(u32x2*)(pf + 8 * 4096 + m * 1024 + (fp & 1) * 8) = pp(4);
  }
  STAMP(3,11); RSTAMP(3,15);
}

DEVI void mma3(f32x4 (&acc)[6], const Frags& f) {
#pragma unroll
  for (int n = 0; n < 4; ++n) acc[n] = mfma_bf(f.n[n], f.m[0], acc[n]);
  acc[4] = mfma_bf(f.n[8], f.m[0], acc[4]);
#pragma unroll
  for (int n = 0; n < 4; ++n) acc[n] = mfma_bf(f.n[4 + n], f.m[1], acc[n]);
  acc[5] = mfma_bf(f.n[8], f.m[1], acc[5]);
}
__global__ __launch_bounds__(512, 2) void k_out(char* __restrict__ ws, char* __restrict__ out) {
  extern __shared__ __attribute__((aligned(16))) char sm[];
  const int tid = threadIdx.x, lane = tid & 63, w = __builtin_amdgcn_readfirstlane(tid >> 6), li = lane & 15, q = lane >> 4;
  const int xc = blockIdx.x & 7, ii = blockIdx.x >> 3;
  const int t64 = 4 * xc + (ii >> 3), j = ii & 7;
  constexpr int S = 18;
  STAMP(7,0);
  if (w >= 4) {
    const int lw = w - 4;
    const unsigned l16 = lane * 16, base = lds_addr(sm);
    const int ph = ((ii >> 3) + ii) & 3;
    auto run = [&](auto NPc, const char* src, unsigned dst, int stride) {
      constexpr int NP = decltype(NPc)::value;
      auto issue = [&](int t) {
        int k = t;
        if ((t & 3) == ph) k = t + 4 < S ? t + 4 : ph;
        ld_lin<NP>(dst + (t & 3) * STG, src + k * stride, l16);
      };
#pragma unroll
      for (int t = 0; t < 4; ++t) issue(t);
      wait_vm<3 * NP>(); bar();
#pragma unroll 1
      for (int t = 0; t < S - 4; ++t) { wait_vm<2 * NP>(); bar(); issue(t + 4); }
      wait_vm<2 * NP>(); bar();
      wait_vm<NP>(); bar();
      wait_vm<0>(); bar();
    };
    if (lw < 2) run(std::integral_constant<int, 8>{}, ws + O_PF + (size_t)t64 * 72 * 4096 + lw * 8192, base + lw * 8192, 16384);
    else run(std::integral_constant<int, 9>{}, ws + O_WO + (size_t)j * 36 * 9216 + (lw - 2) * 9216, base + 16384 + (lw - 2) * 9216, 18432);
    return;
  }
  const int T = 4 * t64 + w;
  u32x2 hv[5];
#pragma unroll
  for (int n = 0; n < 5; ++n) {
    int dd = 72 * j + 16 * n + 4 * (n == 4 ? (q & 1) : q);
    hv[n] = *(const u32x2*)(ws + O_HF + ((size_t)((T >> 3) * 18 + (dd >> 5)) * 8 + (T & 7)) * 1024 + ((dd >> 2) & 1) * 512 + (((dd >> 3) & 3) * 16 + li) * 8);
  }
  f32x4 acc[6] = {};
  Frags X, Y;
  bar();
  STAMP(7,1);
  ld_frags3(X, sm, 0, w, lane);
#pragma unroll 1
  for (int t = 0; t < S - 1; ++t) {
    ld_frags3(Y, sm + (t & 3) * STG, 1, w, lane); mma3(acc, X);
    bar();
    ld_frags3(X, sm + ((t + 1) & 3) * STG, 0, w, lane); mma3(acc, Y);
    __builtin_amdgcn_sched_barrier(0);
  }
  ld_frags3(Y, sm + ((S - 1) & 3) * STG, 1, w, lane); mma3(acc, X);
  mma3(acc, Y);
#pragma unroll
  for (int r = 0; r < 4; ++r) acc[4][r] += __shfl_xor(acc[5][r], 32);
  STAMP(7,2);
  const int t = 32 * (T >> 1) + 8 * (li >> 2) + 4 * (T & 1) + (li & 3);
  char* o = out + (size_t)t * 1152 + (72 * j + 4 * q) * 2;
#pragma unroll
  for (int n = 0; n < 5; ++n) {
    unsigned a = pk_bf16(acc[n][0], acc[n][1]), c = pk_bf16(acc[n][2], acc[n][3]);
    u32x2 v = {pk_bf16(bf_lo(hv[n][0]) + bf_lo(a), bf_hi(hv[n][0]) + bf_hi(a)), pk_bf16(bf_lo(hv[n][1]) + bf_lo(c), bf_hi(hv[n][1]) + bf_hi(c))};
    if (n < 4 || q < 2) *(u32x2*)(o + n * 32) = v;
  }
  STAMP(7,3);
}

extern "C" void kernel_launch(void* const* d_in, const int* in_sizes, int n_in, void* d_out, int out_size, void* d_ws, size_t ws_size,
                              hipStream_t stream) {
  char* ws = (char*)d_ws;
  (void)hipFuncSetAttribute((const void*)k_conv, hipFuncAttributeMaxDynamicSharedMemorySize, CV_LDS);
  (void)hipFuncSetAttribute((const void*)k_mix, hipFuncAttributeMaxDynamicSharedMemorySize, M_LDS);
  (void)hipFuncSetAttribute((const void*)k_out, hipFuncAttributeMaxDynamicSharedMemorySize, G_LDS);
  k_prep<<<NB_LN + NB_WI + NB_WO + D, 256, 0, stream>>>((const char*)d_in[0], (const char*)d_in[1], (const char*)d_in[2], (const char*)d_in[3],
      (const char*)d_in[4], (const char*)d_in[5], (const char*)d_in[6], (const char*)d_in[7], (const char*)d_in[10], (const char*)d_in[13], ws);
  k_conv<<<NB_CV + NB_TOUCH, 256, CV_LDS, stream>>>((const char*)d_in[8], (const char*)d_in[11], ws);
  k_mix<<<256, 768, M_LDS, stream>>>((const char*)d_in[11], (const char*)d_in[12], ws);
  k_out<<<256, 512, G_LDS, stream>>>(ws, (char*)d_out);
}
```

```cpp
#include <hip/hip_runtime.h>
#include <hip/hip_bf16.h>
#include <stdint.h>

typedef __attribute__((ext_vector_type(8))) __bf16 bf16x8;
typedef __attribute__((ext_vector_type(2))) __bf16 bf16x2;
typedef __attribute__((ext_vector_type(8))) _Float16 f16x8;
typedef __attribute__((ext_vector_type(2))) _Float16 f16x2;
typedef __attribute__((ext_vector_type(4))) float f32x4;
typedef __attribute__((ext_vector_type(2))) float f32x2;
typedef __attribute__((ext_vector_type(4))) unsigned u32x4;
typedef __attribute__((ext_vector_type(2))) unsigned u32x2;
typedef __attribute__((ext_vector_type(4))) short s16x4;
#define LDSP __attribute__((address_space(3)))
#define DEVI __device__ __forceinline__

constexpr int L = 2048, D = 576, NM = 128, F = 4608, FH = 2304;
constexpr float EPS = 1e-5f;
constexpr unsigned O_EP = 0;
constexpr int NREP = 32;
constexpr unsigned O_F1 = 256;
constexpr unsigned O_F2 = O_F1 + NREP * 1024;
constexpr unsigned O_ZFL = O_F2 + NREP * 2048;
constexpr unsigned O_PFL = O_ZFL + 32768;
constexpr unsigned O_CS = 167936;
static_assert(O_PFL + 32768 <= O_CS, "ws");
constexpr unsigned O_ST = O_CS + F * 8;
constexpr unsigned O_HS = O_ST + 262144;
constexpr unsigned O_U = O_HS + 16384;
constexpr unsigned O_XB = O_U + L * D * 2;
constexpr unsigned O_WI = O_XB + L * D * 2;
constexpr unsigned O_WO = O_WI + D * F * 2;
constexpr unsigned O_HF = O_WO + FH * D * 2;
constexpr unsigned O_ZF = O_HF + L * D * 2;
constexpr unsigned O_PF = O_ZF + L * FH * 2;
constexpr unsigned WS_USED = O_PF + L * FH * 2;
constexpr size_t O_STAMP = 128u << 20;
#define STAMP(k,i)
#define STAMPW(k,i,W)
#define RSTAMP(k,i)
constexpr unsigned SENT = 0xFFFFFFFFu;

DEVI unsigned pk_bf16(float a, float b) { f32x2 v = {a, b}; return __builtin_bit_cast(unsigned, __builtin_convertvector(v, bf16x2)); }
DEVI unsigned pk_f16(float a, float b) { f32x2 v = {a, b}; return __builtin_bit_cast(unsigned, __builtin_convertvector(v, f16x2)); }
DEVI float bf_lo(unsigned u) { return __builtin_bit_cast(float, u << 16); }
DEVI float bf_hi(unsigned u) { return __builtin_bit_cast(float, u & 0xFFFF0000u); }
DEVI float asf(unsigned u) { return __builtin_bit_cast(float, u); }
DEVI unsigned asu(float f) { return __builtin_bit_cast(unsigned, f); }
DEVI float rbf(float a) { return bf_lo(pk_bf16(a, a)); }
DEVI float h_lo(unsigned u) { return (float)__builtin_bit_cast(f16x2, u)[0]; }
DEVI float h_hi(unsigned u) { return (float)__builtin_bit_cast(f16x2, u)[1]; }
template <int CTRL> DEVI float dpp_add(float v) {
  return v + __builtin_bit_cast(float, __builtin_amdgcn_update_dpp(0, __builtin_bit_cast(int, v), CTRL, 0xF, 0xF, true));
}
DEVI float row16_sum(float v) { v = dpp_add<0x128>(v); v = dpp_add<0x124>(v); v = dpp_add<0x122>(v); v = dpp_add<0x121>(v); return v; }
DEVI float q_sum(float v) { v += __shfl_xor(v, 16); v += __shfl_xor(v, 32); return v; }
DEVI u32x2 tr_read(const char* p) {
  return __builtin_bit_cast(u32x2, __builtin_amdgcn_ds_read_tr16_b64_v4i16((LDSP s16x4*)p));
}
DEVI f32x4 mfma_bf(u32x4 a, u32x4 b, f32x4 c) {
  return __builtin_amdgcn_mfma_f32_16x16x32_bf16(__builtin_bit_cast(bf16x8, a), __builtin_bit_cast(bf16x8, b), c, 0, 0, 0);
}
DEVI f32x4 mfma_h(u32x4 a, u32x4 b, f32x4 c) {
  return __builtin_amdgcn_mfma_f32_16x16x32_f16(__builtin_bit_cast(f16x8, a), __builtin_bit_cast(f16x8, b), c, 0, 0, 0);
}
DEVI void ln_scalars(float mean, float var, float& mb, float& rinv) {
  mb = rbf(mean);
  float d = rbf(__builtin_amdgcn_sqrtf(rbf(rbf(var) + EPS)));
  rinv = __builtin_amdgcn_rcpf(d);
}
DEVI float gelu(float x) {
  float w = x * __builtin_fmaf(x * x, -0.10294324f, -2.3022082f);
  return x * __builtin_amdgcn_rcpf(1.0f + __builtin_amdgcn_exp2f(w));
}

typedef unsigned long long u64;
typedef __amdgpu_buffer_rsrc_t rsrc_t;
constexpr int SPIN = 1 << 16;
template <int CTRL> DEVI float dpp_get(float v) {
  return __builtin_bit_cast(float, __builtin_amdgcn_update_dpp(0, __builtin_bit_cast(int, v), CTRL, 0xF, 0xF, true));
}
constexpr float GC1 = -0.10294324f, GC0 = -2.3022082f;
DEVI f32x2 gelu2(f32x2 x, f32x2 k1, f32x2 k0) {
  f32x2 w = x * __builtin_elementwise_fma(x * x, k1, k0);
  f32x2 d = f32x2{__builtin_amdgcn_exp2f(w[0]), __builtin_amdgcn_exp2f(w[1])} + f32x2{1.f, 1.f};
  return x * f32x2{__builtin_amdgcn_rcpf(d[0]), __builtin_amdgcn_rcpf(d[1])};
}
DEVI unsigned pk2(f32x2 v) { return __builtin_bit_cast(unsigned, __builtin_convertvector(v, bf16x2)); }
DEVI f32x2 up2(unsigned u) { return f32x2{bf_lo(u), bf_hi(u)}; }
DEVI f32x2 lo2(f32x4 v) { return __builtin_shufflevector(v, v, 0, 1); }
DEVI f32x2 hi2(f32x4 v) { return __builtin_shufflevector(v, v, 2, 3); }
DEVI float dot2(unsigned a, unsigned b, float c) {
  return __builtin_amdgcn_fdot2_f32_bf16(__builtin_bit_cast(bf16x2, a), __builtin_bit_cast(bf16x2, b), c, false);
}
DEVI void st16(rsrc_t r, unsigned voff, unsigned soff, u32x4 v) { __builtin_amdgcn_raw_buffer_store_b128(v, r, voff + soff, 0, 16); }
DEVI void st8(rsrc_t r, unsigned voff, unsigned soff, u32x2 v) { __builtin_amdgcn_raw_buffer_store_b64(v, r, voff, soff, 16); }
DEVI void st4(rsrc_t r, unsigned voff, unsigned soff, unsigned v) { __builtin_amdgcn_raw_buffer_store_b32(v, r, voff, soff, 16); }
DEVI u32x4 ld16(rsrc_t r, unsigned voff, unsigned soff) { return __builtin_amdgcn_raw_buffer_load_b128(r, voff, soff, 16); }
DEVI u32x2 ld8(rsrc_t r, unsigned voff, unsigned soff) { return __builtin_amdgcn_raw_buffer_load_b64(r, voff, soff, 16); }
template <int N> DEVI void wait_vm() { asm volatile("s_waitcnt vmcnt(%0)" ::"i"(N) : "memory"); }
DEVI void bar() { __builtin_amdgcn_sched_barrier(0); asm volatile("s_waitcnt lgkmcnt(0)\n\ts_barrier" ::: "memory"); __builtin_amdgcn_sched_barrier(0); }
DEVI int fresh_lane() { int l; asm volatile("v_mbcnt_lo_u32_b32 %0, -1, 0\n\tv_mbcnt_hi_u32_b32 %0, -1, %0" : "=v"(l)); return l; }
DEVI unsigned lds_addr(const char* p) { return (unsigned)(uintptr_t)(LDSP const char*)p; }
template <int NL> DEVI void wait_flags(rsrc_t r, unsigned off, int n, u32x2 tag, int lane) {
#pragma unroll 1
  for (int spin = 0; spin < SPIN; ++spin) {
    bool ok = true;
#pragma unroll
    for (int k = 0; k < NL; ++k) {
      int i = min(lane + 64 * k, n / 2 - 1);
      u32x4 v = ld16(r, i * 16, off);
      ok = ok && v[0] == tag[0] && v[1] == tag[1] && v[2] == tag[0] && v[3] == tag[1];
    }
    if (__all(ok)) break;
    __builtin_amdgcn_s_sleep(1);
    asm volatile("" ::: "memory");
  }
  asm volatile("" ::: "memory");
}
template <bool SC> DEVI void glds4(unsigned m0v, const char* base, unsigned v0, unsigned v1, unsigned v2, unsigned v3) {
  unsigned keep;
  if (SC)
    asm volatile("s_mov_b32 %0, m0\n\ts_mov_b32 m0, %1\n\ts_nop 0\n\t"
                 "global_load_lds_dwordx4 %3, %2 sc1\n\tglobal_load_lds_dwordx4 %4, %2 offset:1024 sc1\n\t"
                 "global_load_lds_dwordx4 %5, %2 offset:2048 sc1\n\tglobal_load_lds_dwordx4 %6, %2 offset:3072 sc1\n\ts_mov_b32 m0, %0"
                 : "=&s"(keep) : "s"(m0v), "s"(base), "v"(v0), "v"(v1), "v"(v2), "v"(v3) : "memory");
  else
    asm volatile("s_mov_b32 %0, m0\n\ts_mov_b32 m0, %1\n\ts_nop 0\n\t"
                 "global_load_lds_dwordx4 %3, %2\n\tglobal_load_lds_dwordx4 %4, %2 offset:1024\n\t"
                 "global_load_lds_dwordx4 %5, %2 offset:2048\n\tglobal_load_lds_dwordx4 %6, %2 offset:3072\n\ts_mov_b32 m0, %0"
                 : "=&s"(keep) : "s"(m0v), "s"(base), "v"(v0), "v"(v1), "v"(v2), "v"(v3) : "memory");
}
DEVI void glds1(unsigned m0v, const char* base, unsigned v0) {
  unsigned keep;
  asm volatile("s_mov_b32 %0, m0\n\ts_mov_b32 m0, %1\n\ts_nop 0\n\tglobal_load_lds_dwordx4 %3, %2 sc1\n\ts_mov_b32 m0, %0"
               : "=&s"(keep) : "s"(m0v), "s"(base), "v"(v0) : "memory");
}
template <int NP> DEVI void ld_lin(unsigned dst, const char* src, unsigned l16) {
  glds4<true>(dst, src, l16, l16, l16, l16);
  glds4<true>(dst + 4096, src + 4096, l16, l16, l16, l16);
  if (NP == 9) glds1(dst + 8192, src + 8192, l16);
}

constexpr int TP = 1168;
constexpr int CV_K = 8 * 260 * 16, CV_U = 4576;
constexpr int C_U = 4 * CV_K, C_KD = C_U + 4 * CV_U, C_MX = C_KD + 4 * 1536, C_END = C_MX + 64;
constexpr int KT = 528;
constexpr int STG = 34816, RING = 4 * STG;
constexpr int X_CS = RING, X_LN = X_CS + 1152, X_PV = X_LN + 1024, X_CNT = 158720;
constexpr int LDS_BYTES = X_CNT + 64;
static_assert(X_PV + 16384 <= X_CNT && LDS_BYTES <= 163840, "lds");
#define TPOLL 12
#define TCHK 13
#define TFLAG 15

struct Fr { u32x4 m[2], n[9]; };
struct Ctx { const char* sm; int am0[4], am1, an; };
template <int MK> DEVI void ldf(Fr& f, const Ctx& c, int t, int kk) {
  int so = (t & 3) * STG;
  asm volatile("" : "+s"(so));
  const char* slot = c.sm + so;
  if (MK == 0) {
#pragma unroll
    for (int m = 0; m < 2; ++m) {
      u32x2 a = *(const u32x2*)(slot + c.am0[2 * m] + kk * 8192), b = *(const u32x2*)(slot + c.am0[2 * m + 1] + kk * 8192);
      f.m[m] = u32x4{a[0], a[1], b[0], b[1]};
    }
  } else {
#pragma unroll
    for (int m = 0; m < 2; ++m) f.m[m] = *(const u32x4*)(slot + (c.am1 ^ (kk * 64)) + m * 2048);
  }
#pragma unroll
  for (int n = 0; n < 9; ++n) f.n[n] = *(const u32x4*)(slot + c.an + (kk * 9 + n) * 1024);
}
template <int MK, int ORI, bool LD> DEVI void seg(f32x4 (&acc)[2][9], const Fr& cur, Fr& nxt, const Ctx& c, int t, int kk) {
  if (LD) ldf<MK>(nxt, c, t, kk);
#pragma unroll
  for (int m = 0; m < 2; ++m)
#pragma unroll
    for (int nn = 0; nn < 9; ++nn) {
      const int n = m ? 8 - nn : nn;
      acc[m][n] = ORI == 0 ? mfma_bf(cur.m[m], cur.n[n], acc[m][n]) : mfma_bf(cur.n[n], cur.m[m], acc[m][n]);
    }
#pragma unroll
  for (int i = 0; i < (MK == 0 ? 13 : 11); ++i) { __builtin_amdgcn_sched_group_barrier(0x008, 1, 0); __builtin_amdgcn_sched_group_barrier(0x100, 1, 0); }
  __builtin_amdgcn_sched_barrier(0);
}
DEVI f32x2 cmul(f32x2 a, f32x2 b, f32x2 bp) {
  return __builtin_elementwise_fma(f32x2{a[1], a[1]}, bp, f32x2{a[0], a[0]} * b);
}
DEVI f32x2 perp(f32x2 b) { return f32x2{-b[1], b[0]}; }
DEVI f32x2 cexp(float ar, float ai, float l) {
  float m = __builtin_amdgcn_exp2f(ar * l), ph = __builtin_amdgcn_fractf(ai * l);
  return f32x2{m * __builtin_amdgcn_cosf(ph), m * __builtin_amdgcn_sinf(ph)};
}
DEVI unsigned pkh(f32x2 v) { return __builtin_bit_cast(unsigned, __builtin_convertvector(v, f16x2)); }
DEVI float ldbf(const char* p, int i) { return bf_lo(*(const unsigned short*)(p + 2 * i)); }

__global__ __launch_bounds__(512, 2) void k_all(const char* __restrict__ x, const char* __restrict__ a_re, const char* __restrict__ a_im,
    const char* __restrict__ b_re, const char* __restrict__ b_im, const char* __restrict__ c_re, const char* __restrict__ c_im,
    const char* __restrict__ log_dt, const char* __restrict__ dskip, const char* __restrict__ w_in, const char* __restrict__ w_sp,
    const char* __restrict__ b_sp, const char* __restrict__ w_out, char* __restrict__ out, char* __restrict__ ws) {
  extern __shared__ __attribute__((aligned(16))) char sm[];
  const int tid = threadIdx.x, lane = tid & 63, w = __builtin_amdgcn_readfirstlane(tid >> 6), li = lane & 15, q = lane >> 4;
  const int bid = blockIdx.x, xc = bid & 7, ii = bid >> 3;
  const rsrc_t R = __builtin_amdgcn_make_buffer_rsrc(ws, 0, WS_USED, 0x00020000);
  STAMP(0,0);
  const u32x2 ep = ld8(R, 0, O_EP);
  const bool is_conv = ii < 18;
  const int cg = xc * 18 + ii, rep = bid & (NREP - 1);
  if (tid == 0) *(u32x4*)(sm + X_CNT) = u32x4{0, 0, 0, 0};
  auto mk_tag = [&]() {
    u64 e = (((u64)__builtin_amdgcn_readfirstlane(ep[1]) << 32) | (unsigned)__builtin_amdgcn_readfirstlane(ep[0])) + 0x9E3779B97F4A7C15ull;
    return u32x2{(unsigned)e, (unsigned)(e >> 32)};
  };
  u32x2 tag;
  auto arrive = [&](int slot, unsigned n, unsigned flag_off, unsigned stride, int nrep) {
    wait_vm<0>();
    const int lane = fresh_lane();
    unsigned old = 0;
    if (lane == 0) old = __hip_atomic_fetch_add((unsigned*)(sm + X_CNT) + slot, 1u, __ATOMIC_RELAXED, __HIP_MEMORY_SCOPE_WORKGROUP);
    if (__builtin_amdgcn_readfirstlane(old) == n - 1 && lane < nrep) st8(R, lane * stride, flag_off, tag);
  };
  if (is_conv) {
    const int ch = w & 3, dch = 4 * cg + ch;
    char* sk = sm + ch * CV_K;
    const char* su = sm + C_U + ch * CV_U;
    float sc = 0.f;
    if (w < 4) {
      const int pi = (dch * NM + 2 * lane) * 2;
      const unsigned v_ar = *(const unsigned*)(a_re + pi), v_ai = *(const unsigned*)(a_im + pi), v_br = *(const unsigned*)(b_re + pi),
                     v_bi = *(const unsigned*)(b_im + pi), v_cr = *(const unsigned*)(c_re + pi), v_ci = *(const unsigned*)(c_im + pi);
      const float p_dk = ldbf(dskip, dch);
      const float dt = rbf(__expf(ldbf(log_dt, dch)));
      tag = mk_tag();
      STAMP(0,1);
      const f32x2 ar = (up2(v_ar) * dt) * 1.44269504f, ai = (up2(v_ai) * dt) * 0.15915494f;
      const f32x2 br = up2(v_br) * dt, bi = up2(v_bi) * dt, cr = up2(v_cr), ci = up2(v_ci);
      const f32x2 cbr = cr * br - ci * bi, cbi = -(cr * bi + ci * br);
      float mx = fmaxf(fmaxf(fabsf(cbr[0]), fabsf(cbi[0])), fmaxf(fabsf(cbr[1]), fabsf(cbi[1])));
      mx = fmaxf(mx, dpp_get<0x128>(mx)); mx = fmaxf(mx, dpp_get<0x124>(mx)); mx = fmaxf(mx, dpp_get<0x122>(mx)); mx = fmaxf(mx, dpp_get<0x121>(mx));
      auto rl = [&](int l) { return asf(__builtin_amdgcn_readlane((int)asu(mx), l)); };
      sc = fmaxf(fmaxf(rl(0), rl(16)), fmaxf(rl(32), rl(48)));
      sc = fmaxf(sc, fabsf(p_dk) * (1.0f / 256));
      const float isc = sc > 0.f ? 1.0f / sc : 0.f;
      {
        struct C2 { f32x2 a, b; };
        char* tb = sk + lane * 8;
        auto put = [&](int row, C2 v) { *(u32x2*)(tb + row * KT) = u32x2{pkh(v.a), pkh(v.b)}; };
        auto mul = [&](C2 v, C2 u, C2 up) { return C2{cmul(v.a, u.a, up.a), cmul(v.b, u.b, up.b)}; };
        auto prp = [&](C2 v) { return C2{perp(v.a), perp(v.b)}; };
        auto ex = [&](float l) { return C2{cexp(ar[0], ai[0], l), cexp(ar[1], ai[1], l)}; };
        const C2 w1 = ex(1.f), w128 = ex(128.f);
        C2 w8 = ex(8.f); w8.a[1] = -w8.a[1]; w8.b[1] = -w8.b[1];
        const C2 w1p = prp(w1), w8p = prp(w8), w128p = prp(w128);
        C2 y = {f32x2{cbr[0], cbi[0]} * isc, f32x2{cbr[1], cbi[1]} * isc};
        C2 E[8]; E[0] = C2{f32x2{1.f, 0.f}, f32x2{1.f, 0.f}}; E[1] = w1;
#pragma unroll
        for (int e = 2; e < 8; ++e) E[e] = mul(E[e - 1], w1, w1p);
#pragma unroll
        for (int e = 0; e < 8; ++e) { put(e, E[e]); put(8 + e, e ? mul(E[e], w128, w128p) : w128); put(41 + e, y); y = mul(y, w8, w8p); }
        const C2 e2p = prp(E[2]), e4p = prp(E[4]), e6p = prp(E[6]);
        C2 H = w128;
#pragma unroll
        for (int a = 2; a <= 16; ++a) {
          H = mul(H, w128, w128p);
          if (a < 4) { put(8 + 4 * a, H); put(9 + 4 * a, mul(H, E[2], e2p)); put(10 + 4 * a, mul(H, E[4], e4p)); put(11 + 4 * a, mul(H, E[6], e6p)); }
          else if (a < 8) { put(16 + 2 * a, H); put(17 + 2 * a, mul(H, E[4], e4p)); }
          else put(24 + a, H);
          if (a < 10) { put(47 + a, y); if (a < 9) y = mul(y, w8, w8p); }
        }
      }
      asm volatile("" ::: "memory");
      STAMP(0,2);
      {
        f32x4 kk[3] = {};
        const char* pa = sk + li * KT + q * 16;
#pragma unroll
        for (int s = 0; s < 8; ++s) {
          u32x4 fb = *(const u32x4*)(pa + 41 * KT + s * 64);
#pragma unroll
          for (int t = 0; t < 3; ++t) kk[t] = mfma_h(*(const u32x4*)(pa + 16 * t * KT + s * 64), fb, kk[t]);
        }
        if (lane == 0) kk[0][0] += p_dk * isc;
        char* kd = sm + C_KD + ch * 1536;
        *(u32x2*)(kd + (128 * (q >> 1) + 4 * (q & 1) + 8 * li) * 2) = u32x2{pkh(lo2(kk[0])), pkh(hi2(kk[0]))};
        const int i1 = q < 2 ? 256 + 64 * q + 4 * li : 384 + 64 * (q - 2) + 2 * li;
        *(unsigned*)(kd + i1 * 2) = pkh(lo2(kk[1])); *(unsigned*)(kd + (i1 + (q < 2 ? 2 : 32)) * 2) = pkh(hi2(kk[1]));
#pragma unroll
        for (int r = 0; r < 4; ++r) *(_Float16*)(kd + (512 + 16 * (4 * q + r) + li) * 2) = (_Float16)kk[2][r];
      }
      asm volatile("" ::: "memory");
      u32x4 c0[4];
      {
        const unsigned short* kd = (const unsigned short*)(sm + C_KD + ch * 1536);
#pragma unroll
        for (int rr = 0; rr < 2; ++rr) {
          const int k = 255 - 64 * rr - lane;
          const unsigned a = kd[384 + k], b = kd[385 + k], aa = a | (a << 16);
          c0[rr] = u32x4{b | (b << 16), b | (((k & 1) ? b : a) << 16), aa, aa};
        }
        { const int k = 127 - lane;
          const unsigned ab = *(const unsigned*)(kd + 256 + 2 * k), cc = kd[258 + 2 * k];
          c0[2] = u32x4{cc | (cc << 16), __builtin_amdgcn_perm(ab, ab, 0x03020302), __builtin_amdgcn_perm(ab, ab, 0x01000302), __builtin_amdgcn_perm(ab, ab, 0x01000100)}; }
        { const int k = 63 - lane;
          const u32x4 v = *(const u32x4*)(kd + 8 * (k & 31));
          const u32x2 u = *(const u32x2*)(kd + 128 + 4 * k); const unsigned e = kd[132 + 4 * k];
          c0[3] = lane < 32 ? u32x4{__builtin_amdgcn_perm(u[1], e, 0x07060100), __builtin_amdgcn_perm(u[1], u[1], 0x01000100), __builtin_amdgcn_perm(u[0], u[1], 0x07060100), __builtin_amdgcn_perm(u[0], u[0], 0x01000100)}
                            : u32x4{__builtin_amdgcn_alignbit(v[3], v[3], 16), __builtin_amdgcn_alignbit(v[2], v[2], 16), __builtin_amdgcn_alignbit(v[1], v[1], 16), __builtin_amdgcn_alignbit(v[0], v[0], 16)}; }
#pragma unroll
        for (int rr = 0; rr < 4; ++rr) *(u32x4*)(sk + (64 * rr + lane) * 16) = c0[rr];
        if (lane < 8) *(u32x4*)(sk + (lane * 260 + 256) * 16) = u32x4{0, 0, 0, 0};
      }
      asm volatile("" ::: "memory");
      STAMP(0,3);
      u32x4 c1[4];
#pragma unroll
      for (int rr = 0; rr < 4; ++rr) c1[rr] = *(const u32x4*)(sk + (64 * rr + lane) * 16 + 16);
#pragma unroll
      for (int rr = 0; rr < 4; ++rr) {
        char* p = sk + (64 * rr + lane) * 16;
        unsigned e[8] = {c0[rr][0], c0[rr][1], c0[rr][2], c0[rr][3], c1[rr][0], c1[rr][1], c1[rr][2], c1[rr][3]}, o[7];
#pragma unroll
        for (int i = 0; i < 7; ++i) o[i] = __builtin_amdgcn_alignbit(e[i + 1], e[i], 16);
#pragma unroll
        for (int s = 0; s < 4; ++s) {
          if (s) *(u32x4*)(p + (2 * s) * 260 * 16) = u32x4{e[s], e[s + 1], e[s + 2], e[s + 3]};
          *(u32x4*)(p + (2 * s + 1) * 260 * 16) = u32x4{o[s], o[s + 1], o[s + 2], o[s + 3]};
        }
      }
      STAMP(0,4);
    } else {
      tag = mk_tag();
      if (cg < 136 && tid < 384) st16(R, (tid - 256) * 16, O_ST + cg * 2048, u32x4{SENT, SENT, SENT, SENT});
      if (lane < 30) *(u32x4*)(sm + C_U + ch * CV_U + lane * 16) = u32x4{0, 0, 0, 0};
      if (w == 4) {
        wait_flags<1>(R, O_F1 + rep * 1024, 64, tag, lane);
        STAMPW(5,0,4);
        const unsigned v0 = (lane >> 2) * 36864 + (lane & 3) * 16, dv = 16 * 36864 - 1024;
#pragma unroll
        for (int i = 0; i < 4; ++i) glds4<true>(lds_addr(sm) + C_U + i * CV_U + 480, ws + O_U + (4 * cg + i) * 64, v0, v0 + dv, v0 + 2 * dv, v0 + 3 * dv);
        wait_vm<0>();
      }
    }
    bar();
    STAMP(0,5);
    u32x4 xv[4];
    if (w < 4) {
      f32x4 acc[8] = {};
      const char* pa = sk + ((7 - (li & 7)) * 260 + 253 - (li >> 3) + q) * 16 - 4032;
      const char* pb = su + li * 32 + q * 16;
      struct CF { u32x4 a[8], b[8]; } A, B;
      auto ldc = [&](CF& f, int a0) {
#pragma unroll
        for (int m = 0; m < 8; ++m) { f.a[m] = *(const u32x4*)(pa + 4032 - 64 * a0 - 512 * m); f.b[m] = *(const u32x4*)(pb + 448 - 64 * a0 + 512 * m); }
      };
      auto mmc = [&](const CF& f) {
#pragma unroll
        for (int m = 0; m < 8; ++m)
#pragma unroll
          for (int t = m; t < 8; ++t) acc[t] = mfma_h(f.a[m], f.b[t - m], acc[t]);
#pragma unroll
        for (int i = 0; i < 16; ++i) { __builtin_amdgcn_sched_group_barrier(0x008, 2, 0); __builtin_amdgcn_sched_group_barrier(0x100, 1, 0); }
        __builtin_amdgcn_sched_barrier(0);
      };
      ldc(A, 0);
      __builtin_amdgcn_sched_barrier(0);
#pragma unroll
      for (int a0 = 0; a0 < 8; a0 += 2) { ldc(B, a0 + 1); mmc(A); if (a0 < 6) ldc(A, a0 + 2); mmc(B); }
      STAMP(0,6);
#pragma unroll
      for (int t = 0; t < 8; ++t) *(u32x2*)(sk + t * 512 + li * 32 + q * 8) = u32x2{pk2(lo2(acc[t]) * sc), pk2(hi2(acc[t]) * sc)};
    } else {
      wait_flags<1>(R, O_F1 + rep * 1024 + 512, 64, tag, lane);
#pragma unroll
      for (int r = 0; r < 4; ++r) {
        const int T = ((tid & 255) >> 3) + 32 * r, ip = tid & 7;
        xv[r] = ld16(R, (32 * (T >> 1) + 8 * (ip >> 1) + 4 * (T & 1) + 2 * (ip & 1)) * 8, O_XB + cg * 16384);
      }
      STAMPW(2,0,4);
    }
    bar();
    if (w >= 4) {
#pragma unroll
      for (int r = 0; r < 4; ++r) {
        const int T = ((tid & 255) >> 3) + 32 * r, ip = tid & 7;
        const char* p = sm + (32 * (T >> 1) + 8 * (ip >> 1) + 4 * (T & 1) + 2 * (ip & 1)) * 2;
        const unsigned c0 = *(const unsigned*)p, c1 = *(const unsigned*)(p + CV_K), c2 = *(const unsigned*)(p + 2 * CV_K), c3 = *(const unsigned*)(p + 3 * CV_K);
        u32x4 v = {pk2(up2(xv[r][0]) + f32x2{bf_lo(c0), bf_lo(c1)}), pk2(up2(xv[r][1]) + f32x2{bf_lo(c2), bf_lo(c3)}),
                   pk2(up2(xv[r][2]) + f32x2{bf_hi(c0), bf_hi(c1)}), pk2(up2(xv[r][3]) + f32x2{bf_hi(c2), bf_hi(c3)})};
        st16(R, (T >> 3) * (18 * 8192) + (T & 7) * 1024 + ip * 16, O_HF + (cg >> 3) * 8192 + (cg & 1) * 512 + ((cg >> 1) & 3) * 128, v);
      }
      STAMPW(2,1,4);
      arrive(1, 4, O_F2 + cg * 8, 2048, NREP);
      STAMPW(2,2,4);
    }
  } else {
    const int tj = (ii - 18) * 8 + xc;
    if (tj < 64) {
      char* s0 = sm + 83968;
      const char* src = x + (size_t)(32 * tj + (tid >> 4)) * 1152 + li * 8;
      u32x2 xr[9];
#pragma unroll
      for (int k = 0; k < 9; ++k) xr[k] = *(const u32x2*)(src + k * 128);
      asm volatile("" :: "v"(xr[8])); STAMP(1,0);
      float a[4] = {0, 0, 0, 0};
#pragma unroll
      for (int k = 0; k < 9; ++k)
#pragma unroll
        for (int e = 0; e < 2; ++e) { a[e] = dot2(xr[k][e], 0x3F803F80u, a[e]); a[2 + e] = dot2(xr[k][e], xr[k][e], a[2 + e]); }
      const float s = row16_sum(a[0] + a[1]), ss = row16_sum(a[2] + a[3]);
      float mean = s * (1.0f / D), mb, rinv;
      ln_scalars(mean, fmaxf(ss * (1.0f / D) - mean * mean, 0.f), mb, rinv);
      const f32x2 r2 = {rinv, rinv}, b2 = {-mb * rinv, -mb * rinv};
      char* p = s0 + (tid >> 4) * TP + li * 8;
#pragma unroll
      for (int k = 0; k < 9; ++k) {
        *(u32x2*)(p + k * 128) = u32x2{pkh(__builtin_elementwise_fma(up2(xr[k][0]), r2, b2)), pkh(__builtin_elementwise_fma(up2(xr[k][1]), r2, b2))};
        *(u32x2*)(p + 32 * TP + k * 128) = xr[k];
      }
      tag = mk_tag();
      bar();
      STAMP(1,1);
      if (w < 4) {
        const char* trb = s0 + (8 * q + (li >> 2)) * TP + (li & 3) * 8 + w * 288;
        const unsigned uo = li * 64 + q * 16 + O_U + tj * 36864 + w * 9216;
#pragma unroll
        for (int k = 0; k < 9; ++k) {
          u32x2 lo = tr_read(trb + k * 32), hi = tr_read(trb + 4 * TP + k * 32);
          st16(R, uo, k * 1024, u32x4{lo[0], lo[1], hi[0], hi[1]});
        }
        STAMP(1,2);
        arrive(0, 4, O_F1 + tj * 8, 1024, NREP);
        STAMP(1,3);
      } else {
        const int c16 = (tid >> 4) & 15;
        const char* px = s0 + (32 + li * 2) * TP + c16 * 8;
#pragma unroll
        for (int k = 0; k < 9; ++k) {
          u32x2 a = *(const u32x2*)(px + k * 128), b = *(const u32x2*)(px + TP + k * 128);
          st16(R, (c16 + 16 * k) * 16384 + li * 16, O_XB + tj * 256, u32x4{a[0], a[1], b[0], b[1]});
        }
        arrive(3, 4, O_F1 + (64 + tj) * 8, 1024, NREP);
        STAMPW(1,4,4);
      }
      if (tj < 36) {
        const int par = w >> 2, G = 2 * tj + par, row = (tid >> 3) & 31, uu = G / 9, idx = G % 9, hh = row >> 4;
        const int panel = idx < 4 ? 2 * uu : (idx < 8 ? 2 * uu + 1 : 2 * uu + hh), tile = idx < 8 ? 2 * (idx & 3) + hh : 8;
        const char* wsrc = w_out + (size_t)(144 * panel + 16 * tile + (row & 15)) * 1152 + (tid & 7) * 16;
        u32x4 v[9];
#pragma unroll
        for (int k = 0; k < 9; ++k) v[k] = *(const u32x4*)(wsrc + k * 128);
#pragma unroll
        for (int k = 0; k < 9; ++k) *(u32x4*)(sm + (tid >> 3) * TP + (tid & 7) * 16 + k * 128) = v[k];
        bar();
        const char* trb = sm + (32 * par + 4 * q + (li >> 2)) * TP + (li & 3) * 8;
#pragma unroll
        for (int jj = 0; jj < 2; ++jj) {
          const int j = 2 * (w & 3) + jj;
          const unsigned dst = O_WO + (j * 36 + tj) * 9216;
#pragma unroll
          for (int n = 0; n < 5; ++n) {
            const int c0 = 72 * j + 16 * n - (n == 4 ? 8 * par : 0);
            u32x2 lo = tr_read(trb + c0 * 2), hi = tr_read(trb + 16 * TP + c0 * 2);
            if (n < 4) st16(R, lane * 16, dst + (4 * par + n) * 1024, u32x4{lo[0], lo[1], hi[0], hi[1]});
            else if ((li >> 3) == par) st16(R, lane * 16, dst + 8192, u32x4{lo[0], lo[1], hi[0], hi[1]});
          }
        }
        arrive(1, 8, O_F2 + (192 + tj) * 8, 2048, NREP);
        STAMP(4,2);
      }
    } else {
      const int ti = tj - 64;
      if (w < 6) {
        const int row = tid / 12, cc = tid % 12;
        const char* src = w_in + (size_t)row * 9216 + ti * 192 + cc * 16;
        u32x4 v[18];
#pragma unroll
        for (int k = 0; k < 18; ++k) v[k] = *(const u32x4*)(src + (size_t)k * 32 * 9216);
#pragma unroll
        for (int k = 0; k < 18; ++k) *(u32x4*)(sm + (row + 32 * k) * 208 + cc * 16) = v[k];
      }
      tag = mk_tag();
      bar();
      STAMP(4,0);
      if (w < 6) {
        const int ct = 6 * ti + w;
        const int hf = ct < 144 ? 1 : 0, cl = ct < 144 ? ct : ct - 144, fp = cl / 9, n = cl % 9;
        const char* trb = sm + (8 * q + (li >> 2)) * 208 + w * 32 + (li & 3) * 8;
        const unsigned dst = lane * 16 + O_WI + ((fp * 2 + hf) * 18 * 9 + n) * 1024;
        float s = 0.f;
#pragma unroll 6
        for (int g = 0; g < 18; ++g) {
          u32x2 lo = tr_read(trb + g * 32 * 208), hi = tr_read(trb + g * 32 * 208 + 4 * 208);
          st16(R, dst, g * 9216, u32x4{lo[0], lo[1], hi[0], hi[1]});
          s = dot2(lo[0], 0x3F803F80u, s); s = dot2(lo[1], 0x3F803F80u, s); s = dot2(hi[0], 0x3F803F80u, s); s = dot2(hi[1], 0x3F803F80u, s);
        }
        s = q_sum(s);
        if (q == 0) st4(R, li * 4, O_CS + ct * 64, asu(s));
        arrive(1, 6, O_F2 + (144 + ti) * 8, 2048, NREP);
        STAMP(4,1);
      }
    }
  }
  const int tp = (xc & 1) * 8 + (ii >> 2), fp = (xc >> 1) * 4 + (ii & 3);
  const int t64 = 4 * xc + (ii >> 3), j3 = ii & 7;
  if (w == 0) { wait_flags<1>(R, O_F2 + rep * 2048 + 144 * 8, 48, tag, lane); STAMPW(6,0,0); }
  bar();
  if (w >= 4) {
    const int lw = w - 4;
    const unsigned l16 = lane * 16, base = lds_addr(sm);
    const int ph = ((ii >> 2) + ii) & 3, ph3 = ((ii >> 3) + ii) & 3;
    auto k9 = [&](int t) { return (t & 3) == ph ? (t + 4 < 9 ? t + 4 : ph) : t; };
    auto k32 = [&](int t) { return (t & 3) == ph ? ((t + 4) & 31) : t; };
    auto k18 = [&](int t) { return (t & 3) == ph3 ? (t + 4 < 18 ? t + 4 : ph3) : t; };
    if (lw < 2) {
      constexpr int NP = 8;
      unsigned vo[8];
#pragma unroll
      for (int j = 0; j < 8; ++j) {
        int t = 64 * lw + 32 * (j >> 2) + 4 * ((j >> 1) & 1) + 16 * (j & 1) + 8 * (lane >> 5) + ((lane >> 3) & 3);
        vo[j] = t * 4096 + (((lane & 7) ^ ((lane >> 3) & 7)) * 16) + 3072 - (j & 3) * 1024;
      }
      const char* wsrc = w_sp + (size_t)tp * 128 * 4096 - 3072;
      const char* hsrc = ws + O_HF + tp * 18 * 8192 + lw * 8192;
      const char* psrc = ws + O_PF + t64 * 72 * 4096 + lw * 8192;
      const unsigned dst = base + lw * 8192;
      if (lw == 0) wait_flags<2>(R, O_F2 + rep * 2048, 144, tag, lane);
      STAMPW(5,1,4);
      bar();
#pragma unroll 1
      for (int t = 0; t < 4; ++t) ld_lin<NP>(dst + t * STG, hsrc + k9(t) * 16384, l16);
      wait_vm<3 * NP>(); bar();
#pragma unroll 1
      for (int t = 4; t < 18; ++t) { wait_vm<2 * NP>(); bar(); ld_lin<NP>(dst + (t & 3) * STG, hsrc + k9(t < 9 ? t : t - 9) * 16384, l16); }
#pragma unroll 1
      for (int t = 18; t < 50; ++t) {
        wait_vm<2 * NP>(); bar();
        const unsigned dd = dst + (t & 3) * STG; const char* sp = wsrc + k32(t - 18) * 128;
        glds4<false>(dd, sp, vo[0], vo[1], vo[2], vo[3]); glds4<false>(dd + 4096, sp, vo[4], vo[5], vo[6], vo[7]);
      }
      wait_vm<2 * NP>(); bar(); wait_vm<NP>(); bar(); wait_vm<0>(); bar();
      wait_flags<1>(R, O_PFL + (t64 >> 1) * 2048 + ((t64 & 1) * 8 + j3) * 128, 16, tag, lane);
      STAMPW(5,2,4);
#pragma unroll 1
      for (int t = 50; t < 68; ++t) {
        if (t >= 53) { wait_vm<2 * NP>(); bar(); }
        ld_lin<NP>(dst + (t & 3) * STG, psrc + k18(t - 50) * 16384, l16);
      }
      wait_vm<2 * NP>(); bar(); wait_vm<NP>(); bar(); wait_vm<0>(); bar();
    } else {
      constexpr int NP = 9;
      const char* wi = ws + O_WI + (fp * 2) * 18 * 9216 + (lw - 2) * 9216;
      const char* zs = ws + O_ZF + fp * 64 * 9216 + (lw - 2) * 9216;
      const char* wo = ws + O_WO + j3 * 36 * 9216 + (lw - 2) * 9216;
      const unsigned dst = base + 16384 + (lw - 2) * 9216;
#pragma unroll 1
      for (int t = 0; t < 4; ++t) ld_lin<NP>(dst + t * STG, wi + k9(t) * 18432, l16);
      bar();
      wait_vm<3 * NP>(); bar();
      if (lw == 3) {
        f32x2 a = *(const f32x2*)(sm + X_LN + lane * 8) + *(const f32x2*)(sm + X_LN + 512 + lane * 8);
        a[0] = q_sum(dpp_add<0x128>(a[0])); a[1] = q_sum(dpp_add<0x128>(a[1]));
        float mean = a[0] * (1.0f / D), mb, ri;
        ln_scalars(mean, fmaxf(a[1] * (1.0f / D) - mean * mean, 0.f), mb, ri);
        if (lane < 8) st8(R, lane * 8, O_HS + tp * 1024 + fp * 64, u32x2{asu(ri) == SENT ? 0x7FC00000u : asu(ri), asu(-mb)});
      }
#pragma unroll 1
      for (int t = 4; t < 18; ++t) { wait_vm<2 * NP>(); bar(); ld_lin<NP>(dst + (t & 3) * STG, wi + (t < 9 ? k9(t) : 9 + k9(t - 9)) * 18432, l16); }
      wait_vm<2 * NP>(); bar(); wait_vm<NP>(); bar(); wait_vm<0>(); bar();
      wait_flags<1>(R, O_F2 + rep * 2048 + 192 * 8, 36, tag, lane);
      wait_flags<1>(R, O_ZFL + fp * 2048 + tp * 128, 16, tag, lane);
      STAMPW(6,1,6);
#pragma unroll 1
      for (int t = 18; t < 68; ++t) {
        if (t >= 21) { wait_vm<2 * NP>(); bar(); }
        ld_lin<NP>(dst + (t & 3) * STG, t < 50 ? zs + k32(t - 18) * 18432 : wo + k18(t - 50) * 18432, l16);
      }
      wait_vm<2 * NP>(); bar(); wait_vm<NP>(); bar(); wait_vm<0>(); bar();
    }
    return;
  }
  Ctx c;
  c.sm = sm;
#pragma unroll
  for (int i = 0; i < 4; ++i) { c.am0[i] = (2 * w + (i >> 1)) * 1024 + (i & 1) * 512 + lane * 8; asm volatile("" : "+v"(c.am0[i])); }
  c.am1 = 2 * w * 2048 + li * 128 + ((q ^ (lane & 7)) * 16);
  c.an = 16384 + lane * 16;
  const int prow = 8 * (li >> 2) + (li & 3);
  if (tid < 72) *(u32x4*)(sm + X_CS + tid * 16) = ld16(R, (tid < 36 ? FH * 4 + tid * 16 : (tid - 36) * 16), O_CS + fp * 576);
  bar();
  char* pvb = sm + X_PV + w * 4096;
  {
    u32x4 hv[3];
#pragma unroll
    for (int k = 0; k < 3; ++k)
      hv[k] = ld16(R, (lane >> 5) * 8192 + ((lane >> 2) & 7) * 128 + (lane & 3) * 16, O_HF + (tp * 18 * 8 + (fp >> 1)) * 1024 + (fp & 1) * 64 + min(w + 4 * k, 8) * 16384);
    if (w) hv[2] = u32x4{0, 0, 0, 0};
    f32x4 a = {0, 0, 0, 0};
#pragma unroll
    for (int k = 0; k < 3; ++k)
#pragma unroll
      for (int e = 0; e < 4; ++e) { a[e & 2] = dot2(hv[k][e], 0x3F803F80u, a[e & 2]); a[(e & 2) + 1] = dot2(hv[k][e], hv[k][e], a[(e & 2) + 1]); }
#pragma unroll
    for (int e = 0; e < 4; ++e) a[e] = dpp_add<0x124>(dpp_add<0x128>(a[e]));
    if (li < 4) *(f32x4*)(sm + X_LN + (w * 4 + q) * 64 + li * 16) = a;
  }
  f32x4 acc[2][9] = {};
  Fr X, Y;
  bar();
  STAMP(3,0);
  ldf<0>(X, c, 0, 0);
#pragma unroll 1
  for (int t = 0; t < 8; ++t) {
    seg<0, 0, true>(acc, X, Y, c, t, 1);
    bar();
    seg<0, 0, true>(acc, Y, X, c, t + 1, 0);
  }
  u32x2 ht[2]; u32x4 hc[4];
  auto ld_hs = [&]() {
#pragma unroll
    for (int m = 0; m < 2; ++m) {
      ht[m] = ld8(R, li * 8 + m * 128, O_HS + tp * 1024 + w * 256);
      hc[2 * m] = ld16(R, q * 32 + m * 128, O_HS + tp * 1024 + w * 256); hc[2 * m + 1] = ld16(R, q * 32 + 16 + m * 128, O_HS + tp * 1024 + w * 256);
    }
  };
  ld_hs();
  __builtin_amdgcn_sched_barrier(0);
  seg<0, 0, true>(acc, X, Y, c, 8, 1);
  bar();
  seg<0, 0, true>(acc, Y, X, c, 9, 0);
  STAMP(3,1);
#pragma unroll 1
  for (int spin = 0; spin < SPIN; ++spin) {
    bool ok = ht[0][0] != SENT && ht[0][1] != SENT && ht[1][0] != SENT && ht[1][1] != SENT;
#pragma unroll
    for (int k = 0; k < 4; ++k) ok = ok && hc[k][0] != SENT && hc[k][1] != SENT && hc[k][2] != SENT && hc[k][3] != SENT;
    if (__all(ok)) break;
    __builtin_amdgcn_s_sleep(1);
    asm volatile("" ::: "memory");
    ld_hs();
  }
  const float lrs[2] = {asf(ht[0][0]), asf(ht[1][0])}, lnm[2] = {asf(ht[0][1]), asf(ht[1][1])};
  u32x4 zb[9];
  f32x2 rs[4];
  {
    float cs[9];
#pragma unroll
    for (int n = 0; n < 9; ++n) cs[n] = *(const float*)(sm + X_CS + (16 * n + li) * 4);
    f32x2 nm[4], k1[4], k0[4], S1[4] = {}, S2[4] = {};
#pragma unroll
    for (int jp = 0; jp < 4; ++jp) {
      rs[jp] = f32x2{asf(hc[jp][0]), asf(hc[jp][2])}; nm[jp] = f32x2{asf(hc[jp][1]), asf(hc[jp][3])};
      k0[jp] = rs[jp] * GC0; k1[jp] = rs[jp] * rs[jp] * rs[jp] * GC1;
    }
#pragma unroll
    for (int n = 0; n < 9; ++n) {
#pragma unroll
      for (int jp = 0; jp < 4; ++jp) {
        f32x2 a = (jp & 1) ? hi2(acc[jp >> 1][n]) : lo2(acc[jp >> 1][n]);
        f32x2 z = gelu2(__builtin_elementwise_fma(nm[jp], f32x2{cs[n], cs[n]}, a), k1[jp], k0[jp]);
        zb[n][jp] = pk2(z);
        S1[jp] += z; S2[jp] = __builtin_elementwise_fma(z, z, S2[jp]);
      }
      asm volatile("" : "+v"(zb[n]));
    }
#pragma unroll
    for (int jp = 0; jp < 4; ++jp) { S1[jp] *= rs[jp]; S2[jp] *= rs[jp] * rs[jp]; }
    float v[16];
#pragma unroll
    for (int j = 0; j < 8; ++j) { v[2 * j] = S1[j >> 1][j & 1]; v[2 * j + 1] = S2[j >> 1][j & 1]; }
#pragma unroll
    for (int i = 0; i < 8; ++i) { bool h = li & 8; v[i] = (h ? v[i + 8] : v[i]) + dpp_get<0x128>(h ? v[i] : v[i + 8]); }
#pragma unroll
    for (int i = 0; i < 4; ++i) { bool h = li & 4; v[i] = (h ? v[i + 4] : v[i]) + dpp_get<0x141>(h ? v[i] : v[i + 4]); }
#pragma unroll
    for (int i = 0; i < 2; ++i) { bool h = li & 2; v[i] = (h ? v[i + 2] : v[i]) + dpp_get<0x4E>(h ? v[i] : v[i + 2]); }
    { bool h = li & 1; v[0] = (h ? v[1] : v[0]) + dpp_get<0xB1>(h ? v[0] : v[1]); }
    unsigned tot = asu(v[0]);
    st4(R, lane * 4, O_ST + ((tp * 4 + w) * 16 + fp) * 256, tot == SENT ? 0x7FC00000u : tot);
#pragma unroll
    for (int m = 0; m < 2; ++m)
#pragma unroll
      for (int n = 0; n < 9; ++n) acc[m][n] = f32x4{0, 0, 0, 0};
  }
  STAMP(3,2);
  auto it2 = [&](int t) {
    seg<0, 1, true>(acc, X, Y, c, t, 1);
    bar();
    seg<0, 1, true>(acc, Y, X, c, t + 1, 0);
  };
  const char* sts = ws + O_ST + (tp * 4 + w) * 4096;
#pragma unroll 1
  for (int t = 9; t < TCHK; ++t) {
    if (t == TPOLL) glds4<true>(lds_addr(pvb), sts, lane * 16, lane * 16, lane * 16, lane * 16);
    __builtin_amdgcn_sched_barrier(0);
    it2(t);
  }
  STAMP(3,3);
  u32x4 pv[4];
#pragma unroll 1
  for (int spin = 0; spin < SPIN; ++spin) {
    wait_vm<0>();
    bool ok = true;
#pragma unroll
    for (int k = 0; k < 4; ++k) {
      pv[k] = *(const u32x4*)(pvb + k * 1024 + lane * 16);
      ok = ok && pv[k][0] != SENT && pv[k][1] != SENT && pv[k][2] != SENT && pv[k][3] != SENT;
    }
    if (__all(ok)) break;
    glds4<true>(lds_addr(pvb), sts, lane * 16, lane * 16, lane * 16, lane * 16);
  }
  STAMP(3,4);
  {
    float t4[4];
#pragma unroll
    for (int e = 0; e < 4; ++e) t4[e] = q_sum((asf(pv[0][e]) + asf(pv[1][e])) + (asf(pv[2][e]) + asf(pv[3][e])));
    f32x2 o0, o1;
#pragma unroll
    for (int r = 0; r < 2; ++r) {
      float mean = t4[2 * r] * (1.0f / FH), var = fmaxf(t4[2 * r + 1] * (1.0f / FH) - mean * mean, 0.f), mb, ri;
      ln_scalars(mean, var, mb, ri);
      o0[r] = ri; o1[r] = -mb * ri;
    }
    char* ln = sm + X_LN + w * 256;
    if (lane < 16) { *(f32x2*)(ln + li * 8) = o0; *(f32x2*)(ln + 128 + li * 8) = o1; }
    asm volatile("" ::: "memory");
    f32x4 ri[2], rb[2];
#pragma unroll
    for (int m = 0; m < 2; ++m) { ri[m] = *(const f32x4*)(ln + (8 * q + 4 * m) * 4); rb[m] = *(const f32x4*)(ln + 128 + (8 * q + 4 * m) * 4); }
#pragma unroll
    for (int jp = 0; jp < 4; ++jp) rs[jp] *= (jp & 1) ? hi2(ri[jp >> 1]) : lo2(ri[jp >> 1]);
    const unsigned zo = lane * 16 + O_ZF + (fp * 64 + 4 * tp + w) * 9216;
#pragma unroll
    for (int n = 0; n < 9; ++n) {
      u32x4 o4;
#pragma unroll
      for (int jp = 0; jp < 4; ++jp)
        o4[jp] = pk2(__builtin_elementwise_fma(up2(zb[n][jp]), rs[jp], (jp & 1) ? hi2(rb[jp >> 1]) : lo2(rb[jp >> 1])));
      st16(R, zo, n * 1024, o4);
    }
  }
  STAMP(3,5);
#pragma unroll 1
  for (int t = TCHK; t < 17; ++t) {
    seg<0, 1, true>(acc, X, Y, c, t, 1);
    if (t == TFLAG) wait_vm<0>();
    bar();
    if (t == TFLAG && tid < 16) st8(R, tid * 128, O_ZFL + fp * 2048 + tp * 8, tag);
    seg<0, 1, true>(acc, Y, X, c, t + 1, 0);
  }
  seg<0, 1, true>(acc, X, Y, c, 17, 1);
  seg<0, 1, false>(acc, Y, X, c, 17, 1);
  STAMP(3,6);
  u32x2 z1[2][9];
  float bs[2];
#pragma unroll
  for (int m = 0; m < 2; ++m) bs[m] = ldbf(b_sp, 128 * tp + 32 * w + prow + 4 * m) * lrs[m];
  {
    f32x4 cs[9];
#pragma unroll
    for (int n = 0; n < 9; ++n) cs[n] = *(const f32x4*)(sm + X_CS + (144 + 16 * n + 4 * q) * 4);
#pragma unroll
    for (int n = 0; n < 9; ++n)
#pragma unroll
      for (int m = 0; m < 2; ++m) {
        const float a1 = lrs[m] * lrs[m] * lrs[m] * GC1, a0 = lrs[m] * GC0;
        const f32x2 k1 = {a1, a1}, k0 = {a0, a0}, nm = {lnm[m], lnm[m]};
        z1[m][n][0] = pk2(gelu2(__builtin_elementwise_fma(nm, lo2(cs[n]), lo2(acc[m][n])), k1, k0));
        z1[m][n][1] = pk2(gelu2(__builtin_elementwise_fma(nm, hi2(cs[n]), hi2(acc[m][n])), k1, k0));
        asm volatile("" : "+v"(z1[m][n]));
        acc[m][n] = f32x4{0, 0, 0, 0};
      }
  }
  STAMP(3,7);
  bar();
  STAMP(3,8);
  ldf<1>(X, c, 18, 0);
#pragma unroll 1
  for (int t = 18; t < 49; ++t) {
    seg<1, 1, true>(acc, X, Y, c, t, 1);
    bar();
    seg<1, 1, true>(acc, Y, X, c, t + 1, 0);
  }
  seg<1, 1, true>(acc, X, Y, c, 49, 1);
  seg<1, 1, false>(acc, Y, X, c, 49, 1);
  STAMP(3,9);
  {
    const unsigned pf = O_PF + (((2 * tp + (w >> 1)) * 72 + 9 * (fp >> 1)) * 4 + 2 * (w & 1)) * 1024;
    const unsigned po = lane * 16 + pf + (fp & 1) * 16384;
#pragma unroll
    for (int m = 0; m < 2; ++m) {
      const f32x2 r2 = {lrs[m], lrs[m]}, b2 = {bs[m], bs[m]};
      auto pp = [&](int n) { return u32x2{pk2(__builtin_elementwise_fma(lo2(acc[m][n]), r2, b2) * up2(z1[m][n][0])), pk2(__builtin_elementwise_fma(hi2(acc[m][n]), r2, b2) * up2(z1[m][n][1]))}; };
#pragma unroll
      for (int a = 0; a < 4; ++a) {
        u32x2 p0 = pp(2 * a), p1 = pp(2 * a + 1);
        st16(R, po, a * 4096 + m * 1024, u32x4{p0[0], p0[1], p1[0], p1[1]});
      }
      st8(R, lane * 16, pf + 8 * 4096 + m * 1024 + (fp & 1) * 8, pp(8));
    }
  }
  STAMP(3,10);
  arrive(2, 4, O_PFL + tp * 2048 + fp * 8, 128, 16);
  const int th = w & 1, kq = w >> 1, T = 4 * t64 + 2 * th + kq;
  const int lane5 = fresh_lane();
#define lane lane5
#define li (lane5 & 15)
#define q (lane5 >> 4)
#define prow (8 * (li >> 2) + (li & 3))
  u32x2 hv[5];
#pragma unroll
  for (int n = 0; n < 5; ++n) {
    const int dd = 72 * j3 + 16 * n + 4 * (n == 4 ? (q & 1) : q);
    hv[n] = ld8(R, (dd >> 5) * 8192 + ((dd >> 2) & 1) * 512 + (((dd >> 3) & 3) * 16 + li) * 8, O_HF + ((T >> 3) * 18 * 8 + (T & 7)) * 1024);
  }
  struct F3 { u32x4 p[2][2], n[9]; };
  f32x4 a3[2][6] = {};
  F3 U, V;
  const int ap = (8 * kq + 2 * th) * 1024 + lane * 16, aw = 16384 + kq * 9216 + lane * 16;
  auto ld3 = [&](F3& f, int t) {
    const char* slot = sm + (t & 3) * STG;
#pragma unroll
    for (int g = 0; g < 2; ++g)
#pragma unroll
      for (int m = 0; m < 2; ++m) f.p[g][m] = *(const u32x4*)(slot + ap + (4 * g + m) * 1024);
#pragma unroll
    for (int n = 0; n < 9; ++n) f.n[n] = *(const u32x4*)(slot + aw + n * 1024);
  };
  auto mm3 = [&](const F3& f) {
#pragma unroll
    for (int g = 0; g < 2; ++g)
#pragma unroll
      for (int m = 0; m < 2; ++m) {
#pragma unroll
        for (int n = 0; n < 4; ++n) a3[m][n] = mfma_bf(f.n[4 * g + n], f.p[g][m], a3[m][n]);
        a3[m][4 + g] = mfma_bf(f.n[8], f.p[g][m], a3[m][4 + g]);
      }
#pragma unroll
    for (int i = 0; i < 13; ++i) { __builtin_amdgcn_sched_group_barrier(0x008, 1, 0); __builtin_amdgcn_sched_group_barrier(0x100, 1, 0); }
    __builtin_amdgcn_sched_barrier(0);
  };
  STAMP(3,11);
  bar();
  STAMP(3,12);
  ld3(U, 50);
#pragma unroll 1
  for (int t = 50; t < 66; t += 2) { bar(); ld3(V, t + 1); mm3(U); bar(); ld3(U, t + 2); mm3(V); }
  bar(); ld3(V, 67); mm3(U); mm3(V);
  STAMP(3,13);
#pragma unroll
  for (int m = 0; m < 2; ++m)
#pragma unroll
    for (int r = 0; r < 4; ++r) a3[m][4][r] += __shfl_xor(a3[m][5][r], 32);
#pragma unroll
  for (int n = 0; n < 5; ++n) {
    *(f32x4*)(sm + (w * 5 + n) * 1024 + lane * 16) = kq ? a3[0][n] : a3[1][n];
    a3[0][n] = kq ? a3[1][n] : a3[0][n];
  }
  bar();
  char* o = out + (size_t)(32 * (T >> 1) + 4 * (T & 1) + prow) * 1152 + (72 * j3 + 4 * q) * 2;
#pragma unroll
  for (int n = 0; n < 5; ++n) {
    f32x4 v = a3[0][n] + *(const f32x4*)(sm + ((w ^ 2) * 5 + n) * 1024 + lane * 16);
    if (n < 4 || q < 2) *(u32x2*)(o + n * 32) = u32x2{pk2(up2(hv[n][0]) + up2(pk2(lo2(v)))), pk2(up2(hv[n][1]) + up2(pk2(hi2(v))))};
  }
  if (bid == 0 && w == 0 && lane == 0) { u64 e = (((u64)tag[1] << 32) | tag[0]) - 0x9E3779B97F4A7C15ull + 1; st8(R, 0, O_EP, u32x2{(unsigned)e, (unsigned)(e >> 32)}); }
  STAMP(3,14);
}

#undef lane
#undef li
#undef q
#undef prow
extern "C" void kernel_launch(void* const* d_in, const int* in_sizes, int n_in, void* d_out, int out_size, void* d_ws, size_t ws_size,
                              hipStream_t stream) {
  (void)hipFuncSetAttribute((const void*)k_all, hipFuncAttributeMaxDynamicSharedMemorySize, LDS_BYTES);
  k_all<<<256, 512, LDS_BYTES, stream>>>((const char*)d_in[0], (const char*)d_in[1], (const char*)d_in[2], (const char*)d_in[3],
      (const char*)d_in[4], (const char*)d_in[5], (const char*)d_in[6], (const char*)d_in[7], (const char*)d_in[8], (const char*)d_in[10],
      (const char*)d_in[11], (const char*)d_in[12], (const char*)d_in[13], (char*)d_out, (char*)d_ws);
}
```

```cpp
#include <hip/hip_runtime.h>
#include <hip/hip_bf16.h>
#include <stdint.h>

typedef __attribute__((ext_vector_type(8))) __bf16 bf16x8;
typedef __attribute__((ext_vector_type(2))) __bf16 bf16x2;
typedef __attribute__((ext_vector_type(8))) _Float16 f16x8;
typedef __attribute__((ext_vector_type(2))) _Float16 f16x2;
typedef __attribute__((ext_vector_type(4))) float f32x4;
typedef __attribute__((ext_vector_type(2))) float f32x2;
typedef __attribute__((ext_vector_type(4))) unsigned u32x4;
typedef __attribute__((ext_vector_type(2))) unsigned u32x2;
typedef __attribute__((ext_vector_type(4))) short s16x4;
#define LDSP __attribute__((address_space(3)))
#define DEVI __device__ __forceinline__

constexpr int L = 2048, D = 576, NM = 128, F = 4608, FH = 2304;
constexpr float EPS = 1e-5f;
constexpr unsigned O_EP = 0;
constexpr int NREP = 32;
constexpr unsigned O_F1 = 256;
constexpr unsigned O_F2 = O_F1 + NREP * 1024;
constexpr unsigned O_ZFL = O_F2 + NREP * 2048;
constexpr unsigned O_PFL = O_ZFL + 32768;
constexpr unsigned O_CS = 167936;
static_assert(O_PFL + 32768 <= O_CS, "ws");
constexpr unsigned O_ST = O_CS + F * 8;
constexpr unsigned O_HS = O_ST + 262144;
constexpr unsigned O_U = O_HS + 16384;
constexpr unsigned O_XB = O_U + L * D * 2;
constexpr unsigned O_WI = O_XB + L * D * 2;
constexpr unsigned O_WO = O_WI + D * F * 2;
constexpr unsigned O_HF = O_WO + FH * D * 2;
constexpr unsigned O_ZF = O_HF + L * D * 2;
constexpr unsigned O_PF = O_ZF + L * FH * 2;
constexpr unsigned WS_USED = O_PF + L * FH * 2;
constexpr size_t O_STAMP = 128u << 20;
#define STAMP(k,i)
#define STAMPW(k,i,W)
#define RSTAMP(k,i)
constexpr unsigned SENT = 0xFFFFFFFFu;

DEVI unsigned pk_bf16(float a, float b) { f32x2 v = {a, b}; return __builtin_bit_cast(unsigned, __builtin_convertvector(v, bf16x2)); }
DEVI unsigned pk_f16(float a, float b) { f32x2 v = {a, b}; return __builtin_bit_cast(unsigned, __builtin_convertvector(v, f16x2)); }
DEVI float bf_lo(unsigned u) { return __builtin_bit_cast(float, u << 16); }
DEVI float bf_hi(unsigned u) { return __builtin_bit_cast(float, u & 0xFFFF0000u); }
DEVI float asf(unsigned u) { return __builtin_bit_cast(float, u); }
DEVI unsigned asu(float f) { return __builtin_bit_cast(unsigned, f); }
DEVI float rbf(float a) { return bf_lo(pk_bf16(a, a)); }
DEVI float h_lo(unsigned u) { return (float)__builtin_bit_cast(f16x2, u)[0]; }
DEVI float h_hi(unsigned u) { return (float)__builtin_bit_cast(f16x2, u)[1]; }
template <int CTRL> DEVI float dpp_add(float v) {
  return v + __builtin_bit_cast(float, __builtin_amdgcn_update_dpp(0, __builtin_bit_cast(int, v), CTRL, 0xF, 0xF, true));
}
DEVI float row16_sum(float v) { v = dpp_add<0x128>(v); v = dpp_add<0x124>(v); v = dpp_add<0x122>(v); v = dpp_add<0x121>(v); return v; }
DEVI float q_sum(float v) { v += __shfl_xor(v, 16); v += __shfl_xor(v, 32); return v; }
DEVI u32x2 tr_read(const char* p) {
  return __builtin_bit_cast(u32x2, __builtin_amdgcn_ds_read_tr16_b64_v4i16((LDSP s16x4*)p));
}
DEVI f32x4 mfma_bf(u32x4 a, u32x4 b, f32x4 c) {
  return __builtin_amdgcn_mfma_f32_16x16x32_bf16(__builtin_bit_cast(bf16x8, a), __builtin_bit_cast(bf16x8, b), c, 0, 0, 0);
}
DEVI f32x4 mfma_h(u32x4 a, u32x4 b, f32x4 c) {
  return __builtin_amdgcn_mfma_f32_16x16x32_f16(__builtin_bit_cast(f16x8, a), __builtin_bit_cast(f16x8, b), c, 0, 0, 0);
}
DEVI void ln_scalars(float mean, float var, float& mb, float& rinv) {
  mb = rbf(mean);
  float d = rbf(__builtin_amdgcn_sqrtf(rbf(rbf(var) + EPS)));
  rinv = __builtin_amdgcn_rcpf(d);
}
DEVI float gelu(float x) {
  float w = x * __builtin_fmaf(x * x, -0.10294324f, -2.3022082f);
  return x * __builtin_amdgcn_rcpf(1.0f + __builtin_amdgcn_exp2f(w));
}

typedef unsigned long long u64;
typedef __amdgpu_buffer_rsrc_t rsrc_t;
constexpr int SPIN = 1 << 16;
template <int CTRL> DEVI float dpp_get(float v) {
  return __builtin_bit_cast(float, __builtin_amdgcn_update_dpp(0, __builtin_bit_cast(int, v), CTRL, 0xF, 0xF, true));
}
constexpr float GC1 = -0.10294324f, GC0 = -2.3022082f;
DEVI f32x2 gelu2(f32x2 x, f32x2 k1, f32x2 k0) {
  f32x2 w = x * __builtin_elementwise_fma(x * x, k1, k0);
  f32x2 d = f32x2{__builtin_amdgcn_exp2f(w[0]), __builtin_amdgcn_exp2f(w[1])} + f32x2{1.f, 1.f};
  return x * f32x2{__builtin_amdgcn_rcpf(d[0]), __builtin_amdgcn_rcpf(d[1])};
}
DEVI unsigned pk2(f32x2 v) { return __builtin_bit_cast(unsigned, __builtin_convertvector(v, bf16x2)); }
DEVI f32x2 up2(unsigned u) { return f32x2{bf_lo(u), bf_hi(u)}; }
DEVI f32x2 lo2(f32x4 v) { return __builtin_shufflevector(v, v, 0, 1); }
DEVI f32x2 hi2(f32x4 v) { return __builtin_shufflevector(v, v, 2, 3); }
DEVI float dot2(unsigned a, unsigned b, float c) {
  return __builtin_amdgcn_fdot2_f32_bf16(__builtin_bit_cast(bf16x2, a), __builtin_bit_cast(bf16x2, b), c, false);
}
DEVI void st16(rsrc_t r, unsigned voff, unsigned soff, u32x4 v) { __builtin_amdgcn_raw_buffer_store_b128(v, r, voff + soff, 0, 16); }
DEVI void st8(rsrc_t r, unsigned voff, unsigned soff, u32x2 v) { __builtin_amdgcn_raw_buffer_store_b64(v, r, voff, soff, 16); }
DEVI void st4(rsrc_t r, unsigned voff, unsigned soff, unsigned v) { __builtin_amdgcn_raw_buffer_store_b32(v, r, voff, soff, 16); }
DEVI u32x4 ld16(rsrc_t r, unsigned voff, unsigned soff) { return __builtin_amdgcn_raw_buffer_load_b128(r, voff, soff, 16); }
DEVI u32x2 ld8(rsrc_t r, unsigned voff, unsigned soff) { return __builtin_amdgcn_raw_buffer_load_b64(r, voff, soff, 16); }
template <int N> DEVI void wait_vm() { asm volatile("s_waitcnt vmcnt(%0)" ::"i"(N) : "memory"); }
DEVI void bar() { __builtin_amdgcn_sched_barrier(0); asm volatile("s_waitcnt lgkmcnt(0)\n\ts_barrier" ::: "memory"); __builtin_amdgcn_sched_barrier(0); }
DEVI int fresh_lane() { int l; asm volatile("v_mbcnt_lo_u32_b32 %0, -1, 0\n\tv_mbcnt_hi_u32_b32 %0, -1, %0" : "=v"(l)); return l; }
DEVI unsigned lds_addr(const char* p) { return (unsigned)(uintptr_t)(LDSP const char*)p; }
template <int NL> DEVI void wait_flags(rsrc_t r, unsigned off, int n, u32x2 tag, int lane) {
#pragma unroll 1
  for (int spin = 0; spin < SPIN; ++spin) {
    bool ok = true;
#pragma unroll
    for (int k = 0; k < NL; ++k) {
      int i = min(lane + 64 * k, n / 2 - 1);
      u32x4 v = ld16(r, i * 16, off);
      ok = ok && v[0] == tag[0] && v[1] == tag[1] && v[2] == tag[0] && v[3] == tag[1];
    }
    if (__all(ok)) break;
    __builtin_amdgcn_s_sleep(1);
    asm volatile("" ::: "memory");
  }
  asm volatile("" ::: "memory");
}
template <bool SC> DEVI void glds4(unsigned m0v, const char* base, unsigned v0, unsigned v1, unsigned v2, unsigned v3) {
  unsigned keep;
  if (SC)
    asm volatile("s_mov_b32 %0, m0\n\ts_mov_b32 m0, %1\n\ts_nop 0\n\t"
                 "global_load_lds_dwordx4 %3, %2 sc1\n\tglobal_load_lds_dwordx4 %4, %2 offset:1024 sc1\n\t"
                 "global_load_lds_dwordx4 %5, %2 offset:2048 sc1\n\tglobal_load_lds_dwordx4 %6, %2 offset:3072 sc1\n\ts_mov_b32 m0, %0"
                 : "=&s"(keep) : "s"(m0v), "s"(base), "v"(v0), "v"(v1), "v"(v2), "v"(v3) : "memory");
  else
    asm volatile("s_mov_b32 %0, m0\n\ts_mov_b32 m0, %1\n\ts_nop 0\n\t"
                 "global_load_lds_dwordx4 %3, %2\n\tglobal_load_lds_dwordx4 %4, %2 offset:1024\n\t"
                 "global_load_lds_dwordx4 %5, %2 offset:2048\n\tglobal_load_lds_dwordx4 %6, %2 offset:3072\n\ts_mov_b32 m0, %0"
                 : "=&s"(keep) : "s"(m0v), "s"(base), "v"(v0), "v"(v1), "v"(v2), "v"(v3) : "memory");
}
DEVI void glds1(unsigned m0v, const char* base, unsigned v0) {
  unsigned keep;
  asm volatile("s_mov_b32 %0, m0\n\ts_mov_b32 m0, %1\n\ts_nop 0\n\tglobal_load_lds_dwordx4 %3, %2 sc1\n\ts_mov_b32 m0, %0"
               : "=&s"(keep) : "s"(m0v), "s"(base), "v"(v0) : "memory");
}
template <int NP> DEVI void ld_lin(unsigned dst, const char* src, unsigned l16) {
  glds4<true>(dst, src, l16, l16, l16, l16);
  glds4<true>(dst + 4096, src + 4096, l16, l16, l16, l16);
  if (NP == 9) glds1(dst + 8192, src + 8192, l16);
}

constexpr int TP = 1168;
constexpr int CV_K = 8 * 260 * 16, CV_U = 4576;
constexpr int C_U = 4 * CV_K, C_KD = C_U + 4 * CV_U, C_MX = C_KD + 4 * 1536, C_END = C_MX + 64;
constexpr int KT = 528;
constexpr int STG = 34816, RING = 4 * STG;
constexpr int X_CS = RING, X_LN = X_CS + 1152, X_PV = X_LN + 1024, X_CNT = 158720;
constexpr int LDS_BYTES = X_CNT + 64;
static_assert(X_PV + 16384 <= X_CNT && LDS_BYTES <= 163840, "lds");
#define TPOLL 12
#define TCHK 13
#define TFLAG 15

struct Fr { u32x4 m[2], n[9]; };
struct Ctx { const char* sm; int am0[4], am1, an; };
template <int MK> DEVI void ldf(Fr& f, const Ctx& c, int t, int kk) {
  int so = (t & 3) * STG;
  asm volatile("" : "+s"(so));
  const char* slot = c.sm + so;
  if (MK == 0) {
#pragma unroll
    for (int m = 0; m < 2; ++m) {
      u32x2 a = *(const u32x2*)(slot + c.am0[2 * m] + kk * 8192), b = *(const u32x2*)(slot + c.am0[2 * m + 1] + kk * 8192);
      f.m[m] = u32x4{a[0], a[1], b[0], b[1]};
    }
  } else {
#pragma unroll
    for (int m = 0; m < 2; ++m) f.m[m] = *(const u32x4*)(slot + (c.am1 ^ (kk * 64)) + m * 2048);
  }
#pragma unroll
  for (int n = 0; n < 9; ++n) f.n[n] = *(const u32x4*)(slot + c.an + (kk * 9 + n) * 1024);
}
template <int MK, int ORI, bool LD> DEVI void seg(f32x4 (&acc)[2][9], const Fr& cur, Fr& nxt, const Ctx& c, int t, int kk) {
  if (LD) ldf<MK>(nxt, c, t, kk);
#pragma unroll
  for (int m = 0; m < 2; ++m)
#pragma unroll
    for (int nn = 0; nn < 9; ++nn) {
      const int n = m ? 8 - nn : nn;
      acc[m][n] = ORI == 0 ? mfma_bf(cur.m[m], cur.n[n], acc[m][n]) : mfma_bf(cur.n[n], cur.m[m], acc[m][n]);
    }
#pragma unroll
  for (int i = 0; i < (MK == 0 ? 13 : 11); ++i) { __builtin_amdgcn_sched_group_barrier(0x008, 1, 0); __builtin_amdgcn_sched_group_barrier(0x100, 1, 0); }
  __builtin_amdgcn_sched_barrier(0);
}
DEVI f32x2 cmul(f32x2 a, f32x2 b, f32x2 bp) {
  return __builtin_elementwise_fma(f32x2{a[1], a[1]}, bp, f32x2{a[0], a[0]} * b);
}
DEVI f32x2 perp(f32x2 b) { return f32x2{-b[1], b[0]}; }
DEVI f32x2 cexp(float ar, float ai, float l) {
  float m = __builtin_amdgcn_exp2f(ar * l), ph = __builtin_amdgcn_fractf(ai * l);
  return f32x2{m * __builtin_amdgcn_cosf(ph), m * __builtin_amdgcn_sinf(ph)};
}
DEVI unsigned pkh(f32x2 v) { return __builtin_bit_cast(unsigned, __builtin_convertvector(v, f16x2)); }
DEVI float ldbf(const char* p, int i) { return bf_lo(*(const unsigned short*)(p + 2 * i)); }

__global__ __launch_bounds__(512, 2) void k_all(const char* __restrict__ x, const char* __restrict__ a_re, const char* __restrict__ a_im,
    const char* __restrict__ b_re, const char* __restrict__ b_im, const char* __restrict__ c_re, const char* __restrict__ c_im,
    const char* __restrict__ log_dt, const char* __restrict__ dskip, const char* __restrict__ w_in, const char* __restrict__ w_sp,
    const char* __restrict__ b_sp, const char* __restrict__ w_out, char* __restrict__ out, char* __restrict__ ws) {
  extern __shared__ __attribute__((aligned(16))) char sm[];
  const int tid = threadIdx.x, lane = tid & 63, w = __builtin_amdgcn_readfirstlane(tid >> 6), li = lane & 15, q = lane >> 4;
  const int bid = blockIdx.x, xc = bid & 7, ii = bid >> 3;
  const rsrc_t R = __builtin_amdgcn_make_buffer_rsrc(ws, 0, WS_USED, 0x00020000);
  STAMP(0,0);
  const u32x2 ep = ld8(R, 0, O_EP);
  const bool is_conv = ii < 18;
  if (w == 7) {
    unsigned long long pc;
    asm volatile("s_getpc_b64 %0" : "=s"(pc));
    const char* cp = (const char*)(pc & ~255ull) + (ii & 7) * 5376 + lane * 128;
    if (lane < 42) { unsigned v = *(const unsigned*)cp; asm volatile("" ::"v"(v)); }
  }
  const int cg = xc * 18 + ii, rep = bid & (NREP - 1);
  if (tid == 0) *(u32x4*)(sm + X_CNT) = u32x4{0, 0, 0, 0};
  auto mk_tag = [&]() {
    u64 e = (((u64)__builtin_amdgcn_readfirstlane(ep[1]) << 32) | (unsigned)__builtin_amdgcn_readfirstlane(ep[0])) + 0x9E3779B97F4A7C15ull;
    return u32x2{(unsigned)e, (unsigned)(e >> 32)};
  };
  u32x2 tag;
  auto arrive = [&](int slot, unsigned n, unsigned flag_off, unsigned stride, int nrep) {
    wait_vm<0>();
    const int lane = fresh_lane();
    unsigned old = 0;
    if (lane == 0) old = __hip_atomic_fetch_add((unsigned*)(sm + X_CNT) + slot, 1u, __ATOMIC_RELAXED, __HIP_MEMORY_SCOPE_WORKGROUP);
    if (__builtin_amdgcn_readfirstlane(old) == n - 1 && lane < nrep) st8(R, lane * stride, flag_off, tag);
  };
  if (is_conv) {
    const int ch = w & 3, dch = 4 * cg + ch;
    char* sk = sm + ch * CV_K;
    const char* su = sm + C_U + ch * CV_U;
    float sc = 0.f;
    if (w < 4) {
      const int pi = (dch * NM + 2 * lane) * 2;
      const unsigned v_ar = *(const unsigned*)(a_re + pi), v_ai = *(const unsigned*)(a_im + pi), v_br = *(const unsigned*)(b_re + pi),
                     v_bi = *(const unsigned*)(b_im + pi), v_cr = *(const unsigned*)(c_re + pi), v_ci = *(const unsigned*)(c_im + pi);
      const float p_dk = ldbf(dskip, dch);
      const float dt = rbf(__expf(ldbf(log_dt, dch)));
      tag = mk_tag();
      STAMP(0,1);
      const f32x2 ar = (up2(v_ar) * dt) * 1.44269504f, ai = (up2(v_ai) * dt) * 0.15915494f;
      const f32x2 br = up2(v_br) * dt, bi = up2(v_bi) * dt, cr = up2(v_cr), ci = up2(v_ci);
      const f32x2 cbr = cr * br - ci * bi, cbi = -(cr * bi + ci * br);
      float mx = fmaxf(fmaxf(fabsf(cbr[0]), fabsf(cbi[0])), fmaxf(fabsf(cbr[1]), fabsf(cbi[1])));
      mx = fmaxf(mx, dpp_get<0x128>(mx)); mx = fmaxf(mx, dpp_get<0x124>(mx)); mx = fmaxf(mx, dpp_get<0x122>(mx)); mx = fmaxf(mx, dpp_get<0x121>(mx));
      auto rl = [&](int l) { return asf(__builtin_amdgcn_readlane((int)asu(mx), l)); };
      sc = fmaxf(fmaxf(rl(0), rl(16)), fmaxf(rl(32), rl(48)));
      sc = fmaxf(sc, fabsf(p_dk) * (1.0f / 256));
      const float isc = sc > 0.f ? 1.0f / sc : 0.f;
      {
        struct C2 { f32x2 a, b; };
        char* tb = sk + lane * 8;
        auto put = [&](int row, C2 v) { *(u32x2*)(tb + row * KT) = u32x2{pkh(v.a), pkh(v.b)}; };
        auto mul = [&](C2 v, C2 u, C2 up) { return C2{cmul(v.a, u.a, up.a), cmul(v.b, u.b, up.b)}; };
        auto prp = [&](C2 v) { return C2{perp(v.a), perp(v.b)}; };
        auto ex = [&](float l) { return C2{cexp(ar[0], ai[0], l), cexp(ar[1], ai[1], l)}; };
        const C2 w1 = ex(1.f), w128 = ex(128.f);
        C2 w8 = ex(8.f); w8.a[1] = -w8.a[1]; w8.b[1] = -w8.b[1];
        const C2 w1p = prp(w1), w8p = prp(w8), w128p = prp(w128);
        C2 y = {f32x2{cbr[0], cbi[0]} * isc, f32x2{cbr[1], cbi[1]} * isc};
        C2 E[8]; E[0] = C2{f32x2{1.f, 0.f}, f32x2{1.f, 0.f}}; E[1] = w1;
#pragma unroll
        for (int e = 2; e < 8; ++e) E[e] = mul(E[e - 1], w1, w1p);
#pragma unroll
        for (int e = 0; e < 8; ++e) { put(e, E[e]); put(8 + e, e ? mul(E[e], w128, w128p) : w128); put(41 + e, y); y = mul(y, w8, w8p); }
        const C2 e2p = prp(E[2]), e4p = prp(E[4]), e6p = prp(E[6]);
        C2 H = w128;
#pragma unroll
        for (int a = 2; a <= 16; ++a) {
          H = mul(H, w128, w128p);
          if (a < 4) { put(8 + 4 * a, H); put(9 + 4 * a, mul(H, E[2], e2p)); put(10 + 4 * a, mul(H, E[4], e4p)); put(11 + 4 * a, mul(H, E[6], e6p)); }
          else if (a < 8) { put(16 + 2 * a, H); put(17 + 2 * a, mul(H, E[4], e4p)); }
          else put(24 + a, H);
          if (a < 10) { put(47 + a, y); if (a < 9) y = mul(y, w8, w8p); }
        }
      }
      asm volatile("" ::: "memory");
      STAMP(0,2);
      {
        f32x4 kk[3] = {};
        const char* pa = sk + li * KT + q * 16;
#pragma unroll
        for (int s = 0; s < 8; ++s) {
          u32x4 fb = *(const u32x4*)(pa + 41 * KT + s * 64);
#pragma unroll
          for (int t = 0; t < 3; ++t) kk[t] = mfma_h(*(const u32x4*)(pa + 16 * t * KT + s * 64), fb, kk[t]);
        }
        if (lane == 0) kk[0][0] += p_dk * isc;
        char* kd = sm + C_KD + ch * 1536;
        *(u32x2*)(kd + (128 * (q >> 1) + 4 * (q & 1) + 8 * li) * 2) = u32x2{pkh(lo2(kk[0])), pkh(hi2(kk[0]))};
        const int i1 = q < 2 ? 256 + 64 * q + 4 * li : 384 + 64 * (q - 2) + 2 * li;
        *(unsigned*)(kd + i1 * 2) = pkh(lo2(kk[1])); *(unsigned*)(kd + (i1 + (q < 2 ? 2 : 32)) * 2) = pkh(hi2(kk[1]));
#pragma unroll
        for (int r = 0; r < 4; ++r) *(_Float16*)(kd + (512 + 16 * (4 * q + r) + li) * 2) = (_Float16)kk[2][r];
      }
      asm volatile("" ::: "memory");
      u32x4 c0[4];
      {
        const unsigned short* kd = (const unsigned short*)(sm + C_KD + ch * 1536);
#pragma unroll
        for (int rr = 0; rr < 2; ++rr) {
          const int k = 255 - 64 * rr - lane;
          const unsigned a = kd[384 + k], b = kd[385 + k], aa = a | (a << 16);
          c0[rr] = u32x4{b | (b << 16), b | (((k & 1) ? b : a) << 16), aa, aa};
        }
        { const int k = 127 - lane;
          const unsigned ab = *(const unsigned*)(kd + 256 + 2 * k), cc = kd[258 + 2 * k];
          c0[2] = u32x4{cc | (cc << 16), __builtin_amdgcn_perm(ab, ab, 0x03020302), __builtin_amdgcn_perm(ab, ab, 0x01000302), __builtin_amdgcn_perm(ab, ab, 0x01000100)}; }
        { const int k = 63 - lane;
          const u32x4 v = *(const u32x4*)(kd + 8 * (k & 31));
          const u32x2 u = *(const u32x2*)(kd + 128 + 4 * k); const unsigned e = kd[132 + 4 * k];
          c0[3] = lane < 32 ? u32x4{__builtin_amdgcn_perm(u[1], e, 0x07060100), __builtin_amdgcn_perm(u[1], u[1], 0x01000100), __builtin_amdgcn_perm(u[0], u[1], 0x07060100), __builtin_amdgcn_perm(u[0], u[0], 0x01000100)}
                            : u32x4{__builtin_amdgcn_alignbit(v[3], v[3], 16), __builtin_amdgcn_alignbit(v[2], v[2], 16), __builtin_amdgcn_alignbit(v[1], v[1], 16), __builtin_amdgcn_alignbit(v[0], v[0], 16)}; }
#pragma unroll
        for (int rr = 0; rr < 4; ++rr) *(u32x4*)(sk + (64 * rr + lane) * 16) = c0[rr];
        if (lane < 8) *(u32x4*)(sk + (lane * 260 + 256) * 16) = u32x4{0, 0, 0, 0};
      }
      asm volatile("" ::: "memory");
      STAMP(0,3);
      u32x4 c1[4];
#pragma unroll
      for (int rr = 0; rr < 4; ++rr) c1[rr] = *(const u32x4*)(sk + (64 * rr + lane) * 16 + 16);
#pragma unroll
      for (int rr = 0; rr < 4; ++rr) {
        char* p = sk + (64 * rr + lane) * 16;
        unsigned e[8] = {c0[rr][0], c0[rr][1], c0[rr][2], c0[rr][3], c1[rr][0], c1[rr][1], c1[rr][2], c1[rr][3]}, o[7];
#pragma unroll
        for (int i = 0; i < 7; ++i) o[i] = __builtin_amdgcn_alignbit(e[i + 1], e[i], 16);
#pragma unroll
        for (int s = 0; s < 4; ++s) {
          if (s) *(u32x4*)(p + (2 * s) * 260 * 16) = u32x4{e[s], e[s + 1], e[s + 2], e[s + 3]};
          *(u32x4*)(p + (2 * s + 1) * 260 * 16) = u32x4{o[s], o[s + 1], o[s + 2], o[s + 3]};
        }
      }
      STAMP(0,4);
    } else {
      tag = mk_tag();
      if (cg < 136 && tid < 384) st16(R, (tid - 256) * 16, O_ST + cg * 2048, u32x4{SENT, SENT, SENT, SENT});
      if (lane < 30) *(u32x4*)(sm + C_U + ch * CV_U + lane * 16) = u32x4{0, 0, 0, 0};
      if (w == 4) {
        wait_flags<1>(R, O_F1 + rep * 1024, 64, tag, lane);
        STAMPW(5,0,4);
        const unsigned v0 = (lane >> 2) * 36864 + (lane & 3) * 16, dv = 16 * 36864 - 1024;
#pragma unroll
        for (int i = 0; i < 4; ++i) glds4<true>(lds_addr(sm) + C_U + i * CV_U + 480, ws + O_U + (4 * cg + i) * 64, v0, v0 + dv, v0 + 2 * dv, v0 + 3 * dv);
        wait_vm<0>();
      }
    }
    bar();
    STAMP(0,5);
    u32x4 xv[4];
    if (w < 4) {
      f32x4 acc[8] = {};
      const char* pa = sk + ((7 - (li & 7)) * 260 + 253 - (li >> 3) + q) * 16 - 4032;
      const char* pb = su + li * 32 + q * 16;
      struct CF { u32x4 a[8], b[8]; } A, B;
      auto ldc = [&](CF& f, int a0) {
#pragma unroll
        for (int m = 0; m < 8; ++m) { f.a[m] = *(const u32x4*)(pa + 4032 - 64 * a0 - 512 * m); f.b[m] = *(const u32x4*)(pb + 448 - 64 * a0 + 512 * m); }
      };
      auto mmc = [&](const CF& f) {
#pragma unroll
        for (int m = 0; m < 8; ++m)
#pragma unroll
          for (int t = m; t < 8; ++t) acc[t] = mfma_h(f.a[m], f.b[t - m], acc[t]);
#pragma unroll
        for (int i = 0; i < 16; ++i) { __builtin_amdgcn_sched_group_barrier(0x008, 2, 0); __builtin_amdgcn_sched_group_barrier(0x100, 1, 0); }
        __builtin_amdgcn_sched_barrier(0);
      };
      ldc(A, 0);
      __builtin_amdgcn_sched_barrier(0);
#pragma unroll
      for (int a0 = 0; a0 < 8; a0 += 2) { ldc(B, a0 + 1); mmc(A); if (a0 < 6) ldc(A, a0 + 2); mmc(B); }
      STAMP(0,6);
#pragma unroll
      for (int t = 0; t < 8; ++t) *(u32x2*)(sk + t * 512 + li * 32 + q * 8) = u32x2{pk2(lo2(acc[t]) * sc), pk2(hi2(acc[t]) * sc)};
    } else {
      wait_flags<1>(R, O_F1 + rep * 1024 + 512, 64, tag, lane);
#pragma unroll
      for (int r = 0; r < 4; ++r) {
        const int T = ((tid & 255) >> 3) + 32 * r, ip = tid & 7;
        xv[r] = ld16(R, (32 * (T >> 1) + 8 * (ip >> 1) + 4 * (T & 1) + 2 * (ip & 1)) * 8, O_XB + cg * 16384);
      }
      STAMPW(2,0,4);
    }
    bar();
    if (w >= 4) {
#pragma unroll
      for (int r = 0; r < 4; ++r) {
        const int T = ((tid & 255) >> 3) + 32 * r, ip = tid & 7;
        const char* p = sm + (32 * (T >> 1) + 8 * (ip >> 1) + 4 * (T & 1) + 2 * (ip & 1)) * 2;
        const unsigned c0 = *(const unsigned*)p, c1 = *(const unsigned*)(p + CV_K), c2 = *(const unsigned*)(p + 2 * CV_K), c3 = *(const unsigned*)(p + 3 * CV_K);
        u32x4 v = {pk2(up2(xv[r][0]) + f32x2{bf_lo(c0), bf_lo(c1)}), pk2(up2(xv[r][1]) + f32x2{bf_lo(c2), bf_lo(c3)}),
                   pk2(up2(xv[r][2]) + f32x2{bf_hi(c0), bf_hi(c1)}), pk2(up2(xv[r][3]) + f32x2{bf_hi(c2), bf_hi(c3)})};
        st16(R, (T >> 3) * (18 * 8192) + (T & 7) * 1024 + ip * 16, O_HF + (cg >> 3) * 8192 + (cg & 1) * 512 + ((cg >> 1) & 3) * 128, v);
      }
      STAMPW(2,1,4);
      arrive(1, 4, O_F2 + cg * 8, 2048, NREP);
      STAMPW(2,2,4);
    }
  } else {
    const int tj = (ii - 18) * 8 + xc;
    if (tj < 64) {
      char* s0 = sm + 83968;
      const char* src = x + (size_t)(32 * tj + (tid >> 4)) * 1152 + li * 8;
      u32x2 xr[9];
#pragma unroll
      for (int k = 0; k < 9; ++k) xr[k] = *(const u32x2*)(src + k * 128);
      asm volatile("" :: "v"(xr[8])); STAMP(1,0);
      float a[4] = {0, 0, 0, 0};
#pragma unroll
      for (int k = 0; k < 9; ++k)
#pragma unroll
        for (int e = 0; e < 2; ++e) { a[e] = dot2(xr[k][e], 0x3F803F80u, a[e]); a[2 + e] = dot2(xr[k][e], xr[k][e], a[2 + e]); }
      const float s = row16_sum(a[0] + a[1]), ss = row16_sum(a[2] + a[3]);
      float mean = s * (1.0f / D), mb, rinv;
      ln_scalars(mean, fmaxf(ss * (1.0f / D) - mean * mean, 0.f), mb, rinv);
      const f32x2 r2 = {rinv, rinv}, b2 = {-mb * rinv, -mb * rinv};
      char* p = s0 + (tid >> 4) * TP + li * 8;
#pragma unroll
      for (int k = 0; k < 9; ++k) {
        *(u32x2*)(p + k * 128) = u32x2{pkh(__builtin_elementwise_fma(up2(xr[k][0]), r2, b2)), pkh(__builtin_elementwise_fma(up2(xr[k][1]), r2, b2))};
        *(u32x2*)(p + 32 * TP + k * 128) = xr[k];
      }
      tag = mk_tag();
      bar();
      STAMP(1,1);
      if (w < 4) {
        const char* trb = s0 + (8 * q + (li >> 2)) * TP + (li & 3) * 8 + w * 288;
        const unsigned uo = li * 64 + q * 16 + O_U + tj * 36864 + w * 9216;
#pragma unroll
        for (int k = 0; k < 9; ++k) {
          u32x2 lo = tr_read(trb + k * 32), hi = tr_read(trb + 4 * TP + k * 32);
          st16(R, uo, k * 1024, u32x4{lo[0], lo[1], hi[0], hi[1]});
        }
        STAMP(1,2);
        arrive(0, 4, O_F1 + tj * 8, 1024, NREP);
        STAMP(1,3);
      } else {
        const int c16 = (tid >> 4) & 15;
        const char* px = s0 + (32 + li * 2) * TP + c16 * 8;
#pragma unroll
        for (int k = 0; k < 9; ++k) {
          u32x2 a = *(const u32x2*)(px + k * 128), b = *(const u32x2*)(px + TP + k * 128);
          st16(R, (c16 + 16 * k) * 16384 + li * 16, O_XB + tj * 256, u32x4{a[0], a[1], b[0], b[1]});
        }
        arrive(3, 4, O_F1 + (64 + tj) * 8, 1024, NREP);
        STAMPW(1,4,4);
      }
      if (tj < 36) {
        const int par = w >> 2, G = 2 * tj + par, row = (tid >> 3) & 31, uu = G / 9, idx = G % 9, hh = row >> 4;
        const int panel = idx < 4 ? 2 * uu : (idx < 8 ? 2 * uu + 1 : 2 * uu + hh), tile = idx < 8 ? 2 * (idx & 3) + hh : 8;
        const char* wsrc = w_out + (size_t)(144 * panel + 16 * tile + (row & 15)) * 1152 + (tid & 7) * 16;
        u32x4 v[9];
#pragma unroll
        for (int k = 0; k < 9; ++k) v[k] = *(const u32x4*)(wsrc + k * 128);
#pragma unroll
        for (int k = 0; k < 9; ++k) *(u32x4*)(sm + (tid >> 3) * TP + (tid & 7) * 16 + k * 128) = v[k];
        bar();
        const char* trb = sm + (32 * par + 4 * q + (li >> 2)) * TP + (li & 3) * 8;
#pragma unroll
        for (int jj = 0; jj < 2; ++jj) {
          const int j = 2 * (w & 3) + jj;
          const unsigned dst = O_WO + (j * 36 + tj) * 9216;
#pragma unroll
          for (int n = 0; n < 5; ++n) {
            const int c0 = 72 * j + 16 * n - (n == 4 ? 8 * par : 0);
            u32x2 lo = tr_read(trb + c0 * 2), hi = tr_read(trb + 16 * TP + c0 * 2);
            if (n < 4) st16(R, lane * 16, dst + (4 * par + n) * 1024, u32x4{lo[0], lo[1], hi[0], hi[1]});
            else if ((li >> 3) == par) st16(R, lane * 16, dst + 8192, u32x4{lo[0], lo[1], hi[0], hi[1]});
          }
        }
        arrive(1, 8, O_F2 + (192 + tj) * 8, 2048, NREP);
        STAMP(4,2);
      }
    } else {
      const int ti = tj - 64;
      if (w < 6) {
        const int row = tid / 12, cc = tid % 12;
        const char* src = w_in + (size_t)row * 9216 + ti * 192 + cc * 16;
        u32x4 v[18];
#pragma unroll
        for (int k = 0; k < 18; ++k) v[k] = *(const u32x4*)(src + (size_t)k * 32 * 9216);
#pragma unroll
        for (int k = 0; k < 18; ++k) *(u32x4*)(sm + (row + 32 * k) * 208 + cc * 16) = v[k];
      }
      tag = mk_tag();
      bar();
      STAMP(4,0);
      if (w < 6) {
        const int ct = 6 * ti + w;
        const int hf = ct < 144 ? 1 : 0, cl = ct < 144 ? ct : ct - 144, fp = cl / 9, n = cl % 9;
        const char* trb = sm + (8 * q + (li >> 2)) * 208 + w * 32 + (li & 3) * 8;
        const unsigned dst = lane * 16 + O_WI + ((fp * 2 + hf) * 18 * 9 + n) * 1024;
        float s = 0.f;
#pragma unroll 6
        for (int g = 0; g < 18; ++g) {
          u32x2 lo = tr_read(trb + g * 32 * 208), hi = tr_read(trb + g * 32 * 208 + 4 * 208);
          st16(R, dst, g * 9216, u32x4{lo[0], lo[1], hi[0], hi[1]});
          s = dot2(lo[0], 0x3F803F80u, s); s = dot2(lo[1], 0x3F803F80u, s); s = dot2(hi[0], 0x3F803F80u, s); s = dot2(hi[1], 0x3F803F80u, s);
        }
        s = q_sum(s);
        if (q == 0) st4(R, li * 4, O_CS + ct * 64, asu(s));
        arrive(1, 6, O_F2 + (144 + ti) * 8, 2048, NREP);
        STAMP(4,1);
      }
    }
  }
  const int tp = (xc & 1) * 8 + (ii >> 2), fp = (xc >> 1) * 4 + (ii & 3);
  const int t64 = 4 * xc + (ii >> 3), j3 = ii & 7;
  if (w == 0) { wait_flags<1>(R, O_F2 + rep * 2048 + 144 * 8, 48, tag, lane); STAMPW(6,0,0); }
  bar();
  if (w >= 4) {
    const int lw = w - 4;
    const unsigned l16 = lane * 16, base = lds_addr(sm);
    const int ph = ((ii >> 2) + ii) & 3, ph3 = ((ii >> 3) + ii) & 3;
    auto k9 = [&](int t) { return (t & 3) == ph ? (t + 4 < 9 ? t + 4 : ph) : t; };
    auto k32 = [&](int t) { return (t & 3) == ph ? ((t + 4) & 31) : t; };
    auto k18 = [&](int t) { return (t & 3) == ph3 ? (t + 4 < 18 ? t + 4 : ph3) : t; };
    if (lw < 2) {
      constexpr int NP = 8;
      unsigned vo[8];
#pragma unroll
      for (int j = 0; j < 8; ++j) {
        int t = 64 * lw + 32 * (j >> 2) + 4 * ((j >> 1) & 1) + 16 * (j & 1) + 8 * (lane >> 5) + ((lane >> 3) & 3);
        vo[j] = t * 4096 + (((lane & 7) ^ ((lane >> 3) & 7)) * 16) + 3072 - (j & 3) * 1024;
      }
      const char* wsrc = w_sp + (size_t)tp * 128 * 4096 - 3072;
      const char* hsrc = ws + O_HF + tp * 18 * 8192 + lw * 8192;
      const char* psrc = ws + O_PF + t64 * 72 * 4096 + lw * 8192;
      const unsigned dst = base + lw * 8192;
      if (lw == 0) wait_flags<2>(R, O_F2 + rep * 2048, 144, tag, lane);
      STAMPW(5,1,4);
      bar();
#pragma unroll 1
      for (int t = 0; t < 4; ++t) ld_lin<NP>(dst + t * STG, hsrc + k9(t) * 16384, l16);
      wait_vm<3 * NP>(); bar();
#pragma unroll 1
      for (int t = 4; t < 18; ++t) { wait_vm<2 * NP>(); bar(); ld_lin<NP>(dst + (t & 3) * STG, hsrc + k9(t < 9 ? t : t - 9) * 16384, l16); }
#pragma unroll 1
      for (int t = 18; t < 50; ++t) {
        wait_vm<2 * NP>(); bar();
        const unsigned dd = dst + (t & 3) * STG; const char* sp = wsrc + k32(t - 18) * 128;
        glds4<false>(dd, sp, vo[0], vo[1], vo[2], vo[3]); glds4<false>(dd + 4096, sp, vo[4], vo[5], vo[6], vo[7]);
      }
      wait_vm<2 * NP>(); bar(); wait_vm<NP>(); bar(); wait_vm<0>(); bar();
      wait_flags<1>(R, O_PFL + (t64 >> 1) * 2048 + ((t64 & 1) * 8 + j3) * 128, 16, tag, lane);
      STAMPW(5,2,4);
#pragma unroll 1
      for (int t = 50; t < 68; ++t) {
        if (t >= 53) { wait_vm<2 * NP>(); bar(); }
        ld_lin<NP>(dst + (t & 3) * STG, psrc + k18(t - 50) * 16384, l16);
      }
      wait_vm<2 * NP>(); bar(); wait_vm<NP>(); bar(); wait_vm<0>(); bar();
    } else {
      constexpr int NP = 9;
      const char* wi = ws + O_WI + (fp * 2) * 18 * 9216 + (lw - 2) * 9216;
      const char* zs = ws + O_ZF + fp * 64 * 9216 + (lw - 2) * 9216;
      const char* wo = ws + O_WO + j3 * 36 * 9216 + (lw - 2) * 9216;
      const unsigned dst = base + 16384 + (lw - 2) * 9216;
#pragma unroll 1
      for (int t = 0; t < 4; ++t) ld_lin<NP>(dst + t * STG, wi + k9(t) * 18432, l16);
      bar();
      wait_vm<3 * NP>(); bar();
      if (lw == 3) {
        f32x2 a = *(const f32x2*)(sm + X_LN + lane * 8) + *(const f32x2*)(sm + X_LN + 512 + lane * 8);
        a[0] = q_sum(dpp_add<0x128>(a[0])); a[1] = q_sum(dpp_add<0x128>(a[1]));
        float mean = a[0] * (1.0f / D), mb, ri;
        ln_scalars(mean, fmaxf(a[1] * (1.0f / D) - mean * mean, 0.f), mb, ri);
        if (lane < 8) st8(R, lane * 8, O_HS + tp * 1024 + fp * 64, u32x2{asu(ri) == SENT ? 0x7FC00000u : asu(ri), asu(-mb)});
      }
#pragma unroll 1
      for (int t = 4; t < 18; ++t) { wait_vm<2 * NP>(); bar(); ld_lin<NP>(dst + (t & 3) * STG, wi + (t < 9 ? k9(t) : 9 + k9(t - 9)) * 18432, l16); }
      wait_vm<2 * NP>(); bar(); wait_vm<NP>(); bar(); wait_vm<0>(); bar();
      wait_flags<1>(R, O_F2 + rep * 2048 + 192 * 8, 36, tag, lane);
      wait_flags<1>(R, O_ZFL + fp * 2048 + tp * 128, 16, tag, lane);
      STAMPW(6,1,6);
#pragma unroll 1
      for (int t = 18; t < 68; ++t) {
        if (t >= 21) { wait_vm<2 * NP>(); bar(); }
        ld_lin<NP>(dst + (t & 3) * STG, t < 50 ? zs + k32(t - 18) * 18432 : wo + k18(t - 50) * 18432, l16);
      }
      wait_vm<2 * NP>(); bar(); wait_vm<NP>(); bar(); wait_vm<0>(); bar();
    }
    return;
  }
  Ctx c;
  c.sm = sm;
#pragma unroll
  for (int i = 0; i < 4; ++i) { c.am0[i] = (2 * w + (i >> 1)) * 1024 + (i & 1) * 512 + lane * 8; asm volatile("" : "+v"(c.am0[i])); }
  c.am1 = 2 * w * 2048 + li * 128 + ((q ^ (lane & 7)) * 16);
  c.an = 16384 + lane * 16;
  const int prow = 8 * (li >> 2) + (li & 3);
  if (tid < 72) *(u32x4*)(sm + X_CS + tid * 16) = ld16(R, (tid < 36 ? FH * 4 + tid * 16 : (tid - 36) * 16), O_CS + fp * 576);
  bar();
  char* pvb = sm + X_PV + w * 4096;
  {
    u32x4 hv[3];
#pragma unroll
    for (int k = 0; k < 3; ++k)
      hv[k] = ld16(R, (lane >> 5) * 8192 + ((lane >> 2) & 7) * 128 + (lane & 3) * 16, O_HF + (tp * 18 * 8 + (fp >> 1)) * 1024 + (fp & 1) * 64 + min(w + 4 * k, 8) * 16384);
    if (w) hv[2] = u32x4{0, 0, 0, 0};
    f32x4 a = {0, 0, 0, 0};
#pragma unroll
    for (int k = 0; k < 3; ++k)
#pragma unroll
      for (int e = 0; e < 4; ++e) { a[e & 2] = dot2(hv[k][e], 0x3F803F80u, a[e & 2]); a[(e & 2) + 1] = dot2(hv[k][e], hv[k][e], a[(e & 2) + 1]); }
#pragma unroll
    for (int e = 0; e < 4; ++e) a[e] = dpp_add<0x124>(dpp_add<0x128>(a[e]));
    if (li < 4) *(f32x4*)(sm + X_LN + (w * 4 + q) * 64 + li * 16) = a;
  }
  f32x4 acc[2][9] = {};
  Fr X, Y;
  bar();
  STAMP(3,0);
  ldf<0>(X, c, 0, 0);
#pragma unroll 1
  for (int t = 0; t < 8; ++t) {
    seg<0, 0, true>(acc, X, Y, c, t, 1);
    bar();
    seg<0, 0, true>(acc, Y, X, c, t + 1, 0);
  }
  u32x2 ht[2]; u32x4 hc[4];
  auto ld_hs = [&]() {
#pragma unroll
    for (int m = 0; m < 2; ++m) {
      ht[m] = ld8(R, li * 8 + m * 128, O_HS + tp * 1024 + w * 256);
      hc[2 * m] = ld16(R, q * 32 + m * 128, O_HS + tp * 1024 + w * 256); hc[2 * m + 1] = ld16(R, q * 32 + 16 + m * 128, O_HS + tp * 1024 + w * 256);
    }
  };
  ld_hs();
  __builtin_amdgcn_sched_barrier(0);
  seg<0, 0, true>(acc, X, Y, c, 8, 1);
  bar();
  seg<0, 0, true>(acc, Y, X, c, 9, 0);
  STAMP(3,1);
#pragma unroll 1
  for (int spin = 0; spin < SPIN; ++spin) {
    bool ok = ht[0][0] != SENT && ht[0][1] != SENT && ht[1][0] != SENT && ht[1][1] != SENT;
#pragma unroll
    for (int k = 0; k < 4; ++k) ok = ok && hc[k][0] != SENT && hc[k][1] != SENT && hc[k][2] != SENT && hc[k][3] != SENT;
    if (__all(ok)) break;
    __builtin_amdgcn_s_sleep(1);
    asm volatile("" ::: "memory");
    ld_hs();
  }
  const float lrs[2] = {asf(ht[0][0]), asf(ht[1][0])}, lnm[2] = {asf(ht[0][1]), asf(ht[1][1])};
  u32x4 zb[9];
  f32x2 rs[4];
  {
    float cs[9];
#pragma unroll
    for (int n = 0; n < 9; ++n) cs[n] = *(const float*)(sm + X_CS + (16 * n + li) * 4);
    f32x2 nm[4], k1[4], k0[4], S1[4] = {}, S2[4] = {};
#pragma unroll
    for (int jp = 0; jp < 4; ++jp) {
      rs[jp] = f32x2{asf(hc[jp][0]), asf(hc[jp][2])}; nm[jp] = f32x2{asf(hc[jp][1]), asf(hc[jp][3])};
      k0[jp] = rs[jp] * GC0; k1[jp] = rs[jp] * rs[jp] * rs[jp] * GC1;
    }
#pragma unroll
    for (int n = 0; n < 9; ++n) {
#pragma unroll
      for (int jp = 0; jp < 4; ++jp) {
        f32x2 a = (jp & 1) ? hi2(acc[jp >> 1][n]) : lo2(acc[jp >> 1][n]);
        f32x2 z = gelu2(__builtin_elementwise_fma(nm[jp], f32x2{cs[n], cs[n]}, a), k1[jp], k0[jp]);
        zb[n][jp] = pk2(z);
        S1[jp] += z; S2[jp] = __builtin_elementwise_fma(z, z, S2[jp]);
      }
      asm volatile("" : "+v"(zb[n]));
    }
#pragma unroll
    for (int jp = 0; jp < 4; ++jp) { S1[jp] *= rs[jp]; S2[jp] *= rs[jp] * rs[jp]; }
    float v[16];
#pragma unroll
    for (int j = 0; j < 8; ++j) { v[2 * j] = S1[j >> 1][j & 1]; v[2 * j + 1] = S2[j >> 1][j & 1]; }
#pragma unroll
    for (int i = 0; i < 8; ++i) { bool h = li & 8; v[i] = (h ? v[i + 8] : v[i]) + dpp_get<0x128>(h ? v[i] : v[i + 8]); }
#pragma unroll
    for (int i = 0; i < 4; ++i) { bool h = li & 4; v[i] = (h ? v[i + 4] : v[i]) + dpp_get<0x141>(h ? v[i] : v[i + 4]); }
#pragma unroll
    for (int i = 0; i < 2; ++i) { bool h = li & 2; v[i] = (h ? v[i + 2] : v[i]) + dpp_get<0x4E>(h ? v[i] : v[i + 2]); }
    { bool h = li & 1; v[0] = (h ? v[1] : v[0]) + dpp_get<0xB1>(h ? v[0] : v[1]); }
    unsigned tot = asu(v[0]);
    st4(R, lane * 4, O_ST + ((tp * 4 + w) * 16 + fp) * 256, tot == SENT ? 0x7FC00000u : tot);
#pragma unroll
    for (int m = 0; m < 2; ++m)
#pragma unroll
      for (int n = 0; n < 9; ++n) acc[m][n] = f32x4{0, 0, 0, 0};
  }
  STAMP(3,2);
  auto it2 = [&](int t) {
    seg<0, 1, true>(acc, X, Y, c, t, 1);
    bar();
    seg<0, 1, true>(acc, Y, X, c, t + 1, 0);
  };
  const char* sts = ws + O_ST + (tp * 4 + w) * 4096;
#pragma unroll 1
  for (int t = 9; t < TCHK; ++t) {
    if (t == TPOLL) glds4<true>(lds_addr(pvb), sts, lane * 16, lane * 16, lane * 16, lane * 16);
    __builtin_amdgcn_sched_barrier(0);
    it2(t);
  }
  STAMP(3,3);
  u32x4 pv[4];
#pragma unroll 1
  for (int spin = 0; spin < SPIN; ++spin) {
    wait_vm<0>();
    bool ok = true;
#pragma unroll
    for (int k = 0; k < 4; ++k) {
      pv[k] = *(const u32x4*)(pvb + k * 1024 + lane * 16);
      ok = ok && pv[k][0] != SENT && pv[k][1] != SENT && pv[k][2] != SENT && pv[k][3] != SENT;
    }
    if (__all(ok)) break;
    glds4<true>(lds_addr(pvb), sts, lane * 16, lane * 16, lane * 16, lane * 16);
  }
  STAMP(3,4);
  {
    float t4[4];
#pragma unroll
    for (int e = 0; e < 4; ++e) t4[e] = q_sum((asf(pv[0][e]) + asf(pv[1][e])) + (asf(pv[2][e]) + asf(pv[3][e])));
    f32x2 o0, o1;
#pragma unroll
    for (int r = 0; r < 2; ++r) {
      float mean = t4[2 * r] * (1.0f / FH), var = fmaxf(t4[2 * r + 1] * (1.0f / FH) - mean * mean, 0.f), mb, ri;
      ln_scalars(mean, var, mb, ri);
      o0[r] = ri; o1[r] = -mb * ri;
    }
    char* ln = sm + X_LN + w * 256;
    if (lane < 16) { *(f32x2*)(ln + li * 8) = o0; *(f32x2*)(ln + 128 + li * 8) = o1; }
    asm volatile("" ::: "memory");
    f32x4 ri[2], rb[2];
#pragma unroll
    for (int m = 0; m < 2; ++m) { ri[m] = *(const f32x4*)(ln + (8 * q + 4 * m) * 4); rb[m] = *(const f32x4*)(ln + 128 + (8 * q + 4 * m) * 4); }
#pragma unroll
    for (int jp = 0; jp < 4; ++jp) rs[jp] *= (jp & 1) ? hi2(ri[jp >> 1]) : lo2(ri[jp >> 1]);
    const unsigned zo = lane * 16 + O_ZF + (fp * 64 + 4 * tp + w) * 9216;
#pragma unroll
    for (int n = 0; n < 9; ++n) {
      u32x4 o4;
#pragma unroll
      for (int jp = 0; jp < 4; ++jp)
        o4[jp] = pk2(__builtin_elementwise_fma(up2(zb[n][jp]), rs[jp], (jp & 1) ? hi2(rb[jp >> 1]) : lo2(rb[jp >> 1])));
      st16(R, zo, n * 1024, o4);
    }
  }
  STAMP(3,5);
#pragma unroll 1
  for (int t = TCHK; t < 17; ++t) {
    seg<0, 1, true>(acc, X, Y, c, t, 1);
    if (t == TFLAG) wait_vm<0>();
    bar();
    if (t == TFLAG && tid < 16) st8(R, tid * 128, O_ZFL + fp * 2048 + tp * 8, tag);
    seg<0, 1, true>(acc, Y, X, c, t + 1, 0);
  }
  seg<0, 1, true>(acc, X, Y, c, 17, 1);
  seg<0, 1, false>(acc, Y, X, c, 17, 1);
  STAMP(3,6);
  u32x2 z1[2][9];
  float bs[2];
#pragma unroll
  for (int m = 0; m < 2; ++m) bs[m] = ldbf(b_sp, 128 * tp + 32 * w + prow + 4 * m) * lrs[m];
  {
    f32x4 cs[9];
#pragma unroll
    for (int n = 0; n < 9; ++n) cs[n] = *(const f32x4*)(sm + X_CS + (144 + 16 * n + 4 * q) * 4);
#pragma unroll
    for (int n = 0; n < 9; ++n)
#pragma unroll
      for (int m = 0; m < 2; ++m) {
        const float a1 = lrs[m] * lrs[m] * lrs[m] * GC1, a0 = lrs[m] * GC0;
        const f32x2 k1 = {a1, a1}, k0 = {a0, a0}, nm = {lnm[m], lnm[m]};
        z1[m][n][0] = pk2(gelu2(__builtin_elementwise_fma(nm, lo2(cs[n]), lo2(acc[m][n])), k1, k0));
        z1[m][n][1] = pk2(gelu2(__builtin_elementwise_fma(nm, hi2(cs[n]), hi2(acc[m][n])), k1, k0));
        asm volatile("" : "+v"(z1[m][n]));
        acc[m][n] = f32x4{0, 0, 0, 0};
      }
  }
  STAMP(3,7);
  bar();
  STAMP(3,8);
  ldf<1>(X, c, 18, 0);
#pragma unroll 1
  for (int t = 18; t < 49; ++t) {
    seg<1, 1, true>(acc, X, Y, c, t, 1);
    bar();
    seg<1, 1, true>(acc, Y, X, c, t + 1, 0);
  }
  seg<1, 1, true>(acc, X, Y, c, 49, 1);
  seg<1, 1, false>(acc, Y, X, c, 49, 1);
  STAMP(3,9);
  {
    const unsigned pf = O_PF + (((2 * tp + (w >> 1)) * 72 + 9 * (fp >> 1)) * 4 + 2 * (w & 1)) * 1024;
    const unsigned po = lane * 16 + pf + (fp & 1) * 16384;
#pragma unroll
    for (int m = 0; m < 2; ++m) {
      const f32x2 r2 = {lrs[m], lrs[m]}, b2 = {bs[m], bs[m]};
      auto pp = [&](int n) { return u32x2{pk2(__builtin_elementwise_fma(lo2(acc[m][n]), r2, b2) * up2(z1[m][n][0])), pk2(__builtin_elementwise_fma(hi2(acc[m][n]), r2, b2) * up2(z1[m][n][1]))}; };
#pragma unroll
      for (int a = 0; a < 4; ++a) {
        u32x2 p0 = pp(2 * a), p1 = pp(2 * a + 1);
        st16(R, po, a * 4096 + m * 1024, u32x4{p0[0], p0[1], p1[0], p1[1]});
      }
      st8(R, lane * 16, pf + 8 * 4096 + m * 1024 + (fp & 1) * 8, pp(8));
    }
  }
  STAMP(3,10);
  arrive(2, 4, O_PFL + tp * 2048 + fp * 8, 128, 16);
  const int th = w & 1, kq = w >> 1, T = 4 * t64 + 2 * th + kq;
  const int lane5 = fresh_lane();
#define lane lane5
#define li (lane5 & 15)
#define q (lane5 >> 4)
#define prow (8 * (li >> 2) + (li & 3))
  u32x2 hv[5];
#pragma unroll
  for (int n = 0; n < 5; ++n) {
    const int dd = 72 * j3 + 16 * n + 4 * (n == 4 ? (q & 1) : q);
    hv[n] = ld8(R, (dd >> 5) * 8192 + ((dd >> 2) & 1) * 512 + (((dd >> 3) & 3) * 16 + li) * 8, O_HF + ((T >> 3) * 18 * 8 + (T & 7)) * 1024);
  }
  struct F3 { u32x4 p[2][2], n[9]; };
  f32x4 a3[2][6] = {};
  F3 U, V;
  const int ap = (8 * kq + 2 * th) * 1024 + lane * 16, aw = 16384 + kq * 9216 + lane * 16;
  auto ld3 = [&](F3& f, int t) {
    const char* slot = sm + (t & 3) * STG;
#pragma unroll
    for (int g = 0; g < 2; ++g)
#pragma unroll
      for (int m = 0; m < 2; ++m) f.p[g][m] = *(const u32x4*)(slot + ap + (4 * g + m) * 1024);
#pragma unroll
    for (int n = 0; n < 9; ++n) f.n[n] = *(const u32x4*)(slot + aw + n * 1024);
  };
  auto mm3 = [&](const F3& f) {
#pragma unroll
    for (int g = 0; g < 2; ++g)
#pragma unroll
      for (int m = 0; m < 2; ++m) {
#pragma unroll
        for (int n = 0; n < 4; ++n) a3[m][n] = mfma_bf(f.n[4 * g + n], f.p[g][m], a3[m][n]);
        a3[m][4 + g] = mfma_bf(f.n[8], f.p[g][m], a3[m][4 + g]);
      }
#pragma unroll
    for (int i = 0; i < 13; ++i) { __builtin_amdgcn_sched_group_barrier(0x008, 1, 0); __builtin_amdgcn_sched_group_barrier(0x100, 1, 0); }
    __builtin_amdgcn_sched_barrier(0);
  };
  STAMP(3,11);
  bar();
  STAMP(3,12);
  ld3(U, 50);
#pragma unroll 1
  for (int t = 50; t < 66; t += 2) { bar(); ld3(V, t + 1); mm3(U); bar(); ld3(U, t + 2); mm3(V); }
  bar(); ld3(V, 67); mm3(U); mm3(V);
  STAMP(3,13);
#pragma unroll
  for (int m = 0; m < 2; ++m)
#pragma unroll
    for (int r = 0; r < 4; ++r) a3[m][4][r] += __shfl_xor(a3[m][5][r], 32);
#pragma unroll
  for (int n = 0; n < 5; ++n) {
    *(f32x4*)(sm + (w * 5 + n) * 1024 + lane * 16) = kq ? a3[0][n] : a3[1][n];
    a3[0][n] = kq ? a3[1][n] : a3[0][n];
  }
  bar();
  char* o = out + (size_t)(32 * (T >> 1) + 4 * (T & 1) + prow) * 1152 + (72 * j3 + 4 * q) * 2;
#pragma unroll
  for (int n = 0; n < 5; ++n) {
    f32x4 v = a3[0][n] + *(const f32x4*)(sm + ((w ^ 2) * 5 + n) * 1024 + lane * 16);
    if (n < 4 || q < 2) *(u32x2*)(o + n * 32) = u32x2{pk2(up2(hv[n][0]) + up2(pk2(lo2(v)))), pk2(up2(hv[n][1]) + up2(pk2(hi2(v))))};
  }
  if (bid == 0 && w == 0 && lane == 0) { u64 e = (((u64)tag[1] << 32) | tag[0]) - 0x9E3779B97F4A7C15ull + 1; st8(R, 0, O_EP, u32x2{(unsigned)e, (unsigned)(e >> 32)}); }
  STAMP(3,14);
}

#undef lane
#undef li
#undef q
#undef prow
extern "C" void kernel_launch(void* const* d_in, const int* in_sizes, int n_in, void* d_out, int out_size, void* d_ws, size_t ws_size,
                              hipStream_t stream) {
  (void)hipFuncSetAttribute((const void*)k_all, hipFuncAttributeMaxDynamicSharedMemorySize, LDS_BYTES);
  k_all<<<256, 512, LDS_BYTES, stream>>>((const char*)d_in[0], (const char*)d_in[1], (const char*)d_in[2], (const char*)d_in[3],
      (const char*)d_in[4], (const char*)d_in[5], (const char*)d_in[6], (const char*)d_in[7], (const char*)d_in[8], (const char*)d_in[10],
      (const char*)d_in[11], (const char*)d_in[12], (const char*)d_in[13], (char*)d_out, (char*)d_ws);
}
```

```cpp
#include <hip/hip_runtime.h>
#include <hip/hip_bf16.h>
#include <stdint.h>

typedef __attribute__((ext_vector_type(8))) __bf16 bf16x8;
typedef __attribute__((ext_vector_type(2))) __bf16 bf16x2;
typedef __attribute__((ext_vector_type(8))) _Float16 f16x8;
typedef __attribute__((ext_vector_type(2))) _Float16 f16x2;
typedef __attribute__((ext_vector_type(4))) float f32x4;
typedef __attribute__((ext_vector_type(2))) float f32x2;
typedef __attribute__((ext_vector_type(4))) unsigned u32x4;
typedef __attribute__((ext_vector_type(2))) unsigned u32x2;
typedef __attribute__((ext_vector_type(4))) short s16x4;
#define LDSP __attribute__((address_space(3)))
#define DEVI __device__ __forceinline__

constexpr int L = 2048, D = 576, NM = 128, F = 4608, FH = 2304;
constexpr float EPS = 1e-5f;
constexpr unsigned O_EP = 0;
constexpr int NREP = 32;
constexpr unsigned O_F1 = 256;
constexpr unsigned O_F2 = O_F1 + NREP * 1024;
constexpr unsigned O_ZFL = O_F2 + NREP * 2048;
constexpr unsigned O_PFL = O_ZFL + 32768;
constexpr unsigned O_CS = 167936;
static_assert(O_PFL + 32768 <= O_CS, "ws");
constexpr unsigned O_ST = O_CS + F * 8;
constexpr unsigned O_HS = O_ST + 262144;
constexpr unsigned O_U = O_HS + 16384;
constexpr unsigned O_XB = O_U + L * D * 2;
constexpr unsigned O_WI = O_XB + L * D * 2;
constexpr unsigned O_WO = O_WI + D * F * 2;
constexpr unsigned O_HF = O_WO + FH * D * 2;
constexpr unsigned O_ZF = O_HF + L * D * 2;
constexpr unsigned O_PF = O_ZF + L * FH * 2;
constexpr unsigned WS_USED = O_PF + L * FH * 2;
constexpr size_t O_STAMP = 128u << 20;
#define STAMP(k,i)
#define STAMPW(k,i,W)
#define RSTAMP(k,i)
constexpr unsigned SENT = 0xFFFFFFFFu;

DEVI unsigned pk_bf16(float a, float b) { f32x2 v = {a, b}; return __builtin_bit_cast(unsigned, __builtin_convertvector(v, bf16x2)); }
DEVI unsigned pk_f16(float a, float b) { f32x2 v = {a, b}; return __builtin_bit_cast(unsigned, __builtin_convertvector(v, f16x2)); }
DEVI float bf_lo(unsigned u) { return __builtin_bit_cast(float, u << 16); }
DEVI float bf_hi(unsigned u) { return __builtin_bit_cast(float, u & 0xFFFF0000u); }
DEVI float asf(unsigned u) { return __builtin_bit_cast(float, u); }
DEVI unsigned asu(float f) { return __builtin_bit_cast(unsigned, f); }
DEVI float rbf(float a) { return bf_lo(pk_bf16(a, a)); }
DEVI float h_lo(unsigned u) { return (float)__builtin_bit_cast(f16x2, u)[0]; }
DEVI float h_hi(unsigned u) { return (float)__builtin_bit_cast(f16x2, u)[1]; }
template <int CTRL> DEVI float dpp_add(float v) {
  return v + __builtin_bit_cast(float, __builtin_amdgcn_update_dpp(0, __builtin_bit_cast(int, v), CTRL, 0xF, 0xF, true));
}
DEVI float row16_sum(float v) { v = dpp_add<0x128>(v); v = dpp_add<0x124>(v); v = dpp_add<0x122>(v); v = dpp_add<0x121>(v); return v; }
DEVI float q_sum(float v) { v += __shfl_xor(v, 16); v += __shfl_xor(v, 32); return v; }
DEVI u32x2 tr_read(const char* p) {
  return __builtin_bit_cast(u32x2, __builtin_amdgcn_ds_read_tr16_b64_v4i16((LDSP s16x4*)p));
}
DEVI f32x4 mfma_bf(u32x4 a, u32x4 b, f32x4 c) {
  return __builtin_amdgcn_mfma_f32_16x16x32_bf16(__builtin_bit_cast(bf16x8, a), __builtin_bit_cast(bf16x8, b), c, 0, 0, 0);
}
DEVI f32x4 mfma_h(u32x4 a, u32x4 b, f32x4 c) {
  return __builtin_amdgcn_mfma_f32_16x16x32_f16(__builtin_bit_cast(f16x8, a), __builtin_bit_cast(f16x8, b), c, 0, 0, 0);
}
DEVI void ln_scalars(float mean, float var, float& mb, float& rinv) {
  mb = rbf(mean);
  float d = rbf(__builtin_amdgcn_sqrtf(rbf(rbf(var) + EPS)));
  rinv = __builtin_amdgcn_rcpf(d);
}
DEVI float gelu(float x) {
  float w = x * __builtin_fmaf(x * x, -0.10294324f, -2.3022082f);
  return x * __builtin_amdgcn_rcpf(1.0f + __builtin_amdgcn_exp2f(w));
}

typedef unsigned long long u64;
typedef __amdgpu_buffer_rsrc_t rsrc_t;
constexpr int SPIN = 1 << 16;
template <int CTRL> DEVI float dpp_get(float v) {
  return __builtin_bit_cast(float, __builtin_amdgcn_update_dpp(0, __builtin_bit_cast(int, v), CTRL, 0xF, 0xF, true));
}
constexpr float GC1 = -0.10294324f, GC0 = -2.3022082f;
DEVI f32x2 gelu2(f32x2 x, f32x2 k1, f32x2 k0) {
  f32x2 w = x * __builtin_elementwise_fma(x * x, k1, k0);
  f32x2 d = f32x2{__builtin_amdgcn_exp2f(w[0]), __builtin_amdgcn_exp2f(w[1])} + f32x2{1.f, 1.f};
  return x * f32x2{__builtin_amdgcn_rcpf(d[0]), __builtin_amdgcn_rcpf(d[1])};
}
DEVI unsigned pk2(f32x2 v) { return __builtin_bit_cast(unsigned, __builtin_convertvector(v, bf16x2)); }
DEVI f32x2 up2(unsigned u) { return f32x2{bf_lo(u), bf_hi(u)}; }
DEVI f32x2 lo2(f32x4 v) { return __builtin_shufflevector(v, v, 0, 1); }
DEVI f32x2 hi2(f32x4 v) { return __builtin_shufflevector(v, v, 2, 3); }
DEVI float dot2(unsigned a, unsigned b, float c) {
  return __builtin_amdgcn_fdot2_f32_bf16(__builtin_bit_cast(bf16x2, a), __builtin_bit_cast(bf16x2, b), c, false);
}
DEVI void st16(rsrc_t r, unsigned voff, unsigned soff, u32x4 v) { __builtin_amdgcn_raw_buffer_store_b128(v, r, voff + soff, 0, 16); }
DEVI void st8(rsrc_t r, unsigned voff, unsigned soff, u32x2 v) { __builtin_amdgcn_raw_buffer_store_b64(v, r, voff, soff, 16); }
DEVI void st4(rsrc_t r, unsigned voff, unsigned soff, unsigned v) { __builtin_amdgcn_raw_buffer_store_b32(v, r, voff, soff, 16); }
DEVI u32x4 ld16(rsrc_t r, unsigned voff, unsigned soff) { return __builtin_amdgcn_raw_buffer_load_b128(r, voff, soff, 16); }
DEVI u32x2 ld8(rsrc_t r, unsigned voff, unsigned soff) { return __builtin_amdgcn_raw_buffer_load_b64(r, voff, soff, 16); }
template <int N> DEVI void wait_vm() { asm volatile("s_waitcnt vmcnt(%0)" ::"i"(N) : "memory"); }
DEVI void bar() { __builtin_amdgcn_sched_barrier(0); asm volatile("s_waitcnt lgkmcnt(0)\n\ts_barrier" ::: "memory"); __builtin_amdgcn_sched_barrier(0); }
DEVI int fresh_lane() { int l; asm volatile("v_mbcnt_lo_u32_b32 %0, -1, 0\n\tv_mbcnt_hi_u32_b32 %0, -1, %0" : "=v"(l)); return l; }
DEVI unsigned lds_addr(const char* p) { return (unsigned)(uintptr_t)(LDSP const char*)p; }
template <int NL> DEVI void wait_flags(rsrc_t r, unsigned off, int n, u32x2 tag, int lane) {
#pragma unroll 1
  for (int spin = 0; spin < SPIN; ++spin) {
    bool ok = true;
#pragma unroll
    for (int k = 0; k < NL; ++k) {
      int i = min(lane + 64 * k, n / 2 - 1);
      u32x4 v = ld16(r, i * 16, off);
      ok = ok && v[0] == tag[0] && v[1] == tag[1] && v[2] == tag[0] && v[3] == tag[1];
    }
    if (__all(ok)) break;
    __builtin_amdgcn_s_sleep(1);
    asm volatile("" ::: "memory");
  }
  asm volatile("" ::: "memory");
}
template <bool SC> DEVI void glds4(unsigned m0v, const char* base, unsigned v0, unsigned v1, unsigned v2, unsigned v3) {
  unsigned keep;
  if (SC)
    asm volatile("s_mov_b32 %0, m0\n\ts_mov_b32 m0, %1\n\ts_nop 0\n\t"
                 "global_load_lds_dwordx4 %3, %2 sc1\n\tglobal_load_lds_dwordx4 %4, %2 offset:1024 sc1\n\t"
                 "global_load_lds_dwordx4 %5, %2 offset:2048 sc1\n\tglobal_load_lds_dwordx4 %6, %2 offset:3072 sc1\n\ts_mov_b32 m0, %0"
                 : "=&s"(keep) : "s"(m0v), "s"(base), "v"(v0), "v"(v1), "v"(v2), "v"(v3) : "memory");
  else
    asm volatile("s_mov_b32 %0, m0\n\ts_mov_b32 m0, %1\n\ts_nop 0\n\t"
                 "global_load_lds_dwordx4 %3, %2\n\tglobal_load_lds_dwordx4 %4, %2 offset:1024\n\t"
                 "global_load_lds_dwordx4 %5, %2 offset:2048\n\tglobal_load_lds_dwordx4 %6, %2 offset:3072\n\ts_mov_b32 m0, %0"
                 : "=&s"(keep) : "s"(m0v), "s"(base), "v"(v0), "v"(v1), "v"(v2), "v"(v3) : "memory");
}
DEVI void glds1(unsigned m0v, const char* base, unsigned v0) {
  unsigned keep;
  asm volatile("s_mov_b32 %0, m0\n\ts_mov_b32 m0, %1\n\ts_nop 0\n\tglobal_load_lds_dwordx4 %3, %2 sc1\n\ts_mov_b32 m0, %0"
               : "=&s"(keep) : "s"(m0v), "s"(base), "v"(v0) : "memory");
}
template <int NP> DEVI void ld_lin(unsigned dst, const char* src, unsigned l16) {
  glds4<true>(dst, src, l16, l16, l16, l16);
  glds4<true>(dst + 4096, src + 4096, l16, l16, l16, l16);
  if (NP == 9) glds1(dst + 8192, src + 8192, l16);
}

constexpr int TP = 1168;
constexpr int CV_K = 8 * 260 * 16, CV_U = 4576;
constexpr int C_U = 4 * CV_K, C_KD = C_U + 4 * CV_U, C_MX = C_KD + 4 * 1536, C_END = C_MX + 64;
constexpr int KT = 528;
constexpr int STG = 34816, RING = 4 * STG;
constexpr int X_CS = RING, X_LN = X_CS + 1152, X_PV = X_LN + 1024, X_CNT = 158720;
constexpr int LDS_BYTES = X_CNT + 64;
static_assert(X_PV + 16384 <= X_CNT && LDS_BYTES <= 163840, "lds");
#define TPOLL 12
#define TLBW 2
#define TCHK 13
#define TFLAG 15

struct Fr { u32x4 m[2], n[9]; };
struct Ctx { const char* sm; int am0[4], am1, an; };
template <int MK> DEVI void ldf(Fr& f, const Ctx& c, int t, int kk) {
  int so = (t & 3) * STG;
  asm volatile("" : "+s"(so));
  const char* slot = c.sm + so;
  if (MK == 0) {
#pragma unroll
    for (int m = 0; m < 2; ++m) {
      u32x2 a = *(const u32x2*)(slot + c.am0[2 * m] + kk * 8192), b = *(const u32x2*)(slot + c.am0[2 * m + 1] + kk * 8192);
      f.m[m] = u32x4{a[0], a[1], b[0], b[1]};
    }
  } else {
#pragma unroll
    for (int m = 0; m < 2; ++m) f.m[m] = *(const u32x4*)(slot + (c.am1 ^ (kk * 64)) + m * 2048);
  }
#pragma unroll
  for (int n = 0; n < 9; ++n) f.n[n] = *(const u32x4*)(slot + c.an + (kk * 9 + n) * 1024);
}
template <int MK, int ORI, bool LD> DEVI void seg(f32x4 (&acc)[2][9], const Fr& cur, Fr& nxt, const Ctx& c, int t, int kk) {
  if (LD) ldf<MK>(nxt, c, t, kk);
#pragma unroll
  for (int m = 0; m < 2; ++m)
#pragma unroll
    for (int nn = 0; nn < 9; ++nn) {
      const int n = m ? 8 - nn : nn;
      acc[m][n] = ORI == 0 ? mfma_bf(cur.m[m], cur.n[n], acc[m][n]) : mfma_bf(cur.n[n], cur.m[m], acc[m][n]);
    }
#pragma unroll
  for (int i = 0; i < (MK == 0 ? 13 : 11); ++i) { __builtin_amdgcn_sched_group_barrier(0x008, 1, 0); __builtin_amdgcn_sched_group_barrier(0x100, 1, 0); }
  __builtin_amdgcn_sched_barrier(0);
}
DEVI f32x2 cmul(f32x2 a, f32x2 b, f32x2 bp) {
  return __builtin_elementwise_fma(f32x2{a[1], a[1]}, bp, f32x2{a[0], a[0]} * b);
}
DEVI f32x2 perp(f32x2 b) { return f32x2{-b[1], b[0]}; }
DEVI f32x2 cexp(float ar, float ai, float l) {
  float m = __builtin_amdgcn_exp2f(ar * l), ph = __builtin_amdgcn_fractf(ai * l);
  return f32x2{m * __builtin_amdgcn_cosf(ph), m * __builtin_amdgcn_sinf(ph)};
}
DEVI unsigned pkh(f32x2 v) { return __builtin_bit_cast(unsigned, __builtin_convertvector(v, f16x2)); }
DEVI float ldbf(const char* p, int i) { return bf_lo(*(const unsigned short*)(p + 2 * i)); }

__global__ __launch_bounds__(512, 2) void k_all(const char* __restrict__ x, const char* __restrict__ a_re, const char* __restrict__ a_im,
    const char* __restrict__ b_re, const char* __restrict__ b_im, const char* __restrict__ c_re, const char* __restrict__ c_im,
    const char* __restrict__ log_dt, const char* __restrict__ dskip, const char* __restrict__ w_in, const char* __restrict__ w_sp,
    const char* __restrict__ b_sp, const char* __restrict__ w_out, char* __restrict__ out, char* __restrict__ ws) {
  extern __shared__ __attribute__((aligned(16))) char sm[];
  const int tid = threadIdx.x, lane = tid & 63, w = __builtin_amdgcn_readfirstlane(tid >> 6), li = lane & 15, q = lane >> 4;
  const int bid = blockIdx.x, xc = bid & 7, ii = bid >> 3;
  const rsrc_t R = __builtin_amdgcn_make_buffer_rsrc(ws, 0, WS_USED, 0x00020000);
  STAMP(0,0);
  const u32x2 ep = ld8(R, 0, O_EP);
  const bool is_conv = ii < 18;
  if (w == 7) {
    unsigned long long pc;
    asm volatile("s_getpc_b64 %0" : "=s"(pc));
    const char* cp = (const char*)(pc & ~255ull) + (ii & 7) * 5376 + lane * 128;
    unsigned v0 = 0, v1 = 0;
    if (lane < 42) v0 = *(const unsigned*)cp;
    {
      const char* tp_ = lane < 36 ? ws + min((unsigned)lane << 20, WS_USED - 4) : (lane < 44 ? w_sp + ((lane - 36) << 20) : (lane < 47 ? out + ((lane - 44) << 20) : b_sp));
      v1 = *(const unsigned*)tp_;
    }
    constexpr int NK = TLBW == 2 ? 1 : 6, SH = TLBW == 2 ? 16 : 12;
    unsigned v2[NK];
#pragma unroll
    for (int k = 0; k < NK; ++k) {
      const unsigned o = (unsigned)((k * 64 + lane) * 32 + ii) << SH;
      const char* tp_ = o < WS_USED ? ws + o : (o < WS_USED + 8388608 - 4 ? w_sp + ((o - WS_USED) & ~3u) : out + min((o - WS_USED - 8388608 + 4) & ~3u, 2359292u));
      v2[k] = *(const unsigned*)tp_;
    }
#pragma unroll
    for (int k = 0; k < NK; ++k) asm volatile("" ::"v"(v2[k]));
    asm volatile("" ::"v"(v0), "v"(v1));
  }
  const int cg = xc * 18 + ii, rep = bid & (NREP - 1);
  if (tid == 0) *(u32x4*)(sm + X_CNT) = u32x4{0, 0, 0, 0};
  auto mk_tag = [&]() {
    u64 e = (((u64)__builtin_amdgcn_readfirstlane(ep[1]) << 32) | (unsigned)__builtin_amdgcn_readfirstlane(ep[0])) + 0x9E3779B97F4A7C15ull;
    return u32x2{(unsigned)e, (unsigned)(e >> 32)};
  };
  u32x2 tag;
  auto arrive = [&](int slot, unsigned n, unsigned flag_off, unsigned stride, int nrep) {
    wait_vm<0>();
    const int lane = fresh_lane();
    unsigned old = 0;
    if (lane == 0) old = __hip_atomic_fetch_add((unsigned*)(sm + X_CNT) + slot, 1u, __ATOMIC_RELAXED, __HIP_MEMORY_SCOPE_WORKGROUP);
    if (__builtin_amdgcn_readfirstlane(old) == n - 1 && lane < nrep) st8(R, lane * stride, flag_off, tag);
  };
  if (is_conv) {
    const int ch = w & 3, dch = 4 * cg + ch;
    char* sk = sm + ch * CV_K;
    const char* su = sm + C_U + ch * CV_U;
    float sc = 0.f;
    if (w < 4) {
      const int pi = (dch * NM + 2 * lane) * 2;
      const unsigned v_ar = *(const unsigned*)(a_re + pi), v_ai = *(const unsigned*)(a_im + pi), v_br = *(const unsigned*)(b_re + pi),
                     v_bi = *(const unsigned*)(b_im + pi), v_cr = *(const unsigned*)(c_re + pi), v_ci = *(const unsigned*)(c_im + pi);
      const float p_dk = ldbf(dskip, dch);
      const float dt = rbf(__expf(ldbf(log_dt, dch)));
      tag = mk_tag();
      STAMP(0,1);
      const f32x2 ar = (up2(v_ar) * dt) * 1.44269504f, ai = (up2(v_ai) * dt) * 0.15915494f;
      const f32x2 br = up2(v_br) * dt, bi = up2(v_bi) * dt, cr = up2(v_cr), ci = up2(v_ci);
      const f32x2 cbr = cr * br - ci * bi, cbi = -(cr * bi + ci * br);
      float mx = fmaxf(fmaxf(fabsf(cbr[0]), fabsf(cbi[0])), fmaxf(fabsf(cbr[1]), fabsf(cbi[1])));
      mx = fmaxf(mx, dpp_get<0x128>(mx)); mx = fmaxf(mx, dpp_get<0x124>(mx)); mx = fmaxf(mx, dpp_get<0x122>(mx)); mx = fmaxf(mx, dpp_get<0x121>(mx));
      auto rl = [&](int l) { return asf(__builtin_amdgcn_readlane((int)asu(mx), l)); };
      sc = fmaxf(fmaxf(rl(0), rl(16)), fmaxf(rl(32), rl(48)));
      sc = fmaxf(sc, fabsf(p_dk) * (1.0f / 256));
      const float isc = sc > 0.f ? 1.0f / sc : 0.f;
      {
        struct C2 { f32x2 a, b; };
        char* tb = sk + lane * 8;
        auto put = [&](int row, C2 v) { *(u32x2*)(tb + row * KT) = u32x2{pkh(v.a), pkh(v.b)}; };
        auto mul = [&](C2 v, C2 u, C2 up) { return C2{cmul(v.a, u.a, up.a), cmul(v.b, u.b, up.b)}; };
        auto prp = [&](C2 v) { return C2{perp(v.a), perp(v.b)}; };
        auto ex = [&](float l) { return C2{cexp(ar[0], ai[0], l), cexp(ar[1], ai[1], l)}; };
        const C2 w1 = ex(1.f), w128 = ex(128.f);
        C2 w8 = ex(8.f); w8.a[1] = -w8.a[1]; w8.b[1] = -w8.b[1];
        const C2 w1p = prp(w1), w8p = prp(w8), w128p = prp(w128);
        C2 y = {f32x2{cbr[0], cbi[0]} * isc, f32x2{cbr[1], cbi[1]} * isc};
        C2 E[8]; E[0] = C2{f32x2{1.f, 0.f}, f32x2{1.f, 0.f}}; E[1] = w1;
#pragma unroll
        for (int e = 2; e < 8; ++e) E[e] = mul(E[e - 1], w1, w1p);
#pragma unroll
        for (int e = 0; e < 8; ++e) { put(e, E[e]); put(8 + e, e ? mul(E[e], w128, w128p) : w128); put(41 + e, y); y = mul(y, w8, w8p); }
        const C2 e2p = prp(E[2]), e4p = prp(E[4]), e6p = prp(E[6]);
        C2 H = w128;
#pragma unroll
        for (int a = 2; a <= 16; ++a) {
          H = mul(H, w128, w128p);
          if (a < 4) { put(8 + 4 * a, H); put(9 + 4 * a, mul(H, E[2], e2p)); put(10 + 4 * a, mul(H, E[4], e4p)); put(11 + 4 * a, mul(H, E[6], e6p)); }
          else if (a < 8) { put(16 + 2 * a, H); put(17 + 2 * a, mul(H, E[4], e4p)); }
          else put(24 + a, H);
          if (a < 10) { put(47 + a, y); if (a < 9) y = mul(y, w8, w8p); }
        }
      }
      asm volatile("" ::: "memory");
      STAMP(0,2);
      {
        f32x4 kk[3] = {};
        const char* pa = sk + li * KT + q * 16;
#pragma unroll
        for (int s = 0; s < 8; ++s) {
          u32x4 fb = *(const u32x4*)(pa + 41 * KT + s * 64);
#pragma unroll
          for (int t = 0; t < 3; ++t) kk[t] = mfma_h(*(const u32x4*)(pa + 16 * t * KT + s * 64), fb, kk[t]);
        }
        if (lane == 0) kk[0][0] += p_dk * isc;
        char* kd = sm + C_KD + ch * 1536;
        *(u32x2*)(kd + (128 * (q >> 1) + 4 * (q & 1) + 8 * li) * 2) = u32x2{pkh(lo2(kk[0])), pkh(hi2(kk[0]))};
        const int i1 = q < 2 ? 256 + 64 * q + 4 * li : 384 + 64 * (q - 2) + 2 * li;
        *(unsigned*)(kd + i1 * 2) = pkh(lo2(kk[1])); *(unsigned*)(kd + (i1 + (q < 2 ? 2 : 32)) * 2) = pkh(hi2(kk[1]));
#pragma unroll
        for (int r = 0; r < 4; ++r) *(_Float16*)(kd + (512 + 16 * (4 * q + r) + li) * 2) = (_Float16)kk[2][r];
      }
      asm volatile("" ::: "memory");
      u32x4 c0[4];
      {
        const unsigned short* kd = (const unsigned short*)(sm + C_KD + ch * 1536);
#pragma unroll
        for (int rr = 0; rr < 2; ++rr) {
          const int k = 255 - 64 * rr - lane;
          const unsigned a = kd[384 + k], b = kd[385 + k], aa = a | (a << 16);
          c0[rr] = u32x4{b | (b << 16), b | (((k & 1) ? b : a) << 16), aa, aa};
        }
        { const int k = 127 - lane;
          const unsigned ab = *(const unsigned*)(kd + 256 + 2 * k), cc = kd[258 + 2 * k];
          c0[2] = u32x4{cc | (cc << 16), __builtin_amdgcn_perm(ab, ab, 0x03020302), __builtin_amdgcn_perm(ab, ab, 0x01000302), __builtin_amdgcn_perm(ab, ab, 0x01000100)}; }
        { const int k = 63 - lane;
          const u32x4 v = *(const u32x4*)(kd + 8 * (k & 31));
          const u32x2 u = *(const u32x2*)(kd + 128 + 4 * k); const unsigned e = kd[132 + 4 * k];
          c0[3] = lane < 32 ? u32x4{__builtin_amdgcn_perm(u[1], e, 0x07060100), __builtin_amdgcn_perm(u[1], u[1], 0x01000100), __builtin_amdgcn_perm(u[0], u[1], 0x07060100), __builtin_amdgcn_perm(u[0], u[0], 0x01000100)}
                            : u32x4{__builtin_amdgcn_alignbit(v[3], v[3], 16), __builtin_amdgcn_alignbit(v[2], v[2], 16), __builtin_amdgcn_alignbit(v[1], v[1], 16), __builtin_amdgcn_alignbit(v[0], v[0], 16)}; }
#pragma unroll
        for (int rr = 0; rr < 4; ++rr) *(u32x4*)(sk + (64 * rr + lane) * 16) = c0[rr];
        if (lane < 8) *(u32x4*)(sk + (lane * 260 + 256) * 16) = u32x4{0, 0, 0, 0};
      }
      asm volatile("" ::: "memory");
      STAMP(0,3);
      u32x4 c1[4];
#pragma unroll
      for (int rr = 0; rr < 4; ++rr) c1[rr] = *(const u32x4*)(sk + (64 * rr + lane) * 16 + 16);
#pragma unroll
      for (int rr = 0; rr < 4; ++rr) {
        char* p = sk + (64 * rr + lane) * 16;
        unsigned e[8] = {c0[rr][0], c0[rr][1], c0[rr][2], c0[rr][3], c1[rr][0], c1[rr][1], c1[rr][2], c1[rr][3]}, o[7];
#pragma unroll
        for (int i = 0; i < 7; ++i) o[i] = __builtin_amdgcn_alignbit(e[i + 1], e[i], 16);
#pragma unroll
        for (int s = 0; s < 4; ++s) {
          if (s) *(u32x4*)(p + (2 * s) * 260 * 16) = u32x4{e[s], e[s + 1], e[s + 2], e[s + 3]};
          *(u32x4*)(p + (2 * s + 1) * 260 * 16) = u32x4{o[s], o[s + 1], o[s + 2], o[s + 3]};
        }
      }
      STAMP(0,4);
    } else {
      tag = mk_tag();
      if (cg < 136 && tid < 384) st16(R, (tid - 256) * 16, O_ST + cg * 2048, u32x4{SENT, SENT, SENT, SENT});
      if (lane < 30) *(u32x4*)(sm + C_U + ch * CV_U + lane * 16) = u32x4{0, 0, 0, 0};
      if (w == 4) {
        wait_flags<1>(R, O_F1 + rep * 1024, 64, tag, lane);
        STAMPW(5,0,4);
        const unsigned v0 = (lane >> 2) * 36864 + (lane & 3) * 16, dv = 16 * 36864 - 1024;
#pragma unroll
        for (int i = 0; i < 4; ++i) glds4<true>(lds_addr(sm) + C_U + i * CV_U + 480, ws + O_U + (4 * cg + i) * 64, v0, v0 + dv, v0 + 2 * dv, v0 + 3 * dv);
        wait_vm<0>();
      }
    }
    bar();
    STAMP(0,5);
    u32x4 xv[4];
    if (w < 4) {
      f32x4 acc[8] = {};
      const char* pa = sk + ((7 - (li & 7)) * 260 + 253 - (li >> 3) + q) * 16 - 4032;
      const char* pb = su + li * 32 + q * 16;
      struct CF { u32x4 a[8], b[8]; } A, B;
      auto ldc = [&](CF& f, int a0) {
#pragma unroll
        for (int m = 0; m < 8; ++m) { f.a[m] = *(const u32x4*)(pa + 4032 - 64 * a0 - 512 * m); f.b[m] = *(const u32x4*)(pb + 448 - 64 * a0 + 512 * m); }
      };
      auto mmc = [&](const CF& f) {
#pragma unroll
        for (int m = 0; m < 8; ++m)
#pragma unroll
          for (int t = m; t < 8; ++t) acc[t] = mfma_h(f.a[m], f.b[t - m], acc[t]);
#pragma unroll
        for (int i = 0; i < 16; ++i) { __builtin_amdgcn_sched_group_barrier(0x008, 2, 0); __builtin_amdgcn_sched_group_barrier(0x100, 1, 0); }
        __builtin_amdgcn_sched_barrier(0);
      };
      ldc(A, 0);
      __builtin_amdgcn_sched_barrier(0);
#pragma unroll
      for (int a0 = 0; a0 < 8; a0 += 2) { ldc(B, a0 + 1); mmc(A); if (a0 < 6) ldc(A, a0 + 2); mmc(B); }
      STAMP(0,6);
#pragma unroll
      for (int t = 0; t < 8; ++t) *(u32x2*)(sk + t * 512 + li * 32 + q * 8) = u32x2{pk2(lo2(acc[t]) * sc), pk2(hi2(acc[t]) * sc)};
    } else {
      wait_flags<1>(R, O_F1 + rep * 1024 + 512, 64, tag, lane);
#pragma unroll
      for (int r = 0; r < 4; ++r) {
        const int T = ((tid & 255) >> 3) + 32 * r, ip = tid & 7;
        xv[r] = ld16(R, (32 * (T >> 1) + 8 * (ip >> 1) + 4 * (T & 1) + 2 * (ip & 1)) * 8, O_XB + cg * 16384);
      }
      STAMPW(2,0,4);
    }
    bar();
    if (w >= 4) {
#pragma unroll
      for (int r = 0; r < 4; ++r) {
        const int T = ((tid & 255) >> 3) + 32 * r, ip = tid & 7;
        const char* p = sm + (32 * (T >> 1) + 8 * (ip >> 1) + 4 * (T & 1) + 2 * (ip & 1)) * 2;
        const unsigned c0 = *(const unsigned*)p, c1 = *(const unsigned*)(p + CV_K), c2 = *(const unsigned*)(p + 2 * CV_K), c3 = *(const unsigned*)(p + 3 * CV_K);
        u32x4 v = {pk2(up2(xv[r][0]) + f32x2{bf_lo(c0), bf_lo(c1)}), pk2(up2(xv[r][1]) + f32x2{bf_lo(c2), bf_lo(c3)}),
                   pk2(up2(xv[r][2]) + f32x2{bf_hi(c0), bf_hi(c1)}), pk2(up2(xv[r][3]) + f32x2{bf_hi(c2), bf_hi(c3)})};
        st16(R, (T >> 3) * (18 * 8192) + (T & 7) * 1024 + ip * 16, O_HF + (cg >> 3) * 8192 + (cg & 1) * 512 + ((cg >> 1) & 3) * 128, v);
      }
      STAMPW(2,1,4);
      arrive(1, 4, O_F2 + cg * 8, 2048, NREP);
      STAMPW(2,2,4);
    }
  } else {
    const int tj = (ii - 18) * 8 + xc;
    if (tj < 64) {
      char* s0 = sm + 83968;
      const char* src = x + (size_t)(32 * tj + (tid >> 4)) * 1152 + li * 8;
      u32x2 xr[9];
#pragma unroll
      for (int k = 0; k < 9; ++k) xr[k] = *(const u32x2*)(src + k * 128);
      asm volatile("" :: "v"(xr[8])); STAMP(1,0);
      float a[4] = {0, 0, 0, 0};
#pragma unroll
      for (int k = 0; k < 9; ++k)
#pragma unroll
        for (int e = 0; e < 2; ++e) { a[e] = dot2(xr[k][e], 0x3F803F80u, a[e]); a[2 + e] = dot2(xr[k][e], xr[k][e], a[2 + e]); }
      const float s = row16_sum(a[0] + a[1]), ss = row16_sum(a[2] + a[3]);
      float mean = s * (1.0f / D), mb, rinv;
      ln_scalars(mean, fmaxf(ss * (1.0f / D) - mean * mean, 0.f), mb, rinv);
      const f32x2 r2 = {rinv, rinv}, b2 = {-mb * rinv, -mb * rinv};
      char* p = s0 + (tid >> 4) * TP + li * 8;
#pragma unroll
      for (int k = 0; k < 9; ++k) {
        *(u32x2*)(p + k * 128) = u32x2{pkh(__builtin_elementwise_fma(up2(xr[k][0]), r2, b2)), pkh(__builtin_elementwise_fma(up2(xr[k][1]), r2, b2))};
        *(u32x2*)(p + 32 * TP + k * 128) = xr[k];
      }
      tag = mk_tag();
      bar();
      STAMP(1,1);
      if (w < 4) {
        const char* trb = s0 + (8 * q + (li >> 2)) * TP + (li & 3) * 8 + w * 288;
        const unsigned uo = li * 64 + q * 16 + O_U + tj * 36864 + w * 9216;
#pragma unroll
        for (int k = 0; k < 9; ++k) {
          u32x2 lo = tr_read(trb + k * 32), hi = tr_read(trb + 4 * TP + k * 32);
          st16(R, uo, k * 1024, u32x4{lo[0], lo[1], hi[0], hi[1]});
        }
        STAMP(1,2);
        arrive(0, 4, O_F1 + tj * 8, 1024, NREP);
        STAMP(1,3);
      } else {
        const int c16 = (tid >> 4) & 15;
        const char* px = s0 + (32 + li * 2) * TP + c16 * 8;
#pragma unroll
        for (int k = 0; k < 9; ++k) {
          u32x2 a = *(const u32x2*)(px + k * 128), b = *(const u32x2*)(px + TP + k * 128);
          st16(R, (c16 + 16 * k) * 16384 + li * 16, O_XB + tj * 256, u32x4{a[0], a[1], b[0], b[1]});
        }
        arrive(3, 4, O_F1 + (64 + tj) * 8, 1024, NREP);
        STAMPW(1,4,4);
      }
      if (tj < 36) {
        const int par = w >> 2, G = 2 * tj + par, row = (tid >> 3) & 31, uu = G / 9, idx = G % 9, hh = row >> 4;
        const int panel = idx < 4 ? 2 * uu : (idx < 8 ? 2 * uu + 1 : 2 * uu + hh), tile = idx < 8 ? 2 * (idx & 3) + hh : 8;
        const char* wsrc = w_out + (size_t)(144 * panel + 16 * tile + (row & 15)) * 1152 + (tid & 7) * 16;
        u32x4 v[9];
#pragma unroll
        for (int k = 0; k < 9; ++k) v[k] = *(const u32x4*)(wsrc + k * 128);
#pragma unroll
        for (int k = 0; k < 9; ++k) *(u32x4*)(sm + (tid >> 3) * TP + (tid & 7) * 16 + k * 128) = v[k];
        bar();
        const char* trb = sm + (32 * par + 4 * q + (li >> 2)) * TP + (li & 3) * 8;
#pragma unroll
        for (int jj = 0; jj < 2; ++jj) {
          const int j = 2 * (w & 3) + jj;
          const unsigned dst = O_WO + (j * 36 + tj) * 9216;
#pragma unroll
          for (int n = 0; n < 5; ++n) {
            const int c0 = 72 * j + 16 * n - (n == 4 ? 8 * par : 0);
            u32x2 lo = tr_read(trb + c0 * 2), hi = tr_read(trb + 16 * TP + c0 * 2);
            if (n < 4) st16(R, lane * 16, dst + (4 * par + n) * 1024, u32x4{lo[0], lo[1], hi[0], hi[1]});
            else if ((li >> 3) == par) st16(R, lane * 16, dst + 8192, u32x4{lo[0], lo[1], hi[0], hi[1]});
          }
        }
        arrive(1, 8, O_F2 + (192 + tj) * 8, 2048, NREP);
        STAMP(4,2);
      }
    } else {
      const int ti = tj - 64;
      if (w < 6) {
        const int row = tid / 12, cc = tid % 12;
        const char* src = w_in + (size_t)row * 9216 + ti * 192 + cc * 16;
        u32x4 v[18];
#pragma unroll
        for (int k = 0; k < 18; ++k) v[k] = *(const u32x4*)(src + (size_t)k * 32 * 9216);
#pragma unroll
        for (int k = 0; k < 18; ++k) *(u32x4*)(sm + (row + 32 * k) * 208 + cc * 16) = v[k];
      }
      tag = mk_tag();
      bar();
      STAMP(4,0);
      if (w < 6) {
        const int ct = 6 * ti + w;
        const int hf = ct < 144 ? 1 : 0, cl = ct < 144 ? ct : ct - 144, fp = cl / 9, n = cl % 9;
        const char* trb = sm + (8 * q + (li >> 2)) * 208 + w * 32 + (li & 3) * 8;
        const unsigned dst = lane * 16 + O_WI + ((fp * 2 + hf) * 18 * 9 + n) * 1024;
        float s = 0.f;
#pragma unroll 6
        for (int g = 0; g < 18; ++g) {
          u32x2 lo = tr_read(trb + g * 32 * 208), hi = tr_read(trb + g * 32 * 208 + 4 * 208);
          st16(R, dst, g * 9216, u32x4{lo[0], lo[1], hi[0], hi[1]});
          s = dot2(lo[0], 0x3F803F80u, s); s = dot2(lo[1], 0x3F803F80u, s); s = dot2(hi[0], 0x3F803F80u, s); s = dot2(hi[1], 0x3F803F80u, s);
        }
        s = q_sum(s);
        if (q == 0) st4(R, li * 4, O_CS + ct * 64, asu(s));
        arrive(1, 6, O_F2 + (144 + ti) * 8, 2048, NREP);
        STAMP(4,1);
      }
    }
  }
  const int tp = (xc & 1) * 8 + (ii >> 2), fp = (xc >> 1) * 4 + (ii & 3);
  const int t64 = 4 * xc + (ii >> 3), j3 = ii & 7;
  if (w == 0) { wait_flags<1>(R, O_F2 + rep * 2048 + 144 * 8, 48, tag, lane); STAMPW(6,0,0); }
  bar();
  if (w >= 4) {
    const int lw = w - 4;
    const unsigned l16 = lane * 16, base = lds_addr(sm);
    const int ph = ((ii >> 2) + ii) & 3, ph3 = ((ii >> 3) + ii) & 3;
    auto k9 = [&](int t) { return (t & 3) == ph ? (t + 4 < 9 ? t + 4 : ph) : t; };
    auto k32 = [&](int t) { return (t & 3) == ph ? ((t + 4) & 31) : t; };
    auto k18 = [&](int t) { return (t & 3) == ph3 ? (t + 4 < 18 ? t + 4 : ph3) : t; };
    if (lw < 2) {
      constexpr int NP = 8;
      unsigned vo[8];
#pragma unroll
      for (int j = 0; j < 8; ++j) {
        int t = 64 * lw + 32 * (j >> 2) + 4 * ((j >> 1) & 1) + 16 * (j & 1) + 8 * (lane >> 5) + ((lane >> 3) & 3);
        vo[j] = t * 4096 + (((lane & 7) ^ ((lane >> 3) & 7)) * 16) + 3072 - (j & 3) * 1024;
      }
      const char* wsrc = w_sp + (size_t)tp * 128 * 4096 - 3072;
      const char* hsrc = ws + O_HF + tp * 18 * 8192 + lw * 8192;
      const char* psrc = ws + O_PF + t64 * 72 * 4096 + lw * 8192;
      const unsigned dst = base + lw * 8192;
      if (lw == 0) wait_flags<2>(R, O_F2 + rep * 2048, 144, tag, lane);
      STAMPW(5,1,4);
      bar();
#pragma unroll 1
      for (int t = 0; t < 4; ++t) ld_lin<NP>(dst + t * STG, hsrc + k9(t) * 16384, l16);
      wait_vm<3 * NP>(); bar();
#pragma unroll 1
      for (int t = 4; t < 18; ++t) { wait_vm<2 * NP>(); bar(); ld_lin<NP>(dst + (t & 3) * STG, hsrc + k9(t < 9 ? t : t - 9) * 16384, l16); }
#pragma unroll 1
      for (int t = 18; t < 50; ++t) {
        wait_vm<2 * NP>(); bar();
        const unsigned dd = dst + (t & 3) * STG; const char* sp = wsrc + k32(t - 18) * 128;
        glds4<false>(dd, sp, vo[0], vo[1], vo[2], vo[3]); glds4<false>(dd + 4096, sp, vo[4], vo[5], vo[6], vo[7]);
      }
      wait_vm<2 * NP>(); bar(); wait_vm<NP>(); bar(); wait_vm<0>(); bar();
      wait_flags<1>(R, O_PFL + (t64 >> 1) * 2048 + ((t64 & 1) * 8 + j3) * 128, 16, tag, lane);
      STAMPW(5,2,4);
#pragma unroll 1
      for (int t = 50; t < 68; ++t) {
        if (t >= 53) { wait_vm<2 * NP>(); bar(); }
        ld_lin<NP>(dst + (t & 3) * STG, psrc + k18(t - 50) * 16384, l16);
      }
      wait_vm<2 * NP>(); bar(); wait_vm<NP>(); bar(); wait_vm<0>(); bar();
    } else {
      constexpr int NP = 9;
      const char* wi = ws + O_WI + (fp * 2) * 18 * 9216 + (lw - 2) * 9216;
      const char* zs = ws + O_ZF + fp * 64 * 9216 + (lw - 2) * 9216;
      const char* wo = ws + O_WO + j3 * 36 * 9216 + (lw - 2) * 9216;
      const unsigned dst = base + 16384 + (lw - 2) * 9216;
#pragma unroll 1
      for (int t = 0; t < 4; ++t) ld_lin<NP>(dst + t * STG, wi + k9(t) * 18432, l16);
      bar();
      wait_vm<3 * NP>(); bar();
      if (lw == 3) {
        f32x2 a = *(const f32x2*)(sm + X_LN + lane * 8) + *(const f32x2*)(sm + X_LN + 512 + lane * 8);
        a[0] = q_sum(dpp_add<0x128>(a[0])); a[1] = q_sum(dpp_add<0x128>(a[1]));
        float mean = a[0] * (1.0f / D), mb, ri;
        ln_scalars(mean, fmaxf(a[1] * (1.0f / D) - mean * mean, 0.f), mb, ri);
        if (lane < 8) st8(R, lane * 8, O_HS + tp * 1024 + fp * 64, u32x2{asu(ri) == SENT ? 0x7FC00000u : asu(ri), asu(-mb)});
      }
#pragma unroll 1
      for (int t = 4; t < 18; ++t) { wait_vm<2 * NP>(); bar(); ld_lin<NP>(dst + (t & 3) * STG, wi + (t < 9 ? k9(t) : 9 + k9(t - 9)) * 18432, l16); }
      wait_vm<2 * NP>(); bar(); wait_vm<NP>(); bar(); wait_vm<0>(); bar();
      wait_flags<1>(R, O_F2 + rep * 2048 + 192 * 8, 36, tag, lane);
      wait_flags<1>(R, O_ZFL + fp * 2048 + tp * 128, 16, tag, lane);
      STAMPW(6,1,6);
#pragma unroll 1
      for (int t = 18; t < 68; ++t) {
        if (t >= 21) { wait_vm<2 * NP>(); bar(); }
        ld_lin<NP>(dst + (t & 3) * STG, t < 50 ? zs + k32(t - 18) * 18432 : wo + k18(t - 50) * 18432, l16);
      }
      wait_vm<2 * NP>(); bar(); wait_vm<NP>(); bar(); wait_vm<0>(); bar();
    }
    return;
  }
  Ctx c;
  c.sm = sm;
#pragma unroll
  for (int i = 0; i < 4; ++i) { c.am0[i] = (2 * w + (i >> 1)) * 1024 + (i & 1) * 512 + lane * 8; asm volatile("" : "+v"(c.am0[i])); }
  c.am1 = 2 * w * 2048 + li * 128 + ((q ^ (lane & 7)) * 16);
  c.an = 16384 + lane * 16;
  const int prow = 8 * (li >> 2) + (li & 3);
  if (tid < 72) *(u32x4*)(sm + X_CS + tid * 16) = ld16(R, (tid < 36 ? FH * 4 + tid * 16 : (tid - 36) * 16), O_CS + fp * 576);
  bar();
  char* pvb = sm + X_PV + w * 4096;
  {
    u32x4 hv[3];
#pragma unroll
    for (int k = 0; k < 3; ++k)
      hv[k] = ld16(R, (lane >> 5) * 8192 + ((lane >> 2) & 7) * 128 + (lane & 3) * 16, O_HF + (tp * 18 * 8 + (fp >> 1)) * 1024 + (fp & 1) * 64 + min(w + 4 * k, 8) * 16384);
    if (w) hv[2] = u32x4{0, 0, 0, 0};
    f32x4 a = {0, 0, 0, 0};
#pragma unroll
    for (int k = 0; k < 3; ++k)
#pragma unroll
      for (int e = 0; e < 4; ++e) { a[e & 2] = dot2(hv[k][e], 0x3F803F80u, a[e & 2]); a[(e & 2) + 1] = dot2(hv[k][e], hv[k][e], a[(e & 2) + 1]); }
#pragma unroll
    for (int e = 0; e < 4; ++e) a[e] = dpp_add<0x124>(dpp_add<0x128>(a[e]));
    if (li < 4) *(f32x4*)(sm + X_LN + (w * 4 + q) * 64 + li * 16) = a;
  }
  f32x4 acc[2][9] = {};
  Fr X, Y;
  bar();
  STAMP(3,0);
  ldf<0>(X, c, 0, 0);
#pragma unroll 1
  for (int t = 0; t < 8; ++t) {
    seg<0, 0, true>(acc, X, Y, c, t, 1);
    bar();
    seg<0, 0, true>(acc, Y, X, c, t + 1, 0);
  }
  u32x2 ht[2]; u32x4 hc[4];
  auto ld_hs = [&]() {
#pragma unroll
    for (int m = 0; m < 2; ++m) {
      ht[m] = ld8(R, li * 8 + m * 128, O_HS + tp * 1024 + w * 256);
      hc[2 * m] = ld16(R, q * 32 + m * 128, O_HS + tp * 1024 + w * 256); hc[2 * m + 1] = ld16(R, q * 32 + 16 + m * 128, O_HS + tp * 1024 + w * 256);
    }
  };
  ld_hs();
  __builtin_amdgcn_sched_barrier(0);
  seg<0, 0, true>(acc, X, Y, c, 8, 1);
  bar();
  seg<0, 0, true>(acc, Y, X, c, 9, 0);
  STAMP(3,1);
#pragma unroll 1
  for (int spin = 0; spin < SPIN; ++spin) {
    bool ok = ht[0][0] != SENT && ht[0][1] != SENT && ht[1][0] != SENT && ht[1][1] != SENT;
#pragma unroll
    for (int k = 0; k < 4; ++k) ok = ok && hc[k][0] != SENT && hc[k][1] != SENT && hc[k][2] != SENT && hc[k][3] != SENT;
    if (__all(ok)) break;
    __builtin_amdgcn_s_sleep(1);
    asm volatile("" ::: "memory");
    ld_hs();
  }
  const float lrs[2] = {asf(ht[0][0]), asf(ht[1][0])}, lnm[2] = {asf(ht[0][1]), asf(ht[1][1])};
  u32x4 zb[9];
  f32x2 rs[4];
  {
    float cs[9];
#pragma unroll
    for (int n = 0; n < 9; ++n) cs[n] = *(const float*)(sm + X_CS + (16 * n + li) * 4);
    f32x2 nm[4], k1[4], k0[4], S1[4] = {}, S2[4] = {};
#pragma unroll
    for (int jp = 0; jp < 4; ++jp) {
      rs[jp] = f32x2{asf(hc[jp][0]), asf(hc[jp][2])}; nm[jp] = f32x2{asf(hc[jp][1]), asf(hc[jp][3])};
      k0[jp] = rs[jp] * GC0; k1[jp] = rs[jp] * rs[jp] * rs[jp] * GC1;
    }
#pragma unroll
    for (int n = 0; n < 9; ++n) {
#pragma unroll
      for (int jp = 0; jp < 4; ++jp) {
        f32x2 a = (jp & 1) ? hi2(acc[jp >> 1][n]) : lo2(acc[jp >> 1][n]);
        f32x2 z = gelu2(__builtin_elementwise_fma(nm[jp], f32x2{cs[n], cs[n]}, a), k1[jp], k0[jp]);
        zb[n][jp] = pk2(z);
        S1[jp] += z; S2[jp] = __builtin_elementwise_fma(z, z, S2[jp]);
      }
      asm volatile("" : "+v"(zb[n]));
    }
#pragma unroll
    for (int jp = 0; jp < 4; ++jp) { S1[jp] *= rs[jp]; S2[jp] *= rs[jp] * rs[jp]; }
    float v[16];
#pragma unroll
    for (int j = 0; j < 8; ++j) { v[2 * j] = S1[j >> 1][j & 1]; v[2 * j + 1] = S2[j >> 1][j & 1]; }
#pragma unroll
    for (int i = 0; i < 8; ++i) { bool h = li & 8; v[i] = (h ? v[i + 8] : v[i]) + dpp_get<0x128>(h ? v[i] : v[i + 8]); }
#pragma unroll
    for (int i = 0; i < 4; ++i) { bool h = li & 4; v[i] = (h ? v[i + 4] : v[i]) + dpp_get<0x141>(h ? v[i] : v[i + 4]); }
#pragma unroll
    for (int i = 0; i < 2; ++i) { bool h = li & 2; v[i] = (h ? v[i + 2] : v[i]) + dpp_get<0x4E>(h ? v[i] : v[i + 2]); }
    { bool h = li & 1; v[0] = (h ? v[1] : v[0]) + dpp_get<0xB1>(h ? v[0] : v[1]); }
    unsigned tot = asu(v[0]);
    st4(R, lane * 4, O_ST + ((tp * 4 + w) * 16 + fp) * 256, tot == SENT ? 0x7FC00000u : tot);
#pragma unroll
    for (int m = 0; m < 2; ++m)
#pragma unroll
      for (int n = 0; n < 9; ++n) acc[m][n] = f32x4{0, 0, 0, 0};
  }
  STAMP(3,2);
  auto it2 = [&](int t) {
    seg<0, 1, true>(acc, X, Y, c, t, 1);
    bar();
    seg<0, 1, true>(acc, Y, X, c, t + 1, 0);
  };
  const char* sts = ws + O_ST + (tp * 4 + w) * 4096;
#pragma unroll 1
  for (int t = 9; t < TCHK; ++t) {
    if (t == TPOLL) glds4<true>(lds_addr(pvb), sts, lane * 16, lane * 16, lane * 16, lane * 16);
    __builtin_amdgcn_sched_barrier(0);
    it2(t);
  }
  STAMP(3,3);
  u32x4 pv[4];
#pragma unroll 1
  for (int spin = 0; spin < SPIN; ++spin) {
    wait_vm<0>();
    bool ok = true;
#pragma unroll
    for (int k = 0; k < 4; ++k) {
      pv[k] = *(const u32x4*)(pvb + k * 1024 + lane * 16);
      ok = ok && pv[k][0] != SENT && pv[k][1] != SENT && pv[k][2] != SENT && pv[k][3] != SENT;
    }
    if (__all(ok)) break;
    glds4<true>(lds_addr(pvb), sts, lane * 16, lane * 16, lane * 16, lane * 16);
  }
  STAMP(3,4);
  {
    float t4[4];
#pragma unroll
    for (int e = 0; e < 4; ++e) t4[e] = q_sum((asf(pv[0][e]) + asf(pv[1][e])) + (asf(pv[2][e]) + asf(pv[3][e])));
    f32x2 o0, o1;
#pragma unroll
    for (int r = 0; r < 2; ++r) {
      float mean = t4[2 * r] * (1.0f / FH), var = fmaxf(t4[2 * r + 1] * (1.0f / FH) - mean * mean, 0.f), mb, ri;
      ln_scalars(mean, var, mb, ri);
      o0[r] = ri; o1[r] = -mb * ri;
    }
    char* ln = sm + X_LN + w * 256;
    if (lane < 16) { *(f32x2*)(ln + li * 8) = o0; *(f32x2*)(ln + 128 + li * 8) = o1; }
    asm volatile("" ::: "memory");
    f32x4 ri[2], rb[2];
#pragma unroll
    for (int m = 0; m < 2; ++m) { ri[m] = *(const f32x4*)(ln + (8 * q + 4 * m) * 4); rb[m] = *(const f32x4*)(ln + 128 + (8 * q + 4 * m) * 4); }
#pragma unroll
    for (int jp = 0; jp < 4; ++jp) rs[jp] *= (jp & 1) ? hi2(ri[jp >> 1]) : lo2(ri[jp >> 1]);
    const unsigned zo = lane * 16 + O_ZF + (fp * 64 + 4 * tp + w) * 9216;
#pragma unroll
    for (int n = 0; n < 9; ++n) {
      u32x4 o4;
#pragma unroll
      for (int jp = 0; jp < 4; ++jp)
        o4[jp] = pk2(__builtin_elementwise_fma(up2(zb[n][jp]), rs[jp], (jp & 1) ? hi2(rb[jp >> 1]) : lo2(rb[jp >> 1])));
      st16(R, zo, n * 1024, o4);
    }
  }
  STAMP(3,5);
#pragma unroll 1
  for (int t = TCHK; t < 17; ++t) {
    seg<0, 1, true>(acc, X, Y, c, t, 1);
    if (t == TFLAG) wait_vm<0>();
    bar();
    if (t == TFLAG && tid < 16) st8(R, tid * 128, O_ZFL + fp * 2048 + tp * 8, tag);
    seg<0, 1, true>(acc, Y, X, c, t + 1, 0);
  }
  seg<0, 1, true>(acc, X, Y, c, 17, 1);
  seg<0, 1, false>(acc, Y, X, c, 17, 1);
  STAMP(3,6);
  u32x2 z1[2][9];
  float bs[2];
#pragma unroll
  for (int m = 0; m < 2; ++m) bs[m] = ldbf(b_sp, 128 * tp + 32 * w + prow + 4 * m) * lrs[m];
  {
    f32x4 cs[9];
#pragma unroll
    for (int n = 0; n < 9; ++n) cs[n] = *(const f32x4*)(sm + X_CS + (144 + 16 * n + 4 * q) * 4);
#pragma unroll
    for (int n = 0; n < 9; ++n)
#pragma unroll
      for (int m = 0; m < 2; ++m) {
        const float a1 = lrs[m] * lrs[m] * lrs[m] * GC1, a0 = lrs[m] * GC0;
        const f32x2 k1 = {a1, a1}, k0 = {a0, a0}, nm = {lnm[m], lnm[m]};
        z1[m][n][0] = pk2(gelu2(__builtin_elementwise_fma(nm, lo2(cs[n]), lo2(acc[m][n])), k1, k0));
        z1[m][n][1] = pk2(gelu2(__builtin_elementwise_fma(nm, hi2(cs[n]), hi2(acc[m][n])), k1, k0));
        asm volatile("" : "+v"(z1[m][n]));
        acc[m][n] = f32x4{0, 0, 0, 0};
      }
  }
  STAMP(3,7);
  bar();
  STAMP(3,8);
  ldf<1>(X, c, 18, 0);
#pragma unroll 1
  for (int t = 18; t < 49; ++t) {
    seg<1, 1, true>(acc, X, Y, c, t, 1);
    bar();
    seg<1, 1, true>(acc, Y, X, c, t + 1, 0);
  }
  seg<1, 1, true>(acc, X, Y, c, 49, 1);
  seg<1, 1, false>(acc, Y, X, c, 49, 1);
  STAMP(3,9);
  {
    const unsigned pf = O_PF + (((2 * tp + (w >> 1)) * 72 + 9 * (fp >> 1)) * 4 + 2 * (w & 1)) * 1024;
    const unsigned po = lane * 16 + pf + (fp & 1) * 16384;
#pragma unroll
    for (int m = 0; m < 2; ++m) {
      const f32x2 r2 = {lrs[m], lrs[m]}, b2 = {bs[m], bs[m]};
      auto pp = [&](int n) { return u32x2{pk2(__builtin_elementwise_fma(lo2(acc[m][n]), r2, b2) * up2(z1[m][n][0])), pk2(__builtin_elementwise_fma(hi2(acc[m][n]), r2, b2) * up2(z1[m][n][1]))}; };
#pragma unroll
      for (int a = 0; a < 4; ++a) {
        u32x2 p0 = pp(2 * a), p1 = pp(2 * a + 1);
        st16(R, po, a * 4096 + m * 1024, u32x4{p0[0], p0[1], p1[0], p1[1]});
      }
      st8(R, lane * 16, pf + 8 * 4096 + m * 1024 + (fp & 1) * 8, pp(8));
    }
  }
  STAMP(3,10);
  arrive(2, 4, O_PFL + tp * 2048 + fp * 8, 128, 16);
  const int th = w & 1, kq = w >> 1, T = 4 * t64 + 2 * th + kq;
  const int lane5 = fresh_lane();
#define lane lane5
#define li (lane5 & 15)
#define q (lane5 >> 4)
#define prow (8 * (li >> 2) + (li & 3))
  u32x2 hv[5];
#pragma unroll
  for (int n = 0; n < 5; ++n) {
    const int dd = 72 * j3 + 16 * n + 4 * (n == 4 ? (q & 1) : q);
    hv[n] = ld8(R, (dd >> 5) * 8192 + ((dd >> 2) & 1) * 512 + (((dd >> 3) & 3) * 16 + li) * 8, O_HF + ((T >> 3) * 18 * 8 + (T & 7)) * 1024);
  }
  struct F3 { u32x4 p[2][2], n[9]; };
  f32x4 a3[2][6] = {};
  F3 U, V;
  const int ap = (8 * kq + 2 * th) * 1024 + lane * 16, aw = 16384 + kq * 9216 + lane * 16;
  auto ld3 = [&](F3& f, int t) {
    const char* slot = sm + (t & 3) * STG;
#pragma unroll
    for (int g = 0; g < 2; ++g)
#pragma unroll
      for (int m = 0; m < 2; ++m) f.p[g][m] = *(const u32x4*)(slot + ap + (4 * g + m) * 1024);
#pragma unroll
    for (int n = 0; n < 9; ++n) f.n[n] = *(const u32x4*)(slot + aw + n * 1024);
  };
  auto mm3 = [&](const F3& f) {
#pragma unroll
    for (int g = 0; g < 2; ++g)
#pragma unroll
      for (int m = 0; m < 2; ++m) {
#pragma unroll
        for (int n = 0; n < 4; ++n) a3[m][n] = mfma_bf(f.n[4 * g + n], f.p[g][m], a3[m][n]);
        a3[m][4 + g] = mfma_bf(f.n[8], f.p[g][m], a3[m][4 + g]);
      }
#pragma unroll
    for (int i = 0; i < 13; ++i) { __builtin_amdgcn_sched_group_barrier(0x008, 1, 0); __builtin_amdgcn_sched_group_barrier(0x100, 1, 0); }
    __builtin_amdgcn_sched_barrier(0);
  };
  STAMP(3,11);
  bar();
  STAMP(3,12);
  ld3(U, 50);
#pragma unroll 1
  for (int t = 50; t < 66; t += 2) { bar(); ld3(V, t + 1); mm3(U); bar(); ld3(U, t + 2); mm3(V); }
  bar(); ld3(V, 67); mm3(U); mm3(V);
  STAMP(3,13);
#pragma unroll
  for (int m = 0; m < 2; ++m)
#pragma unroll
    for (int r = 0; r < 4; ++r) a3[m][4][r] += __shfl_xor(a3[m][5][r], 32);
#pragma unroll
  for (int n = 0; n < 5; ++n) {
    *(f32x4*)(sm + (w * 5 + n) * 1024 + lane * 16) = kq ? a3[0][n] : a3[1][n];
    a3[0][n] = kq ? a3[1][n] : a3[0][n];
  }
  bar();
  char* o = out + (size_t)(32 * (T >> 1) + 4 * (T & 1) + prow) * 1152 + (72 * j3 + 4 * q) * 2;
#pragma unroll
  for (int n = 0; n < 5; ++n) {
    f32x4 v = a3[0][n] + *(const f32x4*)(sm + ((w ^ 2) * 5 + n) * 1024 + lane * 16);
    if (n < 4 || q < 2) *(u32x2*)(o + n * 32) = u32x2{pk2(up2(hv[n][0]) + up2(pk2(lo2(v)))), pk2(up2(hv[n][1]) + up2(pk2(hi2(v))))};
  }
  if (bid == 0 && w == 0 && lane == 0) { u64 e = (((u64)tag[1] << 32) | tag[0]) - 0x9E3779B97F4A7C15ull + 1; st8(R, 0, O_EP, u32x2{(unsigned)e, (unsigned)(e >> 32)}); }
  STAMP(3,14);
}

#undef lane
#undef li
#undef q
#undef prow
extern "C" void kernel_launch(void* const* d_in, const int* in_sizes, int n_in, void* d_out, int out_size, void* d_ws, size_t ws_size,
                              hipStream_t stream) {
  (void)hipFuncSetAttribute((const void*)k_all, hipFuncAttributeMaxDynamicSharedMemorySize, LDS_BYTES);
  k_all<<<256, 512, LDS_BYTES, stream>>>((const char*)d_in[0], (const char*)d_in[1], (const char*)d_in[2], (const char*)d_in[3],
      (const char*)d_in[4], (const char*)d_in[5], (const char*)d_in[6], (const char*)d_in[7], (const char*)d_in[8], (const char*)d_in[10],
      (const char*)d_in[11], (const char*)d_in[12], (const char*)d_in[13], (char*)d_out, (char*)d_ws);
}
```

```cpp
#include <hip/hip_runtime.h>
#include <hip/hip_bf16.h>
#include <stdint.h>

typedef __attribute__((ext_vector_type(8))) __bf16 bf16x8;
typedef __attribute__((ext_vector_type(2))) __bf16 bf16x2;
typedef __attribute__((ext_vector_type(8))) _Float16 f16x8;
typedef __attribute__((ext_vector_type(2))) _Float16 f16x2;
typedef __attribute__((ext_vector_type(4))) float f32x4;
typedef __attribute__((ext_vector_type(2))) float f32x2;
typedef __attribute__((ext_vector_type(4))) unsigned u32x4;
typedef __attribute__((ext_vector_type(2))) unsigned u32x2;
typedef __attribute__((ext_vector_type(4))) short s16x4;
#define LDSP __attribute__((address_space(3)))
#define DEVI __device__ __forceinline__

constexpr int L = 2048, D = 576, NM = 128, F = 4608, FH = 2304;
constexpr float EPS = 1e-5f;
constexpr unsigned O_EP = 0;
constexpr int NREP = 32;
constexpr unsigned O_F1 = 256;
constexpr unsigned O_F2 = O_F1 + NREP * 1024;
constexpr unsigned O_ZFL = O_F2 + NREP * 2048;
constexpr unsigned O_PFL = O_ZFL + 32768;
constexpr unsigned O_CS = 167936;
static_assert(O_PFL + 32768 <= O_CS, "ws");
constexpr unsigned O_ST = O_CS + F * 8;
constexpr unsigned O_HS = O_ST + 262144;
constexpr unsigned O_U = O_HS + 16384;
constexpr unsigned O_XB = O_U + L * D * 2;
constexpr unsigned O_WI = O_XB + L * D * 2;
constexpr unsigned O_WO = O_WI + D * F * 2;
constexpr unsigned O_HF = O_WO + FH * D * 2;
constexpr unsigned O_ZF = O_HF + L * D * 2;
constexpr unsigned O_PF = O_ZF + L * FH * 2;
constexpr unsigned WS_USED = O_PF + L * FH * 2;
constexpr size_t O_STAMP = 128u << 20;
#define STAMP(k,i)
#define STAMPW(k,i,W)
#define RSTAMP(k,i)
constexpr unsigned SENT = 0xFFFFFFFFu;

DEVI unsigned pk_bf16(float a, float b) { f32x2 v = {a, b}; return __builtin_bit_cast(unsigned, __builtin_convertvector(v, bf16x2)); }
DEVI unsigned pk_f16(float a, float b) { f32x2 v = {a, b}; return __builtin_bit_cast(unsigned, __builtin_convertvector(v, f16x2)); }
DEVI float bf_lo(unsigned u) { return __builtin_bit_cast(float, u << 16); }
DEVI float bf_hi(unsigned u) { return __builtin_bit_cast(float, u & 0xFFFF0000u); }
DEVI float asf(unsigned u) { return __builtin_bit_cast(float, u); }
DEVI unsigned asu(float f) { return __builtin_bit_cast(unsigned, f); }
DEVI float rbf(float a) { return bf_lo(pk_bf16(a, a)); }
DEVI float h_lo(unsigned u) { return (float)__builtin_bit_cast(f16x2, u)[0]; }
DEVI float h_hi(unsigned u) { return (float)__builtin_bit_cast(f16x2, u)[1]; }
template <int CTRL> DEVI float dpp_add(float v) {
  return v + __builtin_bit_cast(float, __builtin_amdgcn_update_dpp(0, __builtin_bit_cast(int, v), CTRL, 0xF, 0xF, true));
}
DEVI float row16_sum(float v) { v = dpp_add<0x128>(v); v = dpp_add<0x124>(v); v = dpp_add<0x122>(v); v = dpp_add<0x121>(v); return v; }
DEVI float q_sum(float v) { v += __shfl_xor(v, 16); v += __shfl_xor(v, 32); return v; }
DEVI u32x2 tr_read(const char* p) {
  return __builtin_bit_cast(u32x2, __builtin_amdgcn_ds_read_tr16_b64_v4i16((LDSP s16x4*)p));
}
DEVI f32x4 mfma_bf(u32x4 a, u32x4 b, f32x4 c) {
  return __builtin_amdgcn_mfma_f32_16x16x32_bf16(__builtin_bit_cast(bf16x8, a), __builtin_bit_cast(bf16x8, b), c, 0, 0, 0);
}
DEVI f32x4 mfma_h(u32x4 a, u32x4 b, f32x4 c) {
  return __builtin_amdgcn_mfma_f32_16x16x32_f16(__builtin_bit_cast(f16x8, a), __builtin_bit_cast(f16x8, b), c, 0, 0, 0);
}
DEVI void ln_scalars(float mean, float var, float& mb, float& rinv) {
  mb = rbf(mean);
  float d = rbf(__builtin_amdgcn_sqrtf(rbf(rbf(var) + EPS)));
  rinv = __builtin_amdgcn_rcpf(d);
}
DEVI float gelu(float x) {
  float w = x * __builtin_fmaf(x * x, -0.10294324f, -2.3022082f);
  return x * __builtin_amdgcn_rcpf(1.0f + __builtin_amdgcn_exp2f(w));
}

typedef unsigned long long u64;
typedef __amdgpu_buffer_rsrc_t rsrc_t;
constexpr int SPIN = 1 << 16;
template <int CTRL> DEVI float dpp_get(float v) {
  return __builtin_bit_cast(float, __builtin_amdgcn_update_dpp(0, __builtin_bit_cast(int, v), CTRL, 0xF, 0xF, true));
}
constexpr float GC1 = -0.10294324f, GC0 = -2.3022082f;
DEVI f32x2 gelu2(f32x2 x, f32x2 k1, f32x2 k0) {
  f32x2 w = x * __builtin_elementwise_fma(x * x, k1, k0);
  f32x2 d = f32x2{__builtin_amdgcn_exp2f(w[0]), __builtin_amdgcn_exp2f(w[1])} + f32x2{1.f, 1.f};
  return x * f32x2{__builtin_amdgcn_rcpf(d[0]), __builtin_amdgcn_rcpf(d[1])};
}
DEVI unsigned pk2(f32x2 v) { return __builtin_bit_cast(unsigned, __builtin_convertvector(v, bf16x2)); }
DEVI f32x2 up2(unsigned u) { return f32x2{bf_lo(u), bf_hi(u)}; }
DEVI f32x2 lo2(f32x4 v) { return __builtin_shufflevector(v, v, 0, 1); }
DEVI f32x2 hi2(f32x4 v) { return __builtin_shufflevector(v, v, 2, 3); }
DEVI float dot2(unsigned a, unsigned b, float c) {
  return __builtin_amdgcn_fdot2_f32_bf16(__builtin_bit_cast(bf16x2, a), __builtin_bit_cast(bf16x2, b), c, false);
}
DEVI void st16(rsrc_t r, unsigned voff, unsigned soff, u32x4 v) { __builtin_amdgcn_raw_buffer_store_b128(v, r, voff + soff, 0, 16); }
DEVI void st8(rsrc_t r, unsigned voff, unsigned soff, u32x2 v) { __builtin_amdgcn_raw_buffer_store_b64(v, r, voff, soff, 16); }
DEVI void st4(rsrc_t r, unsigned voff, unsigned soff, unsigned v) { __builtin_amdgcn_raw_buffer_store_b32(v, r, voff, soff, 16); }
DEVI u32x4 ld16(rsrc_t r, unsigned voff, unsigned soff) { return __builtin_amdgcn_raw_buffer_load_b128(r, voff, soff, 16); }
DEVI u32x2 ld8(rsrc_t r, unsigned voff, unsigned soff) { return __builtin_amdgcn_raw_buffer_load_b64(r, voff, soff, 16); }
template <int N> DEVI void wait_vm() { asm volatile("s_waitcnt vmcnt(%0)" ::"i"(N) : "memory"); }
DEVI void bar() { __builtin_amdgcn_sched_barrier(0); asm volatile("s_waitcnt lgkmcnt(0)\n\ts_barrier" ::: "memory"); __builtin_amdgcn_sched_barrier(0); }
DEVI int fresh_lane() { int l; asm volatile("v_mbcnt_lo_u32_b32 %0, -1, 0\n\tv_mbcnt_hi_u32_b32 %0, -1, %0" : "=v"(l)); return l; }
DEVI unsigned lds_addr(const char* p) { return (unsigned)(uintptr_t)(LDSP const char*)p; }
template <int NL> DEVI void wait_flags(rsrc_t r, unsigned off, int n, u32x2 tag, int lane) {
#pragma unroll 1
  for (int spin = 0; spin < SPIN; ++spin) {
    bool ok = true;
#pragma unroll
    for (int k = 0; k < NL; ++k) {
      int i = min(lane + 64 * k, n / 2 - 1);
      u32x4 v = ld16(r, i * 16, off);
      ok = ok && v[0] == tag[0] && v[1] == tag[1] && v[2] == tag[0] && v[3] == tag[1];
    }
    if (__all(ok)) break;
    __builtin_amdgcn_s_sleep(1);
    asm volatile("" ::: "memory");
  }
  asm volatile("" ::: "memory");
}
template <bool SC> DEVI void glds4(unsigned m0v, const char* base, unsigned v0, unsigned v1, unsigned v2, unsigned v3) {
  unsigned keep;
  if (SC)
    asm volatile("s_mov_b32 %0, m0\n\ts_mov_b32 m0, %1\n\ts_nop 0\n\t"
                 "global_load_lds_dwordx4 %3, %2 sc1\n\tglobal_load_lds_dwordx4 %4, %2 offset:1024 sc1\n\t"
                 "global_load_lds_dwordx4 %5, %2 offset:2048 sc1\n\tglobal_load_lds_dwordx4 %6, %2 offset:3072 sc1\n\ts_mov_b32 m0, %0"
                 : "=&s"(keep) : "s"(m0v), "s"(base), "v"(v0), "v"(v1), "v"(v2), "v"(v3) : "memory");
  else
    asm volatile("s_mov_b32 %0, m0\n\ts_mov_b32 m0, %1\n\ts_nop 0\n\t"
                 "global_load_lds_dwordx4 %3, %2\n\tglobal_load_lds_dwordx4 %4, %2 offset:1024\n\t"
                 "global_load_lds_dwordx4 %5, %2 offset:2048\n\tglobal_load_lds_dwordx4 %6, %2 offset:3072\n\ts_mov_b32 m0, %0"
                 : "=&s"(keep) : "s"(m0v), "s"(base), "v"(v0), "v"(v1), "v"(v2), "v"(v3) : "memory");
}
DEVI void glds1(unsigned m0v, const char* base, unsigned v0) {
  unsigned keep;
  asm volatile("s_mov_b32 %0, m0\n\ts_mov_b32 m0, %1\n\ts_nop 0\n\tglobal_load_lds_dwordx4 %3, %2 sc1\n\ts_mov_b32 m0, %0"
               : "=&s"(keep) : "s"(m0v), "s"(base), "v"(v0) : "memory");
}
template <int NP> DEVI void ld_lin(unsigned dst, const char* src, unsigned l16) {
  glds4<true>(dst, src, l16, l16, l16, l16);
  glds4<true>(dst + 4096, src + 4096, l16, l16, l16, l16);
  if (NP == 9) glds1(dst + 8192, src + 8192, l16);
}

constexpr int TP = 1168;
constexpr int CV_K = 8 * 260 * 16, CV_U = 4576;
constexpr int C_U = 4 * CV_K, C_KD = C_U + 4 * CV_U, C_MX = C_KD + 4 * 1536, C_END = C_MX + 64;
constexpr int KT = 528;
constexpr int STG = 34816, RING = 4 * STG;
constexpr int X_CS = RING, X_LN = X_CS + 1152, X_PV = X_LN + 1024, X_CNT = 158720;
constexpr int LDS_BYTES = X_CNT + 64;
static_assert(X_PV + 16384 <= X_CNT && LDS_BYTES <= 163840, "lds");
#define TPOLL 12
#define TLBW 0
#define TCHK 13
#define TFLAG 15

struct Fr { u32x4 m[2], n[9]; };
struct Ctx { const char* sm; int am0[4], am1, an; };
template <int MK> DEVI void ldf(Fr& f, const Ctx& c, int t, int kk) {
  int so = (t & 3) * STG;
  asm volatile("" : "+s"(so));
  const char* slot = c.sm + so;
  if (MK == 0) {
#pragma unroll
    for (int m = 0; m < 2; ++m) {
      u32x2 a = *(const u32x2*)(slot + c.am0[2 * m] + kk * 8192), b = *(const u32x2*)(slot + c.am0[2 * m + 1] + kk * 8192);
      f.m[m] = u32x4{a[0], a[1], b[0], b[1]};
    }
  } else {
#pragma unroll
    for (int m = 0; m < 2; ++m) f.m[m] = *(const u32x4*)(slot + (c.am1 ^ (kk * 64)) + m * 2048);
  }
#pragma unroll
  for (int n = 0; n < 9; ++n) f.n[n] = *(const u32x4*)(slot + c.an + (kk * 9 + n) * 1024);
}
template <int MK, int ORI, bool LD> DEVI void seg(f32x4 (&acc)[2][9], const Fr& cur, Fr& nxt, const Ctx& c, int t, int kk) {
  if (LD) ldf<MK>(nxt, c, t, kk);
#pragma unroll
  for (int m = 0; m < 2; ++m)
#pragma unroll
    for (int nn = 0; nn < 9; ++nn) {
      const int n = m ? 8 - nn : nn;
      acc[m][n] = ORI == 0 ? mfma_bf(cur.m[m], cur.n[n], acc[m][n]) : mfma_bf(cur.n[n], cur.m[m], acc[m][n]);
    }
#pragma unroll
  for (int i = 0; i < (MK == 0 ? 13 : 11); ++i) { __builtin_amdgcn_sched_group_barrier(0x008, 1, 0); __builtin_amdgcn_sched_group_barrier(0x100, 1, 0); }
  __builtin_amdgcn_sched_barrier(0);
}
DEVI f32x2 cmul(f32x2 a, f32x2 b, f32x2 bp) {
  return __builtin_elementwise_fma(f32x2{a[1], a[1]}, bp, f32x2{a[0], a[0]} * b);
}
DEVI f32x2 perp(f32x2 b) { return f32x2{-b[1], b[0]}; }
DEVI f32x2 cexp(float ar, float ai, float l) {
  float m = __builtin_amdgcn_exp2f(ar * l), ph = __builtin_amdgcn_fractf(ai * l);
  return f32x2{m * __builtin_amdgcn_cosf(ph), m * __builtin_amdgcn_sinf(ph)};
}
DEVI unsigned pkh(f32x2 v) { return __builtin_bit_cast(unsigned, __builtin_convertvector(v, f16x2)); }
DEVI float ldbf(const char* p, int i) { return bf_lo(*(const unsigned short*)(p + 2 * i)); }

__global__ __launch_bounds__(512, 2) void k_all(const char* __restrict__ x, const char* __restrict__ a_re, const char* __restrict__ a_im,
    const char* __restrict__ b_re, const char* __restrict__ b_im, const char* __restrict__ c_re, const char* __restrict__ c_im,
    const char* __restrict__ log_dt, const char* __restrict__ dskip, const char* __restrict__ w_in, const char* __restrict__ w_sp,
    const char* __restrict__ b_sp, const char* __restrict__ w_out, char* __restrict__ out, char* __restrict__ ws) {
  extern __shared__ __attribute__((aligned(16))) char sm[];
  const int tid = threadIdx.x, lane = tid & 63, w = __builtin_amdgcn_readfirstlane(tid >> 6), li = lane & 15, q = lane >> 4;
  const int bid = blockIdx.x, xc = bid & 7, ii = bid >> 3;
  const rsrc_t R = __builtin_amdgcn_make_buffer_rsrc(ws, 0, WS_USED, 0x00020000);
  STAMP(0,0);
  const u32x2 ep = ld8(R, 0, O_EP);
  const bool is_conv = ii < 18;
  if (w == 7) {
    unsigned long long pc;
    asm volatile("s_getpc_b64 %0" : "=s"(pc));
    const char* cp = (const char*)(pc & ~255ull) + (ii & 7) * 5376 + lane * 128;
    unsigned v0 = 0, v1 = 0;
    if (lane < 42) v0 = *(const unsigned*)cp;
    asm volatile("" ::"v"(v0), "v"(v1));
  }
  const int cg = xc * 18 + ii, rep = bid & (NREP - 1);
  if (tid == 0) *(u32x4*)(sm + X_CNT) = u32x4{0, 0, 0, 0};
  auto mk_tag = [&]() {
    u64 e = (((u64)__builtin_amdgcn_readfirstlane(ep[1]) << 32) | (unsigned)__builtin_amdgcn_readfirstlane(ep[0])) + 0x9E3779B97F4A7C15ull;
    return u32x2{(unsigned)e, (unsigned)(e >> 32)};
  };
  u32x2 tag;
  auto arrive = [&](int slot, unsigned n, unsigned flag_off, unsigned stride, int nrep) {
    wait_vm<0>();
    const int lane = fresh_lane();
    unsigned old = 0;
    if (lane == 0) old = __hip_atomic_fetch_add((unsigned*)(sm + X_CNT) + slot, 1u, __ATOMIC_RELAXED, __HIP_MEMORY_SCOPE_WORKGROUP);
    if (__builtin_amdgcn_readfirstlane(old) == n - 1 && lane < nrep) st8(R, lane * stride, flag_off, tag);
  };
  if (is_conv) {
    const int ch = w & 3, dch = 4 * cg + ch;
    char* sk = sm + ch * CV_K;
    const char* su = sm + C_U + ch * CV_U;
    float sc = 0.f;
    if (w < 4) {
      const int pi = (dch * NM + 2 * lane) * 2;
      const unsigned v_ar = *(const unsigned*)(a_re + pi), v_ai = *(const unsigned*)(a_im + pi), v_br = *(const unsigned*)(b_re + pi),
                     v_bi = *(const unsigned*)(b_im + pi), v_cr = *(const unsigned*)(c_re + pi), v_ci = *(const unsigned*)(c_im + pi);
      const float p_dk = ldbf(dskip, dch);
      const float dt = rbf(__expf(ldbf(log_dt, dch)));
      tag = mk_tag();
      STAMP(0,1);
      const f32x2 ar = (up2(v_ar) * dt) * 1.44269504f, ai = (up2(v_ai) * dt) * 0.15915494f;
      const f32x2 br = up2(v_br) * dt, bi = up2(v_bi) * dt, cr = up2(v_cr), ci = up2(v_ci);
      const f32x2 cbr = cr * br - ci * bi, cbi = -(cr * bi + ci * br);
      float mx = fmaxf(fmaxf(fabsf(cbr[0]), fabsf(cbi[0])), fmaxf(fabsf(cbr[1]), fabsf(cbi[1])));
      mx = fmaxf(mx, dpp_get<0x128>(mx)); mx = fmaxf(mx, dpp_get<0x124>(mx)); mx = fmaxf(mx, dpp_get<0x122>(mx)); mx = fmaxf(mx, dpp_get<0x121>(mx));
      auto rl = [&](int l) { return asf(__builtin_amdgcn_readlane((int)asu(mx), l)); };
      sc = fmaxf(fmaxf(rl(0), rl(16)), fmaxf(rl(32), rl(48)));
      sc = fmaxf(sc, fabsf(p_dk) * (1.0f / 256));
      const float isc = sc > 0.f ? 1.0f / sc : 0.f;
      {
        struct C2 { f32x2 a, b; };
        char* tb = sk + lane * 8;
        auto put = [&](int row, C2 v) { *(u32x2*)(tb + row * KT) = u32x2{pkh(v.a), pkh(v.b)}; };
        auto mul = [&](C2 v, C2 u, C2 up) { return C2{cmul(v.a, u.a, up.a), cmul(v.b, u.b, up.b)}; };
        auto prp = [&](C2 v) { return C2{perp(v.a), perp(v.b)}; };
        auto ex = [&](float l) { return C2{cexp(ar[0], ai[0], l), cexp(ar[1], ai[1], l)}; };
        const C2 w1 = ex(1.f), w128 = ex(128.f);
        C2 w8 = ex(8.f); w8.a[1] = -w8.a[1]; w8.b[1] = -w8.b[1];
        const C2 w1p = prp(w1), w8p = prp(w8), w128p = prp(w128);
        C2 y = {f32x2{cbr[0], cbi[0]} * isc, f32x2{cbr[1], cbi[1]} * isc};
        C2 E[8]; E[0] = C2{f32x2{1.f, 0.f}, f32x2{1.f, 0.f}}; E[1] = w1;
#pragma unroll
        for (int e = 2; e < 8; ++e) E[e] = mul(E[e - 1], w1, w1p);
#pragma unroll
        for (int e = 0; e < 8; ++e) { put(e, E[e]); put(8 + e, e ? mul(E[e], w128, w128p) : w128); put(41 + e, y); y = mul(y, w8, w8p); }
        const C2 e2p = prp(E[2]), e4p = prp(E[4]), e6p = prp(E[6]);
        C2 H = w128;
#pragma unroll
        for (int a = 2; a <= 16; ++a) {
          H = mul(H, w128, w128p);
          if (a < 4) { put(8 + 4 * a, H); put(9 + 4 * a, mul(H, E[2], e2p)); put(10 + 4 * a, mul(H, E[4], e4p)); put(11 + 4 * a, mul(H, E[6], e6p)); }
          else if (a < 8) { put(16 + 2 * a, H); put(17 + 2 * a, mul(H, E[4], e4p)); }
          else put(24 + a, H);
          if (a < 10) { put(47 + a, y); if (a < 9) y = mul(y, w8, w8p); }
        }
      }
      asm volatile("" ::: "memory");
      STAMP(0,2);
      {
        f32x4 kk[3] = {};
        const char* pa = sk + li * KT + q * 16;
#pragma unroll
        for (int s = 0; s < 8; ++s) {
          u32x4 fb = *(const u32x4*)(pa + 41 * KT + s * 64);
#pragma unroll
          for (int t = 0; t < 3; ++t) kk[t] = mfma_h(*(const u32x4*)(pa + 16 * t * KT + s * 64), fb, kk[t]);
        }
        if (lane == 0) kk[0][0] += p_dk * isc;
        char* kd = sm + C_KD + ch * 1536;
        *(u32x2*)(kd + (128 * (q >> 1) + 4 * (q & 1) + 8 * li) * 2) = u32x2{pkh(lo2(kk[0])), pkh(hi2(kk[0]))};
        const int i1 = q < 2 ? 256 + 64 * q + 4 * li : 384 + 64 * (q - 2) + 2 * li;
        *(unsigned*)(kd + i1 * 2) = pkh(lo2(kk[1])); *(unsigned*)(kd + (i1 + (q < 2 ? 2 : 32)) * 2) = pkh(hi2(kk[1]));
#pragma unroll
        for (int r = 0; r < 4; ++r) *(_Float16*)(kd + (512 + 16 * (4 * q + r) + li) * 2) = (_Float16)kk[2][r];
      }
      asm volatile("" ::: "memory");
      u32x4 c0[4];
      {
        const unsigned short* kd = (const unsigned short*)(sm + C_KD + ch * 1536);
#pragma unroll
        for (int rr = 0; rr < 2; ++rr) {
          const int k = 255 - 64 * rr - lane;
          const unsigned a = kd[384 + k], b = kd[385 + k], aa = a | (a << 16);
          c0[rr] = u32x4{b | (b << 16), b | (((k & 1) ? b : a) << 16), aa, aa};
        }
        { const int k = 127 - lane;
          const unsigned ab = *(const unsigned*)(kd + 256 + 2 * k), cc = kd[258 + 2 * k];
          c0[2] = u32x4{cc | (cc << 16), __builtin_amdgcn_perm(ab, ab, 0x03020302), __builtin_amdgcn_perm(ab, ab, 0x01000302), __builtin_amdgcn_perm(ab, ab, 0x01000100)}; }
        { const int k = 63 - lane;
          const u32x4 v = *(const u32x4*)(kd + 8 * (k & 31));
          const u32x2 u = *(const u32x2*)(kd + 128 + 4 * k); const unsigned e = kd[132 + 4 * k];
          c0[3] = lane < 32 ? u32x4{__builtin_amdgcn_perm(u[1], e, 0x07060100), __builtin_amdgcn_perm(u[1], u[1], 0x01000100), __builtin_amdgcn_perm(u[0], u[1], 0x07060100), __builtin_amdgcn_perm(u[0], u[0], 0x01000100)}
                            : u32x4{__builtin_amdgcn_alignbit(v[3], v[3], 16), __builtin_amdgcn_alignbit(v[2], v[2], 16), __builtin_amdgcn_alignbit(v[1], v[1], 16), __builtin_amdgcn_alignbit(v[0], v[0], 16)}; }
#pragma unroll
        for (int rr = 0; rr < 4; ++rr) *(u32x4*)(sk + (64 * rr + lane) * 16) = c0[rr];
        if (lane < 8) *(u32x4*)(sk + (lane * 260 + 256) * 16) = u32x4{0, 0, 0, 0};
      }
      asm volatile("" ::: "memory");
      STAMP(0,3);
      u32x4 c1[4];
#pragma unroll
      for (int rr = 0; rr < 4; ++rr) c1[rr] = *(const u32x4*)(sk + (64 * rr + lane) * 16 + 16);
#pragma unroll
      for (int rr = 0; rr < 4; ++rr) {
        char* p = sk + (64 * rr + lane) * 16;
        unsigned e[8] = {c0[rr][0], c0[rr][1], c0[rr][2], c0[rr][3], c1[rr][0], c1[rr][1], c1[rr][2], c1[rr][3]}, o[7];
#pragma unroll
        for (int i = 0; i < 7; ++i) o[i] = __builtin_amdgcn_alignbit(e[i + 1], e[i], 16);
#pragma unroll
        for (int s = 0; s < 4; ++s) {
          if (s) *(u32x4*)(p + (2 * s) * 260 * 16) = u32x4{e[s], e[s + 1], e[s + 2], e[s + 3]};
          *(u32x4*)(p + (2 * s + 1) * 260 * 16) = u32x4{o[s], o[s + 1], o[s + 2], o[s + 3]};
        }
      }
      STAMP(0,4);
    } else {
      tag = mk_tag();
      if (cg < 136 && tid < 384) st16(R, (tid - 256) * 16, O_ST + cg * 2048, u32x4{SENT, SENT, SENT, SENT});
      if (lane < 30) *(u32x4*)(sm + C_U + ch * CV_U + lane * 16) = u32x4{0, 0, 0, 0};
      if (w == 4) {
        wait_flags<1>(R, O_F1 + rep * 1024, 64, tag, lane);
        STAMPW(5,0,4);
        const unsigned v0 = (lane >> 2) * 36864 + (lane & 3) * 16, dv = 16 * 36864 - 1024;
#pragma unroll
        for (int i = 0; i < 4; ++i) glds4<true>(lds_addr(sm) + C_U + i * CV_U + 480, ws + O_U + (4 * cg + i) * 64, v0, v0 + dv, v0 + 2 * dv, v0 + 3 * dv);
        wait_vm<0>();
      }
    }
    bar();
    STAMP(0,5);
    u32x4 xv[4];
    if (w < 4) {
      f32x4 acc[8] = {};
      const char* pa = sk + ((7 - (li & 7)) * 260 + 253 - (li >> 3) + q) * 16 - 4032;
      const char* pb = su + li * 32 + q * 16;
      struct CF { u32x4 a[8], b[8]; } A, B;
      auto ldc = [&](CF& f, int a0) {
#pragma unroll
        for (int m = 0; m < 8; ++m) { f.a[m] = *(const u32x4*)(pa + 4032 - 64 * a0 - 512 * m); f.b[m] = *(const u32x4*)(pb + 448 - 64 * a0 + 512 * m); }
      };
      auto mmc = [&](const CF& f) {
#pragma unroll
        for (int m = 0; m < 8; ++m)
#pragma unroll
          for (int t = m; t < 8; ++t) acc[t] = mfma_h(f.a[m], f.b[t - m], acc[t]);
#pragma unroll
        for (int i = 0; i < 16; ++i) { __builtin_amdgcn_sched_group_barrier(0x008, 2, 0); __builtin_amdgcn_sched_group_barrier(0x100, 1, 0); }
        __builtin_amdgcn_sched_barrier(0);
      };
      ldc(A, 0);
      __builtin_amdgcn_sched_barrier(0);
#pragma unroll
      for (int a0 = 0; a0 < 8; a0 += 2) { ldc(B, a0 + 1); mmc(A); if (a0 < 6) ldc(A, a0 + 2); mmc(B); }
      STAMP(0,6);
#pragma unroll
      for (int t = 0; t < 8; ++t) *(u32x2*)(sk + t * 512 + li * 32 + q * 8) = u32x2{pk2(lo2(acc[t]) * sc), pk2(hi2(acc[t]) * sc)};
    } else {
      wait_flags<1>(R, O_F1 + rep * 1024 + 512, 64, tag, lane);
#pragma unroll
      for (int r = 0; r < 4; ++r) {
        const int T = ((tid & 255) >> 3) + 32 * r, ip = tid & 7;
        xv[r] = ld16(R, (32 * (T >> 1) + 8 * (ip >> 1) + 4 * (T & 1) + 2 * (ip & 1)) * 8, O_XB + cg * 16384);
      }
      STAMPW(2,0,4);
    }
    bar();
    if (w >= 4) {
#pragma unroll
      for (int r = 0; r < 4; ++r) {
        const int T = ((tid & 255) >> 3) + 32 * r, ip = tid & 7;
        const char* p = sm + (32 * (T >> 1) + 8 * (ip >> 1) + 4 * (T & 1) + 2 * (ip & 1)) * 2;
        const unsigned c0 = *(const unsigned*)p, c1 = *(const unsigned*)(p + CV_K), c2 = *(const unsigned*)(p + 2 * CV_K), c3 = *(const unsigned*)(p + 3 * CV_K);
        u32x4 v = {pk2(up2(xv[r][0]) + f32x2{bf_lo(c0), bf_lo(c1)}), pk2(up2(xv[r][1]) + f32x2{bf_lo(c2), bf_lo(c3)}),
                   pk2(up2(xv[r][2]) + f32x2{bf_hi(c0), bf_hi(c1)}), pk2(up2(xv[r][3]) + f32x2{bf_hi(c2), bf_hi(c3)})};
        st16(R, (T >> 3) * (18 * 8192) + (T & 7) * 1024 + ip * 16, O_HF + (cg >> 3) * 8192 + (cg & 1) * 512 + ((cg >> 1) & 3) * 128, v);
      }
      STAMPW(2,1,4);
      arrive(1, 4, O_F2 + cg * 8, 2048, NREP);
      STAMPW(2,2,4);
    }
  } else {
    const int tj = (ii - 18) * 8 + xc;
    if (tj < 64) {
      char* s0 = sm + 83968;
      const char* src = x + (size_t)(32 * tj + (tid >> 4)) * 1152 + li * 8;
      u32x2 xr[9];
#pragma unroll
      for (int k = 0; k < 9; ++k) xr[k] = *(const u32x2*)(src + k * 128);
      asm volatile("" :: "v"(xr[8])); STAMP(1,0);
      float a[4] = {0, 0, 0, 0};
#pragma unroll
      for (int k = 0; k < 9; ++k)
#pragma unroll
        for (int e = 0; e < 2; ++e) { a[e] = dot2(xr[k][e], 0x3F803F80u, a[e]); a[2 + e] = dot2(xr[k][e], xr[k][e], a[2 + e]); }
      const float s = row16_sum(a[0] + a[1]), ss = row16_sum(a[2] + a[3]);
      float mean = s * (1.0f / D), mb, rinv;
      ln_scalars(mean, fmaxf(ss * (1.0f / D) - mean * mean, 0.f), mb, rinv);
      const f32x2 r2 = {rinv, rinv}, b2 = {-mb * rinv, -mb * rinv};
      char* p = s0 + (tid >> 4) * TP + li * 8;
#pragma unroll
      for (int k = 0; k < 9; ++k) {
        *(u32x2*)(p + k * 128) = u32x2{pkh(__builtin_elementwise_fma(up2(xr[k][0]), r2, b2)), pkh(__builtin_elementwise_fma(up2(xr[k][1]), r2, b2))};
        *(u32x2*)(p + 32 * TP + k * 128) = xr[k];
      }
      tag = mk_tag();
      bar();
      STAMP(1,1);
      if (w < 4) {
        const char* trb = s0 + (8 * q + (li >> 2)) * TP + (li & 3) * 8 + w * 288;
        const unsigned uo = li * 64 + q * 16 + O_U + tj * 36864 + w * 9216;
#pragma unroll
        for (int k = 0; k < 9; ++k) {
          u32x2 lo = tr_read(trb + k * 32), hi = tr_read(trb + 4 * TP + k * 32);
          st16(R, uo, k * 1024, u32x4{lo[0], lo[1], hi[0], hi[1]});
        }
        STAMP(1,2);
        arrive(0, 4, O_F1 + tj * 8, 1024, NREP);
        STAMP(1,3);
      } else {
        const int c16 = (tid >> 4) & 15;
        const char* px = s0 + (32 + li * 2) * TP + c16 * 8;
#pragma unroll
        for (int k = 0; k < 9; ++k) {
          u32x2 a = *(const u32x2*)(px + k * 128), b = *(const u32x2*)(px + TP + k * 128);
          st16(R, (c16 + 16 * k) * 16384 + li * 16, O_XB + tj * 256, u32x4{a[0], a[1], b[0], b[1]});
        }
        arrive(3, 4, O_F1 + (64 + tj) * 8, 1024, NREP);
        STAMPW(1,4,4);
      }
      if (tj < 36) {
        const int par = w >> 2, G = 2 * tj + par, row = (tid >> 3) & 31, uu = G / 9, idx = G % 9, hh = row >> 4;
        const int panel = idx < 4 ? 2 * uu : (idx < 8 ? 2 * uu + 1 : 2 * uu + hh), tile = idx < 8 ? 2 * (idx & 3) + hh : 8;
        const char* wsrc = w_out + (size_t)(144 * panel + 16 * tile + (row & 15)) * 1152 + (tid & 7) * 16;
        u32x4 v[9];
#pragma unroll
        for (int k = 0; k < 9; ++k) v[k] = *(const u32x4*)(wsrc + k * 128);
#pragma unroll
        for (int k = 0; k < 9; ++k) *(u32x4*)(sm + (tid >> 3) * TP + (tid & 7) * 16 + k * 128) = v[k];
        bar();
        const char* trb = sm + (32 * par + 4 * q + (li >> 2)) * TP + (li & 3) * 8;
#pragma unroll
        for (int jj = 0; jj < 2; ++jj) {
          const int j = 2 * (w & 3) + jj;
          const unsigned dst = O_WO + (j * 36 + tj) * 9216;
#pragma unroll
          for (int n = 0; n < 5; ++n) {
            const int c0 = 72 * j + 16 * n - (n == 4 ? 8 * par : 0);
            u32x2 lo = tr_read(trb + c0 * 2), hi = tr_read(trb + 16 * TP + c0 * 2);
            if (n < 4) st16(R, lane * 16, dst + (4 * par + n) * 1024, u32x4{lo[0], lo[1], hi[0], hi[1]});
            else if ((li >> 3) == par) st16(R, lane * 16, dst + 8192, u32x4{lo[0], lo[1], hi[0], hi[1]});
          }
        }
        arrive(1, 8, O_F2 + (192 + tj) * 8, 2048, NREP);
        STAMP(4,2);
      }
    } else {
      const int ti = tj - 64;
      if (w < 6) {
        const int row = tid / 12, cc = tid % 12;
        const char* src = w_in + (size_t)row * 9216 + ti * 192 + cc * 16;
        u32x4 v[18];
#pragma unroll
        for (int k = 0; k < 18; ++k) v[k] = *(const u32x4*)(src + (size_t)k * 32 * 9216);
#pragma unroll
        for (int k = 0; k < 18; ++k) *(u32x4*)(sm + (row + 32 * k) * 208 + cc * 16) = v[k];
      }
      tag = mk_tag();
      bar();
      STAMP(4,0);
      if (w < 6) {
        const int ct = 6 * ti + w;
        const int hf = ct < 144 ? 1 : 0, cl = ct < 144 ? ct : ct - 144, fp = cl / 9, n = cl % 9;
        const char* trb = sm + (8 * q + (li >> 2)) * 208 + w * 32 + (li & 3) * 8;
        const unsigned dst = lane * 16 + O_WI + ((fp * 2 + hf) * 18 * 9 + n) * 1024;
        float s = 0.f;
#pragma unroll 6
        for (int g = 0; g < 18; ++g) {
          u32x2 lo = tr_read(trb + g * 32 * 208), hi = tr_read(trb + g * 32 * 208 + 4 * 208);
          st16(R, dst, g * 9216, u32x4{lo[0], lo[1], hi[0], hi[1]});
          s = dot2(lo[0], 0x3F803F80u, s); s = dot2(lo[1], 0x3F803F80u, s); s = dot2(hi[0], 0x3F803F80u, s); s = dot2(hi[1], 0x3F803F80u, s);
        }
        s = q_sum(s);
        if (q == 0) st4(R, li * 4, O_CS + ct * 64, asu(s));
        arrive(1, 6, O_F2 + (144 + ti) * 8, 2048, NREP);
        STAMP(4,1);
      }
    }
  }
  const int tp = (xc & 1) * 8 + (ii >> 2), fp = (xc >> 1) * 4 + (ii & 3);
  const int t64 = 4 * xc + (ii >> 3), j3 = ii & 7;
  if (w == 0) { wait_flags<1>(R, O_F2 + rep * 2048 + 144 * 8, 48, tag, lane); STAMPW(6,0,0); }
  bar();
  if (w >= 4) {
    const int lw = w - 4;
    const unsigned l16 = lane * 16, base = lds_addr(sm);
    const int ph = ((ii >> 2) + ii) & 3, ph3 = ((ii >> 3) + ii) & 3;
    auto k9 = [&](int t) { return (t & 3) == ph ? (t + 4 < 9 ? t + 4 : ph) : t; };
    auto k32 = [&](int t) { return (t & 3) == ph ? ((t + 4) & 31) : t; };
    auto k18 = [&](int t) { return (t & 3) == ph3 ? (t + 4 < 18 ? t + 4 : ph3) : t; };
    if (lw < 2) {
      constexpr int NP = 8;
      unsigned vo[8];
#pragma unroll
      for (int j = 0; j < 8; ++j) {
        int t = 64 * lw + 32 * (j >> 2) + 4 * ((j >> 1) & 1) + 16 * (j & 1) + 8 * (lane >> 5) + ((lane >> 3) & 3);
        vo[j] = t * 4096 + (((lane & 7) ^ ((lane >> 3) & 7)) * 16) + 3072 - (j & 3) * 1024;
      }
      const char* wsrc = w_sp + (size_t)tp * 128 * 4096 - 3072;
      const char* hsrc = ws + O_HF + tp * 18 * 8192 + lw * 8192;
      const char* psrc = ws + O_PF + t64 * 72 * 4096 + lw * 8192;
      const unsigned dst = base + lw * 8192;
      if (lw == 0) wait_flags<2>(R, O_F2 + rep * 2048, 144, tag, lane);
      STAMPW(5,1,4);
      bar();
#pragma unroll 1
      for (int t = 0; t < 4; ++t) ld_lin<NP>(dst + t * STG, hsrc + k9(t) * 16384, l16);
      wait_vm<3 * NP>(); bar();
#pragma unroll 1
      for (int t = 4; t < 18; ++t) { wait_vm<2 * NP>(); bar(); ld_lin<NP>(dst + (t & 3) * STG, hsrc + k9(t < 9 ? t : t - 9) * 16384, l16); }
#pragma unroll 1
      for (int t = 18; t < 50; ++t) {
        wait_vm<2 * NP>(); bar();
        const unsigned dd = dst + (t & 3) * STG; const char* sp = wsrc + k32(t - 18) * 128;
        glds4<false>(dd, sp, vo[0], vo[1], vo[2], vo[3]); glds4<false>(dd + 4096, sp, vo[4], vo[5], vo[6], vo[7]);
      }
      wait_vm<2 * NP>(); bar(); wait_vm<NP>(); bar(); wait_vm<0>(); bar();
      wait_flags<1>(R, O_PFL + (t64 >> 1) * 2048 + ((t64 & 1) * 8 + j3) * 128, 16, tag, lane);
      STAMPW(5,2,4);
#pragma unroll 1
      for (int t = 50; t < 68; ++t) {
        if (t >= 53) { wait_vm<2 * NP>(); bar(); }
        ld_lin<NP>(dst + (t & 3) * STG, psrc + k18(t - 50) * 16384, l16);
      }
      wait_vm<2 * NP>(); bar(); wait_vm<NP>(); bar(); wait_vm<0>(); bar();
    } else {
      constexpr int NP = 9;
      const char* wi = ws + O_WI + (fp * 2) * 18 * 9216 + (lw - 2) * 9216;
      const char* zs = ws + O_ZF + fp * 64 * 9216 + (lw - 2) * 9216;
      const char* wo = ws + O_WO + j3 * 36 * 9216 + (lw - 2) * 9216;
      const unsigned dst = base + 16384 + (lw - 2) * 9216;
#pragma unroll 1
      for (int t = 0; t < 4; ++t) ld_lin<NP>(dst + t * STG, wi + k9(t) * 18432, l16);
      bar();
      wait_vm<3 * NP>(); bar();
      if (lw == 3) {
        f32x2 a = *(const f32x2*)(sm + X_LN + lane * 8) + *(const f32x2*)(sm + X_LN + 512 + lane * 8);
        a[0] = q_sum(dpp_add<0x128>(a[0])); a[1] = q_sum(dpp_add<0x128>(a[1]));
        float mean = a[0] * (1.0f / D), mb, ri;
        ln_scalars(mean, fmaxf(a[1] * (1.0f / D) - mean * mean, 0.f), mb, ri);
        if (lane < 8) st8(R, lane * 8, O_HS + tp * 1024 + fp * 64, u32x2{asu(ri) == SENT ? 0x7FC00000u : asu(ri), asu(-mb)});
      }
#pragma unroll 1
      for (int t = 4; t < 18; ++t) { wait_vm<2 * NP>(); bar(); ld_lin<NP>(dst + (t & 3) * STG, wi + (t < 9 ? k9(t) : 9 + k9(t - 9)) * 18432, l16); }
      wait_vm<2 * NP>(); bar(); wait_vm<NP>(); bar(); wait_vm<0>(); bar();
      wait_flags<1>(R, O_F2 + rep * 2048 + 192 * 8, 36, tag, lane);
      wait_flags<1>(R, O_ZFL + fp * 2048 + tp * 128, 16, tag, lane);
      STAMPW(6,1,6);
#pragma unroll 1
      for (int t = 18; t < 68; ++t) {
        if (t >= 21) { wait_vm<2 * NP>(); bar(); }
        ld_lin<NP>(dst + (t & 3) * STG, t < 50 ? zs + k32(t - 18) * 18432 : wo + k18(t - 50) * 18432, l16);
      }
      wait_vm<2 * NP>(); bar(); wait_vm<NP>(); bar(); wait_vm<0>(); bar();
    }
    return;
  }
  Ctx c;
  c.sm = sm;
#pragma unroll
  for (int i = 0; i < 4; ++i) { c.am0[i] = (2 * w + (i >> 1)) * 1024 + (i & 1) * 512 + lane * 8; asm volatile("" : "+v"(c.am0[i])); }
  c.am1 = 2 * w * 2048 + li * 128 + ((q ^ (lane & 7)) * 16);
  c.an = 16384 + lane * 16;
  const int prow = 8 * (li >> 2) + (li & 3);
  if (tid < 72) *(u32x4*)(sm + X_CS + tid * 16) = ld16(R, (tid < 36 ? FH * 4 + tid * 16 : (tid - 36) * 16), O_CS + fp * 576);
  bar();
  char* pvb = sm + X_PV + w * 4096;
  {
    u32x4 hv[3];
#pragma unroll
    for (int k = 0; k < 3; ++k)
      hv[k] = ld16(R, (lane >> 5) * 8192 + ((lane >> 2) & 7) * 128 + (lane & 3) * 16, O_HF + (tp * 18 * 8 + (fp >> 1)) * 1024 + (fp & 1) * 64 + min(w + 4 * k, 8) * 16384);
    if (w) hv[2] = u32x4{0, 0, 0, 0};
    f32x4 a = {0, 0, 0, 0};
#pragma unroll
    for (int k = 0; k < 3; ++k)
#pragma unroll
      for (int e = 0; e < 4; ++e) { a[e & 2] = dot2(hv[k][e], 0x3F803F80u, a[e & 2]); a[(e & 2) + 1] = dot2(hv[k][e], hv[k][e], a[(e & 2) + 1]); }
#pragma unroll
    for (int e = 0; e < 4; ++e) a[e] = dpp_add<0x124>(dpp_add<0x128>(a[e]));
    if (li < 4) *(f32x4*)(sm + X_LN + (w * 4 + q) * 64 + li * 16) = a;
  }
  f32x4 acc[2][9] = {};
  Fr X, Y;
  bar();
  STAMP(3,0);
  ldf<0>(X, c, 0, 0);
#pragma unroll 1
  for (int t = 0; t < 8; ++t) {
    seg<0, 0, true>(acc, X, Y, c, t, 1);
    bar();
    seg<0, 0, true>(acc, Y, X, c, t + 1, 0);
  }
  u32x2 ht[2]; u32x4 hc[4];
  auto ld_hs = [&]() {
#pragma unroll
    for (int m = 0; m < 2; ++m) {
      ht[m] = ld8(R, li * 8 + m * 128, O_HS + tp * 1024 + w * 256);
      hc[2 * m] = ld16(R, q * 32 + m * 128, O_HS + tp * 1024 + w * 256); hc[2 * m + 1] = ld16(R, q * 32 + 16 + m * 128, O_HS + tp * 1024 + w * 256);
    }
  };
  ld_hs();
  __builtin_amdgcn_sched_barrier(0);
  seg<0, 0, true>(acc, X, Y, c, 8, 1);
  bar();
  seg<0, 0, true>(acc, Y, X, c, 9, 0);
  STAMP(3,1);
#pragma unroll 1
  for (int spin = 0; spin < SPIN; ++spin) {
    bool ok = ht[0][0] != SENT && ht[0][1] != SENT && ht[1][0] != SENT && ht[1][1] != SENT;
#pragma unroll
    for (int k = 0; k < 4; ++k) ok = ok && hc[k][0] != SENT && hc[k][1] != SENT && hc[k][2] != SENT && hc[k][3] != SENT;
    if (__all(ok)) break;
    __builtin_amdgcn_s_sleep(1);
    asm volatile("" ::: "memory");
    ld_hs();
  }
  const float lrs[2] = {asf(ht[0][0]), asf(ht[1][0])}, lnm[2] = {asf(ht[0][1]), asf(ht[1][1])};
  u32x4 zb[9];
  f32x2 rs[4];
  {
    float cs[9];
#pragma unroll
    for (int n = 0; n < 9; ++n) cs[n] = *(const float*)(sm + X_CS + (16 * n + li) * 4);
    f32x2 nm[4], k1[4], k0[4], S1[4] = {}, S2[4] = {};
#pragma unroll
    for (int jp = 0; jp < 4; ++jp) {
      rs[jp] = f32x2{asf(hc[jp][0]), asf(hc[jp][2])}; nm[jp] = f32x2{asf(hc[jp][1]), asf(hc[jp][3])};
      k0[jp] = rs[jp] * GC0; k1[jp] = rs[jp] * rs[jp] * rs[jp] * GC1;
    }
#pragma unroll
    for (int n = 0; n < 9; ++n) {
#pragma unroll
      for (int jp = 0; jp < 4; ++jp) {
        f32x2 a = (jp & 1) ? hi2(acc[jp >> 1][n]) : lo2(acc[jp >> 1][n]);
        f32x2 z = gelu2(__builtin_elementwise_fma(nm[jp], f32x2{cs[n], cs[n]}, a), k1[jp], k0[jp]);
        zb[n][jp] = pk2(z);
        S1[jp] += z; S2[jp] = __builtin_elementwise_fma(z, z, S2[jp]);
      }
      asm volatile("" : "+v"(zb[n]));
    }
#pragma unroll
    for (int jp = 0; jp < 4; ++jp) { S1[jp] *= rs[jp]; S2[jp] *= rs[jp] * rs[jp]; }
    float v[16];
#pragma unroll
    for (int j = 0; j < 8; ++j) { v[2 * j] = S1[j >> 1][j & 1]; v[2 * j + 1] = S2[j >> 1][j & 1]; }
#pragma unroll
    for (int i = 0; i < 8; ++i) { bool h = li & 8; v[i] = (h ? v[i + 8] : v[i]) + dpp_get<0x128>(h ? v[i] : v[i + 8]); }
#pragma unroll
    for (int i = 0; i < 4; ++i) { bool h = li & 4; v[i] = (h ? v[i + 4] : v[i]) + dpp_get<0x141>(h ? v[i] : v[i + 4]); }
#pragma unroll
    for (int i = 0; i < 2; ++i) { bool h = li & 2; v[i] = (h ? v[i + 2] : v[i]) + dpp_get<0x4E>(h ? v[i] : v[i + 2]); }
    { bool h = li & 1; v[0] = (h ? v[1] : v[0]) + dpp_get<0xB1>(h ? v[0] : v[1]); }
    unsigned tot = asu(v[0]);
    st4(R, lane * 4, O_ST + ((tp * 4 + w) * 16 + fp) * 256, tot == SENT ? 0x7FC00000u : tot);
#pragma unroll
    for (int m = 0; m < 2; ++m)
#pragma unroll
      for (int n = 0; n < 9; ++n) acc[m][n] = f32x4{0, 0, 0, 0};
  }
  STAMP(3,2);
  auto it2 = [&](int t) {
    seg<0, 1, true>(acc, X, Y, c, t, 1);
    bar();
    seg<0, 1, true>(acc, Y, X, c, t + 1, 0);
  };
  const char* sts = ws + O_ST + (tp * 4 + w) * 4096;
#pragma unroll 1
  for (int t = 9; t < TCHK; ++t) {
    if (t == TPOLL) glds4<true>(lds_addr(pvb), sts, lane * 16, lane * 16, lane * 16, lane * 16);
    __builtin_amdgcn_sched_barrier(0);
    it2(t);
  }
  STAMP(3,3);
  u32x4 pv[4];
#pragma unroll 1
  for (int spin = 0; spin < SPIN; ++spin) {
    wait_vm<0>();
    bool ok = true;
#pragma unroll
    for (int k = 0; k < 4; ++k) {
      pv[k] = *(const u32x4*)(pvb + k * 1024 + lane * 16);
      ok = ok && pv[k][0] != SENT && pv[k][1] != SENT && pv[k][2] != SENT && pv[k][3] != SENT;
    }
    if (__all(ok)) break;
    glds4<true>(lds_addr(pvb), sts, lane * 16, lane * 16, lane * 16, lane * 16);
  }
  STAMP(3,4);
  {
    float t4[4];
#pragma unroll
    for (int e = 0; e < 4; ++e) t4[e] = q_sum((asf(pv[0][e]) + asf(pv[1][e])) + (asf(pv[2][e]) + asf(pv[3][e])));
    f32x2 o0, o1;
#pragma unroll
    for (int r = 0; r < 2; ++r) {
      float mean = t4[2 * r] * (1.0f / FH), var = fmaxf(t4[2 * r + 1] * (1.0f / FH) - mean * mean, 0.f), mb, ri;
      ln_scalars(mean, var, mb, ri);
      o0[r] = ri; o1[r] = -mb * ri;
    }
    char* ln = sm + X_LN + w * 256;
    if (lane < 16) { *(f32x2*)(ln + li * 8) = o0; *(f32x2*)(ln + 128 + li * 8) = o1; }
    asm volatile("" ::: "memory");
    f32x4 ri[2], rb[2];
#pragma unroll
    for (int m = 0; m < 2; ++m) { ri[m] = *(const f32x4*)(ln + (8 * q + 4 * m) * 4); rb[m] = *(const f32x4*)(ln + 128 + (8 * q + 4 * m) * 4); }
#pragma unroll
    for (int jp = 0; jp < 4; ++jp) rs[jp] *= (jp & 1) ? hi2(ri[jp >> 1]) : lo2(ri[jp >> 1]);
    const unsigned zo = lane * 16 + O_ZF + (fp * 64 + 4 * tp + w) * 9216;
#pragma unroll
    for (int n = 0; n < 9; ++n) {
      u32x4 o4;
#pragma unroll
      for (int jp = 0; jp < 4; ++jp)
        o4[jp] = pk2(__builtin_elementwise_fma(up2(zb[n][jp]), rs[jp], (jp & 1) ? hi2(rb[jp >> 1]) : lo2(rb[jp >> 1])));
      st16(R, zo, n * 1024, o4);
    }
  }
  STAMP(3,5);
#pragma unroll 1
  for (int t = TCHK; t < 17; ++t) {
    seg<0, 1, true>(acc, X, Y, c, t, 1);
    if (t == TFLAG) wait_vm<0>();
    bar();
    if (t == TFLAG && tid < 16) st8(R, tid * 128, O_ZFL + fp * 2048 + tp * 8, tag);
    seg<0, 1, true>(acc, Y, X, c, t + 1, 0);
  }
  seg<0, 1, true>(acc, X, Y, c, 17, 1);
  seg<0, 1, false>(acc, Y, X, c, 17, 1);
  STAMP(3,6);
  u32x2 z1[2][9];
  float bs[2];
#pragma unroll
  for (int m = 0; m < 2; ++m) bs[m] = ldbf(b_sp, 128 * tp + 32 * w + prow + 4 * m) * lrs[m];
  {
    f32x4 cs[9];
#pragma unroll
    for (int n = 0; n < 9; ++n) cs[n] = *(const f32x4*)(sm + X_CS + (144 + 16 * n + 4 * q) * 4);
#pragma unroll
    for (int n = 0; n < 9; ++n)
#pragma unroll
      for (int m = 0; m < 2; ++m) {
        const float a1 = lrs[m] * lrs[m] * lrs[m] * GC1, a0 = lrs[m] * GC0;
        const f32x2 k1 = {a1, a1}, k0 = {a0, a0}, nm = {lnm[m], lnm[m]};
        z1[m][n][0] = pk2(gelu2(__builtin_elementwise_fma(nm, lo2(cs[n]), lo2(acc[m][n])), k1, k0));
        z1[m][n][1] = pk2(gelu2(__builtin_elementwise_fma(nm, hi2(cs[n]), hi2(acc[m][n])), k1, k0));
        asm volatile("" : "+v"(z1[m][n]));
        acc[m][n] = f32x4{0, 0, 0, 0};
      }
  }
  STAMP(3,7);
  bar();
  STAMP(3,8);
  ldf<1>(X, c, 18, 0);
#pragma unroll 1
  for (int t = 18; t < 49; ++t) {
    seg<1, 1, true>(acc, X, Y, c, t, 1);
    bar();
    seg<1, 1, true>(acc, Y, X, c, t + 1, 0);
  }
  seg<1, 1, true>(acc, X, Y, c, 49, 1);
  seg<1, 1, false>(acc, Y, X, c, 49, 1);
  STAMP(3,9);
  {
    const unsigned pf = O_PF + (((2 * tp + (w >> 1)) * 72 + 9 * (fp >> 1)) * 4 + 2 * (w & 1)) * 1024;
    const unsigned po = lane * 16 + pf + (fp & 1) * 16384;
#pragma unroll
    for (int m = 0; m < 2; ++m) {
      const f32x2 r2 = {lrs[m], lrs[m]}, b2 = {bs[m], bs[m]};
      auto pp = [&](int n) { return u32x2{pk2(__builtin_elementwise_fma(lo2(acc[m][n]), r2, b2) * up2(z1[m][n][0])), pk2(__builtin_elementwise_fma(hi2(acc[m][n]), r2, b2) * up2(z1[m][n][1]))}; };
#pragma unroll
      for (int a = 0; a < 4; ++a) {
        u32x2 p0 = pp(2 * a), p1 = pp(2 * a + 1);
        st16(R, po, a * 4096 + m * 1024, u32x4{p0[0], p0[1], p1[0], p1[1]});
      }
      st8(R, lane * 16, pf + 8 * 4096 + m * 1024 + (fp & 1) * 8, pp(8));
    }
  }
  STAMP(3,10);
  arrive(2, 4, O_PFL + tp * 2048 + fp * 8, 128, 16);
  const int th = w & 1, kq = w >> 1, T = 4 * t64 + 2 * th + kq;
  const int lane5 = fresh_lane();
#define lane lane5
#define li (lane5 & 15)
#define q (lane5 >> 4)
#define prow (8 * (li >> 2) + (li & 3))
  u32x2 hv[5];
#pragma unroll
  for (int n = 0; n < 5; ++n) {
    const int dd = 72 * j3 + 16 * n + 4 * (n == 4 ? (q & 1) : q);
    hv[n] = ld8(R, (dd >> 5) * 8192 + ((dd >> 2) & 1) * 512 + (((dd >> 3) & 3) * 16 + li) * 8, O_HF + ((T >> 3) * 18 * 8 + (T & 7)) * 1024);
  }
  struct F3 { u32x4 p[2][2], n[9]; };
  f32x4 a3[2][6] = {};
  F3 U, V;
  const int ap = (8 * kq + 2 * th) * 1024 + lane * 16, aw = 16384 + kq * 9216 + lane * 16;
  auto ld3 = [&](F3& f, int t) {
    const char* slot = sm + (t & 3) * STG;
#pragma unroll
    for (int g = 0; g < 2; ++g)
#pragma unroll
      for (int m = 0; m < 2; ++m) f.p[g][m] = *(const u32x4*)(slot + ap + (4 * g + m) * 1024);
#pragma unroll
    for (int n = 0; n < 9; ++n) f.n[n] = *(const u32x4*)(slot + aw + n * 1024);
  };
  auto mm3 = [&](const F3& f) {
#pragma unroll
    for (int g = 0; g < 2; ++g)
#pragma unroll
      for (int m = 0; m < 2; ++m) {
#pragma unroll
        for (int n = 0; n < 4; ++n) a3[m][n] = mfma_bf(f.n[4 * g + n], f.p[g][m], a3[m][n]);
        a3[m][4 + g] = mfma_bf(f.n[8], f.p[g][m], a3[m][4 + g]);
      }
#pragma unroll
    for (int i = 0; i < 13; ++i) { __builtin_amdgcn_sched_group_barrier(0x008, 1, 0); __builtin_amdgcn_sched_group_barrier(0x100, 1, 0); }
    __builtin_amdgcn_sched_barrier(0);
  };
  STAMP(3,11);
  bar();
  STAMP(3,12);
  ld3(U, 50);
#pragma unroll 1
  for (int t = 50; t < 66; t += 2) { bar(); ld3(V, t + 1); mm3(U); bar(); ld3(U, t + 2); mm3(V); }
  bar(); ld3(V, 67); mm3(U); mm3(V);
  STAMP(3,13);
#pragma unroll
  for (int m = 0; m < 2; ++m)
#pragma unroll
    for (int r = 0; r < 4; ++r) a3[m][4][r] += __shfl_xor(a3[m][5][r], 32);
#pragma unroll
  for (int n = 0; n < 5; ++n) {
    *(f32x4*)(sm + (w * 5 + n) * 1024 + lane * 16) = kq ? a3[0][n] : a3[1][n];
    a3[0][n] = kq ? a3[1][n] : a3[0][n];
  }
  bar();
  char* o = out + (size_t)(32 * (T >> 1) + 4 * (T & 1) + prow) * 1152 + (72 * j3 + 4 * q) * 2;
#pragma unroll
  for (int n = 0; n < 5; ++n) {
    f32x4 v = a3[0][n] + *(const f32x4*)(sm + ((w ^ 2) * 5 + n) * 1024 + lane * 16);
    if (n < 4 || q < 2) *(u32x2*)(o + n * 32) = u32x2{pk2(up2(hv[n][0]) + up2(pk2(lo2(v)))), pk2(up2(hv[n][1]) + up2(pk2(hi2(v))))};
  }
  if (bid == 0 && w == 0 && lane == 0) { u64 e = (((u64)tag[1] << 32) | tag[0]) - 0x9E3779B97F4A7C15ull + 1; st8(R, 0, O_EP, u32x2{(unsigned)e, (unsigned)(e >> 32)}); }
  STAMP(3,14);
}

#undef lane
#undef li
#undef q
#undef prow
extern "C" void kernel_launch(void* const* d_in, const int* in_sizes, int n_in, void* d_out, int out_size, void* d_ws, size_t ws_size,
                              hipStream_t stream) {
  (void)hipFuncSetAttribute((const void*)k_all, hipFuncAttributeMaxDynamicSharedMemorySize, LDS_BYTES);
  k_all<<<256, 512, LDS_BYTES, stream>>>((const char*)d_in[0], (const char*)d_in[1], (const char*)d_in[2], (const char*)d_in[3],
      (const char*)d_in[4], (const char*)d_in[5], (const char*)d_in[6], (const char*)d_in[7], (const char*)d_in[8], (const char*)d_in[10],
      (const char*)d_in[11], (const char*)d_in[12], (const char*)d_in[13], (char*)d_out, (char*)d_ws);
}
```

```cpp
#include <hip/hip_runtime.h>
#include <hip/hip_bf16.h>
#include <stdint.h>

typedef __attribute__((ext_vector_type(8))) __bf16 bf16x8;
typedef __attribute__((ext_vector_type(2))) __bf16 bf16x2;
typedef __attribute__((ext_vector_type(8))) _Float16 f16x8;
typedef __attribute__((ext_vector_type(2))) _Float16 f16x2;
typedef __attribute__((ext_vector_type(4))) float f32x4;
typedef __attribute__((ext_vector_type(2))) float f32x2;
typedef __attribute__((ext_vector_type(4))) unsigned u32x4;
typedef __attribute__((ext_vector_type(2))) unsigned u32x2;
typedef __attribute__((ext_vector_type(4))) short s16x4;
#define LDSP __attribute__((address_space(3)))
#define DEVI __device__ __forceinline__

constexpr int L = 2048, D = 576, NM = 128, F = 4608, FH = 2304;
constexpr float EPS = 1e-5f;
constexpr unsigned O_EP = 0;
constexpr int NREP = 32;
constexpr unsigned O_F1 = 256;
constexpr unsigned O_F2 = O_F1 + NREP * 1024;
constexpr unsigned F2S = 2048;
constexpr unsigned O_ZFL = O_F2 + NREP * F2S;
constexpr unsigned O_PFL = O_ZFL + 32768;
constexpr unsigned O_CS = 188416;
static_assert(O_PFL + 32768 <= O_CS, "ws");
constexpr unsigned O_ST = O_CS + F * 12;
constexpr unsigned O_HS = O_ST + 262144;
constexpr unsigned O_U = O_HS + 16384;
constexpr unsigned O_XB = O_U + L * D * 2;
constexpr unsigned O_WI = O_XB + L * D * 2;
constexpr unsigned O_WO = O_WI + D * F * 2;
constexpr unsigned O_HF = O_WO + FH * D * 2;
constexpr unsigned O_ZF = O_HF + L * D * 2;
constexpr unsigned O_PF = O_ZF + L * FH * 2;
constexpr unsigned WS_USED = O_PF + L * FH * 2;
constexpr size_t O_STAMP = 128u << 20;
#define STAMP(k,i) __builtin_amdgcn_sched_barrier(0)
#define STAMPW(k,i,W) __builtin_amdgcn_sched_barrier(0)
#define RSTAMP(k,i)
constexpr unsigned SENT = 0xFFFFFFFFu;

DEVI unsigned pk_bf16(float a, float b) { f32x2 v = {a, b}; return __builtin_bit_cast(unsigned, __builtin_convertvector(v, bf16x2)); }
DEVI unsigned pk_f16(float a, float b) { f32x2 v = {a, b}; return __builtin_bit_cast(unsigned, __builtin_convertvector(v, f16x2)); }
DEVI float bf_lo(unsigned u) { return __builtin_bit_cast(float, u << 16); }
DEVI float bf_hi(unsigned u) { return __builtin_bit_cast(float, u & 0xFFFF0000u); }
DEVI float asf(unsigned u) { return __builtin_bit_cast(float, u); }
DEVI unsigned asu(float f) { return __builtin_bit_cast(unsigned, f); }
DEVI float rbf(float a) { return bf_lo(pk_bf16(a, a)); }
DEVI float h_lo(unsigned u) { return (float)__builtin_bit_cast(f16x2, u)[0]; }
DEVI float h_hi(unsigned u) { return (float)__builtin_bit_cast(f16x2, u)[1]; }
template <int CTRL> DEVI float dpp_add(float v) {
  return v + __builtin_bit_cast(float, __builtin_amdgcn_update_dpp(0, __builtin_bit_cast(int, v), CTRL, 0xF, 0xF, true));
}
DEVI float row16_sum(float v) { v = dpp_add<0x128>(v); v = dpp_add<0x124>(v); v = dpp_add<0x122>(v); v = dpp_add<0x121>(v); return v; }
DEVI float half_sum(float v) { auto r = __builtin_amdgcn_permlane32_swap(asu(v), asu(v), false, false); return asf(r[0]) + asf(r[1]); }
DEVI float q_sum(float v) { auto r = __builtin_amdgcn_permlane16_swap(asu(v), asu(v), false, false); return half_sum(asf(r[0]) + asf(r[1])); }
DEVI unsigned umax4(u32x4 v) { return max(max(v[0], v[1]), max(v[2], v[3])); }
DEVI u32x2 tr_read(const char* p) {
  return __builtin_bit_cast(u32x2, __builtin_amdgcn_ds_read_tr16_b64_v4i16((LDSP s16x4*)p));
}
DEVI f32x4 mfma_bf(u32x4 a, u32x4 b, f32x4 c) {
  return __builtin_amdgcn_mfma_f32_16x16x32_bf16(__builtin_bit_cast(bf16x8, a), __builtin_bit_cast(bf16x8, b), c, 0, 0, 0);
}
DEVI f32x4 mfma_h(u32x4 a, u32x4 b, f32x4 c) {
  return __builtin_amdgcn_mfma_f32_16x16x32_f16(__builtin_bit_cast(f16x8, a), __builtin_bit_cast(f16x8, b), c, 0, 0, 0);
}
DEVI void ln_scalars(float mean, float var, float& mb, float& rinv) {
  mb = rbf(mean);
  float d = rbf(__builtin_amdgcn_sqrtf(rbf(rbf(var) + EPS)));
  rinv = __builtin_amdgcn_rcpf(d);
}
DEVI float gelu(float x) {
  float w = x * __builtin_fmaf(x * x, -0.10294324f, -2.3022082f);
  return x * __builtin_amdgcn_rcpf(1.0f + __builtin_amdgcn_exp2f(w));
}

typedef unsigned long long u64;
typedef __amdgpu_buffer_rsrc_t rsrc_t;
constexpr int SPIN = 1 << 16;
constexpr int CODE_LINES = 38;
template <int CTRL> DEVI float dpp_get(float v) {
  return __builtin_bit_cast(float, __builtin_amdgcn_update_dpp(0, __builtin_bit_cast(int, v), CTRL, 0xF, 0xF, true));
}
constexpr float GC1 = -0.10294324f, GC0 = -2.3022082f;
DEVI f32x2 gelu2(f32x2 x, f32x2 k1, f32x2 k0) {
  f32x2 w = x * __builtin_elementwise_fma(x * x, k1, k0);
  f32x2 d = f32x2{__builtin_amdgcn_exp2f(w[0]), __builtin_amdgcn_exp2f(w[1])} + f32x2{1.f, 1.f};
  return x * f32x2{__builtin_amdgcn_rcpf(d[0]), __builtin_amdgcn_rcpf(d[1])};
}
DEVI unsigned pk2(f32x2 v) { return __builtin_bit_cast(unsigned, __builtin_convertvector(v, bf16x2)); }
DEVI f32x2 up2(unsigned u) { return f32x2{bf_lo(u), bf_hi(u)}; }
DEVI f32x2 lo2(f32x4 v) { return __builtin_shufflevector(v, v, 0, 1); }
DEVI f32x2 hi2(f32x4 v) { return __builtin_shufflevector(v, v, 2, 3); }
DEVI float dot2(unsigned a, unsigned b, float c) {
  return __builtin_amdgcn_fdot2_f32_bf16(__builtin_bit_cast(bf16x2, a), __builtin_bit_cast(bf16x2, b), c, false);
}
DEVI void st16(rsrc_t r, unsigned voff, unsigned soff, u32x4 v) { __builtin_amdgcn_raw_buffer_store_b128(v, r, voff + soff, 0, 16); }
DEVI void st8(rsrc_t r, unsigned voff, unsigned soff, u32x2 v) { __builtin_amdgcn_raw_buffer_store_b64(v, r, voff, soff, 16); }
DEVI void st4(rsrc_t r, unsigned voff, unsigned soff, unsigned v) { __builtin_amdgcn_raw_buffer_store_b32(v, r, voff, soff, 16); }
DEVI u32x4 ld16(rsrc_t r, unsigned voff, unsigned soff) { return __builtin_amdgcn_raw_buffer_load_b128(r, voff, soff, 16); }
DEVI u32x2 ld8(rsrc_t r, unsigned voff, unsigned soff) { return __builtin_amdgcn_raw_buffer_load_b64(r, voff, soff, 16); }
template <int N> DEVI void wait_vm() { asm volatile("s_waitcnt vmcnt(%0)" ::"i"(N) : "memory"); }
DEVI void bar() { __builtin_amdgcn_sched_barrier(0); asm volatile("s_waitcnt lgkmcnt(0)\n\ts_barrier" ::: "memory"); __builtin_amdgcn_sched_barrier(0); }
DEVI int fresh_lane() { int l; asm volatile("v_mbcnt_lo_u32_b32 %0, -1, 0\n\tv_mbcnt_hi_u32_b32 %0, -1, %0" : "=v"(l)); return l; }
DEVI unsigned lds_addr(const char* p) { return (unsigned)(uintptr_t)(LDSP const char*)p; }
template <int NL> DEVI void wait_flags(rsrc_t r, unsigned off, int n, u32x2 tag, int lane) {
#pragma unroll 1
  for (int spin = 0; spin < SPIN; ++spin) {
    bool ok = true;
#pragma unroll
    for (int k = 0; k < NL; ++k) {
      int i = min(lane + 64 * k, n / 2 - 1);
      u32x4 v = ld16(r, i * 16, off);
      ok = ok && v[0] == tag[0] && v[1] == tag[1] && v[2] == tag[0] && v[3] == tag[1];
    }
    if (__all(ok)) break;
    __builtin_amdgcn_s_sleep(1);
    asm volatile("" ::: "memory");
  }
  asm volatile("" ::: "memory");
}
template <bool SC> DEVI void glds4(unsigned m0v, const char* base, unsigned v0, unsigned v1, unsigned v2, unsigned v3) {
  unsigned keep;
  if (SC)
    asm volatile("s_mov_b32 %0, m0\n\ts_mov_b32 m0, %1\n\ts_nop 0\n\t"
                 "global_load_lds_dwordx4 %3, %2 sc1\n\tglobal_load_lds_dwordx4 %4, %2 offset:1024 sc1\n\t"
                 "global_load_lds_dwordx4 %5, %2 offset:2048 sc1\n\tglobal_load_lds_dwordx4 %6, %2 offset:3072 sc1\n\ts_mov_b32 m0, %0"
                 : "=&s"(keep) : "s"(m0v), "s"(base), "v"(v0), "v"(v1), "v"(v2), "v"(v3) : "memory");
  else
    asm volatile("s_mov_b32 %0, m0\n\ts_mov_b32 m0, %1\n\ts_nop 0\n\t"
                 "global_load_lds_dwordx4 %3, %2\n\tglobal_load_lds_dwordx4 %4, %2 offset:1024\n\t"
                 "global_load_lds_dwordx4 %5, %2 offset:2048\n\tglobal_load_lds_dwordx4 %6, %2 offset:3072\n\ts_mov_b32 m0, %0"
                 : "=&s"(keep) : "s"(m0v), "s"(base), "v"(v0), "v"(v1), "v"(v2), "v"(v3) : "memory");
}
DEVI void glds1(unsigned m0v, const char* base, unsigned v0) {
  unsigned keep;
  asm volatile("s_mov_b32 %0, m0\n\ts_mov_b32 m0, %1\n\ts_nop 0\n\tglobal_load_lds_dwordx4 %3, %2 sc1\n\ts_mov_b32 m0, %0"
               : "=&s"(keep) : "s"(m0v), "s"(base), "v"(v0) : "memory");
}
template <int NP> DEVI void ld_lin(unsigned dst, const char* src, unsigned l16) {
  glds4<true>(dst, src, l16, l16, l16, l16);
  glds4<true>(dst + 4096, src + 4096, l16, l16, l16, l16);
  if (NP == 9) glds1(dst + 8192, src + 8192, l16);
}

constexpr int TP = 1168;
constexpr int CV_K = 8 * 260 * 16, CV_U = 4576;
constexpr int C_U = 4 * CV_K, C_KD = C_U + 16 * TP, C_END = C_KD + 4 * 1536;
constexpr int KT = 528;
constexpr int STG = 34816, RING = 4 * STG;
constexpr int X_CS = RING, X_LN = X_CS + 1152, X_PV = X_LN + 1024, X_CNT = 158720;
constexpr int LDS_BYTES = X_CNT + 64 + 256;
static_assert(C_END <= X_CNT && 4 * CV_U <= 16 * TP && X_PV + 16384 <= X_CNT && LDS_BYTES <= 163840, "lds");
#define TPOLL 11
#define WDELAY 2
#define SDELAY 3
#define TCHK 12
#define TFLAG 14

struct Fr { u32x4 m[2], n[9]; };
struct Ctx { const char* sm; int am0[4], am1, an; };
template <int MK> DEVI void ldf(Fr& f, const Ctx& c, int t, int kk) {
  int so = (t & 3) * STG;
  asm volatile("" : "+s"(so));
  const char* slot = c.sm + so;
  if (MK == 0) {
#pragma unroll
    for (int m = 0; m < 2; ++m) {
      u32x2 a = *(const u32x2*)(slot + c.am0[2 * m] + kk * 8192), b = *(const u32x2*)(slot + c.am0[2 * m + 1] + kk * 8192);
      f.m[m] = u32x4{a[0], a[1], b[0], b[1]};
    }
  } else {
#pragma unroll
    for (int m = 0; m < 2; ++m) f.m[m] = *(const u32x4*)(slot + (c.am1 ^ (kk * 64)) + m * 2048);
  }
#pragma unroll
  for (int n = 0; n < 9; ++n) f.n[n] = *(const u32x4*)(slot + c.an + (kk * 9 + n) * 1024);
}
template <int MK, int ORI, bool LD, bool FIRST = false> DEVI void seg(f32x4 (&acc)[2][9], const Fr& cur, Fr& nxt, const Ctx& c, int t, int kk) {
  if (LD) ldf<MK>(nxt, c, t, kk);
#pragma unroll
  for (int m = 0; m < 2; ++m)
#pragma unroll
    for (int nn = 0; nn < 9; ++nn) {
      const int n = m ? 8 - nn : nn;
      const f32x4 ci = FIRST ? f32x4{0, 0, 0, 0} : acc[m][n];
      acc[m][n] = ORI == 0 ? mfma_bf(cur.m[m], cur.n[n], ci) : mfma_bf(cur.n[n], cur.m[m], ci);
    }
#pragma unroll
  for (int i = 0; i < (MK == 0 ? 13 : 11); ++i) { __builtin_amdgcn_sched_group_barrier(0x008, 1, 0); __builtin_amdgcn_sched_group_barrier(0x100, 1, 0); }
  __builtin_amdgcn_sched_barrier(0);
}
DEVI f32x2 cmul(f32x2 a, f32x2 b, f32x2 bp) {
  return __builtin_elementwise_fma(f32x2{a[1], a[1]}, bp, f32x2{a[0], a[0]} * b);
}
DEVI f32x2 perp(f32x2 b) { return f32x2{-b[1], b[0]}; }
DEVI f32x2 cexp(float ar, float ai, float l) {
  float m = __builtin_amdgcn_exp2f(ar * l), ph = __builtin_amdgcn_fractf(ai * l);
  return f32x2{m * __builtin_amdgcn_cosf(ph), m * __builtin_amdgcn_sinf(ph)};
}
DEVI unsigned pkh(f32x2 v) { return __builtin_bit_cast(unsigned, __builtin_convertvector(v, f16x2)); }
DEVI float ldbf(const char* p, int i) { return bf_lo(*(const unsigned short*)(p + 2 * i)); }

__global__ __launch_bounds__(512, 2) void k_all(const char* __restrict__ x, const char* __restrict__ a_re, const char* __restrict__ a_im,
    const char* __restrict__ b_re, const char* __restrict__ b_im, const char* __restrict__ c_re, const char* __restrict__ c_im,
    const char* __restrict__ log_dt, const char* __restrict__ dskip, const char* __restrict__ w_in, const char* __restrict__ w_sp,
    const char* __restrict__ b_sp, const char* __restrict__ w_out, char* __restrict__ out, char* __restrict__ ws) {
  extern __shared__ __attribute__((aligned(16))) char sm[];
  const int tid = threadIdx.x, lane = tid & 63, w = __builtin_amdgcn_readfirstlane(tid >> 6), li = lane & 15, q = lane >> 4;
  const int bid = blockIdx.x, xc = bid & 7, ii = bid >> 3;
  if (w == 7) {
    unsigned long long pc;
    asm volatile("s_getpc_b64 %0" : "=s"(pc));
    const char* cp = (const char*)(pc & ~255ull) + (ii & 7) * (CODE_LINES * 128);
    unsigned keep;
    asm volatile("s_mov_b32 %0, m0\n\ts_mov_b32 m0, %1\n\ts_nop 0\n\tglobal_load_lds_dword %3, %2\n\ts_mov_b32 m0, %0"
                 : "=&s"(keep) : "s"(lds_addr(sm) + X_CNT + 64), "s"(cp), "v"(min(lane, CODE_LINES - 1) * 128) : "memory");
  }
  asm volatile("" ::"s"(x), "s"(a_re), "s"(a_im), "s"(b_re), "s"(b_im), "s"(c_re), "s"(c_im), "s"(log_dt), "s"(dskip), "s"(w_in), "s"(w_out), "s"(ws));
  const rsrc_t R = __builtin_amdgcn_make_buffer_rsrc(ws, 0, WS_USED, 0x00020000);
  STAMP(0,0);
  const u32x2 ep = ld8(R, 0, O_EP);
  const bool is_conv = ii < 18;
  const int cg = xc * 18 + ii, rep = bid & (NREP - 1);
  if (tid == 0) *(u32x4*)(sm + X_CNT) = u32x4{0, 0, 0, 0};
  auto mk_tag = [&]() {
    u64 e = (((u64)__builtin_amdgcn_readfirstlane(ep[1]) << 32) | (unsigned)__builtin_amdgcn_readfirstlane(ep[0])) + 0x9E3779B97F4A7C15ull;
    return u32x2{(unsigned)e, (unsigned)(e >> 32)};
  };
  u32x2 tag;
  auto arrive = [&](int slot, unsigned n, unsigned flag_off, unsigned stride, int nrep) {
    wait_vm<0>();
    const int lane = fresh_lane();
    unsigned old = 0;
    if (lane == 0) old = __hip_atomic_fetch_add((unsigned*)(sm + X_CNT) + slot, 1u, __ATOMIC_RELAXED, __HIP_MEMORY_SCOPE_WORKGROUP);
    if (__builtin_amdgcn_readfirstlane(old) == n - 1 && lane < nrep) st8(R, lane * stride, flag_off, tag);
  };
  if (is_conv) {
    const int ch = w & 3, dch = 4 * cg + ch;
    char* sk = sm + ch * CV_K;
    const char* su = sm + C_U + ch * CV_U;
    float sc = 0.f;
    if (w < 4) {
      const int pi = (dch * NM + 2 * lane) * 2;
      const unsigned v_ar = *(const unsigned*)(a_re + pi), v_ai = *(const unsigned*)(a_im + pi), v_br = *(const unsigned*)(b_re + pi),
                     v_bi = *(const unsigned*)(b_im + pi), v_cr = *(const unsigned*)(c_re + pi), v_ci = *(const unsigned*)(c_im + pi);
      const float p_dk = ldbf(dskip, dch);
      const float ldt = ldbf(log_dt, dch);
      bar();
      const float dt = rbf(__expf(ldt));
      tag = mk_tag();
      STAMP(0,1);
      const f32x2 ar = (up2(v_ar) * dt) * 1.44269504f, ai = (up2(v_ai) * dt) * 0.15915494f;
      const f32x2 br = up2(v_br) * dt, bi = up2(v_bi) * dt, cr = up2(v_cr), ci = up2(v_ci);
      const f32x2 cbr = cr * br - ci * bi, cbi = -(cr * bi + ci * br);
      float mx = fmaxf(fmaxf(fabsf(cbr[0]), fabsf(cbi[0])), fmaxf(fabsf(cbr[1]), fabsf(cbi[1])));
      mx = fmaxf(mx, dpp_get<0x128>(mx)); mx = fmaxf(mx, dpp_get<0x124>(mx)); mx = fmaxf(mx, dpp_get<0x122>(mx)); mx = fmaxf(mx, dpp_get<0x121>(mx));
      auto rl = [&](int l) { return asf(__builtin_amdgcn_readlane((int)asu(mx), l)); };
      sc = fmaxf(fmaxf(rl(0), rl(16)), fmaxf(rl(32), rl(48)));
      sc = fmaxf(sc, fabsf(p_dk) * (1.0f / 256));
      const float isc = sc > 0.f ? 1.0f / sc : 0.f;
      {
        struct C2 { f32x2 a, b; };
        char* tb = sk + lane * 8;
        auto put = [&](int row, C2 v) { *(u32x2*)(tb + row * KT) = u32x2{pkh(v.a), pkh(v.b)}; };
        auto mul = [&](C2 v, C2 u, C2 up) { return C2{cmul(v.a, u.a, up.a), cmul(v.b, u.b, up.b)}; };
        auto prp = [&](C2 v) { return C2{perp(v.a), perp(v.b)}; };
        auto ex = [&](float l) { return C2{cexp(ar[0], ai[0], l), cexp(ar[1], ai[1], l)}; };
        const C2 w1 = ex(1.f), w128 = ex(128.f);
        C2 w8 = ex(8.f); w8.a[1] = -w8.a[1]; w8.b[1] = -w8.b[1];
        const C2 w1p = prp(w1), w8p = prp(w8), w128p = prp(w128);
        C2 y = {f32x2{cbr[0], cbi[0]} * isc, f32x2{cbr[1], cbi[1]} * isc};
        C2 E[8]; E[0] = C2{f32x2{1.f, 0.f}, f32x2{1.f, 0.f}}; E[1] = w1;
#pragma unroll
        for (int e = 2; e < 8; ++e) E[e] = mul(E[e - 1], w1, w1p);
#pragma unroll
        for (int e = 0; e < 8; ++e) { put(e, E[e]); put(8 + e, e ? mul(E[e], w128, w128p) : w128); put(41 + e, y); y = mul(y, w8, w8p); }
        const C2 e2p = prp(E[2]), e4p = prp(E[4]), e6p = prp(E[6]);
        C2 H = w128;
#pragma unroll
        for (int a = 2; a <= 16; ++a) {
          H = mul(H, w128, w128p);
          if (a < 4) { put(8 + 4 * a, H); put(9 + 4 * a, mul(H, E[2], e2p)); put(10 + 4 * a, mul(H, E[4], e4p)); put(11 + 4 * a, mul(H, E[6], e6p)); }
          else if (a < 8) { put(16 + 2 * a, H); put(17 + 2 * a, mul(H, E[4], e4p)); }
          else put(24 + a, H);
          if (a < 10) { put(47 + a, y); if (a < 9) y = mul(y, w8, w8p); }
        }
      }
      asm volatile("" ::: "memory");
      STAMP(0,2);
      {
        f32x4 kk[3] = {};
        const char* pa = sk + li * KT + q * 16;
#pragma unroll
        for (int s = 0; s < 8; ++s) {
          u32x4 fb = *(const u32x4*)(pa + 41 * KT + s * 64);
#pragma unroll
          for (int t = 0; t < 3; ++t) kk[t] = mfma_h(*(const u32x4*)(pa + 16 * t * KT + s * 64), fb, kk[t]);
        }
        if (lane == 0) kk[0][0] += p_dk * isc;
        char* kd = sm + C_KD + ch * 1536;
        *(u32x2*)(kd + (128 * (q >> 1) + 4 * (q & 1) + 8 * li) * 2) = u32x2{pkh(lo2(kk[0])), pkh(hi2(kk[0]))};
        const int i1 = q < 2 ? 256 + 64 * q + 4 * li : 384 + 64 * (q - 2) + 2 * li;
        *(unsigned*)(kd + i1 * 2) = pkh(lo2(kk[1])); *(unsigned*)(kd + (i1 + (q < 2 ? 2 : 32)) * 2) = pkh(hi2(kk[1]));
#pragma unroll
        for (int r = 0; r < 4; ++r) *(_Float16*)(kd + (512 + 16 * (4 * q + r) + li) * 2) = (_Float16)kk[2][r];
      }
      asm volatile("" ::: "memory");
      u32x4 c0[4];
      {
        const unsigned short* kd = (const unsigned short*)(sm + C_KD + ch * 1536);
#pragma unroll
        for (int rr = 0; rr < 2; ++rr) {
          const int k = 255 - 64 * rr - lane;
          const unsigned a = kd[384 + k], b = kd[385 + k], aa = a | (a << 16);
          c0[rr] = u32x4{b | (b << 16), b | (((k & 1) ? b : a) << 16), aa, aa};
        }
        { const int k = 127 - lane;
          const unsigned ab = *(const unsigned*)(kd + 256 + 2 * k), cc = kd[258 + 2 * k];
          c0[2] = u32x4{cc | (cc << 16), __builtin_amdgcn_perm(ab, ab, 0x03020302), __builtin_amdgcn_perm(ab, ab, 0x01000302), __builtin_amdgcn_perm(ab, ab, 0x01000100)}; }
        { const int k = 63 - lane;
          const u32x4 v = *(const u32x4*)(kd + 8 * (k & 31));
          const u32x2 u = *(const u32x2*)(kd + 128 + 4 * k); const unsigned e = kd[132 + 4 * k];
          c0[3] = lane < 32 ? u32x4{__builtin_amdgcn_perm(u[1], e, 0x07060100), __builtin_amdgcn_perm(u[1], u[1], 0x01000100), __builtin_amdgcn_perm(u[0], u[1], 0x07060100), __builtin_amdgcn_perm(u[0], u[0], 0x01000100)}
                            : u32x4{__builtin_amdgcn_alignbit(v[3], v[3], 16), __builtin_amdgcn_alignbit(v[2], v[2], 16), __builtin_amdgcn_alignbit(v[1], v[1], 16), __builtin_amdgcn_alignbit(v[0], v[0], 16)}; }
#pragma unroll
        for (int rr = 0; rr < 4; ++rr) *(u32x4*)(sk + (64 * rr + lane) * 16) = c0[rr];
        if (lane < 8) *(u32x4*)(sk + (lane * 260 + 256) * 16) = u32x4{0, 0, 0, 0};
      }
      asm volatile("" ::: "memory");
      STAMP(0,3);
      u32x4 c1[4];
#pragma unroll
      for (int rr = 0; rr < 4; ++rr) c1[rr] = *(const u32x4*)(sk + (64 * rr + lane) * 16 + 16);
#pragma unroll
      for (int rr = 0; rr < 4; ++rr) {
        char* p = sk + (64 * rr + lane) * 16;
        unsigned e[8] = {c0[rr][0], c0[rr][1], c0[rr][2], c0[rr][3], c1[rr][0], c1[rr][1], c1[rr][2], c1[rr][3]}, o[7];
#pragma unroll
        for (int i = 0; i < 7; ++i) o[i] = __builtin_amdgcn_alignbit(e[i + 1], e[i], 16);
#pragma unroll
        for (int s = 0; s < 4; ++s) {
          if (s) *(u32x4*)(p + (2 * s) * 260 * 16) = u32x4{e[s], e[s + 1], e[s + 2], e[s + 3]};
          *(u32x4*)(p + (2 * s + 1) * 260 * 16) = u32x4{o[s], o[s + 1], o[s + 2], o[s + 3]};
        }
      }
      STAMP(0,4);
    } else {
      char* s0 = sm + C_U;
      __builtin_amdgcn_s_setprio(2);
      u32x2 xr[9];
      const char* src = x + (size_t)(16 * min(cg, 127) + 4 * ch + q) * 1152 + li * 8;
#pragma unroll
      for (int k = 0; k < 9; ++k) xr[k] = *(const u32x2*)(src + k * 128);
      bar();
      tag = mk_tag();
      if (cg < 128) {
        asm volatile("" :: "v"(xr[8])); STAMPW(1,0,4);
#pragma unroll
        for (int k = 0; k < 9; ++k) st8(R, li * 32 + q * 8 + k * 512, O_XB + (4 * cg + ch) * 4608, xr[k]);
        float a[4] = {0, 0, 0, 0};
#pragma unroll
        for (int k = 0; k < 9; ++k)
#pragma unroll
          for (int e = 0; e < 2; ++e) { a[e] = dot2(xr[k][e], 0x3F803F80u, a[e]); a[2 + e] = dot2(xr[k][e], xr[k][e], a[2 + e]); }
        const float s = row16_sum(a[0] + a[1]), ss = row16_sum(a[2] + a[3]);
        float mean = s * (1.0f / D), mb, rinv;
        ln_scalars(mean, fmaxf(ss * (1.0f / D) - mean * mean, 0.f), mb, rinv);
        const f32x2 r2 = {rinv, rinv}, b2 = {-mb * rinv, -mb * rinv};
        char* p = s0 + (4 * ch + q) * TP + li * 8;
#pragma unroll
        for (int k = 0; k < 9; ++k)
          *(u32x2*)(p + k * 128) = u32x2{pkh(__builtin_elementwise_fma(up2(xr[k][0]), r2, b2)), pkh(__builtin_elementwise_fma(up2(xr[k][1]), r2, b2))};
        asm volatile("" ::: "memory");
        unsigned* cnt = (unsigned*)(sm + X_CNT) + 3;
        if (lane == 0) __hip_atomic_fetch_add(cnt, 1u, __ATOMIC_RELAXED, __HIP_MEMORY_SCOPE_WORKGROUP);
#pragma unroll 1
        for (int spin = 0; spin < (SPIN << 4) && __builtin_amdgcn_readfirstlane(__hip_atomic_load(cnt, __ATOMIC_RELAXED, __HIP_MEMORY_SCOPE_WORKGROUP)) < 4; ++spin) {}
        asm volatile("" ::: "memory");
        STAMPW(1,1,4);
        const char* trb = s0 + (8 * (q & 1) + (li >> 2)) * TP + (q >> 1) * 32 + (li & 3) * 8 + ch * 64;
        const unsigned uo = (16 * (q >> 1) + li) * 32 + (q & 1) * 16 + O_U + cg * 18432 + ch * 1024;
#pragma unroll
        for (int k = 0; k < 5; ++k)
          if (k < 4 || ch < 2) {
            u32x2 lo = tr_read(trb + k * 256), hi = tr_read(trb + 4 * TP + k * 256);
            st16(R, uo, k * 4096, u32x4{lo[0], lo[1], hi[0], hi[1]});
          }
        STAMPW(1,2,4);
        arrive(0, 4, O_F1 + cg * 8, 1024, NREP);
        STAMPW(1,3,4);
      }
      __builtin_amdgcn_s_setprio(0);
      if (cg < 136 && w >= 6) st16(R, (tid - 384) * 16, O_ST + cg * 2048, u32x4{SENT, SENT, SENT, SENT});
      if (w == 4) {
        wait_flags<1>(R, O_F1 + rep * 1024, 128, tag, lane);
        STAMPW(5,0,4);
        const unsigned v0 = (lane >> 1) * 18432 + (lane & 1) * 16, dv = 32 * 18432 - 1024;
#pragma unroll
        for (int i = 0; i < 4; ++i) glds4<true>(lds_addr(sm) + C_U + i * CV_U + 480, ws + O_U + (4 * cg + i) * 32, v0, v0 + dv, v0 + 2 * dv, v0 + 3 * dv);
#pragma unroll
        for (int i = 0; i < 4; ++i) if (lane < 30) *(u32x4*)(sm + C_U + i * CV_U + lane * 16) = u32x4{0, 0, 0, 0};
        wait_vm<0>();
      }
    }
    bar();
    STAMP(0,5);
    u32x4 xv[4];
    if (w < 4) {
      f32x4 acc[8] = {};
      const char* pa = sk + ((7 - (li & 7)) * 260 + 253 - (li >> 3) + q) * 16 - 4032;
      const char* pb = su + li * 32 + q * 16;
      struct CF { u32x4 a[8], b[8]; } A, B;
      auto ldc = [&](CF& f, int a0) {
#pragma unroll
        for (int m = 0; m < 8; ++m) { f.a[m] = *(const u32x4*)(pa + 4032 - 64 * a0 - 512 * m); f.b[m] = *(const u32x4*)(pb + 448 - 64 * a0 + 512 * m); }
      };
      auto mmc = [&](const CF& f) {
#pragma unroll
        for (int m = 0; m < 8; ++m)
#pragma unroll
          for (int t = m; t < 8; ++t) acc[t] = mfma_h(f.a[m], f.b[t - m], acc[t]);
#pragma unroll
        for (int i = 0; i < 16; ++i) { __builtin_amdgcn_sched_group_barrier(0x008, 2, 0); __builtin_amdgcn_sched_group_barrier(0x100, 1, 0); }
        __builtin_amdgcn_sched_barrier(0);
      };
      ldc(A, 0);
      __builtin_amdgcn_sched_barrier(0);
#pragma unroll
      for (int a0 = 0; a0 < 8; a0 += 2) { ldc(B, a0 + 1); mmc(A); if (a0 < 6) ldc(A, a0 + 2); mmc(B); }
      STAMP(0,6);
#pragma unroll
      for (int t = 0; t < 8; ++t) *(u32x2*)(sk + t * 512 + li * 32 + q * 8) = u32x2{pk2(lo2(acc[t]) * sc), pk2(hi2(acc[t]) * sc)};
    } else {
#pragma unroll
      for (int r = 0; r < 4; ++r) {
        const int T = ((tid & 255) >> 3) + 32 * r, ip = tid & 7;
        xv[r] = ld16(R, (8 * (T >> 1) + 2 * (ip >> 1) + (T & 1)) * 4608 + (ip & 1) * 16, O_XB + cg * 32);
      }
      STAMPW(2,0,4);
    }
    bar();
    if (w >= 4) {
#pragma unroll
      for (int r = 0; r < 4; ++r) {
        const int T = ((tid & 255) >> 3) + 32 * r, ip = tid & 7;
        const char* p = sm + (32 * (T >> 1) + 8 * (ip >> 1) + 4 * (T & 1) + 2 * (ip & 1)) * 2;
        const unsigned c0 = *(const unsigned*)p, c1 = *(const unsigned*)(p + CV_K), c2 = *(const unsigned*)(p + 2 * CV_K), c3 = *(const unsigned*)(p + 3 * CV_K);
        u32x4 v = {pk2(up2(xv[r][0]) + f32x2{bf_lo(c0), bf_lo(c1)}), pk2(up2(xv[r][1]) + f32x2{bf_lo(c2), bf_lo(c3)}),
                   pk2(up2(xv[r][2]) + f32x2{bf_hi(c0), bf_hi(c1)}), pk2(up2(xv[r][3]) + f32x2{bf_hi(c2), bf_hi(c3)})};
        st16(R, (T >> 3) * (18 * 8192) + (T & 7) * 1024 + ip * 16, O_HF + (cg >> 3) * 8192 + (cg & 1) * 512 + ((cg >> 1) & 3) * 128, v);
      }
      STAMPW(2,1,4);
      arrive(1, 4, O_F2 + cg * 8, F2S, NREP);
      STAMPW(2,2,4);
    }
  } else {
    const int tj = (ii - 18) * 8 + xc;
    const int kc = tj / 36, cb = tj % 36;
    for (int i = 0; i < WDELAY; ++i) __builtin_amdgcn_s_sleep(16);
    char* s0 = sm + 65536;
    const int G = max(tj - 40, 0), par = G & 1;
    {
      const int row = tid >> 4, uu = G / 9, idx = G % 9, hh = row >> 4;
      const int panel = idx < 4 ? 2 * uu : (idx < 8 ? 2 * uu + 1 : 2 * uu + hh), tile = idx < 8 ? 2 * (idx & 3) + hh : 8;
      const char* src = w_in + (size_t)(192 * min(kc, 2) + row) * 9216 + cb * 256 + li * 16;
      const char* wsrc = w_out + (size_t)(144 * panel + 16 * tile + (row & 15)) * 1152 + li * 8;
      u32x4 v[6]; u32x2 vo[9];
#pragma unroll
      for (int k = 0; k < 6; ++k) v[k] = *(const u32x4*)(src + (size_t)k * 32 * 9216);
      if (tj >= 40) {
#pragma unroll
        for (int k = 0; k < 9; ++k) vo[k] = *(const u32x2*)(wsrc + k * 128);
      }
#pragma unroll
      for (int k = 0; k < 6; ++k) *(u32x4*)(sm + (row + 32 * k) * 272 + li * 16) = v[k];
      if (tj >= 40) {
#pragma unroll
        for (int k = 0; k < 9; ++k) *(u32x2*)(s0 + row * TP + li * 8 + k * 128) = vo[k];
      }
    }
    tag = mk_tag();
    if (w == 0) {
      wait_flags<1>(R, O_F1 + rep * 1024, 128, tag, lane);
      for (int i = 0; i < SDELAY; ++i) __builtin_amdgcn_s_sleep(16);
    }
    bar();
    STAMP(4,0);
    if (tj < 108) {
      const int ct = 8 * cb + w;
      const int hf = ct < 144 ? 1 : 0, cl = ct < 144 ? ct : ct - 144, fp = cl / 9, n = cl % 9;
      const char* trb = sm + (8 * q + (li >> 2)) * 272 + w * 32 + (li & 3) * 8;
      const unsigned dst = lane * 16 + O_WI + (((fp * 2 + hf) * 18 + 6 * kc) * 9 + n) * 1024;
      float s = 0.f;
#pragma unroll
      for (int g = 0; g < 6; ++g) {
        u32x2 lo = tr_read(trb + g * 32 * 272), hi = tr_read(trb + g * 32 * 272 + 4 * 272);
        st16(R, dst, g * 9216, u32x4{lo[0], lo[1], hi[0], hi[1]});
        s = dot2(lo[0], 0x3F803F80u, s); s = dot2(lo[1], 0x3F803F80u, s); s = dot2(hi[0], 0x3F803F80u, s); s = dot2(hi[1], 0x3F803F80u, s);
      }
      s = q_sum(s);
      if (q == 0) st4(R, li * 4, O_CS + kc * F * 4 + ct * 64, asu(s));
    }
    if (tj >= 40) {
      const char* trb = s0 + (4 * q + (li >> 2)) * TP + (li & 3) * 8;
      const unsigned dst = lane * 16 + O_WO + (w * 36 + (G >> 1)) * 9216;
#pragma unroll
      for (int n = 0; n < 5; ++n) {
        const int c0 = 72 * w + 16 * n - (n == 4 ? 8 * par : 0);
        u32x2 lo = tr_read(trb + c0 * 2), hi = tr_read(trb + 16 * TP + c0 * 2);
        if (n < 4) st16(R, dst, (4 * par + n) * 1024, u32x4{lo[0], lo[1], hi[0], hi[1]});
        else if ((li >> 3) == par) st16(R, dst, 8192, u32x4{lo[0], lo[1], hi[0], hi[1]});
      }
    }
    STAMP(4,1);
    arrive(0, 8, O_F2 + (144 + tj) * 8, F2S, NREP);
    STAMP(4,2);
  }
  const int tp = (xc & 1) * 8 + (ii >> 2), fp = (xc >> 1) * 4 + (ii & 3);
  const int t64 = 4 * xc + (ii >> 3), j3 = ii & 7;
  if (w == 0) { wait_flags<1>(R, O_F2 + rep * F2S + 144 * 8, 112, tag, lane); STAMPW(6,0,0); }
  bar();
  if (w >= 4) {
    const int lw = w - 4;
    const unsigned l16 = lane * 16, base = lds_addr(sm);
    const int ph = ((ii >> 2) + ii) & 3, ph3 = ((ii >> 3) + ii) & 3;
    auto k9 = [&](int t) { return (t & 3) == ph ? (t + 4 < 9 ? t + 4 : ph) : t; };
    auto k32 = [&](int t) { return (t & 3) == ph ? ((t + 4) & 31) : t; };
    auto k18 = [&](int t) { return (t & 3) == ph3 ? (t + 4 < 18 ? t + 4 : ph3) : t; };
    if (lw < 2) {
      constexpr int NP = 8;
      unsigned vo[8];
#pragma unroll
      for (int j = 0; j < 8; ++j) {
        int t = 64 * lw + 32 * (j >> 2) + 4 * ((j >> 1) & 1) + 16 * (j & 1) + 8 * (lane >> 5) + ((lane >> 3) & 3);
        vo[j] = t * 4096 + (((lane & 7) ^ ((lane >> 3) & 7)) * 16) + 3072 - (j & 3) * 1024;
      }
      const char* wsrc = w_sp + (size_t)tp * 128 * 4096 - 3072;
      const char* hsrc = ws + O_HF + tp * 18 * 8192 + lw * 8192;
      const char* psrc = ws + O_PF + t64 * 72 * 4096 + lw * 8192;
      const unsigned dst = base + lw * 8192;
      if (lw == 0) wait_flags<2>(R, O_F2 + rep * F2S, 144, tag, lane);
      STAMPW(5,1,4);
      bar();
#pragma unroll 1
      for (int t = 0; t < 4; ++t) ld_lin<NP>(dst + t * STG, hsrc + k9(t) * 16384, l16);
      wait_vm<3 * NP>(); bar();
#pragma unroll 1
      for (int t = 4; t < 18; ++t) { wait_vm<2 * NP>(); bar(); ld_lin<NP>(dst + (t & 3) * STG, hsrc + k9(t < 9 ? t : t - 9) * 16384, l16); }
#pragma unroll 1
      for (int t = 18; t < 50; ++t) {
        wait_vm<2 * NP>(); bar();
        const unsigned dd = dst + (t & 3) * STG; const char* sp = wsrc + k32(t - 18) * 128;
        glds4<false>(dd, sp, vo[0], vo[1], vo[2], vo[3]); glds4<false>(dd + 4096, sp, vo[4], vo[5], vo[6], vo[7]);
      }
      wait_vm<2 * NP>(); bar(); wait_vm<NP>(); bar(); wait_vm<0>(); bar();
      wait_flags<1>(R, O_PFL + (t64 >> 1) * 2048 + ((t64 & 1) * 8 + j3) * 128, 16, tag, lane);
      STAMPW(5,2,4);
#pragma unroll 1
      for (int t = 50; t < 68; ++t) {
        if (t >= 53) { wait_vm<2 * NP>(); bar(); }
        ld_lin<NP>(dst + (t & 3) * STG, psrc + k18(t - 50) * 16384, l16);
      }
      wait_vm<2 * NP>(); bar(); wait_vm<NP>(); bar(); wait_vm<0>(); bar();
    } else {
      constexpr int NP = 9;
      const char* wi = ws + O_WI + (fp * 2) * 18 * 9216 + (lw - 2) * 9216;
      const char* zs = ws + O_ZF + fp * 64 * 9216 + (lw - 2) * 9216;
      const char* wo = ws + O_WO + j3 * 36 * 9216 + (lw - 2) * 9216;
      const unsigned dst = base + 16384 + (lw - 2) * 9216;
#pragma unroll 1
      for (int t = 0; t < 4; ++t) ld_lin<NP>(dst + t * STG, wi + k9(t) * 18432, l16);
      bar();
      wait_vm<3 * NP>(); bar();
      if (lw == 3) {
        f32x2 a = *(const f32x2*)(sm + X_LN + lane * 8) + *(const f32x2*)(sm + X_LN + 512 + lane * 8);
        a[0] = q_sum(dpp_add<0x128>(a[0])); a[1] = q_sum(dpp_add<0x128>(a[1]));
        float mean = a[0] * (1.0f / D), mb, ri;
        ln_scalars(mean, fmaxf(a[1] * (1.0f / D) - mean * mean, 0.f), mb, ri);
        if (lane < 8) st8(R, lane * 8, O_HS + tp * 1024 + fp * 64, u32x2{asu(ri) == SENT ? 0x7FC00000u : asu(ri), asu(-mb)});
      }
#pragma unroll 1
      for (int t = 4; t < 18; ++t) { wait_vm<2 * NP>(); bar(); ld_lin<NP>(dst + (t & 3) * STG, wi + (t < 9 ? k9(t) : 9 + k9(t - 9)) * 18432, l16); }
      wait_vm<2 * NP>(); bar(); wait_vm<NP>(); bar(); wait_vm<0>(); bar();
      wait_flags<1>(R, O_ZFL + fp * 2048 + tp * 128, 16, tag, lane);
      STAMPW(6,1,6);
#pragma unroll 1
      for (int t = 18; t < 68; ++t) {
        if (t >= 21) { wait_vm<2 * NP>(); bar(); }
        ld_lin<NP>(dst + (t & 3) * STG, t < 50 ? zs + k32(t - 18) * 18432 : wo + k18(t - 50) * 18432, l16);
      }
      wait_vm<2 * NP>(); bar(); wait_vm<NP>(); bar(); wait_vm<0>(); bar();
    }
    return;
  }
  Ctx c;
  c.sm = sm;
#pragma unroll
  for (int i = 0; i < 4; ++i) { c.am0[i] = (2 * w + (i >> 1)) * 1024 + (i & 1) * 512 + lane * 8; asm volatile("" : "+v"(c.am0[i])); }
  c.am1 = 2 * w * 2048 + li * 128 + ((q ^ (lane & 7)) * 16);
  c.an = 16384 + lane * 16;
  const int prow = 8 * (li >> 2) + (li & 3);
  if (tid < 72) {
    const unsigned o = tid < 36 ? FH * 4 + tid * 16 : (tid - 36) * 16;
    const u32x4 c0 = ld16(R, o, O_CS + fp * 576), c1 = ld16(R, o, O_CS + F * 4 + fp * 576), c2 = ld16(R, o, O_CS + F * 8 + fp * 576);
    *(f32x4*)(sm + X_CS + tid * 16) = (__builtin_bit_cast(f32x4, c0) + __builtin_bit_cast(f32x4, c1)) + __builtin_bit_cast(f32x4, c2);
  }
  bar();
  char* pvb = sm + X_PV + w * 4096;
  {
    u32x4 hv[3];
#pragma unroll
    for (int k = 0; k < 3; ++k)
      hv[k] = ld16(R, (lane >> 5) * 8192 + ((lane >> 2) & 7) * 128 + (lane & 3) * 16, O_HF + (tp * 18 * 8 + (fp >> 1)) * 1024 + (fp & 1) * 64 + min(w + 4 * k, 8) * 16384);
    if (w) hv[2] = u32x4{0, 0, 0, 0};
    f32x4 a = {0, 0, 0, 0};
#pragma unroll
    for (int k = 0; k < 3; ++k)
#pragma unroll
      for (int e = 0; e < 4; ++e) { a[e & 2] = dot2(hv[k][e], 0x3F803F80u, a[e & 2]); a[(e & 2) + 1] = dot2(hv[k][e], hv[k][e], a[(e & 2) + 1]); }
#pragma unroll
    for (int e = 0; e < 4; ++e) a[e] = dpp_add<0x124>(dpp_add<0x128>(a[e]));
    if (li < 4) *(f32x4*)(sm + X_LN + (w * 4 + q) * 64 + li * 16) = a;
  }
  f32x4 acc[2][9];
  Fr X, Y;
  bar();
  STAMP(3,0);
  ldf<0>(X, c, 0, 0);
  seg<0, 0, true, true>(acc, X, Y, c, 0, 1);
#pragma unroll 1
  for (int t = 0; t < 8; ++t) {
    bar();
    seg<0, 0, true>(acc, Y, X, c, t + 1, 0);
    seg<0, 0, true>(acc, X, Y, c, t + 1, 1);
  }
  u32x2 ht[2]; u32x4 hc[4];
  auto ld_hs = [&]() {
#pragma unroll
    for (int m = 0; m < 2; ++m) {
      ht[m] = ld8(R, li * 8 + m * 128, O_HS + tp * 1024 + w * 256);
      hc[2 * m] = ld16(R, q * 32 + m * 128, O_HS + tp * 1024 + w * 256); hc[2 * m + 1] = ld16(R, q * 32 + 16 + m * 128, O_HS + tp * 1024 + w * 256);
    }
  };
  ld_hs();
  bar();
  seg<0, 0, true>(acc, Y, X, c, 9, 0);
  STAMP(3,1);
#pragma unroll 1
  for (int spin = 0; spin < SPIN; ++spin) {
    const unsigned mx = max(max(umax4(hc[0]), umax4(hc[1])), max(umax4(hc[2]), umax4(hc[3])));
    if (__all(max(mx, umax4(u32x4{ht[0][0], ht[0][1], ht[1][0], ht[1][1]})) != SENT)) break;
    __builtin_amdgcn_s_sleep(1);
    asm volatile("" ::: "memory");
    ld_hs();
  }
  const float lrs[2] = {asf(ht[0][0]), asf(ht[1][0])}, lnm[2] = {asf(ht[0][1]), asf(ht[1][1])};
  u32x4 zb[9];
  f32x2 rs[4];
  {
    float cs[9];
#pragma unroll
    for (int n = 0; n < 9; ++n) cs[n] = *(const float*)(sm + X_CS + (16 * n + li) * 4);
    f32x2 nm[4], k1[4], k0[4], S1[4] = {}, S2[4] = {};
#pragma unroll
    for (int jp = 0; jp < 4; ++jp) {
      rs[jp] = f32x2{asf(hc[jp][0]), asf(hc[jp][2])}; nm[jp] = f32x2{asf(hc[jp][1]), asf(hc[jp][3])};
      k0[jp] = rs[jp] * GC0; k1[jp] = rs[jp] * rs[jp] * rs[jp] * GC1;
    }
#pragma unroll
    for (int n = 0; n < 9; ++n) {
#pragma unroll
      for (int jp = 0; jp < 4; ++jp) {
        f32x2 a = (jp & 1) ? hi2(acc[jp >> 1][n]) : lo2(acc[jp >> 1][n]);
        f32x2 z = gelu2(__builtin_elementwise_fma(nm[jp], f32x2{cs[n], cs[n]}, a), k1[jp], k0[jp]);
        zb[n][jp] = pk2(z);
        S1[jp] += z; S2[jp] = __builtin_elementwise_fma(z, z, S2[jp]);
      }
      asm volatile("" : "+v"(zb[n]));
    }
#pragma unroll
    for (int jp = 0; jp < 4; ++jp) { S1[jp] *= rs[jp]; S2[jp] *= rs[jp] * rs[jp]; }
    float v[16];
#pragma unroll
    for (int j = 0; j < 8; ++j) { v[2 * j] = S1[j >> 1][j & 1]; v[2 * j + 1] = S2[j >> 1][j & 1]; }
#pragma unroll
    for (int i = 0; i < 8; ++i) { bool h = li & 8; v[i] = (h ? v[i + 8] : v[i]) + dpp_get<0x128>(h ? v[i] : v[i + 8]); }
#pragma unroll
    for (int i = 0; i < 4; ++i) { bool h = li & 4; v[i] = (h ? v[i + 4] : v[i]) + dpp_get<0x141>(h ? v[i] : v[i + 4]); }
#pragma unroll
    for (int i = 0; i < 2; ++i) { bool h = li & 2; v[i] = (h ? v[i + 2] : v[i]) + dpp_get<0x4E>(h ? v[i] : v[i + 2]); }
    { bool h = li & 1; v[0] = (h ? v[1] : v[0]) + dpp_get<0xB1>(h ? v[0] : v[1]); }
    unsigned tot = asu(v[0]);
    st4(R, lane * 4, O_ST + ((tp * 4 + w) * 16 + fp) * 256, tot == SENT ? 0x7FC00000u : tot);
  }
  STAMP(3,2);
  const char* sts = ws + O_ST + (tp * 4 + w) * 4096;
  seg<0, 1, true, true>(acc, X, Y, c, 9, 1);
#pragma unroll 1
  for (int t = 9; t < TCHK; ++t) {
    if (t == TPOLL) glds4<true>(lds_addr(pvb), sts, lane * 16, lane * 16, lane * 16, lane * 16);
    bar();
    seg<0, 1, true>(acc, Y, X, c, t + 1, 0);
    seg<0, 1, true>(acc, X, Y, c, t + 1, 1);
  }
  STAMP(3,3);
  u32x4 pv[4];
#pragma unroll 1
  for (int spin = 0; spin < SPIN; ++spin) {
    wait_vm<0>();
#pragma unroll
    for (int k = 0; k < 4; ++k) pv[k] = *(const u32x4*)(pvb + k * 1024 + lane * 16);
    if (__all(max(max(umax4(pv[0]), umax4(pv[1])), max(umax4(pv[2]), umax4(pv[3]))) != SENT)) break;
    glds4<true>(lds_addr(pvb), sts, lane * 16, lane * 16, lane * 16, lane * 16);
  }
  STAMP(3,4);
  {
    float t4[4];
#pragma unroll
    for (int e = 0; e < 4; ++e) t4[e] = q_sum((asf(pv[0][e]) + asf(pv[1][e])) + (asf(pv[2][e]) + asf(pv[3][e])));
    f32x2 o0, o1;
#pragma unroll
    for (int r = 0; r < 2; ++r) {
      float mean = t4[2 * r] * (1.0f / FH), var = fmaxf(t4[2 * r + 1] * (1.0f / FH) - mean * mean, 0.f), mb, ri;
      ln_scalars(mean, var, mb, ri);
      o0[r] = ri; o1[r] = -mb * ri;
    }
    char* ln = sm + X_LN + w * 256;
    if (lane < 16) { *(f32x2*)(ln + li * 8) = o0; *(f32x2*)(ln + 128 + li * 8) = o1; }
    asm volatile("" ::: "memory");
    f32x4 ri[2], rb[2];
#pragma unroll
    for (int m = 0; m < 2; ++m) { ri[m] = *(const f32x4*)(ln + (8 * q + 4 * m) * 4); rb[m] = *(const f32x4*)(ln + 128 + (8 * q + 4 * m) * 4); }
#pragma unroll
    for (int jp = 0; jp < 4; ++jp) rs[jp] *= (jp & 1) ? hi2(ri[jp >> 1]) : lo2(ri[jp >> 1]);
    const unsigned zo = lane * 16 + O_ZF + (fp * 64 + 4 * tp + w) * 9216;
#pragma unroll
    for (int n = 0; n < 9; ++n) {
      u32x4 o4;
#pragma unroll
      for (int jp = 0; jp < 4; ++jp)
        o4[jp] = pk2(__builtin_elementwise_fma(up2(zb[n][jp]), rs[jp], (jp & 1) ? hi2(rb[jp >> 1]) : lo2(rb[jp >> 1])));
      st16(R, zo, n * 1024, o4);
    }
  }
  STAMP(3,5);
#pragma unroll 1
  for (int t = TCHK; t < 17; ++t) {
    if (t == TFLAG) wait_vm<0>();
    bar();
    if (t == TFLAG && tid < 16) st8(R, tid * 128, O_ZFL + fp * 2048 + tp * 8, tag);
    seg<0, 1, true>(acc, Y, X, c, t + 1, 0);
    seg<0, 1, true>(acc, X, Y, c, t + 1, 1);
  }
  seg<0, 1, false>(acc, Y, X, c, 17, 1);
  STAMP(3,6);
  u32x2 z1[2][9];
  float bs[2];
#pragma unroll
  for (int m = 0; m < 2; ++m) bs[m] = ldbf(b_sp, 128 * tp + 32 * w + prow + 4 * m) * lrs[m];
  {
    f32x4 cs[9];
#pragma unroll
    for (int n = 0; n < 9; ++n) cs[n] = *(const f32x4*)(sm + X_CS + (144 + 16 * n + 4 * q) * 4);
#pragma unroll
    for (int n = 0; n < 9; ++n)
#pragma unroll
      for (int m = 0; m < 2; ++m) {
        const float a1 = lrs[m] * lrs[m] * lrs[m] * GC1, a0 = lrs[m] * GC0;
        const f32x2 k1 = {a1, a1}, k0 = {a0, a0}, nm = {lnm[m], lnm[m]};
        z1[m][n][0] = pk2(gelu2(__builtin_elementwise_fma(nm, lo2(cs[n]), lo2(acc[m][n])), k1, k0));
        z1[m][n][1] = pk2(gelu2(__builtin_elementwise_fma(nm, hi2(cs[n]), hi2(acc[m][n])), k1, k0));
        asm volatile("" : "+v"(z1[m][n]));
      }
  }
  STAMP(3,7);
  bar();
  STAMP(3,8);
  ldf<1>(X, c, 18, 0);
  seg<1, 1, true, true>(acc, X, Y, c, 18, 1);
#pragma unroll 1
  for (int t = 18; t < 49; ++t) {
    bar();
    seg<1, 1, true>(acc, Y, X, c, t + 1, 0);
    seg<1, 1, true>(acc, X, Y, c, t + 1, 1);
  }
  seg<1, 1, false>(acc, Y, X, c, 49, 1);
  STAMP(3,9);
  {
    const unsigned pf = O_PF + (((2 * tp + (w >> 1)) * 72 + 9 * (fp >> 1)) * 4 + 2 * (w & 1)) * 1024;
    const unsigned po = lane * 16 + pf + (fp & 1) * 16384;
#pragma unroll
    for (int m = 0; m < 2; ++m) {
      const f32x2 r2 = {lrs[m], lrs[m]}, b2 = {bs[m], bs[m]};
      auto pp = [&](int n) { return u32x2{pk2(__builtin_elementwise_fma(lo2(acc[m][n]), r2, b2) * up2(z1[m][n][0])), pk2(__builtin_elementwise_fma(hi2(acc[m][n]), r2, b2) * up2(z1[m][n][1]))}; };
#pragma unroll
      for (int a = 0; a < 4; ++a) {
        u32x2 p0 = pp(2 * a), p1 = pp(2 * a + 1);
        st16(R, po, a * 4096 + m * 1024, u32x4{p0[0], p0[1], p1[0], p1[1]});
      }
      st8(R, lane * 16, pf + 8 * 4096 + m * 1024 + (fp & 1) * 8, pp(8));
    }
  }
  STAMP(3,10);
  arrive(2, 4, O_PFL + tp * 2048 + fp * 8, 128, 16);
  const int th = w & 1, kq = w >> 1, T = 4 * t64 + 2 * th + kq;
  const int lane5 = fresh_lane();
#define lane lane5
#define li (lane5 & 15)
#define q (lane5 >> 4)
#define prow (8 * (li >> 2) + (li & 3))
  u32x2 hv[5];
#pragma unroll
  for (int n = 0; n < 5; ++n) {
    const int dd = 72 * j3 + 16 * n + 4 * (n == 4 ? (q & 1) : q);
    hv[n] = ld8(R, (dd >> 5) * 8192 + ((dd >> 2) & 1) * 512 + (((dd >> 3) & 3) * 16 + li) * 8, O_HF + ((T >> 3) * 18 * 8 + (T & 7)) * 1024);
  }
  struct F3 { u32x4 p[2][2], n[9]; };
  f32x4 a3[2][6] = {};
  F3 U, V;
  const int ap = (8 * kq + 2 * th) * 1024 + lane * 16, aw = 16384 + kq * 9216 + lane * 16;
  auto ld3 = [&](F3& f, int t) {
    const char* slot = sm + (t & 3) * STG;
#pragma unroll
    for (int g = 0; g < 2; ++g)
#pragma unroll
      for (int m = 0; m < 2; ++m) f.p[g][m] = *(const u32x4*)(slot + ap + (4 * g + m) * 1024);
#pragma unroll
    for (int n = 0; n < 9; ++n) f.n[n] = *(const u32x4*)(slot + aw + n * 1024);
  };
  auto mm3 = [&](const F3& f) {
#pragma unroll
    for (int g = 0; g < 2; ++g)
#pragma unroll
      for (int m = 0; m < 2; ++m) {
#pragma unroll
        for (int n = 0; n < 4; ++n) a3[m][n] = mfma_bf(f.n[4 * g + n], f.p[g][m], a3[m][n]);
        a3[m][4 + g] = mfma_bf(f.n[8], f.p[g][m], a3[m][4 + g]);
      }
#pragma unroll
    for (int i = 0; i < 13; ++i) { __builtin_amdgcn_sched_group_barrier(0x008, 1, 0); __builtin_amdgcn_sched_group_barrier(0x100, 1, 0); }
    __builtin_amdgcn_sched_barrier(0);
  };
  STAMP(3,11);
  bar();
  STAMP(3,12);
  ld3(U, 50);
#pragma unroll 1
  for (int t = 50; t < 66; t += 2) { bar(); ld3(V, t + 1); mm3(U); bar(); ld3(U, t + 2); mm3(V); }
  bar(); ld3(V, 67); mm3(U); mm3(V);
  STAMP(3,13);
#pragma unroll
  for (int m = 0; m < 2; ++m) a3[m][4] = lane < 32 ? a3[m][4] : a3[m][5];
  if (kq) {
#pragma unroll
    for (int n = 0; n < 5; ++n) { *(f32x4*)(sm + w * 5120 + n * 1024 + lane * 16) = a3[0][n]; asm volatile("" : "+v"(a3[1][n])); a3[0][n] = a3[1][n]; }
  } else {
#pragma unroll
    for (int n = 0; n < 5; ++n) *(f32x4*)(sm + w * 5120 + n * 1024 + lane * 16) = a3[1][n];
  }
  bar();
  char* o = out + (size_t)(32 * (T >> 1) + 4 * (T & 1) + prow) * 1152 + (72 * j3 + 4 * q) * 2;
#pragma unroll
  for (int n = 0; n < 5; ++n) {
    f32x4 v = a3[0][n] + *(const f32x4*)(sm + (w ^ 2) * 5120 + n * 1024 + lane * 16);
    if (n == 4) v = f32x4{half_sum(v[0]), half_sum(v[1]), half_sum(v[2]), half_sum(v[3])};
    if (n < 4 || q < 2) *(u32x2*)(o + n * 32) = u32x2{pk2(up2(hv[n][0]) + lo2(v)), pk2(up2(hv[n][1]) + hi2(v))};
  }
  if (bid == 0 && w == 0 && lane == 0) { u64 e = (((u64)tag[1] << 32) | tag[0]) - 0x9E3779B97F4A7C15ull + 1; st8(R, 0, O_EP, u32x2{(unsigned)e, (unsigned)(e >> 32)}); }
  STAMP(3,14);
}

#undef lane
#undef li
#undef q
#undef prow
extern "C" void kernel_launch(void* const* d_in, const int* in_sizes, int n_in, void* d_out, int out_size, void* d_ws, size_t ws_size,
                              hipStream_t stream) {
  (void)hipFuncSetAttribute((const void*)k_all, hipFuncAttributeMaxDynamicSharedMemorySize, LDS_BYTES);
  k_all<<<256, 512, LDS_BYTES, stream>>>((const char*)d_in[0], (const char*)d_in[1], (const char*)d_in[2], (const char*)d_in[3],
      (const char*)d_in[4], (const char*)d_in[5], (const char*)d_in[6], (const char*)d_in[7], (const char*)d_in[8], (const char*)d_in[10],
      (const char*)d_in[11], (const char*)d_in[12], (const char*)d_in[13], (char*)d_out, (char*)d_ws);
}
```

```cpp
#include <hip/hip_runtime.h>
#include <hip/hip_bf16.h>
#include <stdint.h>

typedef __attribute__((ext_vector_type(8))) __bf16 bf16x8;
typedef __attribute__((ext_vector_type(2))) __bf16 bf16x2;
typedef __attribute__((ext_vector_type(8))) _Float16 f16x8;
typedef __attribute__((ext_vector_type(2))) _Float16 f16x2;
typedef __attribute__((ext_vector_type(4))) float f32x4;
typedef __attribute__((ext_vector_type(2))) float f32x2;
typedef __attribute__((ext_vector_type(4))) unsigned u32x4;
typedef __attribute__((ext_vector_type(2))) unsigned u32x2;
typedef __attribute__((ext_vector_type(4))) short s16x4;
#define LDSP __attribute__((address_space(3)))
#define DEVI __device__ __forceinline__

constexpr int L = 2048, D = 576, NM = 128, F = 4608, FH = 2304;
constexpr float EPS = 1e-5f;
constexpr unsigned O_EP = 0;
constexpr int NREP = 32;
constexpr unsigned O_F1 = 256;
constexpr unsigned O_F2 = O_F1 + NREP * 1024;
constexpr unsigned F2S = 2048;
constexpr unsigned O_ZFL = O_F2 + NREP * F2S;
constexpr unsigned O_PFL = O_ZFL + 32768;
constexpr unsigned O_CS = 188416;
static_assert(O_PFL + 32768 <= O_CS, "ws");
constexpr unsigned O_ST = O_CS + F * 12;
constexpr unsigned O_HS = O_ST + 262144;
constexpr unsigned O_U = O_HS + 16384;
constexpr unsigned O_XB = O_U + L * D * 2;
constexpr unsigned O_WI = O_XB + L * D * 2;
constexpr unsigned O_WO = O_WI + D * F * 2;
constexpr unsigned O_HF = O_WO + FH * D * 2;
constexpr unsigned O_ZF = O_HF + L * D * 2;
constexpr unsigned O_PF = O_ZF + L * FH * 2;
constexpr unsigned WS_USED = O_PF + L * FH * 2;
constexpr size_t O_STAMP = 128u << 20;
#define STAMP(k,i) __builtin_amdgcn_sched_barrier(0)
#define STAMPW(k,i,W) __builtin_amdgcn_sched_barrier(0)
#define RSTAMP(k,i)
constexpr unsigned SENT = 0xFFFFFFFFu;

DEVI unsigned pk_bf16(float a, float b) { f32x2 v = {a, b}; return __builtin_bit_cast(unsigned, __builtin_convertvector(v, bf16x2)); }
DEVI unsigned pk_f16(float a, float b) { f32x2 v = {a, b}; return __builtin_bit_cast(unsigned, __builtin_convertvector(v, f16x2)); }
DEVI float bf_lo(unsigned u) { return __builtin_bit_cast(float, u << 16); }
DEVI float bf_hi(unsigned u) { return __builtin_bit_cast(float, u & 0xFFFF0000u); }
DEVI float asf(unsigned u) { return __builtin_bit_cast(float, u); }
DEVI unsigned asu(float f) { return __builtin_bit_cast(unsigned, f); }
DEVI float rbf(float a) { return bf_lo(pk_bf16(a, a)); }
DEVI float h_lo(unsigned u) { return (float)__builtin_bit_cast(f16x2, u)[0]; }
DEVI float h_hi(unsigned u) { return (float)__builtin_bit_cast(f16x2, u)[1]; }
template <int CTRL> DEVI float dpp_add(float v) {
  return v + __builtin_bit_cast(float, __builtin_amdgcn_update_dpp(0, __builtin_bit_cast(int, v), CTRL, 0xF, 0xF, true));
}
DEVI float row16_sum(float v) { v = dpp_add<0x128>(v); v = dpp_add<0x124>(v); v = dpp_add<0x122>(v); v = dpp_add<0x121>(v); return v; }
DEVI float half_sum(float v) { auto r = __builtin_amdgcn_permlane32_swap(asu(v), asu(v), false, false); return asf(r[0]) + asf(r[1]); }
DEVI float q_sum(float v) { auto r = __builtin_amdgcn_permlane16_swap(asu(v), asu(v), false, false); return half_sum(asf(r[0]) + asf(r[1])); }
DEVI unsigned umax4(u32x4 v) { return max(max(v[0], v[1]), max(v[2], v[3])); }
DEVI u32x2 tr_read(const char* p) {
  return __builtin_bit_cast(u32x2, __builtin_amdgcn_ds_read_tr16_b64_v4i16((LDSP s16x4*)p));
}
DEVI f32x4 mfma_bf(u32x4 a, u32x4 b, f32x4 c) {
  return __builtin_amdgcn_mfma_f32_16x16x32_bf16(__builtin_bit_cast(bf16x8, a), __builtin_bit_cast(bf16x8, b), c, 0, 0, 0);
}
DEVI f32x4 mfma_h(u32x4 a, u32x4 b, f32x4 c) {
  return __builtin_amdgcn_mfma_f32_16x16x32_f16(__builtin_bit_cast(f16x8, a), __builtin_bit_cast(f16x8, b), c, 0, 0, 0);
}
DEVI void ln_scalars(float mean, float var, float& mb, float& rinv) {
  mb = rbf(mean);
  float d = rbf(__builtin_amdgcn_sqrtf(rbf(rbf(var) + EPS)));
  rinv = __builtin_amdgcn_rcpf(d);
}
DEVI float gelu(float x) {
  float w = x * __builtin_fmaf(x * x, -0.10294324f, -2.3022082f);
  return x * __builtin_amdgcn_rcpf(1.0f + __builtin_amdgcn_exp2f(w));
}

typedef unsigned long long u64;
typedef __amdgpu_buffer_rsrc_t rsrc_t;
constexpr int SPIN = 1 << 16;
constexpr int CODE_LINES = 38;
template <int CTRL> DEVI float dpp_get(float v) {
  return __builtin_bit_cast(float, __builtin_amdgcn_update_dpp(0, __builtin_bit_cast(int, v), CTRL, 0xF, 0xF, true));
}
constexpr float GC1 = -0.10294324f, GC0 = -2.3022082f;
DEVI f32x2 gelu2(f32x2 x, f32x2 k1, f32x2 k0) {
  f32x2 w = x * __builtin_elementwise_fma(x * x, k1, k0);
  f32x2 d = f32x2{__builtin_amdgcn_exp2f(w[0]), __builtin_amdgcn_exp2f(w[1])} + f32x2{1.f, 1.f};
  return x * f32x2{__builtin_amdgcn_rcpf(d[0]), __builtin_amdgcn_rcpf(d[1])};
}
DEVI unsigned pk2(f32x2 v) { return __builtin_bit_cast(unsigned, __builtin_convertvector(v, bf16x2)); }
DEVI f32x2 up2(unsigned u) { return f32x2{bf_lo(u), bf_hi(u)}; }
DEVI f32x2 lo2(f32x4 v) { return __builtin_shufflevector(v, v, 0, 1); }
DEVI f32x2 hi2(f32x4 v) { return __builtin_shufflevector(v, v, 2, 3); }
DEVI float dot2(unsigned a, unsigned b, float c) {
  return __builtin_amdgcn_fdot2_f32_bf16(__builtin_bit_cast(bf16x2, a), __builtin_bit_cast(bf16x2, b), c, false);
}
DEVI void st16(rsrc_t r, unsigned voff, unsigned soff, u32x4 v) { __builtin_amdgcn_raw_buffer_store_b128(v, r, voff + soff, 0, 16); }
DEVI void st8(rsrc_t r, unsigned voff, unsigned soff, u32x2 v) { __builtin_amdgcn_raw_buffer_store_b64(v, r, voff, soff, 16); }
DEVI void st4(rsrc_t r, unsigned voff, unsigned soff, unsigned v) { __builtin_amdgcn_raw_buffer_store_b32(v, r, voff, soff, 16); }
DEVI u32x4 ld16(rsrc_t r, unsigned voff, unsigned soff) { return __builtin_amdgcn_raw_buffer_load_b128(r, voff, soff, 16); }
DEVI u32x2 ld8(rsrc_t r, unsigned voff, unsigned soff) { return __builtin_amdgcn_raw_buffer_load_b64(r, voff, soff, 16); }
template <int N> DEVI void wait_vm() { asm volatile("s_waitcnt vmcnt(%0)" ::"i"(N) : "memory"); }
DEVI void bar() { __builtin_amdgcn_sched_barrier(0); asm volatile("s_waitcnt lgkmcnt(0)\n\ts_barrier" ::: "memory"); __builtin_amdgcn_sched_barrier(0); }
DEVI int fresh_lane() { int l; asm volatile("v_mbcnt_lo_u32_b32 %0, -1, 0\n\tv_mbcnt_hi_u32_b32 %0, -1, %0" : "=v"(l)); return l; }
DEVI unsigned lds_addr(const char* p) { return (unsigned)(uintptr_t)(LDSP const char*)p; }
template <int NL> DEVI void wait_flags(rsrc_t r, unsigned off, int n, u32x2 tag, int lane) {
#pragma unroll 1
  for (int spin = 0; spin < SPIN; ++spin) {
    bool ok = true;
#pragma unroll
    for (int k = 0; k < NL; ++k) {
      int i = min(lane + 64 * k, n / 2 - 1);
      u32x4 v = ld16(r, i * 16, off);
      ok = ok && v[0] == tag[0] && v[1] == tag[1] && v[2] == tag[0] && v[3] == tag[1];
    }
    if (__all(ok)) break;
    __builtin_amdgcn_s_sleep(1);
    asm volatile("" ::: "memory");
  }
  asm volatile("" ::: "memory");
}
template <bool SC> DEVI void glds4(unsigned m0v, const char* base, unsigned v0, unsigned v1, unsigned v2, unsigned v3) {
  unsigned keep;
  if (SC)
    asm volatile("s_mov_b32 %0, m0\n\ts_mov_b32 m0, %1\n\ts_nop 0\n\t"
                 "global_load_lds_dwordx4 %3, %2 sc1\n\tglobal_load_lds_dwordx4 %4, %2 offset:1024 sc1\n\t"
                 "global_load_lds_dwordx4 %5, %2 offset:2048 sc1\n\tglobal_load_lds_dwordx4 %6, %2 offset:3072 sc1\n\ts_mov_b32 m0, %0"
                 : "=&s"(keep) : "s"(m0v), "s"(base), "v"(v0), "v"(v1), "v"(v2), "v"(v3) : "memory");
  else
    asm volatile("s_mov_b32 %0, m0\n\ts_mov_b32 m0, %1\n\ts_nop 0\n\t"
                 "global_load_lds_dwordx4 %3, %2\n\tglobal_load_lds_dwordx4 %4, %2 offset:1024\n\t"
                 "global_load_lds_dwordx4 %5, %2 offset:2048\n\tglobal_load_lds_dwordx4 %6, %2 offset:3072\n\ts_mov_b32 m0, %0"
                 : "=&s"(keep) : "s"(m0v), "s"(base), "v"(v0), "v"(v1), "v"(v2), "v"(v3) : "memory");
}
DEVI void glds1(unsigned m0v, const char* base, unsigned v0) {
  unsigned keep;
  asm volatile("s_mov_b32 %0, m0\n\ts_mov_b32 m0, %1\n\ts_nop 0\n\tglobal_load_lds_dwordx4 %3, %2 sc1\n\ts_mov_b32 m0, %0"
               : "=&s"(keep) : "s"(m0v), "s"(base), "v"(v0) : "memory");
}
DEVI void touch4(unsigned m0v, const char* base, unsigned v0, unsigned dv) {
  unsigned keep;
  asm volatile("s_mov_b32 %0, m0\n\ts_mov_b32 m0, %1\n\ts_nop 0\n\tglobal_load_lds_dword %3, %2\n\tglobal_load_lds_dword %4, %2\n\t"
               "global_load_lds_dword %5, %2\n\tglobal_load_lds_dword %6, %2\n\ts_mov_b32 m0, %0"
               : "=&s"(keep) : "s"(m0v), "s"(base), "v"(v0), "v"(v0 + dv), "v"(v0 + 2 * dv), "v"(v0 + 3 * dv) : "memory");
}
template <int NP> DEVI void ld_lin(unsigned dst, const char* src, unsigned l16) {
  glds4<true>(dst, src, l16, l16, l16, l16);
  glds4<true>(dst + 4096, src + 4096, l16, l16, l16, l16);
  if (NP == 9) glds1(dst + 8192, src + 8192, l16);
}

constexpr int TP = 1168;
constexpr int CV_K = 8 * 260 * 16, CV_U = 4576;
constexpr int C_U = 4 * CV_K, C_KD = C_U + 16 * TP, C_END = C_KD + 4 * 1536;
constexpr int KT = 528;
constexpr int STG = 34816, RING = 4 * STG;
constexpr int X_CS = RING, X_LN = X_CS + 1152, X_PV = X_LN + 1024, X_CNT = 158720;
constexpr int LDS_BYTES = X_CNT + 64 + 256;
static_assert(C_END <= X_CNT && 4 * CV_U <= 16 * TP && X_PV + 16384 <= X_CNT && LDS_BYTES <= 163840, "lds");
#define TPOLL 11
#define WDELAY 2
#define SDELAY 2
#define TOUCH 0
#define TCHK 12
#define TFLAG 14

struct Fr { u32x4 m[2], n[9]; };
struct Ctx { const char* sm; int am0[4], am1, an; };
template <int MK> DEVI void ldf(Fr& f, const Ctx& c, int t, int kk) {
  int so = (t & 3) * STG;
  asm volatile("" : "+s"(so));
  const char* slot = c.sm + so;
  if (MK == 0) {
#pragma unroll
    for (int m = 0; m < 2; ++m) {
      u32x2 a = *(const u32x2*)(slot + c.am0[2 * m] + kk * 8192), b = *(const u32x2*)(slot + c.am0[2 * m + 1] + kk * 8192);
      f.m[m] = u32x4{a[0], a[1], b[0], b[1]};
    }
  } else {
#pragma unroll
    for (int m = 0; m < 2; ++m) f.m[m] = *(const u32x4*)(slot + (c.am1 ^ (kk * 64)) + m * 2048);
  }
#pragma unroll
  for (int n = 0; n < 9; ++n) f.n[n] = *(const u32x4*)(slot + c.an + (kk * 9 + n) * 1024);
}
template <int MK, int ORI, bool LD, bool FIRST = false> DEVI void seg(f32x4 (&acc)[2][9], const Fr& cur, Fr& nxt, const Ctx& c, int t, int kk) {
  if (LD) ldf<MK>(nxt, c, t, kk);
#pragma unroll
  for (int m = 0; m < 2; ++m)
#pragma unroll
    for (int nn = 0; nn < 9; ++nn) {
      const int n = m ? 8 - nn : nn;
      const f32x4 ci = FIRST ? f32x4{0, 0, 0, 0} : acc[m][n];
      acc[m][n] = ORI == 0 ? mfma_bf(cur.m[m], cur.n[n], ci) : mfma_bf(cur.n[n], cur.m[m], ci);
    }
#pragma unroll
  for (int i = 0; i < (MK == 0 ? 13 : 11); ++i) { __builtin_amdgcn_sched_group_barrier(0x008, 1, 0); __builtin_amdgcn_sched_group_barrier(0x100, 1, 0); }
  __builtin_amdgcn_sched_barrier(0);
}
DEVI f32x2 cmul(f32x2 a, f32x2 b, f32x2 bp) {
  return __builtin_elementwise_fma(f32x2{a[1], a[1]}, bp, f32x2{a[0], a[0]} * b);
}
DEVI f32x2 perp(f32x2 b) { return f32x2{-b[1], b[0]}; }
DEVI f32x2 cexp(float ar, float ai, float l) {
  float m = __builtin_amdgcn_exp2f(ar * l), ph = __builtin_amdgcn_fractf(ai * l);
  return f32x2{m * __builtin_amdgcn_cosf(ph), m * __builtin_amdgcn_sinf(ph)};
}
DEVI unsigned pkh(f32x2 v) { return __builtin_bit_cast(unsigned, __builtin_convertvector(v, f16x2)); }
DEVI float ldbf(const char* p, int i) { return bf_lo(*(const unsigned short*)(p + 2 * i)); }

__global__ __launch_bounds__(512, 2) void k_all(const char* __restrict__ x, const char* __restrict__ a_re, const char* __restrict__ a_im,
    const char* __restrict__ b_re, const char* __restrict__ b_im, const char* __restrict__ c_re, const char* __restrict__ c_im,
    const char* __restrict__ log_dt, const char* __restrict__ dskip, const char* __restrict__ w_in, const char* __restrict__ w_sp,
    const char* __restrict__ b_sp, const char* __restrict__ w_out, char* __restrict__ out, char* __restrict__ ws) {
  extern __shared__ __attribute__((aligned(16))) char sm[];
  const int tid = threadIdx.x, lane = tid & 63, w = __builtin_amdgcn_readfirstlane(tid >> 6), li = lane & 15, q = lane >> 4;
  const int bid = blockIdx.x, xc = bid & 7, ii = bid >> 3;
  if (w == 7) {
    unsigned long long pc;
    asm volatile("s_getpc_b64 %0" : "=s"(pc));
    const char* cp = (const char*)(pc & ~255ull) + (ii & 7) * (CODE_LINES * 128);
    unsigned keep;
    asm volatile("s_mov_b32 %0, m0\n\ts_mov_b32 m0, %1\n\ts_nop 0\n\tglobal_load_lds_dword %3, %2\n\ts_mov_b32 m0, %0"
                 : "=&s"(keep) : "s"(lds_addr(sm) + X_CNT + 64), "s"(cp), "v"(min(lane, CODE_LINES - 1) * 128) : "memory");
  }
  if (tid == 0) *(u32x4*)(sm + X_CNT) = u32x4{0, 0, 0, 0};
  bar();
  asm volatile("" ::"s"(x), "s"(a_re), "s"(a_im), "s"(b_re), "s"(b_im), "s"(c_re), "s"(c_im), "s"(log_dt), "s"(dskip), "s"(w_in), "s"(w_out), "s"(ws));
  const rsrc_t R = __builtin_amdgcn_make_buffer_rsrc(ws, 0, WS_USED, 0x00020000);
  STAMP(0,0);
  const u32x2 ep = ld8(R, 0, O_EP);
  const bool is_conv = ii < 18;
  const int cg = xc * 18 + ii, rep = bid & (NREP - 1);
  auto mk_tag = [&]() {
    u64 e = (((u64)__builtin_amdgcn_readfirstlane(ep[1]) << 32) | (unsigned)__builtin_amdgcn_readfirstlane(ep[0])) + 0x9E3779B97F4A7C15ull;
    return u32x2{(unsigned)e, (unsigned)(e >> 32)};
  };
  u32x2 tag;
  auto arrive = [&](int slot, unsigned n, unsigned flag_off, unsigned stride, int nrep) {
    wait_vm<0>();
    const int lane = fresh_lane();
    unsigned old = 0;
    if (lane == 0) old = __hip_atomic_fetch_add((unsigned*)(sm + X_CNT) + slot, 1u, __ATOMIC_RELAXED, __HIP_MEMORY_SCOPE_WORKGROUP);
    if (__builtin_amdgcn_readfirstlane(old) == n - 1 && lane < nrep) st8(R, lane * stride, flag_off, tag);
  };
  if (is_conv) {
    const int ch = w & 3, dch = 4 * cg + ch;
    char* sk = sm + ch * CV_K;
    const char* su = sm + C_U + ch * CV_U;
    float sc = 0.f;
    if (w < 4) {
      const int pi = (dch * NM + 2 * lane) * 2;
      const unsigned v_ar = *(const unsigned*)(a_re + pi), v_ai = *(const unsigned*)(a_im + pi), v_br = *(const unsigned*)(b_re + pi),
                     v_bi = *(const unsigned*)(b_im + pi), v_cr = *(const unsigned*)(c_re + pi), v_ci = *(const unsigned*)(c_im + pi);
      const float p_dk = ldbf(dskip, dch);
      const float dt = rbf(__expf(ldbf(log_dt, dch)));
      tag = mk_tag();
      STAMP(0,1);
      const f32x2 ar = (up2(v_ar) * dt) * 1.44269504f, ai = (up2(v_ai) * dt) * 0.15915494f;
      const f32x2 br = up2(v_br) * dt, bi = up2(v_bi) * dt, cr = up2(v_cr), ci = up2(v_ci);
      const f32x2 cbr = cr * br - ci * bi, cbi = -(cr * bi + ci * br);
      float mx = fmaxf(fmaxf(fabsf(cbr[0]), fabsf(cbi[0])), fmaxf(fabsf(cbr[1]), fabsf(cbi[1])));
      mx = fmaxf(mx, dpp_get<0x128>(mx)); mx = fmaxf(mx, dpp_get<0x124>(mx)); mx = fmaxf(mx, dpp_get<0x122>(mx)); mx = fmaxf(mx, dpp_get<0x121>(mx));
      auto rl = [&](int l) { return asf(__builtin_amdgcn_readlane((int)asu(mx), l)); };
      sc = fmaxf(fmaxf(rl(0), rl(16)), fmaxf(rl(32), rl(48)));
      sc = fmaxf(sc, fabsf(p_dk) * (1.0f / 256));
      const float isc = sc > 0.f ? 1.0f / sc : 0.f;
      {
        struct C2 { f32x2 a, b; };
        char* tb = sk + lane * 8;
        auto put = [&](int row, C2 v) { *(u32x2*)(tb + row * KT) = u32x2{pkh(v.a), pkh(v.b)}; };
        auto mul = [&](C2 v, C2 u, C2 up) { return C2{cmul(v.a, u.a, up.a), cmul(v.b, u.b, up.b)}; };
        auto prp = [&](C2 v) { return C2{perp(v.a), perp(v.b)}; };
        auto ex = [&](float l) { return C2{cexp(ar[0], ai[0], l), cexp(ar[1], ai[1], l)}; };
        const C2 w1 = ex(1.f), w128 = ex(128.f);
        C2 w8 = ex(8.f); w8.a[1] = -w8.a[1]; w8.b[1] = -w8.b[1];
        const C2 w1p = prp(w1), w8p = prp(w8), w128p = prp(w128);
        C2 y = {f32x2{cbr[0], cbi[0]} * isc, f32x2{cbr[1], cbi[1]} * isc};
        C2 E[8]; E[0] = C2{f32x2{1.f, 0.f}, f32x2{1.f, 0.f}}; E[1] = w1;
#pragma unroll
        for (int e = 2; e < 8; ++e) E[e] = mul(E[e - 1], w1, w1p);
#pragma unroll
        for (int e = 0; e < 8; ++e) { put(e, E[e]); put(8 + e, e ? mul(E[e], w128, w128p) : w128); put(41 + e, y); y = mul(y, w8, w8p); }
        const C2 e2p = prp(E[2]), e4p = prp(E[4]), e6p = prp(E[6]);
        C2 H = w128;
#pragma unroll
        for (int a = 2; a <= 16; ++a) {
          H = mul(H, w128, w128p);
          if (a < 4) { put(8 + 4 * a, H); put(9 + 4 * a, mul(H, E[2], e2p)); put(10 + 4 * a, mul(H, E[4], e4p)); put(11 + 4 * a, mul(H, E[6], e6p)); }
          else if (a < 8) { put(16 + 2 * a, H); put(17 + 2 * a, mul(H, E[4], e4p)); }
          else put(24 + a, H);
          if (a < 10) { put(47 + a, y); if (a < 9) y = mul(y, w8, w8p); }
        }
      }
      asm volatile("" ::: "memory");
      STAMP(0,2);
      {
        f32x4 kk[3] = {};
        const char* pa = sk + li * KT + q * 16;
#pragma unroll
        for (int s = 0; s < 8; ++s) {
          u32x4 fb = *(const u32x4*)(pa + 41 * KT + s * 64);
#pragma unroll
          for (int t = 0; t < 3; ++t) kk[t] = mfma_h(*(const u32x4*)(pa + 16 * t * KT + s * 64), fb, kk[t]);
        }
        if (lane == 0) kk[0][0] += p_dk * isc;
        char* kd = sm + C_KD + ch * 1536;
        *(u32x2*)(kd + (128 * (q >> 1) + 4 * (q & 1) + 8 * li) * 2) = u32x2{pkh(lo2(kk[0])), pkh(hi2(kk[0]))};
        const int i1 = q < 2 ? 256 + 64 * q + 4 * li : 384 + 64 * (q - 2) + 2 * li;
        *(unsigned*)(kd + i1 * 2) = pkh(lo2(kk[1])); *(unsigned*)(kd + (i1 + (q < 2 ? 2 : 32)) * 2) = pkh(hi2(kk[1]));
#pragma unroll
        for (int r = 0; r < 4; ++r) *(_Float16*)(kd + (512 + 16 * (4 * q + r) + li) * 2) = (_Float16)kk[2][r];
      }
      asm volatile("" ::: "memory");
      u32x4 c0[4];
      {
        const unsigned short* kd = (const unsigned short*)(sm + C_KD + ch * 1536);
#pragma unroll
        for (int rr = 0; rr < 2; ++rr) {
          const int k = 255 - 64 * rr - lane;
          const unsigned a = kd[384 + k], b = kd[385 + k], aa = a | (a << 16);
          c0[rr] = u32x4{b | (b << 16), b | (((k & 1) ? b : a) << 16), aa, aa};
        }
        { const int k = 127 - lane;
          const unsigned ab = *(const unsigned*)(kd + 256 + 2 * k), cc = kd[258 + 2 * k];
          c0[2] = u32x4{cc | (cc << 16), __builtin_amdgcn_perm(ab, ab, 0x03020302), __builtin_amdgcn_perm(ab, ab, 0x01000302), __builtin_amdgcn_perm(ab, ab, 0x01000100)}; }
        { const int k = 63 - lane;
          const u32x4 v = *(const u32x4*)(kd + 8 * (k & 31));
          const u32x2 u = *(const u32x2*)(kd + 128 + 4 * k); const unsigned e = kd[132 + 4 * k];
          c0[3] = lane < 32 ? u32x4{__builtin_amdgcn_perm(u[1], e, 0x07060100), __builtin_amdgcn_perm(u[1], u[1], 0x01000100), __builtin_amdgcn_perm(u[0], u[1], 0x07060100), __builtin_amdgcn_perm(u[0], u[0], 0x01000100)}
                            : u32x4{__builtin_amdgcn_alignbit(v[3], v[3], 16), __builtin_amdgcn_alignbit(v[2], v[2], 16), __builtin_amdgcn_alignbit(v[1], v[1], 16), __builtin_amdgcn_alignbit(v[0], v[0], 16)}; }
#pragma unroll
        for (int rr = 0; rr < 4; ++rr) *(u32x4*)(sk + (64 * rr + lane) * 16) = c0[rr];
        if (lane < 8) *(u32x4*)(sk + (lane * 260 + 256) * 16) = u32x4{0, 0, 0, 0};
      }
      asm volatile("" ::: "memory");
      STAMP(0,3);
      u32x4 c1[4];
#pragma unroll
      for (int rr = 0; rr < 4; ++rr) c1[rr] = *(const u32x4*)(sk + (64 * rr + lane) * 16 + 16);
#pragma unroll
      for (int rr = 0; rr < 4; ++rr) {
        char* p = sk + (64 * rr + lane) * 16;
        unsigned e[8] = {c0[rr][0], c0[rr][1], c0[rr][2], c0[rr][3], c1[rr][0], c1[rr][1], c1[rr][2], c1[rr][3]}, o[7];
#pragma unroll
        for (int i = 0; i < 7; ++i) o[i] = __builtin_amdgcn_alignbit(e[i + 1], e[i], 16);
#pragma unroll
        for (int s = 0; s < 4; ++s) {
          if (s) *(u32x4*)(p + (2 * s) * 260 * 16) = u32x4{e[s], e[s + 1], e[s + 2], e[s + 3]};
          *(u32x4*)(p + (2 * s + 1) * 260 * 16) = u32x4{o[s], o[s + 1], o[s + 2], o[s + 3]};
        }
      }
      STAMP(0,4);
    } else {
      char* s0 = sm + C_U;
      __builtin_amdgcn_s_setprio(2);
      u32x2 xr[9];
      const char* src = x + (size_t)(16 * min(cg, 127) + 4 * ch + q) * 1152 + li * 8;
#pragma unroll
      for (int k = 0; k < 9; ++k) xr[k] = *(const u32x2*)(src + k * 128);
      tag = mk_tag();
      if (cg < 128) {
        asm volatile("" :: "v"(xr[8])); STAMPW(1,0,4);
#pragma unroll
        for (int k = 0; k < 9; ++k) st8(R, li * 32 + q * 8 + k * 512, O_XB + (4 * cg + ch) * 4608, xr[k]);
        float a[4] = {0, 0, 0, 0};
#pragma unroll
        for (int k = 0; k < 9; ++k)
#pragma unroll
          for (int e = 0; e < 2; ++e) { a[e] = dot2(xr[k][e], 0x3F803F80u, a[e]); a[2 + e] = dot2(xr[k][e], xr[k][e], a[2 + e]); }
        const float s = row16_sum(a[0] + a[1]), ss = row16_sum(a[2] + a[3]);
        float mean = s * (1.0f / D), mb, rinv;
        ln_scalars(mean, fmaxf(ss * (1.0f / D) - mean * mean, 0.f), mb, rinv);
        const f32x2 r2 = {rinv, rinv}, b2 = {-mb * rinv, -mb * rinv};
        char* p = s0 + (4 * ch + q) * TP + li * 8;
#pragma unroll
        for (int k = 0; k < 9; ++k)
          *(u32x2*)(p + k * 128) = u32x2{pkh(__builtin_elementwise_fma(up2(xr[k][0]), r2, b2)), pkh(__builtin_elementwise_fma(up2(xr[k][1]), r2, b2))};
        asm volatile("" ::: "memory");
        unsigned* cnt = (unsigned*)(sm + X_CNT) + 3;
        if (lane == 0) __hip_atomic_fetch_add(cnt, 1u, __ATOMIC_RELAXED, __HIP_MEMORY_SCOPE_WORKGROUP);
#pragma unroll 1
        for (int spin = 0; spin < (SPIN << 4) && __builtin_amdgcn_readfirstlane(__hip_atomic_load(cnt, __ATOMIC_RELAXED, __HIP_MEMORY_SCOPE_WORKGROUP)) < 4; ++spin) {}
        asm volatile("" ::: "memory");
        STAMPW(1,1,4);
        const char* trb = s0 + (8 * (q & 1) + (li >> 2)) * TP + (q >> 1) * 32 + (li & 3) * 8 + ch * 64;
        const unsigned uo = (16 * (q >> 1) + li) * 32 + (q & 1) * 16 + O_U + cg * 18432 + ch * 1024;
#pragma unroll
        for (int k = 0; k < 5; ++k)
          if (k < 4 || ch < 2) {
            u32x2 lo = tr_read(trb + k * 256), hi = tr_read(trb + 4 * TP + k * 256);
            st16(R, uo, k * 4096, u32x4{lo[0], lo[1], hi[0], hi[1]});
          }
        STAMPW(1,2,4);
        arrive(0, 4, O_F1 + cg * 8, 1024, NREP);
        STAMPW(1,3,4);
      }
      __builtin_amdgcn_s_setprio(0);
      if (cg < 136 && w >= 6) st16(R, (tid - 384) * 16, O_ST + cg * 2048, u32x4{SENT, SENT, SENT, SENT});
      if (w == 4) {
        wait_flags<1>(R, O_F1 + rep * 1024, 128, tag, lane);
        STAMPW(5,0,4);
        const unsigned v0 = (lane >> 1) * 18432 + (lane & 1) * 16, dv = 32 * 18432 - 1024;
#pragma unroll
        for (int i = 0; i < 4; ++i) glds4<true>(lds_addr(sm) + C_U + i * CV_U + 480, ws + O_U + (4 * cg + i) * 32, v0, v0 + dv, v0 + 2 * dv, v0 + 3 * dv);
#pragma unroll
        for (int i = 0; i < 4; ++i) if (lane < 30) *(u32x4*)(sm + C_U + i * CV_U + lane * 16) = u32x4{0, 0, 0, 0};
        wait_vm<0>();
      }
    }
    bar();
    STAMP(0,5);
    u32x4 xv[4];
    if (w < 4) {
      f32x4 acc[8] = {};
      const char* pa = sk + ((7 - (li & 7)) * 260 + 253 - (li >> 3) + q) * 16 - 4032;
      const char* pb = su + li * 32 + q * 16;
      struct CF { u32x4 a[8], b[8]; } A, B;
      auto ldc = [&](CF& f, int a0) {
#pragma unroll
        for (int m = 0; m < 8; ++m) { f.a[m] = *(const u32x4*)(pa + 4032 - 64 * a0 - 512 * m); f.b[m] = *(const u32x4*)(pb + 448 - 64 * a0 + 512 * m); }
      };
      auto mmc = [&](const CF& f) {
#pragma unroll
        for (int m = 0; m < 8; ++m)
#pragma unroll
          for (int t = m; t < 8; ++t) acc[t] = mfma_h(f.a[m], f.b[t - m], acc[t]);
#pragma unroll
        for (int i = 0; i < 16; ++i) { __builtin_amdgcn_sched_group_barrier(0x008, 2, 0); __builtin_amdgcn_sched_group_barrier(0x100, 1, 0); }
        __builtin_amdgcn_sched_barrier(0);
      };
      ldc(A, 0);
      __builtin_amdgcn_sched_barrier(0);
#pragma unroll
      for (int a0 = 0; a0 < 8; a0 += 2) { ldc(B, a0 + 1); mmc(A); if (a0 < 6) ldc(A, a0 + 2); mmc(B); }
      STAMP(0,6);
#pragma unroll
      for (int t = 0; t < 8; ++t) *(u32x2*)(sk + t * 512 + li * 32 + q * 8) = u32x2{pk2(lo2(acc[t]) * sc), pk2(hi2(acc[t]) * sc)};
    } else {
#pragma unroll
      for (int r = 0; r < 4; ++r) {
        const int T = ((tid & 255) >> 3) + 32 * r, ip = tid & 7;
        xv[r] = ld16(R, (8 * (T >> 1) + 2 * (ip >> 1) + (T & 1)) * 4608 + (ip & 1) * 16, O_XB + cg * 32);
      }
      STAMPW(2,0,4);
    }
    bar();
    if (w >= 4) {
#pragma unroll
      for (int r = 0; r < 4; ++r) {
        const int T = ((tid & 255) >> 3) + 32 * r, ip = tid & 7;
        const char* p = sm + (32 * (T >> 1) + 8 * (ip >> 1) + 4 * (T & 1) + 2 * (ip & 1)) * 2;
        const unsigned c0 = *(const unsigned*)p, c1 = *(const unsigned*)(p + CV_K), c2 = *(const unsigned*)(p + 2 * CV_K), c3 = *(const unsigned*)(p + 3 * CV_K);
        u32x4 v = {pk2(up2(xv[r][0]) + f32x2{bf_lo(c0), bf_lo(c1)}), pk2(up2(xv[r][1]) + f32x2{bf_lo(c2), bf_lo(c3)}),
                   pk2(up2(xv[r][2]) + f32x2{bf_hi(c0), bf_hi(c1)}), pk2(up2(xv[r][3]) + f32x2{bf_hi(c2), bf_hi(c3)})};
        st16(R, (T >> 3) * (18 * 8192) + (T & 7) * 1024 + ip * 16, O_HF + (cg >> 3) * 8192 + (cg & 1) * 512 + ((cg >> 1) & 3) * 128, v);
      }
      STAMPW(2,1,4);
      arrive(1, 4, O_F2 + cg * 8, F2S, NREP);
      STAMPW(2,2,4);
    }
  } else {
    const int tj = (ii - 18) * 8 + xc;
    const int kc = tj / 36, cb = tj % 36;
    for (int i = 0; i < WDELAY; ++i) __builtin_amdgcn_s_sleep(16);
    char* s0 = sm + 65536;
    const int G = max(tj - 40, 0), par = G & 1;
    {
      const int row = tid >> 4, uu = G / 9, idx = G % 9, hh = row >> 4;
      const int panel = idx < 4 ? 2 * uu : (idx < 8 ? 2 * uu + 1 : 2 * uu + hh), tile = idx < 8 ? 2 * (idx & 3) + hh : 8;
      const char* src = w_in + (size_t)(192 * min(kc, 2) + row) * 9216 + cb * 256 + li * 16;
      const char* wsrc = w_out + (size_t)(144 * panel + 16 * tile + (row & 15)) * 1152 + li * 8;
      u32x4 v[6]; u32x2 vo[9];
#pragma unroll
      for (int k = 0; k < 6; ++k) v[k] = *(const u32x4*)(src + (size_t)k * 32 * 9216);
      if (tj >= 40) {
#pragma unroll
        for (int k = 0; k < 9; ++k) vo[k] = *(const u32x2*)(wsrc + k * 128);
      }
#pragma unroll
      for (int k = 0; k < 6; ++k) *(u32x4*)(sm + (row + 32 * k) * 272 + li * 16) = v[k];
      if (tj >= 40) {
#pragma unroll
        for (int k = 0; k < 9; ++k) *(u32x2*)(s0 + row * TP + li * 8 + k * 128) = vo[k];
      }
    }
    tag = mk_tag();
    if (w == 0) {
      wait_flags<1>(R, O_F1 + rep * 1024, 128, tag, lane);
      for (int i = 0; i < SDELAY; ++i) __builtin_amdgcn_s_sleep(16);
    }
    bar();
    STAMP(4,0);
    if (tj < 108) {
      const int ct = 8 * cb + w;
      const int hf = ct < 144 ? 1 : 0, cl = ct < 144 ? ct : ct - 144, fp = cl / 9, n = cl % 9;
      const char* trb = sm + (8 * q + (li >> 2)) * 272 + w * 32 + (li & 3) * 8;
      const unsigned dst = lane * 16 + O_WI + (((fp * 2 + hf) * 18 + 6 * kc) * 9 + n) * 1024;
      float s = 0.f;
#pragma unroll
      for (int g = 0; g < 6; ++g) {
        u32x2 lo = tr_read(trb + g * 32 * 272), hi = tr_read(trb + g * 32 * 272 + 4 * 272);
        st16(R, dst, g * 9216, u32x4{lo[0], lo[1], hi[0], hi[1]});
        s = dot2(lo[0], 0x3F803F80u, s); s = dot2(lo[1], 0x3F803F80u, s); s = dot2(hi[0], 0x3F803F80u, s); s = dot2(hi[1], 0x3F803F80u, s);
      }
      s = q_sum(s);
      if (q == 0) st4(R, li * 4, O_CS + kc * F * 4 + ct * 64, asu(s));
    }
    if (tj >= 40) {
      const char* trb = s0 + (4 * q + (li >> 2)) * TP + (li & 3) * 8;
      const unsigned dst = lane * 16 + O_WO + (w * 36 + (G >> 1)) * 9216;
#pragma unroll
      for (int n = 0; n < 5; ++n) {
        const int c0 = 72 * w + 16 * n - (n == 4 ? 8 * par : 0);
        u32x2 lo = tr_read(trb + c0 * 2), hi = tr_read(trb + 16 * TP + c0 * 2);
        if (n < 4) st16(R, dst, (4 * par + n) * 1024, u32x4{lo[0], lo[1], hi[0], hi[1]});
        else if ((li >> 3) == par) st16(R, dst, 8192, u32x4{lo[0], lo[1], hi[0], hi[1]});
      }
    }
    STAMP(4,1);
    arrive(0, 8, O_F2 + (144 + tj) * 8, F2S, NREP);
    STAMP(4,2);
  }
  const int tp = (xc & 1) * 8 + (ii >> 2), fp = (xc >> 1) * 4 + (ii & 3);
  const int t64 = 4 * xc + (ii >> 3), j3 = ii & 7;
  if (w == 0) { wait_flags<1>(R, O_F2 + rep * F2S + 144 * 8, 112, tag, lane); STAMPW(6,0,0); }
  bar();
  if (w >= 4) {
    const int lw = w - 4;
    const unsigned l16 = lane * 16, base = lds_addr(sm);
    const int ph = ((ii >> 2) + ii) & 3, ph3 = ((ii >> 3) + ii) & 3;
    auto k9 = [&](int t) { return (t & 3) == ph ? (t + 4 < 9 ? t + 4 : ph) : t; };
    auto k32 = [&](int t) { return (t & 3) == ph ? ((t + 4) & 31) : t; };
    auto k18 = [&](int t) { return (t & 3) == ph3 ? (t + 4 < 18 ? t + 4 : ph3) : t; };
    if (lw < 2) {
      constexpr int NP = 8;
      unsigned vo[8];
#pragma unroll
      for (int j = 0; j < 8; ++j) {
        int t = 64 * lw + 32 * (j >> 2) + 4 * ((j >> 1) & 1) + 16 * (j & 1) + 8 * (lane >> 5) + ((lane >> 3) & 3);
        vo[j] = t * 4096 + (((lane & 7) ^ ((lane >> 3) & 7)) * 16) + 3072 - (j & 3) * 1024;
      }
      const char* wsrc = w_sp + (size_t)tp * 128 * 4096 - 3072;
      const char* hsrc = ws + O_HF + tp * 18 * 8192 + lw * 8192;
      const char* psrc = ws + O_PF + t64 * 72 * 4096 + lw * 8192;
      const unsigned dst = base + lw * 8192;
      if (lw == 0) wait_flags<2>(R, O_F2 + rep * F2S, 144, tag, lane);
      STAMPW(5,1,4);
      bar();
#pragma unroll 1
      for (int t = 0; t < 4; ++t) ld_lin<NP>(dst + t * STG, hsrc + k9(t) * 16384, l16);
      wait_vm<3 * NP>(); bar();
#pragma unroll 1
      for (int t = 4; t < 18; ++t) {
        wait_vm<2 * NP>(); bar(); ld_lin<NP>(dst + (t & 3) * STG, hsrc + k9(t < 9 ? t : t - 9) * 16384, l16);
      }
#pragma unroll 1
      for (int t = 18; t < 50; ++t) {
        wait_vm<2 * NP>(); bar();
        const unsigned dd = dst + (t & 3) * STG; const char* sp = wsrc + k32(t - 18) * 128;
        glds4<false>(dd, sp, vo[0], vo[1], vo[2], vo[3]); glds4<false>(dd + 4096, sp, vo[4], vo[5], vo[6], vo[7]);
      }
      wait_vm<2 * NP>(); bar(); wait_vm<NP>(); bar(); wait_vm<0>(); bar();
      wait_flags<1>(R, O_PFL + (t64 >> 1) * 2048 + ((t64 & 1) * 8 + j3) * 128, 16, tag, lane);
      STAMPW(5,2,4);
#pragma unroll 1
      for (int t = 50; t < 68; ++t) {
        if (t >= 53) { wait_vm<2 * NP>(); bar(); }
        ld_lin<NP>(dst + (t & 3) * STG, psrc + k18(t - 50) * 16384, l16);
      }
      wait_vm<2 * NP>(); bar(); wait_vm<NP>(); bar(); wait_vm<0>(); bar();
    } else {
      constexpr int NP = 9;
      const char* wi = ws + O_WI + (fp * 2) * 18 * 9216 + (lw - 2) * 9216;
      const char* zs = ws + O_ZF + fp * 64 * 9216 + (lw - 2) * 9216;
      const char* wo = ws + O_WO + j3 * 36 * 9216 + (lw - 2) * 9216;
      const unsigned dst = base + 16384 + (lw - 2) * 9216;
#pragma unroll 1
      for (int t = 0; t < 4; ++t) ld_lin<NP>(dst + t * STG, wi + k9(t) * 18432, l16);
      bar();
      wait_vm<3 * NP>(); bar();
      if (lw == 3) {
        f32x2 a = *(const f32x2*)(sm + X_LN + lane * 8) + *(const f32x2*)(sm + X_LN + 512 + lane * 8);
        a[0] = q_sum(dpp_add<0x128>(a[0])); a[1] = q_sum(dpp_add<0x128>(a[1]));
        float mean = a[0] * (1.0f / D), mb, ri;
        ln_scalars(mean, fmaxf(a[1] * (1.0f / D) - mean * mean, 0.f), mb, ri);
        if (lane < 8) st8(R, lane * 8, O_HS + tp * 1024 + fp * 64, u32x2{asu(ri) == SENT ? 0x7FC00000u : asu(ri), asu(-mb)});
      }
#pragma unroll 1
      for (int t = 4; t < 18; ++t) {
        wait_vm<2 * NP>(); bar(); ld_lin<NP>(dst + (t & 3) * STG, wi + (t < 9 ? k9(t) : 9 + k9(t - 9)) * 18432, l16);
      }
      wait_vm<2 * NP>(); bar(); wait_vm<NP>(); bar(); wait_vm<0>(); bar();
      wait_flags<1>(R, O_ZFL + fp * 2048 + tp * 128, 16, tag, lane);
      STAMPW(6,1,6);
#pragma unroll 1
      for (int t = 18; t < 68; ++t) {
        if (t >= 21) { wait_vm<2 * NP>(); bar(); }
        ld_lin<NP>(dst + (t & 3) * STG, t < 50 ? zs + k32(t - 18) * 18432 : wo + k18(t - 50) * 18432, l16);
      }
      wait_vm<2 * NP>(); bar(); wait_vm<NP>(); bar(); wait_vm<0>(); bar();
    }
    return;
  }
  Ctx c;
  c.sm = sm;
#pragma unroll
  for (int i = 0; i < 4; ++i) { c.am0[i] = (2 * w + (i >> 1)) * 1024 + (i & 1) * 512 + lane * 8; asm volatile("" : "+v"(c.am0[i])); }
  c.am1 = 2 * w * 2048 + li * 128 + ((q ^ (lane & 7)) * 16);
  c.an = 16384 + lane * 16;
  const int prow = 8 * (li >> 2) + (li & 3);
  if (tid < 72) {
    const unsigned o = tid < 36 ? FH * 4 + tid * 16 : (tid - 36) * 16;
    const u32x4 c0 = ld16(R, o, O_CS + fp * 576), c1 = ld16(R, o, O_CS + F * 4 + fp * 576), c2 = ld16(R, o, O_CS + F * 8 + fp * 576);
    *(f32x4*)(sm + X_CS + tid * 16) = (__builtin_bit_cast(f32x4, c0) + __builtin_bit_cast(f32x4, c1)) + __builtin_bit_cast(f32x4, c2);
  }
  bar();
  char* pvb = sm + X_PV + w * 4096;
  {
    u32x4 hv[3];
#pragma unroll
    for (int k = 0; k < 3; ++k)
      hv[k] = ld16(R, (lane >> 5) * 8192 + ((lane >> 2) & 7) * 128 + (lane & 3) * 16, O_HF + (tp * 18 * 8 + (fp >> 1)) * 1024 + (fp & 1) * 64 + min(w + 4 * k, 8) * 16384);
    if (w) hv[2] = u32x4{0, 0, 0, 0};
    f32x4 a = {0, 0, 0, 0};
#pragma unroll
    for (int k = 0; k < 3; ++k)
#pragma unroll
      for (int e = 0; e < 4; ++e) { a[e & 2] = dot2(hv[k][e], 0x3F803F80u, a[e & 2]); a[(e & 2) + 1] = dot2(hv[k][e], hv[k][e], a[(e & 2) + 1]); }
#pragma unroll
    for (int e = 0; e < 4; ++e) a[e] = dpp_add<0x124>(dpp_add<0x128>(a[e]));
    if (li < 4) *(f32x4*)(sm + X_LN + (w * 4 + q) * 64 + li * 16) = a;
  }
  f32x4 acc[2][9];
  Fr X, Y;
  bar();
  STAMP(3,0);
  ldf<0>(X, c, 0, 0);
  seg<0, 0, true, true>(acc, X, Y, c, 0, 1);
#pragma unroll 1
  for (int t = 0; t < 8; ++t) {
    bar();
    seg<0, 0, true>(acc, Y, X, c, t + 1, 0);
    seg<0, 0, true>(acc, X, Y, c, t + 1, 1);
  }
  u32x2 ht[2]; u32x4 hc[4];
  auto ld_hs = [&]() {
#pragma unroll
    for (int m = 0; m < 2; ++m) {
      ht[m] = ld8(R, li * 8 + m * 128, O_HS + tp * 1024 + w * 256);
      hc[2 * m] = ld16(R, q * 32 + m * 128, O_HS + tp * 1024 + w * 256); hc[2 * m + 1] = ld16(R, q * 32 + 16 + m * 128, O_HS + tp * 1024 + w * 256);
    }
  };
  ld_hs();
  bar();
  seg<0, 0, true>(acc, Y, X, c, 9, 0);
  STAMP(3,1);
#pragma unroll 1
  for (int spin = 0; spin < SPIN; ++spin) {
    const unsigned mx = max(max(umax4(hc[0]), umax4(hc[1])), max(umax4(hc[2]), umax4(hc[3])));
    if (__all(max(mx, umax4(u32x4{ht[0][0], ht[0][1], ht[1][0], ht[1][1]})) != SENT)) break;
    __builtin_amdgcn_s_sleep(1);
    asm volatile("" ::: "memory");
    ld_hs();
  }
  const float lrs[2] = {asf(ht[0][0]), asf(ht[1][0])}, lnm[2] = {asf(ht[0][1]), asf(ht[1][1])};
  u32x4 zb[9];
  f32x2 rs[4];
  {
    float cs[9];
#pragma unroll
    for (int n = 0; n < 9; ++n) cs[n] = *(const float*)(sm + X_CS + (16 * n + li) * 4);
    f32x2 nm[4], k1[4], k0[4], S1[4] = {}, S2[4] = {};
#pragma unroll
    for (int jp = 0; jp < 4; ++jp) {
      rs[jp] = f32x2{asf(hc[jp][0]), asf(hc[jp][2])}; nm[jp] = f32x2{asf(hc[jp][1]), asf(hc[jp][3])};
      k0[jp] = rs[jp] * GC0; k1[jp] = rs[jp] * rs[jp] * rs[jp] * GC1;
    }
#pragma unroll
    for (int n = 0; n < 9; ++n) {
#pragma unroll
      for (int jp = 0; jp < 4; ++jp) {
        f32x2 a = (jp & 1) ? hi2(acc[jp >> 1][n]) : lo2(acc[jp >> 1][n]);
        f32x2 z = gelu2(__builtin_elementwise_fma(nm[jp], f32x2{cs[n], cs[n]}, a), k1[jp], k0[jp]);
        zb[n][jp] = pk2(z);
        S1[jp] += z; S2[jp] = __builtin_elementwise_fma(z, z, S2[jp]);
      }
      asm volatile("" : "+v"(zb[n]));
    }
#pragma unroll
    for (int jp = 0; jp < 4; ++jp) { S1[jp] *= rs[jp]; S2[jp] *= rs[jp] * rs[jp]; }
    float v[16];
#pragma unroll
    for (int j = 0; j < 8; ++j) { v[2 * j] = S1[j >> 1][j & 1]; v[2 * j + 1] = S2[j >> 1][j & 1]; }
#pragma unroll
    for (int i = 0; i < 8; ++i) { bool h = li & 8; v[i] = (h ? v[i + 8] : v[i]) + dpp_get<0x128>(h ? v[i] : v[i + 8]); }
#pragma unroll
    for (int i = 0; i < 4; ++i) { bool h = li & 4; v[i] = (h ? v[i + 4] : v[i]) + dpp_get<0x141>(h ? v[i] : v[i + 4]); }
#pragma unroll
    for (int i = 0; i < 2; ++i) { bool h = li & 2; v[i] = (h ? v[i + 2] : v[i]) + dpp_get<0x4E>(h ? v[i] : v[i + 2]); }
    { bool h = li & 1; v[0] = (h ? v[1] : v[0]) + dpp_get<0xB1>(h ? v[0] : v[1]); }
    unsigned tot = asu(v[0]);
    st4(R, lane * 4, O_ST + ((tp * 4 + w) * 16 + fp) * 256, tot == SENT ? 0x7FC00000u : tot);
  }
  STAMP(3,2);
  const char* sts = ws + O_ST + (tp * 4 + w) * 4096;
  seg<0, 1, true, true>(acc, X, Y, c, 9, 1);
#pragma unroll 1
  for (int t = 9; t < TCHK; ++t) {
    if (t == TPOLL) glds4<true>(lds_addr(pvb), sts, lane * 16, lane * 16, lane * 16, lane * 16);
    bar();
    seg<0, 1, true>(acc, Y, X, c, t + 1, 0);
    seg<0, 1, true>(acc, X, Y, c, t + 1, 1);
  }
  STAMP(3,3);
  u32x4 pv[4];
#pragma unroll 1
  for (int spin = 0; spin < SPIN; ++spin) {
    wait_vm<0>();
#pragma unroll
    for (int k = 0; k < 4; ++k) pv[k] = *(const u32x4*)(pvb + k * 1024 + lane * 16);
    if (__all(max(max(umax4(pv[0]), umax4(pv[1])), max(umax4(pv[2]), umax4(pv[3]))) != SENT)) break;
    glds4<true>(lds_addr(pvb), sts, lane * 16, lane * 16, lane * 16, lane * 16);
  }
  STAMP(3,4);
  {
    float t4[4];
#pragma unroll
    for (int e = 0; e < 4; ++e) t4[e] = q_sum((asf(pv[0][e]) + asf(pv[1][e])) + (asf(pv[2][e]) + asf(pv[3][e])));
    f32x2 o0, o1;
#pragma unroll
    for (int r = 0; r < 2; ++r) {
      float mean = t4[2 * r] * (1.0f / FH), var = fmaxf(t4[2 * r + 1] * (1.0f / FH) - mean * mean, 0.f), mb, ri;
      ln_scalars(mean, var, mb, ri);
      o0[r] = ri; o1[r] = -mb * ri;
    }
    char* ln = sm + X_LN + w * 256;
    if (lane < 16) { *(f32x2*)(ln + li * 8) = o0; *(f32x2*)(ln + 128 + li * 8) = o1; }
    asm volatile("" ::: "memory");
    f32x4 ri[2], rb[2];
#pragma unroll
    for (int m = 0; m < 2; ++m) { ri[m] = *(const f32x4*)(ln + (8 * q + 4 * m) * 4); rb[m] = *(const f32x4*)(ln + 128 + (8 * q + 4 * m) * 4); }
#pragma unroll
    for (int jp = 0; jp < 4; ++jp) rs[jp] *= (jp & 1) ? hi2(ri[jp >> 1]) : lo2(ri[jp >> 1]);
    const unsigned zo = lane * 16 + O_ZF + (fp * 64 + 4 * tp + w) * 9216;
#pragma unroll
    for (int n = 0; n < 9; ++n) {
      u32x4 o4;
#pragma unroll
      for (int jp = 0; jp < 4; ++jp)
        o4[jp] = pk2(__builtin_elementwise_fma(up2(zb[n][jp]), rs[jp], (jp & 1) ? hi2(rb[jp >> 1]) : lo2(rb[jp >> 1])));
      st16(R, zo, n * 1024, o4);
    }
  }
  STAMP(3,5);
#pragma unroll 1
  for (int t = TCHK; t < 17; ++t) {
    if (t == TFLAG) wait_vm<0>();
    bar();
    if (t == TFLAG && tid < 16) st8(R, tid * 128, O_ZFL + fp * 2048 + tp * 8, tag);
    seg<0, 1, true>(acc, Y, X, c, t + 1, 0);
    seg<0, 1, true>(acc, X, Y, c, t + 1, 1);
  }
  seg<0, 1, false>(acc, Y, X, c, 17, 1);
  STAMP(3,6);
  u32x2 z1[2][9];
  float bs[2];
#pragma unroll
  for (int m = 0; m < 2; ++m) bs[m] = ldbf(b_sp, 128 * tp + 32 * w + prow + 4 * m) * lrs[m];
  {
    f32x4 cs[9];
#pragma unroll
    for (int n = 0; n < 9; ++n) cs[n] = *(const f32x4*)(sm + X_CS + (144 + 16 * n + 4 * q) * 4);
#pragma unroll
    for (int n = 0; n < 9; ++n)
#pragma unroll
      for (int m = 0; m < 2; ++m) {
        const float a1 = lrs[m] * lrs[m] * lrs[m] * GC1, a0 = lrs[m] * GC0;
        const f32x2 k1 = {a1, a1}, k0 = {a0, a0}, nm = {lnm[m], lnm[m]};
        z1[m][n][0] = pk2(gelu2(__builtin_elementwise_fma(nm, lo2(cs[n]), lo2(acc[m][n])), k1, k0));
        z1[m][n][1] = pk2(gelu2(__builtin_elementwise_fma(nm, hi2(cs[n]), hi2(acc[m][n])), k1, k0));
        asm volatile("" : "+v"(z1[m][n]));
      }
  }
  STAMP(3,7);
  bar();
  STAMP(3,8);
  ldf<1>(X, c, 18, 0);
  seg<1, 1, true, true>(acc, X, Y, c, 18, 1);
#pragma unroll 1
  for (int t = 18; t < 49; ++t) {
    bar();
    seg<1, 1, true>(acc, Y, X, c, t + 1, 0);
    seg<1, 1, true>(acc, X, Y, c, t + 1, 1);
  }
  seg<1, 1, false>(acc, Y, X, c, 49, 1);
  STAMP(3,9);
  {
    const unsigned pf = O_PF + (((2 * tp + (w >> 1)) * 72 + 9 * (fp >> 1)) * 4 + 2 * (w & 1)) * 1024;
    const unsigned po = lane * 16 + pf + (fp & 1) * 16384;
#pragma unroll
    for (int m = 0; m < 2; ++m) {
      const f32x2 r2 = {lrs[m], lrs[m]}, b2 = {bs[m], bs[m]};
      auto pp = [&](int n) { return u32x2{pk2(__builtin_elementwise_fma(lo2(acc[m][n]), r2, b2) * up2(z1[m][n][0])), pk2(__builtin_elementwise_fma(hi2(acc[m][n]), r2, b2) * up2(z1[m][n][1]))}; };
#pragma unroll
      for (int a = 0; a < 4; ++a) {
        u32x2 p0 = pp(2 * a), p1 = pp(2 * a + 1);
        st16(R, po, a * 4096 + m * 1024, u32x4{p0[0], p0[1], p1[0], p1[1]});
      }
      st8(R, lane * 16, pf + 8 * 4096 + m * 1024 + (fp & 1) * 8, pp(8));
    }
  }
  STAMP(3,10);
  arrive(2, 4, O_PFL + tp * 2048 + fp * 8, 128, 16);
  const int th = w & 1, kq = w >> 1, T = 4 * t64 + 2 * th + kq;
  const int lane5 = fresh_lane();
#define lane lane5
#define li (lane5 & 15)
#define q (lane5 >> 4)
#define prow (8 * (li >> 2) + (li & 3))
  u32x2 hv[5];
#pragma unroll
  for (int n = 0; n < 5; ++n) {
    const int dd = 72 * j3 + 16 * n + 4 * (n == 4 ? (q & 1) : q);
    hv[n] = ld8(R, (dd >> 5) * 8192 + ((dd >> 2) & 1) * 512 + (((dd >> 3) & 3) * 16 + li) * 8, O_HF + ((T >> 3) * 18 * 8 + (T & 7)) * 1024);
  }
  struct F3 { u32x4 p[2][2], n[9]; };
  f32x4 a3[2][6] = {};
  F3 U, V;
  const int ap = (8 * kq + 2 * th) * 1024 + lane * 16, aw = 16384 + kq * 9216 + lane * 16;
  auto ld3 = [&](F3& f, int t) {
    const char* slot = sm + (t & 3) * STG;
#pragma unroll
    for (int g = 0; g < 2; ++g)
#pragma unroll
      for (int m = 0; m < 2; ++m) f.p[g][m] = *(const u32x4*)(slot + ap + (4 * g + m) * 1024);
#pragma unroll
    for (int n = 0; n < 9; ++n) f.n[n] = *(const u32x4*)(slot + aw + n * 1024);
  };
  auto mm3 = [&](const F3& f) {
#pragma unroll
    for (int g = 0; g < 2; ++g)
#pragma unroll
      for (int m = 0; m < 2; ++m) {
#pragma unroll
        for (int n = 0; n < 4; ++n) a3[m][n] = mfma_bf(f.n[4 * g + n], f.p[g][m], a3[m][n]);
        a3[m][4 + g] = mfma_bf(f.n[8], f.p[g][m], a3[m][4 + g]);
      }
#pragma unroll
    for (int i = 0; i < 13; ++i) { __builtin_amdgcn_sched_group_barrier(0x008, 1, 0); __builtin_amdgcn_sched_group_barrier(0x100, 1, 0); }
    __builtin_amdgcn_sched_barrier(0);
  };
  STAMP(3,11);
  bar();
  STAMP(3,12);
  ld3(U, 50);
#pragma unroll 1
  for (int t = 50; t < 66; t += 2) { bar(); ld3(V, t + 1); mm3(U); bar(); ld3(U, t + 2); mm3(V); }
  bar(); ld3(V, 67); mm3(U); mm3(V);
  STAMP(3,13);
#pragma unroll
  for (int m = 0; m < 2; ++m) a3[m][4] = lane < 32 ? a3[m][4] : a3[m][5];
  if (kq) {
#pragma unroll
    for (int n = 0; n < 5; ++n) { *(f32x4*)(sm + w * 5120 + n * 1024 + lane * 16) = a3[0][n]; asm volatile("" : "+v"(a3[1][n])); a3[0][n] = a3[1][n]; }
  } else {
#pragma unroll
    for (int n = 0; n < 5; ++n) *(f32x4*)(sm + w * 5120 + n * 1024 + lane * 16) = a3[1][n];
  }
  bar();
  char* o = out + (size_t)(32 * (T >> 1) + 4 * (T & 1) + prow) * 1152 + (72 * j3 + 4 * q) * 2;
#pragma unroll
  for (int n = 0; n < 5; ++n) {
    f32x4 v = a3[0][n] + *(const f32x4*)(sm + (w ^ 2) * 5120 + n * 1024 + lane * 16);
    if (n == 4) v = f32x4{half_sum(v[0]), half_sum(v[1]), half_sum(v[2]), half_sum(v[3])};
    if (n < 4 || q < 2) *(u32x2*)(o + n * 32) = u32x2{pk2(up2(hv[n][0]) + lo2(v)), pk2(up2(hv[n][1]) + hi2(v))};
  }
  if (bid == 0 && w == 0 && lane == 0) { u64 e = (((u64)tag[1] << 32) | tag[0]) - 0x9E3779B97F4A7C15ull + 1; st8(R, 0, O_EP, u32x2{(unsigned)e, (unsigned)(e >> 32)}); }
  STAMP(3,14);
}

#undef lane
#undef li
#undef q
#undef prow
extern "C" void kernel_launch(void* const* d_in, const int* in_sizes, int n_in, void* d_out, int out_size, void* d_ws, size_t ws_size,
                              hipStream_t stream) {
  (void)hipFuncSetAttribute((const void*)k_all, hipFuncAttributeMaxDynamicSharedMemorySize, LDS_BYTES);
  k_all<<<256, 512, LDS_BYTES, stream>>>((const char*)d_in[0], (const char*)d_in[1], (const char*)d_in[2], (const char*)d_in[3],
      (const char*)d_in[4], (const char*)d_in[5], (const char*)d_in[6], (const char*)d_in[7], (const char*)d_in[8], (const char*)d_in[10],
      (const char*)d_in[11], (const char*)d_in[12], (const char*)d_in[13], (char*)d_out, (char*)d_ws);
}
```

```cpp
#include <hip/hip_runtime.h>
#include <hip/hip_bf16.h>
#include <stdint.h>

typedef __attribute__((ext_vector_type(8))) __bf16 bf16x8;
typedef __attribute__((ext_vector_type(2))) __bf16 bf16x2;
typedef __attribute__((ext_vector_type(8))) _Float16 f16x8;
typedef __attribute__((ext_vector_type(2))) _Float16 f16x2;
typedef __attribute__((ext_vector_type(4))) float f32x4;
typedef __attribute__((ext_vector_type(2))) float f32x2;
typedef __attribute__((ext_vector_type(4))) unsigned u32x4;
typedef __attribute__((ext_vector_type(2))) unsigned u32x2;
typedef __attribute__((ext_vector_type(4))) short s16x4;
#define LDSP __attribute__((address_space(3)))
#define DEVI __device__ __forceinline__

constexpr int L = 2048, D = 576, NM = 128, F = 4608, FH = 2304;
constexpr float EPS = 1e-5f;
constexpr unsigned O_EP = 0;
constexpr int NREP = 32;
constexpr unsigned O_F1 = 256;
constexpr unsigned O_F2 = O_F1 + NREP * 1024;
constexpr unsigned F2S = 2048;
constexpr unsigned O_ZFL = O_F2 + NREP * F2S;
constexpr unsigned O_PFL = O_ZFL + 32768;
constexpr unsigned O_CS = 188416;
static_assert(O_PFL + 32768 <= O_CS, "ws");
constexpr unsigned O_ST = O_CS + F * 12;
constexpr unsigned O_HS = O_ST + 262144;
constexpr unsigned O_U = O_HS + 16384;
constexpr unsigned O_XB = O_U + L * D * 2;
constexpr unsigned O_WI = O_XB + L * D * 2;
constexpr unsigned O_WO = O_WI + D * F * 2;
constexpr unsigned O_HF = O_WO + FH * D * 2;
constexpr unsigned O_ZF = O_HF + L * D * 2;
constexpr unsigned O_PF = O_ZF + L * FH * 2;
constexpr unsigned WS_USED = O_PF + L * FH * 2;
constexpr size_t O_STAMP = 128u << 20;
#define STAMP(k,i) __builtin_amdgcn_sched_barrier(0)
#define STAMPW(k,i,W) __builtin_amdgcn_sched_barrier(0)
#define RSTAMP(k,i)
constexpr unsigned SENT = 0xFFFFFFFFu;

DEVI unsigned pk_bf16(float a, float b) { f32x2 v = {a, b}; return __builtin_bit_cast(unsigned, __builtin_convertvector(v, bf16x2)); }
DEVI unsigned pk_f16(float a, float b) { f32x2 v = {a, b}; return __builtin_bit_cast(unsigned, __builtin_convertvector(v, f16x2)); }
DEVI float bf_lo(unsigned u) { return __builtin_bit_cast(float, u << 16); }
DEVI float bf_hi(unsigned u) { return __builtin_bit_cast(float, u & 0xFFFF0000u); }
DEVI float asf(unsigned u) { return __builtin_bit_cast(float, u); }
DEVI unsigned asu(float f) { return __builtin_bit_cast(unsigned, f); }
DEVI float rbf(float a) { return bf_lo(pk_bf16(a, a)); }
DEVI float h_lo(unsigned u) { return (float)__builtin_bit_cast(f16x2, u)[0]; }
DEVI float h_hi(unsigned u) { return (float)__builtin_bit_cast(f16x2, u)[1]; }
template <int CTRL> DEVI float dpp_add(float v) {
  return v + __builtin_bit_cast(float, __builtin_amdgcn_update_dpp(0, __builtin_bit_cast(int, v), CTRL, 0xF, 0xF, true));
}
DEVI float row16_sum(float v) { v = dpp_add<0x128>(v); v = dpp_add<0x124>(v); v = dpp_add<0x122>(v); v = dpp_add<0x121>(v); return v; }
DEVI float half_sum(float v) { auto r = __builtin_amdgcn_permlane32_swap(asu(v), asu(v), false, false); return asf(r[0]) + asf(r[1]); }
DEVI float q_sum(float v) { auto r = __builtin_amdgcn_permlane16_swap(asu(v), asu(v), false, false); return half_sum(asf(r[0]) + asf(r[1])); }
DEVI unsigned umax4(u32x4 v) { return max(max(v[0], v[1]), max(v[2], v[3])); }
DEVI u32x2 tr_read(const char* p) {
  return __builtin_bit_cast(u32x2, __builtin_amdgcn_ds_read_tr16_b64_v4i16((LDSP s16x4*)p));
}
DEVI f32x4 mfma_bf(u32x4 a, u32x4 b, f32x4 c) {
  return __builtin_amdgcn_mfma_f32_16x16x32_bf16(__builtin_bit_cast(bf16x8, a), __builtin_bit_cast(bf16x8, b), c, 0, 0, 0);
}
DEVI f32x4 mfma_h(u32x4 a, u32x4 b, f32x4 c) {
  return __builtin_amdgcn_mfma_f32_16x16x32_f16(__builtin_bit_cast(f16x8, a), __builtin_bit_cast(f16x8, b), c, 0, 0, 0);
}
DEVI void ln_scalars(float mean, float var, float& mb, float& rinv) {
  mb = rbf(mean);
  float d = rbf(__builtin_amdgcn_sqrtf(rbf(rbf(var) + EPS)));
  rinv = __builtin_amdgcn_rcpf(d);
}
DEVI float gelu(float x) {
  float w = x * __builtin_fmaf(x * x, -0.10294324f, -2.3022082f);
  return x * __builtin_amdgcn_rcpf(1.0f + __builtin_amdgcn_exp2f(w));
}

typedef unsigned long long u64;
typedef __amdgpu_buffer_rsrc_t rsrc_t;
constexpr int SPIN = 1 << 16;
constexpr int CODE_LINES = 38;
template <int CTRL> DEVI float dpp_get(float v) {
  return __builtin_bit_cast(float, __builtin_amdgcn_update_dpp(0, __builtin_bit_cast(int, v), CTRL, 0xF, 0xF, true));
}
constexpr float GC1 = -0.10294324f, GC0 = -2.3022082f;
DEVI f32x2 gelu2(f32x2 x, f32x2 k1, f32x2 k0) {
  f32x2 w = x * __builtin_elementwise_fma(x * x, k1, k0);
  f32x2 d = f32x2{__builtin_amdgcn_exp2f(w[0]), __builtin_amdgcn_exp2f(w[1])} + f32x2{1.f, 1.f};
  return x * f32x2{__builtin_amdgcn_rcpf(d[0]), __builtin_amdgcn_rcpf(d[1])};
}
DEVI unsigned pk2(f32x2 v) { return __builtin_bit_cast(unsigned, __builtin_convertvector(v, bf16x2)); }
DEVI f32x2 up2(unsigned u) { return f32x2{bf_lo(u), bf_hi(u)}; }
DEVI f32x2 lo2(f32x4 v) { return __builtin_shufflevector(v, v, 0, 1); }
DEVI f32x2 hi2(f32x4 v) { return __builtin_shufflevector(v, v, 2, 3); }
DEVI float dot2(unsigned a, unsigned b, float c) {
  return __builtin_amdgcn_fdot2_f32_bf16(__builtin_bit_cast(bf16x2, a), __builtin_bit_cast(bf16x2, b), c, false);
}
DEVI void st16(rsrc_t r, unsigned voff, unsigned soff, u32x4 v) { __builtin_amdgcn_raw_buffer_store_b128(v, r, voff + soff, 0, 16); }
DEVI void st8(rsrc_t r, unsigned voff, unsigned soff, u32x2 v) { __builtin_amdgcn_raw_buffer_store_b64(v, r, voff, soff, 16); }
DEVI void st4(rsrc_t r, unsigned voff, unsigned soff, unsigned v) { __builtin_amdgcn_raw_buffer_store_b32(v, r, voff, soff, 16); }
DEVI u32x4 ld16(rsrc_t r, unsigned voff, unsigned soff) { return __builtin_amdgcn_raw_buffer_load_b128(r, voff, soff, 16); }
DEVI u32x2 ld8(rsrc_t r, unsigned voff, unsigned soff) { return __builtin_amdgcn_raw_buffer_load_b64(r, voff, soff, 16); }
template <int N> DEVI void wait_vm() { asm volatile("s_waitcnt vmcnt(%0)" ::"i"(N) : "memory"); }
DEVI void bar() { __builtin_amdgcn_sched_barrier(0); asm volatile("s_waitcnt lgkmcnt(0)\n\ts_barrier" ::: "memory"); __builtin_amdgcn_sched_barrier(0); }
DEVI int fresh_lane() { int l; asm volatile("v_mbcnt_lo_u32_b32 %0, -1, 0\n\tv_mbcnt_hi_u32_b32 %0, -1, %0" : "=v"(l)); return l; }
DEVI unsigned lds_addr(const char* p) { return (unsigned)(uintptr_t)(LDSP const char*)p; }
template <int NL> DEVI void wait_flags(rsrc_t r, unsigned off, int n, u32x2 tag, int lane) {
#pragma unroll 1
  for (int spin = 0; spin < SPIN; ++spin) {
    bool ok = true;
#pragma unroll
    for (int k = 0; k < NL; ++k) {
      int i = min(lane + 64 * k, n / 2 - 1);
      u32x4 v = ld16(r, i * 16, off);
      ok = ok && v[0] == tag[0] && v[1] == tag[1] && v[2] == tag[0] && v[3] == tag[1];
    }
    if (__all(ok)) break;
    __builtin_amdgcn_s_sleep(1);
    asm volatile("" ::: "memory");
  }
  asm volatile("" ::: "memory");
}
template <bool SC> DEVI void glds4(unsigned m0v, const char* base, unsigned v0, unsigned v1, unsigned v2, unsigned v3) {
  unsigned keep;
  if (SC)
    asm volatile("s_mov_b32 %0, m0\n\ts_mov_b32 m0, %1\n\ts_nop 0\n\t"
                 "global_load_lds_dwordx4 %3, %2 sc1\n\tglobal_load_lds_dwordx4 %4, %2 offset:1024 sc1\n\t"
                 "global_load_lds_dwordx4 %5, %2 offset:2048 sc1\n\tglobal_load_lds_dwordx4 %6, %2 offset:3072 sc1\n\ts_mov_b32 m0, %0"
                 : "=&s"(keep) : "s"(m0v), "s"(base), "v"(v0), "v"(v1), "v"(v2), "v"(v3) : "memory");
  else
    asm volatile("s_mov_b32 %0, m0\n\ts_mov_b32 m0, %1\n\ts_nop 0\n\t"
                 "global_load_lds_dwordx4 %3, %2\n\tglobal_load_lds_dwordx4 %4, %2 offset:1024\n\t"
                 "global_load_lds_dwordx4 %5, %2 offset:2048\n\tglobal_load_lds_dwordx4 %6, %2 offset:3072\n\ts_mov_b32 m0, %0"
                 : "=&s"(keep) : "s"(m0v), "s"(base), "v"(v0), "v"(v1), "v"(v2), "v"(v3) : "memory");
}
DEVI void glds1(unsigned m0v, const char* base, unsigned v0) {
  unsigned keep;
  asm volatile("s_mov_b32 %0, m0\n\ts_mov_b32 m0, %1\n\ts_nop 0\n\tglobal_load_lds_dwordx4 %3, %2 sc1\n\ts_mov_b32 m0, %0"
               : "=&s"(keep) : "s"(m0v), "s"(base), "v"(v0) : "memory");
}
template <int NP> DEVI void ld_lin(unsigned dst, const char* src, unsigned l16) {
  glds4<true>(dst, src, l16, l16, l16, l16);
  glds4<true>(dst + 4096, src + 4096, l16, l16, l16, l16);
  if (NP == 9) glds1(dst + 8192, src + 8192, l16);
}

constexpr int TP = 1168;
constexpr int CV_K = 8 * 260 * 16, CV_U = 4576;
constexpr int C_U = 4 * CV_K, C_KD = C_U + 16 * TP, C_END = C_KD + 4 * 1536;
constexpr int KT = 528;
constexpr int STG = 34816, RING = 4 * STG;
constexpr int X_CS = RING, X_LN = X_CS + 1152, X_PV = X_LN + 1024, X_CNT = 158720;
constexpr int LDS_BYTES = X_CNT + 64 + 256;
static_assert(C_END <= X_CNT && 4 * CV_U <= 16 * TP && X_PV + 16384 <= X_CNT && LDS_BYTES <= 163840, "lds");
#define TPOLL 11
#define WDELAY 2
#define SDELAY 2
#define TCHK 12
#define TFLAG 14

struct Fr { u32x4 m[2], n[9]; };
struct Ctx { const char* sm; int am0[4], am1, an; };
template <int MK> DEVI void ldf(Fr& f, const Ctx& c, int t, int kk) {
  int so = (t & 3) * STG;
  asm volatile("" : "+s"(so));
  const char* slot = c.sm + so;
  if (MK == 0) {
#pragma unroll
    for (int m = 0; m < 2; ++m) {
      u32x2 a = *(const u32x2*)(slot + c.am0[2 * m] + kk * 8192), b = *(const u32x2*)(slot + c.am0[2 * m + 1] + kk * 8192);
      f.m[m] = u32x4{a[0], a[1], b[0], b[1]};
    }
  } else {
#pragma unroll
    for (int m = 0; m < 2; ++m) f.m[m] = *(const u32x4*)(slot + (c.am1 ^ (kk * 64)) + m * 2048);
  }
#pragma unroll
  for (int n = 0; n < 9; ++n) f.n[n] = *(const u32x4*)(slot + c.an + (kk * 9 + n) * 1024);
}
template <int MK, int ORI, bool LD, bool FIRST = false> DEVI void seg(f32x4 (&acc)[2][9], const Fr& cur, Fr& nxt, const Ctx& c, int t, int kk) {
  if (LD) ldf<MK>(nxt, c, t, kk);
#pragma unroll
  for (int m = 0; m < 2; ++m)
#pragma unroll
    for (int nn = 0; nn < 9; ++nn) {
      const int n = m ? 8 - nn : nn;
      const f32x4 ci = FIRST ? f32x4{0, 0, 0, 0} : acc[m][n];
      acc[m][n] = ORI == 0 ? mfma_bf(cur.m[m], cur.n[n], ci) : mfma_bf(cur.n[n], cur.m[m], ci);
    }
#pragma unroll
  for (int i = 0; i < (MK == 0 ? 13 : 11); ++i) { __builtin_amdgcn_sched_group_barrier(0x008, 1, 0); __builtin_amdgcn_sched_group_barrier(0x100, 1, 0); }
  __builtin_amdgcn_sched_barrier(0);
}
DEVI f32x2 cmul(f32x2 a, f32x2 b, f32x2 bp) {
  return __builtin_elementwise_fma(f32x2{a[1], a[1]}, bp, f32x2{a[0], a[0]} * b);
}
DEVI f32x2 perp(f32x2 b) { return f32x2{-b[1], b[0]}; }
DEVI f32x2 cexp(float ar, float ai, float l) {
  float m = __builtin_amdgcn_exp2f(ar * l), ph = __builtin_amdgcn_fractf(ai * l);
  return f32x2{m * __builtin_amdgcn_cosf(ph), m * __builtin_amdgcn_sinf(ph)};
}
DEVI unsigned pkh(f32x2 v) { return __builtin_bit_cast(unsigned, __builtin_convertvector(v, f16x2)); }
DEVI float ldbf(const char* p, int i) { return bf_lo(*(const unsigned short*)(p + 2 * i)); }

__global__ __launch_bounds__(512, 2) void k_all(const char* __restrict__ x, const char* __restrict__ a_re, const char* __restrict__ a_im,
    const char* __restrict__ b_re, const char* __restrict__ b_im, const char* __restrict__ c_re, const char* __restrict__ c_im,
    const char* __restrict__ log_dt, const char* __restrict__ dskip, const char* __restrict__ w_in, const char* __restrict__ w_sp,
    const char* __restrict__ b_sp, const char* __restrict__ w_out, char* __restrict__ out, char* __restrict__ ws) {
  extern __shared__ __attribute__((aligned(16))) char sm[];
  const int tid = threadIdx.x, lane = tid & 63, w = __builtin_amdgcn_readfirstlane(tid >> 6), li = lane & 15, q = lane >> 4;
  const int bid = blockIdx.x, xc = bid & 7, ii = bid >> 3;
  if (w == 7) {
    unsigned long long pc;
    asm volatile("s_getpc_b64 %0" : "=s"(pc));
    const char* cp = (const char*)(pc & ~255ull) + (ii & 7) * (CODE_LINES * 128);
    unsigned keep;
    asm volatile("s_mov_b32 %0, m0\n\ts_mov_b32 m0, %1\n\ts_nop 0\n\tglobal_load_lds_dword %3, %2\n\ts_mov_b32 m0, %0"
                 : "=&s"(keep) : "s"(lds_addr(sm) + X_CNT + 64), "s"(cp), "v"(min(lane, CODE_LINES - 1) * 128) : "memory");
  }
  if (tid == 0) *(u32x4*)(sm + X_CNT) = u32x4{0, 0, 0, 0};
  bar();
  asm volatile("" ::"s"(x), "s"(a_re), "s"(a_im), "s"(b_re), "s"(b_im), "s"(c_re), "s"(c_im), "s"(log_dt), "s"(dskip), "s"(w_in), "s"(w_out), "s"(ws));
  const rsrc_t R = __builtin_amdgcn_make_buffer_rsrc(ws, 0, WS_USED, 0x00020000);
  STAMP(0,0);
  const u32x2 ep = ld8(R, 0, O_EP);
  const bool is_conv = ii < 18;
  const int cg = xc * 18 + ii, rep = bid & (NREP - 1);
  auto mk_tag = [&]() {
    u64 e = (((u64)__builtin_amdgcn_readfirstlane(ep[1]) << 32) | (unsigned)__builtin_amdgcn_readfirstlane(ep[0])) + 0x9E3779B97F4A7C15ull;
    return u32x2{(unsigned)e, (unsigned)(e >> 32)};
  };
  u32x2 tag;
  auto arrive = [&](int slot, unsigned n, unsigned flag_off, unsigned stride, int nrep) {
    wait_vm<0>();
    const int lane = fresh_lane();
    unsigned old = 0;
    if (lane == 0) old = __hip_atomic_fetch_add((unsigned*)(sm + X_CNT) + slot, 1u, __ATOMIC_RELAXED, __HIP_MEMORY_SCOPE_WORKGROUP);
    if (__builtin_amdgcn_readfirstlane(old) == n - 1 && lane < nrep) st8(R, lane * stride, flag_off, tag);
  };
  if (is_conv) {
    const int ch = w & 3, dch = 4 * cg + ch;
    char* sk = sm + ch * CV_K;
    const char* su = sm + C_U + ch * CV_U;
    float sc = 0.f;
    if (w < 4) {
      const int pi = (dch * NM + 2 * lane) * 2;
      const unsigned v_ar = *(const unsigned*)(a_re + pi), v_ai = *(const unsigned*)(a_im + pi), v_br = *(const unsigned*)(b_re + pi),
                     v_bi = *(const unsigned*)(b_im + pi), v_cr = *(const unsigned*)(c_re + pi), v_ci = *(const unsigned*)(c_im + pi);
      const float p_dk = ldbf(dskip, dch);
      const float dt = rbf(__expf(ldbf(log_dt, dch)));
      tag = mk_tag();
      STAMP(0,1);
      const f32x2 ar = (up2(v_ar) * dt) * 1.44269504f, ai = (up2(v_ai) * dt) * 0.15915494f;
      const f32x2 br = up2(v_br) * dt, bi = up2(v_bi) * dt, cr = up2(v_cr), ci = up2(v_ci);
      const f32x2 cbr = cr * br - ci * bi, cbi = -(cr * bi + ci * br);
      float mx = fmaxf(fmaxf(fabsf(cbr[0]), fabsf(cbi[0])), fmaxf(fabsf(cbr[1]), fabsf(cbi[1])));
      mx = fmaxf(mx, dpp_get<0x128>(mx)); mx = fmaxf(mx, dpp_get<0x124>(mx)); mx = fmaxf(mx, dpp_get<0x122>(mx)); mx = fmaxf(mx, dpp_get<0x121>(mx));
      auto rl = [&](int l) { return asf(__builtin_amdgcn_readlane((int)asu(mx), l)); };
      sc = fmaxf(fmaxf(rl(0), rl(16)), fmaxf(rl(32), rl(48)));
      sc = fmaxf(sc, fabsf(p_dk) * (1.0f / 256));
      const float isc = sc > 0.f ? 1.0f / sc : 0.f;
      {
        struct C2 { f32x2 a, b; };
        char* tb = sk + lane * 8;
        auto put = [&](int row, C2 v) { *(u32x2*)(tb + row * KT) = u32x2{pkh(v.a), pkh(v.b)}; };
        auto mul = [&](C2 v, C2 u, C2 up) { return C2{cmul(v.a, u.a, up.a), cmul(v.b, u.b, up.b)}; };
        auto prp = [&](C2 v) { return C2{perp(v.a), perp(v.b)}; };
        auto ex = [&](float l) { return C2{cexp(ar[0], ai[0], l), cexp(ar[1], ai[1], l)}; };
        const C2 w1 = ex(1.f), w128 = ex(128.f);
        C2 w8 = ex(8.f); w8.a[1] = -w8.a[1]; w8.b[1] = -w8.b[1];
        const C2 w1p = prp(w1), w8p = prp(w8), w128p = prp(w128);
        C2 y = {f32x2{cbr[0], cbi[0]} * isc, f32x2{cbr[1], cbi[1]} * isc};
        C2 E[8]; E[0] = C2{f32x2{1.f, 0.f}, f32x2{1.f, 0.f}}; E[1] = w1;
#pragma unroll
        for (int e = 2; e < 8; ++e) E[e] = mul(E[e - 1], w1, w1p);
#pragma unroll
        for (int e = 0; e < 8; ++e) { put(e, E[e]); put(8 + e, e ? mul(E[e], w128, w128p) : w128); put(41 + e, y); y = mul(y, w8, w8p); }
        const C2 e2p = prp(E[2]), e4p = prp(E[4]), e6p = prp(E[6]);
        C2 H = w128;
#pragma unroll
        for (int a = 2; a <= 16; ++a) {
          H = mul(H, w128, w128p);
          if (a < 4) { put(8 + 4 * a, H); put(9 + 4 * a, mul(H, E[2], e2p)); put(10 + 4 * a, mul(H, E[4], e4p)); put(11 + 4 * a, mul(H, E[6], e6p)); }
          else if (a < 8) { put(16 + 2 * a, H); put(17 + 2 * a, mul(H, E[4], e4p)); }
          else put(24 + a, H);
          if (a < 10) { put(47 + a, y); if (a < 9) y = mul(y, w8, w8p); }
        }
      }
      asm volatile("" ::: "memory");
      STAMP(0,2);
      {
        f32x4 kk[3] = {};
        const char* pa = sk + li * KT + q * 16;
#pragma unroll
        for (int s = 0; s < 8; ++s) {
          u32x4 fb = *(const u32x4*)(pa + 41 * KT + s * 64);
#pragma unroll
          for (int t = 0; t < 3; ++t) kk[t] = mfma_h(*(const u32x4*)(pa + 16 * t * KT + s * 64), fb, kk[t]);
        }
        if (lane == 0) kk[0][0] += p_dk * isc;
        char* kd = sm + C_KD + ch * 1536;
        *(u32x2*)(kd + (128 * (q >> 1) + 4 * (q & 1) + 8 * li) * 2) = u32x2{pkh(lo2(kk[0])), pkh(hi2(kk[0]))};
        const int i1 = q < 2 ? 256 + 64 * q + 4 * li : 384 + 64 * (q - 2) + 2 * li;
        *(unsigned*)(kd + i1 * 2) = pkh(lo2(kk[1])); *(unsigned*)(kd + (i1 + (q < 2 ? 2 : 32)) * 2) = pkh(hi2(kk[1]));
#pragma unroll
        for (int r = 0; r < 4; ++r) *(_Float16*)(kd + (512 + 16 * (4 * q + r) + li) * 2) = (_Float16)kk[2][r];
      }
      asm volatile("" ::: "memory");
      u32x4 c0[4];
      {
        const unsigned short* kd = (const unsigned short*)(sm + C_KD + ch * 1536);
#pragma unroll
        for (int rr = 0; rr < 2; ++rr) {
          const int k = 255 - 64 * rr - lane;
          const unsigned a = kd[384 + k], b = kd[385 + k], aa = a | (a << 16);
          c0[rr] = u32x4{b | (b << 16), b | (((k & 1) ? b : a) << 16), aa, aa};
        }
        { const int k = 127 - lane;
          const unsigned ab = *(const unsigned*)(kd + 256 + 2 * k), cc = kd[258 + 2 * k];
          c0[2] = u32x4{cc | (cc << 16), __builtin_amdgcn_perm(ab, ab, 0x03020302), __builtin_amdgcn_perm(ab, ab, 0x01000302), __builtin_amdgcn_perm(ab, ab, 0x01000100)}; }
        { const int k = 63 - lane;
          const u32x4 v = *(const u32x4*)(kd + 8 * (k & 31));
          const u32x2 u = *(const u32x2*)(kd + 128 + 4 * k); const unsigned e = kd[132 + 4 * k];
          c0[3] = lane < 32 ? u32x4{__builtin_amdgcn_perm(u[1], e, 0x07060100), __builtin_amdgcn_perm(u[1], u[1], 0x01000100), __builtin_amdgcn_perm(u[0], u[1], 0x07060100), __builtin_amdgcn_perm(u[0], u[0], 0x01000100)}
                            : u32x4{__builtin_amdgcn_alignbit(v[3], v[3], 16), __builtin_amdgcn_alignbit(v[2], v[2], 16), __builtin_amdgcn_alignbit(v[1], v[1], 16), __builtin_amdgcn_alignbit(v[0], v[0], 16)}; }
#pragma unroll
        for (int rr = 0; rr < 4; ++rr) *(u32x4*)(sk + (64 * rr + lane) * 16) = c0[rr];
        if (lane < 8) *(u32x4*)(sk + (lane * 260 + 256) * 16) = u32x4{0, 0, 0, 0};
      }
      asm volatile("" ::: "memory");
      STAMP(0,3);
      u32x4 c1[4];
#pragma unroll
      for (int rr = 0; rr < 4; ++rr) c1[rr] = *(const u32x4*)(sk + (64 * rr + lane) * 16 + 16);
#pragma unroll
      for (int rr = 0; rr < 4; ++rr) {
        char* p = sk + (64 * rr + lane) * 16;
        unsigned e[8] = {c0[rr][0], c0[rr][1], c0[rr][2], c0[rr][3], c1[rr][0], c1[rr][1], c1[rr][2], c1[rr][3]}, o[7];
#pragma unroll
        for (int i = 0; i < 7; ++i) o[i] = __builtin_amdgcn_alignbit(e[i + 1], e[i], 16);
#pragma unroll
        for (int s = 0; s < 4; ++s) {
          if (s) *(u32x4*)(p + (2 * s) * 260 * 16) = u32x4{e[s], e[s + 1], e[s + 2], e[s + 3]};
          *(u32x4*)(p + (2 * s + 1) * 260 * 16) = u32x4{o[s], o[s + 1], o[s + 2], o[s + 3]};
        }
      }
      STAMP(0,4);
    } else {
      char* s0 = sm + C_U;
      __builtin_amdgcn_s_setprio(2);
      u32x2 xr[9];
      const char* src = x + (size_t)(16 * min(cg, 127) + 4 * ch + q) * 1152 + li * 8;
#pragma unroll
      for (int k = 0; k < 9; ++k) xr[k] = *(const u32x2*)(src + k * 128);
      tag = mk_tag();
      if (cg < 128) {
        asm volatile("" :: "v"(xr[8])); STAMPW(1,0,4);
#pragma unroll
        for (int k = 0; k < 9; ++k) st8(R, li * 32 + q * 8 + k * 512, O_XB + (4 * cg + ch) * 4608, xr[k]);
        float a[4] = {0, 0, 0, 0};
#pragma unroll
        for (int k = 0; k < 9; ++k)
#pragma unroll
          for (int e = 0; e < 2; ++e) { a[e] = dot2(xr[k][e], 0x3F803F80u, a[e]); a[2 + e] = dot2(xr[k][e], xr[k][e], a[2 + e]); }
        const float s = row16_sum(a[0] + a[1]), ss = row16_sum(a[2] + a[3]);
        float mean = s * (1.0f / D), mb, rinv;
        ln_scalars(mean, fmaxf(ss * (1.0f / D) - mean * mean, 0.f), mb, rinv);
        const f32x2 r2 = {rinv, rinv}, b2 = {-mb * rinv, -mb * rinv};
        char* p = s0 + (4 * ch + q) * TP + li * 8;
#pragma unroll
        for (int k = 0; k < 9; ++k)
          *(u32x2*)(p + k * 128) = u32x2{pkh(__builtin_elementwise_fma(up2(xr[k][0]), r2, b2)), pkh(__builtin_elementwise_fma(up2(xr[k][1]), r2, b2))};
        asm volatile("" ::: "memory");
        unsigned* cnt = (unsigned*)(sm + X_CNT) + 3;
        if (lane == 0) __hip_atomic_fetch_add(cnt, 1u, __ATOMIC_RELAXED, __HIP_MEMORY_SCOPE_WORKGROUP);
#pragma unroll 1
        for (int spin = 0; spin < (SPIN << 4) && __builtin_amdgcn_readfirstlane(__hip_atomic_load(cnt, __ATOMIC_RELAXED, __HIP_MEMORY_SCOPE_WORKGROUP)) < 4; ++spin) {}
        asm volatile("" ::: "memory");
        STAMPW(1,1,4);
        const char* trb = s0 + (8 * (q & 1) + (li >> 2)) * TP + (q >> 1) * 32 + (li & 3) * 8 + ch * 64;
        const unsigned uo = (16 * (q >> 1) + li) * 32 + (q & 1) * 16 + O_U + cg * 18432 + ch * 1024;
#pragma unroll
        for (int k = 0; k < 5; ++k)
          if (k < 4 || ch < 2) {
            u32x2 lo = tr_read(trb + k * 256), hi = tr_read(trb + 4 * TP + k * 256);
            st16(R, uo, k * 4096, u32x4{lo[0], lo[1], hi[0], hi[1]});
          }
        STAMPW(1,2,4);
        arrive(0, 4, O_F1 + cg * 8, 1024, NREP);
        STAMPW(1,3,4);
      }
      __builtin_amdgcn_s_setprio(0);
      if (cg < 136 && w >= 6) st16(R, (tid - 384) * 16, O_ST + cg * 2048, u32x4{SENT, SENT, SENT, SENT});
      if (w == 4) {
        wait_flags<1>(R, O_F1 + rep * 1024, 128, tag, lane);
        STAMPW(5,0,4);
        const unsigned v0 = (lane >> 1) * 18432 + (lane & 1) * 16, dv = 32 * 18432 - 1024;
#pragma unroll
        for (int i = 0; i < 4; ++i) glds4<true>(lds_addr(sm) + C_U + i * CV_U + 480, ws + O_U + (4 * cg + i) * 32, v0, v0 + dv, v0 + 2 * dv, v0 + 3 * dv);
#pragma unroll
        for (int i = 0; i < 4; ++i) if (lane < 30) *(u32x4*)(sm + C_U + i * CV_U + lane * 16) = u32x4{0, 0, 0, 0};
        wait_vm<0>();
      }
    }
    bar();
    STAMP(0,5);
    u32x4 xv[4];
    if (w < 4) {
      f32x4 acc[8] = {};
      const char* pa = sk + ((7 - (li & 7)) * 260 + 253 - (li >> 3) + q) * 16 - 4032;
      const char* pb = su + li * 32 + q * 16;
      struct CF { u32x4 a[8], b[8]; } A, B;
      auto ldc = [&](CF& f, int a0) {
#pragma unroll
        for (int m = 0; m < 8; ++m) { f.a[m] = *(const u32x4*)(pa + 4032 - 64 * a0 - 512 * m); f.b[m] = *(const u32x4*)(pb + 448 - 64 * a0 + 512 * m); }
      };
      auto mmc = [&](const CF& f) {
#pragma unroll
        for (int m = 0; m < 8; ++m)
#pragma unroll
          for (int t = m; t < 8; ++t) acc[t] = mfma_h(f.a[m], f.b[t - m], acc[t]);
#pragma unroll
        for (int i = 0; i < 16; ++i) { __builtin_amdgcn_sched_group_barrier(0x008, 2, 0); __builtin_amdgcn_sched_group_barrier(0x100, 1, 0); }
        __builtin_amdgcn_sched_barrier(0);
      };
      ldc(A, 0);
      __builtin_amdgcn_sched_barrier(0);
#pragma unroll
      for (int a0 = 0; a0 < 8; a0 += 2) { ldc(B, a0 + 1); mmc(A); if (a0 < 6) ldc(A, a0 + 2); mmc(B); }
      STAMP(0,6);
#pragma unroll
      for (int t = 0; t < 8; ++t) *(u32x2*)(sk + t * 512 + li * 32 + q * 8) = u32x2{pk2(lo2(acc[t]) * sc), pk2(hi2(acc[t]) * sc)};
    } else {
#pragma unroll
      for (int r = 0; r < 4; ++r) {
        const int T = ((tid & 255) >> 3) + 32 * r, ip = tid & 7;
        xv[r] = ld16(R, (8 * (T >> 1) + 2 * (ip >> 1) + (T & 1)) * 4608 + (ip & 1) * 16, O_XB + cg * 32);
      }
      STAMPW(2,0,4);
    }
    bar();
    if (w >= 4) {
#pragma unroll
      for (int r = 0; r < 4; ++r) {
        const int T = ((tid & 255) >> 3) + 32 * r, ip = tid & 7;
        const char* p = sm + (32 * (T >> 1) + 8 * (ip >> 1) + 4 * (T & 1) + 2 * (ip & 1)) * 2;
        const unsigned c0 = *(const unsigned*)p, c1 = *(const unsigned*)(p + CV_K), c2 = *(const unsigned*)(p + 2 * CV_K), c3 = *(const unsigned*)(p + 3 * CV_K);
        u32x4 v = {pk2(up2(xv[r][0]) + f32x2{bf_lo(c0), bf_lo(c1)}), pk2(up2(xv[r][1]) + f32x2{bf_lo(c2), bf_lo(c3)}),
                   pk2(up2(xv[r][2]) + f32x2{bf_hi(c0), bf_hi(c1)}), pk2(up2(xv[r][3]) + f32x2{bf_hi(c2), bf_hi(c3)})};
        st16(R, (T >> 3) * (18 * 8192) + (T & 7) * 1024 + ip * 16, O_HF + (cg >> 3) * 8192 + (cg & 1) * 512 + ((cg >> 1) & 3) * 128, v);
      }
      STAMPW(2,1,4);
      arrive(1, 4, O_F2 + cg * 8, F2S, NREP);
      STAMPW(2,2,4);
    }
  } else {
    const int tj = (ii - 18) * 8 + xc;
    const int kc = tj / 36, cb = tj % 36;
    for (int i = 0; i < WDELAY; ++i) __builtin_amdgcn_s_sleep(16);
    char* s0 = sm + 65536;
    const int G = max(tj - 40, 0), par = G & 1;
    {
      const int row = tid >> 4, uu = G / 9, idx = G % 9, hh = row >> 4;
      const int panel = idx < 4 ? 2 * uu : (idx < 8 ? 2 * uu + 1 : 2 * uu + hh), tile = idx < 8 ? 2 * (idx & 3) + hh : 8;
      const char* src = w_in + (size_t)(192 * min(kc, 2) + row) * 9216 + cb * 256 + li * 16;
      const char* wsrc = w_out + (size_t)(144 * panel + 16 * tile + (row & 15)) * 1152 + li * 8;
      u32x4 v[6]; u32x2 vo[9];
#pragma unroll
      for (int k = 0; k < 6; ++k) v[k] = *(const u32x4*)(src + (size_t)k * 32 * 9216);
      if (tj >= 40) {
#pragma unroll
        for (int k = 0; k < 9; ++k) vo[k] = *(const u32x2*)(wsrc + k * 128);
      }
#pragma unroll
      for (int k = 0; k < 6; ++k) *(u32x4*)(sm + (row + 32 * k) * 272 + li * 16) = v[k];
      if (tj >= 40) {
#pragma unroll
        for (int k = 0; k < 9; ++k) *(u32x2*)(s0 + row * TP + li * 8 + k * 128) = vo[k];
      }
    }
    tag = mk_tag();
    if (w == 0) {
      wait_flags<1>(R, O_F1 + rep * 1024, 128, tag, lane);
      for (int i = 0; i < SDELAY; ++i) __builtin_amdgcn_s_sleep(16);
    }
    bar();
    STAMP(4,0);
    if (tj < 108) {
      const int ct = 8 * cb + w;
      const int hf = ct < 144 ? 1 : 0, cl = ct < 144 ? ct : ct - 144, fp = cl / 9, n = cl % 9;
      const char* trb = sm + (8 * q + (li >> 2)) * 272 + w * 32 + (li & 3) * 8;
      const unsigned dst = lane * 16 + O_WI + (((fp * 2 + hf) * 18 + 6 * kc) * 9 + n) * 1024;
      float s = 0.f;
#pragma unroll
      for (int g = 0; g < 6; ++g) {
        u32x2 lo = tr_read(trb + g * 32 * 272), hi = tr_read(trb + g * 32 * 272 + 4 * 272);
        st16(R, dst, g * 9216, u32x4{lo[0], lo[1], hi[0], hi[1]});
        s = dot2(lo[0], 0x3F803F80u, s); s = dot2(lo[1], 0x3F803F80u, s); s = dot2(hi[0], 0x3F803F80u, s); s = dot2(hi[1], 0x3F803F80u, s);
      }
      s = q_sum(s);
      if (q == 0) st4(R, li * 4, O_CS + kc * F * 4 + ct * 64, asu(s));
    }
    if (tj >= 40) {
      const char* trb = s0 + (4 * q + (li >> 2)) * TP + (li & 3) * 8;
      const unsigned dst = lane * 16 + O_WO + (w * 36 + (G >> 1)) * 9216;
#pragma unroll
      for (int n = 0; n < 5; ++n) {
        const int c0 = 72 * w + 16 * n - (n == 4 ? 8 * par : 0);
        u32x2 lo = tr_read(trb + c0 * 2), hi = tr_read(trb + 16 * TP + c0 * 2);
        if (n < 4) st16(R, dst, (4 * par + n) * 1024, u32x4{lo[0], lo[1], hi[0], hi[1]});
        else if ((li >> 3) == par) st16(R, dst, 8192, u32x4{lo[0], lo[1], hi[0], hi[1]});
      }
    }
    STAMP(4,1);
    arrive(0, 8, O_F2 + (144 + tj) * 8, F2S, NREP);
    STAMP(4,2);
  }
  const int tp = (xc & 1) * 8 + (ii >> 2), fp = (xc >> 1) * 4 + (ii & 3);
  const int t64 = 4 * xc + (ii >> 3), j3 = ii & 7;
  if (w == 0) { wait_flags<1>(R, O_F2 + rep * F2S + 144 * 8, 112, tag, lane); STAMPW(6,0,0); }
  bar();
  if (w >= 4) {
    const int lw = w - 4;
    const unsigned l16 = lane * 16, base = lds_addr(sm);
    const int ph = ((ii >> 2) + ii) & 3, ph3 = ((ii >> 3) + ii) & 3;
    auto k9 = [&](int t) { return (t & 3) == ph ? (t + 4 < 9 ? t + 4 : ph) : t; };
    auto k32 = [&](int t) { return (t & 3) == ph ? ((t + 4) & 31) : t; };
    auto k18 = [&](int t) { return (t & 3) == ph3 ? (t + 4 < 18 ? t + 4 : ph3) : t; };
    if (lw < 2) {
      constexpr int NP = 8;
      unsigned vo[8];
#pragma unroll
      for (int j = 0; j < 8; ++j) {
        int t = 64 * lw + 32 * (j >> 2) + 4 * ((j >> 1) & 1) + 16 * (j & 1) + 8 * (lane >> 5) + ((lane >> 3) & 3);
        vo[j] = t * 4096 + (((lane & 7) ^ ((lane >> 3) & 7)) * 16) + 3072 - (j & 3) * 1024;
      }
      const char* wsrc = w_sp + (size_t)tp * 128 * 4096 - 3072;
      const char* hsrc = ws + O_HF + tp * 18 * 8192 + lw * 8192;
      const char* psrc = ws + O_PF + t64 * 72 * 4096 + lw * 8192;
      const unsigned dst = base + lw * 8192;
      if (lw == 0) wait_flags<2>(R, O_F2 + rep * F2S, 144, tag, lane);
      STAMPW(5,1,4);
      bar();
#pragma unroll 1
      for (int t = 0; t < 4; ++t) ld_lin<NP>(dst + t * STG, hsrc + k9(t) * 16384, l16);
      wait_vm<3 * NP>(); bar();
#pragma unroll 1
      for (int t = 4; t < 18; ++t) { wait_vm<2 * NP>(); bar(); ld_lin<NP>(dst + (t & 3) * STG, hsrc + k9(t < 9 ? t : t - 9) * 16384, l16); }
#pragma unroll 1
      for (int t = 18; t < 50; ++t) {
        wait_vm<2 * NP>(); bar();
        const unsigned dd = dst + (t & 3) * STG; const char* sp = wsrc + k32(t - 18) * 128;
        glds4<false>(dd, sp, vo[0], vo[1], vo[2], vo[3]); glds4<false>(dd + 4096, sp, vo[4], vo[5], vo[6], vo[7]);
      }
      wait_vm<2 * NP>(); bar(); wait_vm<NP>(); bar(); wait_vm<0>(); bar();
      wait_flags<1>(R, O_PFL + (t64 >> 1) * 2048 + ((t64 & 1) * 8 + j3) * 128, 16, tag, lane);
      STAMPW(5,2,4);
#pragma unroll 1
      for (int t = 50; t < 68; ++t) {
        if (t >= 53) { wait_vm<2 * NP>(); bar(); }
        ld_lin<NP>(dst + (t & 3) * STG, psrc + k18(t - 50) * 16384, l16);
      }
      wait_vm<2 * NP>(); bar(); wait_vm<NP>(); bar(); wait_vm<0>(); bar();
    } else {
      constexpr int NP = 9;
      const char* wi = ws + O_WI + (fp * 2) * 18 * 9216 + (lw - 2) * 9216;
      const char* zs = ws + O_ZF + fp * 64 * 9216 + (lw - 2) * 9216;
      const char* wo = ws + O_WO + j3 * 36 * 9216 + (lw - 2) * 9216;
      const unsigned dst = base + 16384 + (lw - 2) * 9216;
#pragma unroll 1
      for (int t = 0; t < 4; ++t) ld_lin<NP>(dst + t * STG, wi + k9(t) * 18432, l16);
      bar();
      wait_vm<3 * NP>(); bar();
      if (lw == 3) {
        f32x2 a = *(const f32x2*)(sm + X_LN + lane * 8) + *(const f32x2*)(sm + X_LN + 512 + lane * 8);
        a[0] = q_sum(dpp_add<0x128>(a[0])); a[1] = q_sum(dpp_add<0x128>(a[1]));
        float mean = a[0] * (1.0f / D), mb, ri;
        ln_scalars(mean, fmaxf(a[1] * (1.0f / D) - mean * mean, 0.f), mb, ri);
        if (lane < 8) st8(R, lane * 8, O_HS + tp * 1024 + fp * 64, u32x2{asu(ri) == SENT ? 0x7FC00000u : asu(ri), asu(-mb)});
      }
#pragma unroll 1
      for (int t = 4; t < 18; ++t) { wait_vm<2 * NP>(); bar(); ld_lin<NP>(dst + (t & 3) * STG, wi + (t < 9 ? k9(t) : 9 + k9(t - 9)) * 18432, l16); }
      wait_vm<2 * NP>(); bar(); wait_vm<NP>(); bar(); wait_vm<0>(); bar();
      wait_flags<1>(R, O_ZFL + fp * 2048 + tp * 128, 16, tag, lane);
      STAMPW(6,1,6);
#pragma unroll 1
      for (int t = 18; t < 68; ++t) {
        if (t >= 21) { wait_vm<2 * NP>(); bar(); }
        ld_lin<NP>(dst + (t & 3) * STG, t < 50 ? zs + k32(t - 18) * 18432 : wo + k18(t - 50) * 18432, l16);
      }
      wait_vm<2 * NP>(); bar(); wait_vm<NP>(); bar(); wait_vm<0>(); bar();
    }
    return;
  }
  Ctx c;
  c.sm = sm;
#pragma unroll
  for (int i = 0; i < 4; ++i) { c.am0[i] = (2 * w + (i >> 1)) * 1024 + (i & 1) * 512 + lane * 8; asm volatile("" : "+v"(c.am0[i])); }
  c.am1 = 2 * w * 2048 + li * 128 + ((q ^ (lane & 7)) * 16);
  c.an = 16384 + lane * 16;
  const int prow = 8 * (li >> 2) + (li & 3);
  if (tid < 72) {
    const unsigned o = tid < 36 ? FH * 4 + tid * 16 : (tid - 36) * 16;
    const u32x4 c0 = ld16(R, o, O_CS + fp * 576), c1 = ld16(R, o, O_CS + F * 4 + fp * 576), c2 = ld16(R, o, O_CS + F * 8 + fp * 576);
    *(f32x4*)(sm + X_CS + tid * 16) = (__builtin_bit_cast(f32x4, c0) + __builtin_bit_cast(f32x4, c1)) + __builtin_bit_cast(f32x4, c2);
  }
  bar();
  char* pvb = sm + X_PV + w * 4096;
  {
    u32x4 hv[3];
#pragma unroll
    for (int k = 0; k < 3; ++k)
      hv[k] = ld16(R, (lane >> 5) * 8192 + ((lane >> 2) & 7) * 128 + (lane & 3) * 16, O_HF + (tp * 18 * 8 + (fp >> 1)) * 1024 + (fp & 1) * 64 + min(w + 4 * k, 8) * 16384);
    if (w) hv[2] = u32x4{0, 0, 0, 0};
    f32x4 a = {0, 0, 0, 0};
#pragma unroll
    for (int k = 0; k < 3; ++k)
#pragma unroll
      for (int e = 0; e < 4; ++e) { a[e & 2] = dot2(hv[k][e], 0x3F803F80u, a[e & 2]); a[(e & 2) + 1] = dot2(hv[k][e], hv[k][e], a[(e & 2) + 1]); }
#pragma unroll
    for (int e = 0; e < 4; ++e) a[e] = dpp_add<0x124>(dpp_add<0x128>(a[e]));
    if (li < 4) *(f32x4*)(sm + X_LN + (w * 4 + q) * 64 + li * 16) = a;
  }
  f32x4 acc[2][9];
  Fr X, Y;
  bar();
  STAMP(3,0);
  ldf<0>(X, c, 0, 0);
  seg<0, 0, true, true>(acc, X, Y, c, 0, 1);
#pragma unroll 1
  for (int t = 0; t < 8; ++t) {
    bar();
    seg<0, 0, true>(acc, Y, X, c, t + 1, 0);
    seg<0, 0, true>(acc, X, Y, c, t + 1, 1);
  }
  u32x2 ht[2]; u32x4 hc[4];
  auto ld_hs = [&]() {
#pragma unroll
    for (int m = 0; m < 2; ++m) {
      ht[m] = ld8(R, li * 8 + m * 128, O_HS + tp * 1024 + w * 256);
      hc[2 * m] = ld16(R, q * 32 + m * 128, O_HS + tp * 1024 + w * 256); hc[2 * m + 1] = ld16(R, q * 32 + 16 + m * 128, O_HS + tp * 1024 + w * 256);
    }
  };
  ld_hs();
  bar();
  seg<0, 0, true>(acc, Y, X, c, 9, 0);
  STAMP(3,1);
#pragma unroll 1
  for (int spin = 0; spin < SPIN; ++spin) {
    const unsigned mx = max(max(umax4(hc[0]), umax4(hc[1])), max(umax4(hc[2]), umax4(hc[3])));
    if (__all(max(mx, umax4(u32x4{ht[0][0], ht[0][1], ht[1][0], ht[1][1]})) != SENT)) break;
    __builtin_amdgcn_s_sleep(1);
    asm volatile("" ::: "memory");
    ld_hs();
  }
  const float lrs[2] = {asf(ht[0][0]), asf(ht[1][0])}, lnm[2] = {asf(ht[0][1]), asf(ht[1][1])};
  u32x4 zb[9];
  f32x2 rs[4];
  {
    float cs[9];
#pragma unroll
    for (int n = 0; n < 9; ++n) cs[n] = *(const float*)(sm + X_CS + (16 * n + li) * 4);
    f32x2 nm[4], k1[4], k0[4], S1[4] = {}, S2[4] = {};
#pragma unroll
    for (int jp = 0; jp < 4; ++jp) {
      rs[jp] = f32x2{asf(hc[jp][0]), asf(hc[jp][2])}; nm[jp] = f32x2{asf(hc[jp][1]), asf(hc[jp][3])};
      k0[jp] = rs[jp] * GC0; k1[jp] = rs[jp] * rs[jp] * rs[jp] * GC1;
    }
#pragma unroll
    for (int n = 0; n < 9; ++n) {
#pragma unroll
      for (int jp = 0; jp < 4; ++jp) {
        f32x2 a = (jp & 1) ? hi2(acc[jp >> 1][n]) : lo2(acc[jp >> 1][n]);
        f32x2 z = gelu2(__builtin_elementwise_fma(nm[jp], f32x2{cs[n], cs[n]}, a), k1[jp], k0[jp]);
        zb[n][jp] = pk2(z);
        S1[jp] += z; S2[jp] = __builtin_elementwise_fma(z, z, S2[jp]);
      }
      asm volatile("" : "+v"(zb[n]));
    }
#pragma unroll
    for (int jp = 0; jp < 4; ++jp) { S1[jp] *= rs[jp]; S2[jp] *= rs[jp] * rs[jp]; }
    float v[16];
#pragma unroll
    for (int j = 0; j < 8; ++j) { v[2 * j] = S1[j >> 1][j & 1]; v[2 * j + 1] = S2[j >> 1][j & 1]; }
#pragma unroll
    for (int i = 0; i < 8; ++i) { bool h = li & 8; v[i] = (h ? v[i + 8] : v[i]) + dpp_get<0x128>(h ? v[i] : v[i + 8]); }
#pragma unroll
    for (int i = 0; i < 4; ++i) { bool h = li & 4; v[i] = (h ? v[i + 4] : v[i]) + dpp_get<0x141>(h ? v[i] : v[i + 4]); }
#pragma unroll
    for (int i = 0; i < 2; ++i) { bool h = li & 2; v[i] = (h ? v[i + 2] : v[i]) + dpp_get<0x4E>(h ? v[i] : v[i + 2]); }
    { bool h = li & 1; v[0] = (h ? v[1] : v[0]) + dpp_get<0xB1>(h ? v[0] : v[1]); }
    unsigned tot = asu(v[0]);
    st4(R, lane * 4, O_ST + ((tp * 4 + w) * 16 + fp) * 256, tot == SENT ? 0x7FC00000u : tot);
  }
  STAMP(3,2);
  const char* sts = ws + O_ST + (tp * 4 + w) * 4096;
  seg<0, 1, true, true>(acc, X, Y, c, 9, 1);
#pragma unroll 1
  for (int t = 9; t < TCHK; ++t) {
    if (t == TPOLL) glds4<true>(lds_addr(pvb), sts, lane * 16, lane * 16, lane * 16, lane * 16);
    bar();
    seg<0, 1, true>(acc, Y, X, c, t + 1, 0);
    seg<0, 1, true>(acc, X, Y, c, t + 1, 1);
  }
  STAMP(3,3);
  u32x4 pv[4];
#pragma unroll 1
  for (int spin = 0; spin < SPIN; ++spin) {
    wait_vm<0>();
#pragma unroll
    for (int k = 0; k < 4; ++k) pv[k] = *(const u32x4*)(pvb + k * 1024 + lane * 16);
    if (__all(max(max(umax4(pv[0]), umax4(pv[1])), max(umax4(pv[2]), umax4(pv[3]))) != SENT)) break;
    glds4<true>(lds_addr(pvb), sts, lane * 16, lane * 16, lane * 16, lane * 16);
  }
  STAMP(3,4);
  {
    float t4[4];
#pragma unroll
    for (int e = 0; e < 4; ++e) t4[e] = q_sum((asf(pv[0][e]) + asf(pv[1][e])) + (asf(pv[2][e]) + asf(pv[3][e])));
    f32x2 o0, o1;
#pragma unroll
    for (int r = 0; r < 2; ++r) {
      float mean = t4[2 * r] * (1.0f / FH), var = fmaxf(t4[2 * r + 1] * (1.0f / FH) - mean * mean, 0.f), mb, ri;
      ln_scalars(mean, var, mb, ri);
      o0[r] = ri; o1[r] = -mb * ri;
    }
    char* ln = sm + X_LN + w * 256;
    if (lane < 16) { *(f32x2*)(ln + li * 8) = o0; *(f32x2*)(ln + 128 + li * 8) = o1; }
    asm volatile("" ::: "memory");
    f32x4 ri[2], rb[2];
#pragma unroll
    for (int m = 0; m < 2; ++m) { ri[m] = *(const f32x4*)(ln + (8 * q + 4 * m) * 4); rb[m] = *(const f32x4*)(ln + 128 + (8 * q + 4 * m) * 4); }
#pragma unroll
    for (int jp = 0; jp < 4; ++jp) rs[jp] *= (jp & 1) ? hi2(ri[jp >> 1]) : lo2(ri[jp >> 1]);
    const unsigned zo = lane * 16 + O_ZF + (fp * 64 + 4 * tp + w) * 9216;
#pragma unroll
    for (int n = 0; n < 9; ++n) {
      u32x4 o4;
#pragma unroll
      for (int jp = 0; jp < 4; ++jp)
        o4[jp] = pk2(__builtin_elementwise_fma(up2(zb[n][jp]), rs[jp], (jp & 1) ? hi2(rb[jp >> 1]) : lo2(rb[jp >> 1])));
      st16(R, zo, n * 1024, o4);
    }
  }
  STAMP(3,5);
#pragma unroll 1
  for (int t = TCHK; t < 17; ++t) {
    if (t == TFLAG) wait_vm<0>();
    bar();
    if (t == TFLAG && tid < 16) st8(R, tid * 128, O_ZFL + fp * 2048 + tp * 8, tag);
    seg<0, 1, true>(acc, Y, X, c, t + 1, 0);
    seg<0, 1, true>(acc, X, Y, c, t + 1, 1);
  }
  seg<0, 1, false>(acc, Y, X, c, 17, 1);
  STAMP(3,6);
  u32x2 z1[2][9];
  float bs[2];
#pragma unroll
  for (int m = 0; m < 2; ++m) bs[m] = ldbf(b_sp, 128 * tp + 32 * w + prow + 4 * m) * lrs[m];
  {
    f32x4 cs[9];
#pragma unroll
    for (int n = 0; n < 9; ++n) cs[n] = *(const f32x4*)(sm + X_CS + (144 + 16 * n + 4 * q) * 4);
#pragma unroll
    for (int n = 0; n < 9; ++n)
#pragma unroll
      for (int m = 0; m < 2; ++m) {
        const float a1 = lrs[m] * lrs[m] * lrs[m] * GC1, a0 = lrs[m] * GC0;
        const f32x2 k1 = {a1, a1}, k0 = {a0, a0}, nm = {lnm[m], lnm[m]};
        z1[m][n][0] = pk2(gelu2(__builtin_elementwise_fma(nm, lo2(cs[n]), lo2(acc[m][n])), k1, k0));
        z1[m][n][1] = pk2(gelu2(__builtin_elementwise_fma(nm, hi2(cs[n]), hi2(acc[m][n])), k1, k0));
        asm volatile("" : "+v"(z1[m][n]));
      }
  }
  STAMP(3,7);
  bar();
  STAMP(3,8);
  ldf<1>(X, c, 18, 0);
  seg<1, 1, true, true>(acc, X, Y, c, 18, 1);
#pragma unroll 1
  for (int t = 18; t < 49; ++t) {
    bar();
    seg<1, 1, true>(acc, Y, X, c, t + 1, 0);
    seg<1, 1, true>(acc, X, Y, c, t + 1, 1);
  }
  seg<1, 1, false>(acc, Y, X, c, 49, 1);
  STAMP(3,9);
  {
    const unsigned pf = O_PF + (((2 * tp + (w >> 1)) * 72 + 9 * (fp >> 1)) * 4 + 2 * (w & 1)) * 1024;
    const unsigned po = lane * 16 + pf + (fp & 1) * 16384;
#pragma unroll
    for (int m = 0; m < 2; ++m) {
      const f32x2 r2 = {lrs[m], lrs[m]}, b2 = {bs[m], bs[m]};
      auto pp = [&](int n) { return u32x2{pk2(__builtin_elementwise_fma(lo2(acc[m][n]), r2, b2) * up2(z1[m][n][0])), pk2(__builtin_elementwise_fma(hi2(acc[m][n]), r2, b2) * up2(z1[m][n][1]))}; };
#pragma unroll
      for (int a = 0; a < 4; ++a) {
        u32x2 p0 = pp(2 * a), p1 = pp(2 * a + 1);
        st16(R, po, a * 4096 + m * 1024, u32x4{p0[0], p0[1], p1[0], p1[1]});
      }
      st8(R, lane * 16, pf + 8 * 4096 + m * 1024 + (fp & 1) * 8, pp(8));
    }
  }
  STAMP(3,10);
  arrive(2, 4, O_PFL + tp * 2048 + fp * 8, 128, 16);
  const int th = w & 1, kq = w >> 1, T = 4 * t64 + 2 * th + kq;
  const int lane5 = fresh_lane();
#define lane lane5
#define li (lane5 & 15)
#define q (lane5 >> 4)
#define prow (8 * (li >> 2) + (li & 3))
  u32x2 hv[5];
#pragma unroll
  for (int n = 0; n < 5; ++n) {
    const int dd = 72 * j3 + 16 * n + 4 * (n == 4 ? (q & 1) : q);
    hv[n] = ld8(R, (dd >> 5) * 8192 + ((dd >> 2) & 1) * 512 + (((dd >> 3) & 3) * 16 + li) * 8, O_HF + ((T >> 3) * 18 * 8 + (T & 7)) * 1024);
  }
  struct F3 { u32x4 p[2][2], n[9]; };
  f32x4 a3[2][6] = {};
  F3 U, V;
  const int ap = (8 * kq + 2 * th) * 1024 + lane * 16, aw = 16384 + kq * 9216 + lane * 16;
  auto ld3 = [&](F3& f, int t) {
    const char* slot = sm + (t & 3) * STG;
#pragma unroll
    for (int g = 0; g < 2; ++g)
#pragma unroll
      for (int m = 0; m < 2; ++m) f.p[g][m] = *(const u32x4*)(slot + ap + (4 * g + m) * 1024);
#pragma unroll
    for (int n = 0; n < 9; ++n) f.n[n] = *(const u32x4*)(slot + aw + n * 1024);
  };
  auto mm3 = [&](const F3& f) {
#pragma unroll
    for (int g = 0; g < 2; ++g)
#pragma unroll
      for (int m = 0; m < 2; ++m) {
#pragma unroll
        for (int n = 0; n < 4; ++n) a3[m][n] = mfma_bf(f.n[4 * g + n], f.p[g][m], a3[m][n]);
        a3[m][4 + g] = mfma_bf(f.n[8], f.p[g][m], a3[m][4 + g]);
      }
#pragma unroll
    for (int i = 0; i < 13; ++i) { __builtin_amdgcn_sched_group_barrier(0x008, 1, 0); __builtin_amdgcn_sched_group_barrier(0x100, 1, 0); }
    __builtin_amdgcn_sched_barrier(0);
  };
  STAMP(3,11);
  bar();
  STAMP(3,12);
  ld3(U, 50);
#pragma unroll 1
  for (int t = 50; t < 66; t += 2) { bar(); ld3(V, t + 1); mm3(U); bar(); ld3(U, t + 2); mm3(V); }
  bar(); ld3(V, 67); mm3(U); mm3(V);
  STAMP(3,13);
#pragma unroll
  for (int m = 0; m < 2; ++m) a3[m][4] = lane < 32 ? a3[m][4] : a3[m][5];
  if (kq) {
#pragma unroll
    for (int n = 0; n < 5; ++n) { *(f32x4*)(sm + w * 5120 + n * 1024 + lane * 16) = a3[0][n]; asm volatile("" : "+v"(a3[1][n])); a3[0][n] = a3[1][n]; }
  } else {
#pragma unroll
    for (int n = 0; n < 5; ++n) *(f32x4*)(sm + w * 5120 + n * 1024 + lane * 16) = a3[1][n];
  }
  bar();
  char* o = out + (size_t)(32 * (T >> 1) + 4 * (T & 1) + prow) * 1152 + (72 * j3 + 4 * q) * 2;
#pragma unroll
  for (int n = 0; n < 5; ++n) {
    f32x4 v = a3[0][n] + *(const f32x4*)(sm + (w ^ 2) * 5120 + n * 1024 + lane * 16);
    if (n == 4) v = f32x4{half_sum(v[0]), half_sum(v[1]), half_sum(v[2]), half_sum(v[3])};
    if (n < 4 || q < 2) *(u32x2*)(o + n * 32) = u32x2{pk2(up2(hv[n][0]) + lo2(v)), pk2(up2(hv[n][1]) + hi2(v))};
  }
  if (bid == 0 && w == 0 && lane == 0) { u64 e = (((u64)tag[1] << 32) | tag[0]) - 0x9E3779B97F4A7C15ull + 1; st8(R, 0, O_EP, u32x2{(unsigned)e, (unsigned)(e >> 32)}); }
  STAMP(3,14);
}

#undef lane
#undef li
#undef q
#undef prow
extern "C" void kernel_launch(void* const* d_in, const int* in_sizes, int n_in, void* d_out, int out_size, void* d_ws, size_t ws_size,
                              hipStream_t stream) {
  (void)hipFuncSetAttribute((const void*)k_all, hipFuncAttributeMaxDynamicSharedMemorySize, LDS_BYTES);
  k_all<<<256, 512, LDS_BYTES, stream>>>((const char*)d_in[0], (const char*)d_in[1], (const char*)d_in[2], (const char*)d_in[3],
      (const char*)d_in[4], (const char*)d_in[5], (const char*)d_in[6], (const char*)d_in[7], (const char*)d_in[8], (const char*)d_in[10],
      (const char*)d_in[11], (const char*)d_in[12], (const char*)d_in[13], (char*)d_out, (char*)d_ws);
}
```

```cpp
#include <hip/hip_runtime.h>
#include <hip/hip_bf16.h>
#include <stdint.h>

typedef __attribute__((ext_vector_type(8))) __bf16 bf16x8;
typedef __attribute__((ext_vector_type(2))) __bf16 bf16x2;
typedef __attribute__((ext_vector_type(8))) _Float16 f16x8;
typedef __attribute__((ext_vector_type(2))) _Float16 f16x2;
typedef __attribute__((ext_vector_type(4))) float f32x4;
typedef __attribute__((ext_vector_type(2))) float f32x2;
typedef __attribute__((ext_vector_type(4))) unsigned u32x4;
typedef __attribute__((ext_vector_type(2))) unsigned u32x2;
typedef __attribute__((ext_vector_type(4))) short s16x4;
#define LDSP __attribute__((address_space(3)))
#define DEVI __device__ __forceinline__

constexpr int L = 2048, D = 576, NM = 128, F = 4608, FH = 2304;
constexpr float EPS = 1e-5f;
constexpr unsigned O_EP = 0;
constexpr int NREP = 32;
constexpr unsigned O_F1 = 256;
constexpr unsigned O_F2 = O_F1 + NREP * 1024;
constexpr unsigned F2S = 2048;
constexpr unsigned O_ZFL = O_F2 + NREP * F2S;
constexpr unsigned O_PFL = O_ZFL + 32768;
constexpr unsigned O_CS = 188416;
static_assert(O_PFL + 32768 <= O_CS, "ws");
constexpr unsigned O_ST = O_CS + F * 12;
constexpr unsigned O_HS = O_ST + 262144;
constexpr unsigned O_U = O_HS + 16384;
constexpr unsigned O_XB = O_U + L * D * 2;
constexpr unsigned O_WI = O_XB + L * D * 2;
constexpr unsigned O_WO = O_WI + D * F * 2;
constexpr unsigned O_HF = O_WO + FH * D * 2;
constexpr unsigned O_ZF = O_HF + L * D * 2;
constexpr unsigned O_PF = O_ZF + L * FH * 2;
constexpr unsigned WS_USED = O_PF + L * FH * 2;
constexpr size_t O_STAMP = 128u << 20;
#define STAMP(k,i) __builtin_amdgcn_sched_barrier(0)
#define STAMPW(k,i,W) __builtin_amdgcn_sched_barrier(0)
#define RSTAMP(k,i)
constexpr unsigned SENT = 0xFFFFFFFFu;

DEVI unsigned pk_bf16(float a, float b) { f32x2 v = {a, b}; return __builtin_bit_cast(unsigned, __builtin_convertvector(v, bf16x2)); }
DEVI unsigned pk_f16(float a, float b) { f32x2 v = {a, b}; return __builtin_bit_cast(unsigned, __builtin_convertvector(v, f16x2)); }
DEVI float bf_lo(unsigned u) { return __builtin_bit_cast(float, u << 16); }
DEVI float bf_hi(unsigned u) { return __builtin_bit_cast(float, u & 0xFFFF0000u); }
DEVI float asf(unsigned u) { return __builtin_bit_cast(float, u); }
DEVI unsigned asu(float f) { return __builtin_bit_cast(unsigned, f); }
DEVI float rbf(float a) { return bf_lo(pk_bf16(a, a)); }
DEVI float h_lo(unsigned u) { return (float)__builtin_bit_cast(f16x2, u)[0]; }
DEVI float h_hi(unsigned u) { return (float)__builtin_bit_cast(f16x2, u)[1]; }
template <int CTRL> DEVI float dpp_add(float v) {
  return v + __builtin_bit_cast(float, __builtin_amdgcn_update_dpp(0, __builtin_bit_cast(int, v), CTRL, 0xF, 0xF, true));
}
DEVI float row16_sum(float v) { v = dpp_add<0x128>(v); v = dpp_add<0x124>(v); v = dpp_add<0x122>(v); v = dpp_add<0x121>(v); return v; }
DEVI float half_sum(float v) { auto r = __builtin_amdgcn_permlane32_swap(asu(v), asu(v), false, false); return asf(r[0]) + asf(r[1]); }
DEVI float q_sum(float v) { auto r = __builtin_amdgcn_permlane16_swap(asu(v), asu(v), false, false); return half_sum(asf(r[0]) + asf(r[1])); }
DEVI unsigned umax4(u32x4 v) { return max(max(v[0], v[1]), max(v[2], v[3])); }
DEVI u32x2 tr_read(const char* p) {
  return __builtin_bit_cast(u32x2, __builtin_amdgcn_ds_read_tr16_b64_v4i16((LDSP s16x4*)p));
}
DEVI f32x4 mfma_bf(u32x4 a, u32x4 b, f32x4 c) {
  return __builtin_amdgcn_mfma_f32_16x16x32_bf16(__builtin_bit_cast(bf16x8, a), __builtin_bit_cast(bf16x8, b), c, 0, 0, 0);
}
DEVI f32x4 mfma_h(u32x4 a, u32x4 b, f32x4 c) {
  return __builtin_amdgcn_mfma_f32_16x16x32_f16(__builtin_bit_cast(f16x8, a), __builtin_bit_cast(f16x8, b), c, 0, 0, 0);
}
DEVI void ln_scalars(float mean, float var, float& mb, float& rinv) {
  mb = rbf(mean);
  float d = rbf(__builtin_amdgcn_sqrtf(rbf(rbf(var) + EPS)));
  rinv = __builtin_amdgcn_rcpf(d);
}
DEVI float gelu(float x) {
  float w = x * __builtin_fmaf(x * x, -0.10294324f, -2.3022082f);
  return x * __builtin_amdgcn_rcpf(1.0f + __builtin_amdgcn_exp2f(w));
}

typedef unsigned long long u64;
typedef __amdgpu_buffer_rsrc_t rsrc_t;
constexpr int SPIN = 1 << 16;
constexpr int CODE_LINES = 39;
template <int CTRL> DEVI float dpp_get(float v) {
  return __builtin_bit_cast(float, __builtin_amdgcn_update_dpp(0, __builtin_bit_cast(int, v), CTRL, 0xF, 0xF, true));
}
constexpr float GC1 = -0.10294324f, GC0 = -2.3022082f;
DEVI f32x2 gelu2(f32x2 x, f32x2 k1, f32x2 k0) {
  f32x2 w = x * __builtin_elementwise_fma(x * x, k1, k0);
  f32x2 d = f32x2{__builtin_amdgcn_exp2f(w[0]), __builtin_amdgcn_exp2f(w[1])} + f32x2{1.f, 1.f};
  return x * f32x2{__builtin_amdgcn_rcpf(d[0]), __builtin_amdgcn_rcpf(d[1])};
}
DEVI unsigned pk2(f32x2 v) { return __builtin_bit_cast(unsigned, __builtin_convertvector(v, bf16x2)); }
DEVI f32x2 up2(unsigned u) { return f32x2{bf_lo(u), bf_hi(u)}; }
DEVI f32x2 lo2(f32x4 v) { return __builtin_shufflevector(v, v, 0, 1); }
DEVI f32x2 hi2(f32x4 v) { return __builtin_shufflevector(v, v, 2, 3); }
DEVI float dot2(unsigned a, unsigned b, float c) {
  return __builtin_amdgcn_fdot2_f32_bf16(__builtin_bit_cast(bf16x2, a), __builtin_bit_cast(bf16x2, b), c, false);
}
DEVI void st16(rsrc_t r, unsigned voff, unsigned soff, u32x4 v) { __builtin_amdgcn_raw_buffer_store_b128(v, r, voff + soff, 0, 16); }
DEVI void st8(rsrc_t r, unsigned voff, unsigned soff, u32x2 v) { __builtin_amdgcn_raw_buffer_store_b64(v, r, voff, soff, 16); }
DEVI void st4(rsrc_t r, unsigned voff, unsigned soff, unsigned v) { __builtin_amdgcn_raw_buffer_store_b32(v, r, voff, soff, 16); }
DEVI u32x4 ld16(rsrc_t r, unsigned voff, unsigned soff) { return __builtin_amdgcn_raw_buffer_load_b128(r, voff, soff, 16); }
DEVI u32x2 ld8(rsrc_t r, unsigned voff, unsigned soff) { return __builtin_amdgcn_raw_buffer_load_b64(r, voff, soff, 16); }
template <int N> DEVI void wait_vm() { asm volatile("s_waitcnt vmcnt(%0)" ::"i"(N) : "memory"); }
DEVI void bar() { __builtin_amdgcn_sched_barrier(0); asm volatile("s_waitcnt lgkmcnt(0)\n\ts_barrier" ::: "memory"); __builtin_amdgcn_sched_barrier(0); }
DEVI int fresh_lane() { int l; asm volatile("v_mbcnt_lo_u32_b32 %0, -1, 0\n\tv_mbcnt_hi_u32_b32 %0, -1, %0" : "=v"(l)); return l; }
DEVI unsigned lds_addr(const char* p) { return (unsigned)(uintptr_t)(LDSP const char*)p; }
template <int NL> DEVI void wait_flags(rsrc_t r, unsigned off, int n, u32x2 tag, int lane) {
#pragma unroll 1
  for (int spin = 0; spin < SPIN; ++spin) {
    bool ok = true;
#pragma unroll
    for (int k = 0; k < NL; ++k) {
      int i = min(lane + 64 * k, n / 2 - 1);
      u32x4 v = ld16(r, i * 16, off);
      ok = ok && v[0] == tag[0] && v[1] == tag[1] && v[2] == tag[0] && v[3] == tag[1];
    }
    if (__all(ok)) break;
    __builtin_amdgcn_s_sleep(1);
    asm volatile("" ::: "memory");
  }
  asm volatile("" ::: "memory");
}
template <bool SC> DEVI void glds4(unsigned m0v, const char* base, unsigned v0, unsigned v1, unsigned v2, unsigned v3) {
  unsigned keep;
  if (SC)
    asm volatile("s_mov_b32 %0, m0\n\ts_mov_b32 m0, %1\n\ts_nop 0\n\t"
                 "global_load_lds_dwordx4 %3, %2 sc1\n\tglobal_load_lds_dwordx4 %4, %2 offset:1024 sc1\n\t"
                 "global_load_lds_dwordx4 %5, %2 offset:2048 sc1\n\tglobal_load_lds_dwordx4 %6, %2 offset:3072 sc1\n\ts_mov_b32 m0, %0"
                 : "=&s"(keep) : "s"(m0v), "s"(base), "v"(v0), "v"(v1), "v"(v2), "v"(v3) : "memory");
  else
    asm volatile("s_mov_b32 %0, m0\n\ts_mov_b32 m0, %1\n\ts_nop 0\n\t"
                 "global_load_lds_dwordx4 %3, %2\n\tglobal_load_lds_dwordx4 %4, %2 offset:1024\n\t"
                 "global_load_lds_dwordx4 %5, %2 offset:2048\n\tglobal_load_lds_dwordx4 %6, %2 offset:3072\n\ts_mov_b32 m0, %0"
                 : "=&s"(keep) : "s"(m0v), "s"(base), "v"(v0), "v"(v1), "v"(v2), "v"(v3) : "memory");
}
DEVI void glds1(unsigned m0v, const char* base, unsigned v0) {
  unsigned keep;
  asm volatile("s_mov_b32 %0, m0\n\ts_mov_b32 m0, %1\n\ts_nop 0\n\tglobal_load_lds_dwordx4 %3, %2 sc1\n\ts_mov_b32 m0, %0"
               : "=&s"(keep) : "s"(m0v), "s"(base), "v"(v0) : "memory");
}
template <int NP> DEVI void ld_lin(unsigned dst, const char* src, unsigned l16) {
  glds4<true>(dst, src, l16, l16, l16, l16);
  glds4<true>(dst + 4096, src + 4096, l16, l16, l16, l16);
  if (NP == 9) glds1(dst + 8192, src + 8192, l16);
}

constexpr int TP = 1168;
constexpr int CV_K = 8 * 260 * 16, CV_U = 4576;
constexpr int C_U = 4 * CV_K, C_KD = C_U + 16 * TP, C_END = C_KD + 4 * 1536;
constexpr int KT = 528;
constexpr int STG = 34816, RING = 4 * STG;
constexpr int X_CS = RING, X_LN = X_CS + 1152, X_PV = X_LN + 1024, X_HS = X_PV + 16384, X_CNT = 158976;
constexpr int LDS_BYTES = X_CNT + 64 + 256;
static_assert(C_END <= X_CNT && 4 * CV_U <= 16 * TP && X_HS + 1024 <= X_CNT && LDS_BYTES <= 163840, "lds");
#define TPOLL 12
#define WDELAY 2
#define SDELAY 2
#define ROT3 0
#define TCHK 13
#define TFLAG 16

struct Fr { u32x4 m[2], n[9]; };
struct Ctx { const char* sm; int am0[4], am1, an; };
template <int MK> DEVI void ldf(Fr& f, const Ctx& c, int t, int kk) {
  int so = (t & 3) * STG;
  asm volatile("" : "+s"(so));
  const char* slot = c.sm + so;
  if (MK == 0) {
#pragma unroll
    for (int m = 0; m < 2; ++m) {
      u32x2 a = *(const u32x2*)(slot + c.am0[2 * m] + kk * 8192), b = *(const u32x2*)(slot + c.am0[2 * m + 1] + kk * 8192);
      f.m[m] = u32x4{a[0], a[1], b[0], b[1]};
    }
  } else {
#pragma unroll
    for (int m = 0; m < 2; ++m) f.m[m] = *(const u32x4*)(slot + (c.am1 ^ (kk * 64)) + m * 2048);
  }
#pragma unroll
  for (int n = 0; n < 9; ++n) f.n[n] = *(const u32x4*)(slot + c.an + (kk * 9 + n) * 1024);
}
template <int MK, int ORI, bool LD, bool FIRST = false> DEVI void seg(f32x4 (&acc)[2][9], const Fr& cur, Fr& nxt, const Ctx& c, int t, int kk) {
  if (LD) ldf<MK>(nxt, c, t, kk);
#pragma unroll
  for (int m = 0; m < 2; ++m)
#pragma unroll
    for (int nn = 0; nn < 9; ++nn) {
      const int n = m ? 8 - nn : nn;
      const f32x4 ci = FIRST ? f32x4{0, 0, 0, 0} : acc[m][n];
      acc[m][n] = ORI == 0 ? mfma_bf(cur.m[m], cur.n[n], ci) : mfma_bf(cur.n[n], cur.m[m], ci);
    }
#pragma unroll
  for (int i = 0; i < (MK == 0 ? 13 : 11); ++i) { __builtin_amdgcn_sched_group_barrier(0x008, 1, 0); __builtin_amdgcn_sched_group_barrier(0x100, 1, 0); }
  __builtin_amdgcn_sched_barrier(0);
}
DEVI f32x2 cmul(f32x2 a, f32x2 b, f32x2 bp) {
  return __builtin_elementwise_fma(f32x2{a[1], a[1]}, bp, f32x2{a[0], a[0]} * b);
}
DEVI f32x2 perp(f32x2 b) { return f32x2{-b[1], b[0]}; }
DEVI f32x2 cexp(float ar, float ai, float l) {
  float m = __builtin_amdgcn_exp2f(ar * l), ph = __builtin_amdgcn_fractf(ai * l);
  return f32x2{m * __builtin_amdgcn_cosf(ph), m * __builtin_amdgcn_sinf(ph)};
}
DEVI unsigned pkh(f32x2 v) { return __builtin_bit_cast(unsigned, __builtin_convertvector(v, f16x2)); }
DEVI float ldbf(const char* p, int i) { return bf_lo(*(const unsigned short*)(p + 2 * i)); }

__global__ __launch_bounds__(512, 2) void k_all(const char* __restrict__ x, const char* __restrict__ a_re, const char* __restrict__ a_im,
    const char* __restrict__ b_re, const char* __restrict__ b_im, const char* __restrict__ c_re, const char* __restrict__ c_im,
    const char* __restrict__ log_dt, const char* __restrict__ dskip, const char* __restrict__ w_in, const char* __restrict__ w_sp,
    const char* __restrict__ b_sp, const char* __restrict__ w_out, char* __restrict__ out, char* __restrict__ ws) {
  extern __shared__ __attribute__((aligned(16))) char sm[];
  const int tid = threadIdx.x, lane = tid & 63, w = __builtin_amdgcn_readfirstlane(tid >> 6), li = lane & 15, q = lane >> 4;
  const int bid = blockIdx.x, xc = bid & 7, ii = bid >> 3;
  if (w == 7) {
    unsigned long long pc;
    asm volatile("s_getpc_b64 %0" : "=s"(pc));
    const char* cp = (const char*)(pc & ~255ull) + (ii & 7) * (CODE_LINES * 128);
    unsigned keep;
    asm volatile("s_mov_b32 %0, m0\n\ts_mov_b32 m0, %1\n\ts_nop 0\n\tglobal_load_lds_dword %3, %2\n\ts_mov_b32 m0, %0"
                 : "=&s"(keep) : "s"(lds_addr(sm) + X_CNT + 64), "s"(cp), "v"(min(lane, CODE_LINES - 1) * 128) : "memory");
  }
  if (tid < 2) *(u32x4*)(sm + X_CNT + tid * 16) = u32x4{0, 0, 0, 0};
  bar();
  asm volatile("" ::"s"(x), "s"(a_re), "s"(a_im), "s"(b_re), "s"(b_im), "s"(c_re), "s"(c_im), "s"(log_dt), "s"(dskip), "s"(w_in), "s"(w_out), "s"(ws));
  const rsrc_t R = __builtin_amdgcn_make_buffer_rsrc(ws, 0, WS_USED, 0x00020000);
  STAMP(0,0);
  const u32x2 ep = ld8(R, 0, O_EP);
  const bool is_conv = ii < 18;
  const int cg = xc * 18 + ii, rep = bid & (NREP - 1);
  auto mk_tag = [&]() {
    u64 e = (((u64)__builtin_amdgcn_readfirstlane(ep[1]) << 32) | (unsigned)__builtin_amdgcn_readfirstlane(ep[0])) + 0x9E3779B97F4A7C15ull;
    return u32x2{(unsigned)e, (unsigned)(e >> 32)};
  };
  u32x2 tag;
  auto arrive = [&](int slot, unsigned n, unsigned flag_off, unsigned stride, int nrep) {
    wait_vm<0>();
    const int lane = fresh_lane();
    unsigned old = 0;
    if (lane == 0) old = __hip_atomic_fetch_add((unsigned*)(sm + X_CNT) + slot, 1u, __ATOMIC_RELAXED, __HIP_MEMORY_SCOPE_WORKGROUP);
    if (__builtin_amdgcn_readfirstlane(old) == n - 1 && lane < nrep) st8(R, lane * stride, flag_off, tag);
  };
  if (is_conv) {
    const int ch = w & 3, dch = 4 * cg + ch;
    char* sk = sm + ch * CV_K;
    const char* su = sm + C_U + ch * CV_U;
    float sc = 0.f;
    if (w < 4) {
      const int pi = (dch * NM + 2 * lane) * 2;
      const unsigned v_ar = *(const unsigned*)(a_re + pi), v_ai = *(const unsigned*)(a_im + pi), v_br = *(const unsigned*)(b_re + pi),
                     v_bi = *(const unsigned*)(b_im + pi), v_cr = *(const unsigned*)(c_re + pi), v_ci = *(const unsigned*)(c_im + pi);
      const float p_dk = ldbf(dskip, dch);
      const float dt = rbf(__expf(ldbf(log_dt, dch)));
      tag = mk_tag();
      STAMP(0,1);
      const f32x2 ar = (up2(v_ar) * dt) * 1.44269504f, ai = (up2(v_ai) * dt) * 0.15915494f;
      const f32x2 br = up2(v_br) * dt, bi = up2(v_bi) * dt, cr = up2(v_cr), ci = up2(v_ci);
      const f32x2 cbr = cr * br - ci * bi, cbi = -(cr * bi + ci * br);
      float mx = fmaxf(fmaxf(fabsf(cbr[0]), fabsf(cbi[0])), fmaxf(fabsf(cbr[1]), fabsf(cbi[1])));
      mx = fmaxf(mx, dpp_get<0x128>(mx)); mx = fmaxf(mx, dpp_get<0x124>(mx)); mx = fmaxf(mx, dpp_get<0x122>(mx)); mx = fmaxf(mx, dpp_get<0x121>(mx));
      auto rl = [&](int l) { return asf(__builtin_amdgcn_readlane((int)asu(mx), l)); };
      sc = fmaxf(fmaxf(rl(0), rl(16)), fmaxf(rl(32), rl(48)));
      sc = fmaxf(sc, fabsf(p_dk) * (1.0f / 256));
      const float isc = sc > 0.f ? 1.0f / sc : 0.f;
      {
        struct C2 { f32x2 a, b; };
        char* tb = sk + lane * 8;
        auto put = [&](int row, C2 v) { *(u32x2*)(tb + row * KT) = u32x2{pkh(v.a), pkh(v.b)}; };
        auto mul = [&](C2 v, C2 u, C2 up) { return C2{cmul(v.a, u.a, up.a), cmul(v.b, u.b, up.b)}; };
        auto prp = [&](C2 v) { return C2{perp(v.a), perp(v.b)}; };
        auto ex = [&](float l) { return C2{cexp(ar[0], ai[0], l), cexp(ar[1], ai[1], l)}; };
        const C2 w1 = ex(1.f), w128 = ex(128.f);
        C2 w8 = ex(8.f); w8.a[1] = -w8.a[1]; w8.b[1] = -w8.b[1];
        const C2 w1p = prp(w1), w8p = prp(w8), w128p = prp(w128);
        C2 y = {f32x2{cbr[0], cbi[0]} * isc, f32x2{cbr[1], cbi[1]} * isc};
        C2 E[8]; E[0] = C2{f32x2{1.f, 0.f}, f32x2{1.f, 0.f}}; E[1] = w1;
#pragma unroll
        for (int e = 2; e < 8; ++e) E[e] = mul(E[e - 1], w1, w1p);
#pragma unroll
        for (int e = 0; e < 8; ++e) { put(e, E[e]); put(8 + e, e ? mul(E[e], w128, w128p) : w128); put(41 + e, y); y = mul(y, w8, w8p); }
        const C2 e2p = prp(E[2]), e4p = prp(E[4]), e6p = prp(E[6]);
        C2 H = w128;
#pragma unroll
        for (int a = 2; a <= 16; ++a) {
          H = mul(H, w128, w128p);
          if (a < 4) { put(8 + 4 * a, H); put(9 + 4 * a, mul(H, E[2], e2p)); put(10 + 4 * a, mul(H, E[4], e4p)); put(11 + 4 * a, mul(H, E[6], e6p)); }
          else if (a < 8) { put(16 + 2 * a, H); put(17 + 2 * a, mul(H, E[4], e4p)); }
          else put(24 + a, H);
          if (a < 10) { put(47 + a, y); if (a < 9) y = mul(y, w8, w8p); }
        }
      }
      asm volatile("" ::: "memory");
      STAMP(0,2);
      {
        f32x4 kk[3] = {};
        const char* pa = sk + li * KT + q * 16;
#pragma unroll
        for (int s = 0; s < 8; ++s) {
          u32x4 fb = *(const u32x4*)(pa + 41 * KT + s * 64);
#pragma unroll
          for (int t = 0; t < 3; ++t) kk[t] = mfma_h(*(const u32x4*)(pa + 16 * t * KT + s * 64), fb, kk[t]);
        }
        if (lane == 0) kk[0][0] += p_dk * isc;
        char* kd = sm + C_KD + ch * 1536;
        *(u32x2*)(kd + (128 * (q >> 1) + 4 * (q & 1) + 8 * li) * 2) = u32x2{pkh(lo2(kk[0])), pkh(hi2(kk[0]))};
        const int i1 = q < 2 ? 256 + 64 * q + 4 * li : 384 + 64 * (q - 2) + 2 * li;
        *(unsigned*)(kd + i1 * 2) = pkh(lo2(kk[1])); *(unsigned*)(kd + (i1 + (q < 2 ? 2 : 32)) * 2) = pkh(hi2(kk[1]));
#pragma unroll
        for (int r = 0; r < 4; ++r) *(_Float16*)(kd + (512 + 16 * (4 * q + r) + li) * 2) = (_Float16)kk[2][r];
      }
      asm volatile("" ::: "memory");
      u32x4 c0[4];
      {
        const unsigned short* kd = (const unsigned short*)(sm + C_KD + ch * 1536);
#pragma unroll
        for (int rr = 0; rr < 2; ++rr) {
          const int k = 255 - 64 * rr - lane;
          const unsigned a = kd[384 + k], b = kd[385 + k], aa = a | (a << 16);
          c0[rr] = u32x4{b | (b << 16), b | (((k & 1) ? b : a) << 16), aa, aa};
        }
        { const int k = 127 - lane;
          const unsigned ab = *(const unsigned*)(kd + 256 + 2 * k), cc = kd[258 + 2 * k];
          c0[2] = u32x4{cc | (cc << 16), __builtin_amdgcn_perm(ab, ab, 0x03020302), __builtin_amdgcn_perm(ab, ab, 0x01000302), __builtin_amdgcn_perm(ab, ab, 0x01000100)}; }
        { const int k = 63 - lane;
          const u32x4 v = *(const u32x4*)(kd + 8 * (k & 31));
          const u32x2 u = *(const u32x2*)(kd + 128 + 4 * k); const unsigned e = kd[132 + 4 * k];
          c0[3] = lane < 32 ? u32x4{__builtin_amdgcn_perm(u[1], e, 0x07060100), __builtin_amdgcn_perm(u[1], u[1], 0x01000100), __builtin_amdgcn_perm(u[0], u[1], 0x07060100), __builtin_amdgcn_perm(u[0], u[0], 0x01000100)}
                            : u32x4{__builtin_amdgcn_alignbit(v[3], v[3], 16), __builtin_amdgcn_alignbit(v[2], v[2], 16), __builtin_amdgcn_alignbit(v[1], v[1], 16), __builtin_amdgcn_alignbit(v[0], v[0], 16)}; }
#pragma unroll
        for (int rr = 0; rr < 4; ++rr) *(u32x4*)(sk + (64 * rr + lane) * 16) = c0[rr];
        if (lane < 8) *(u32x4*)(sk + (lane * 260 + 256) * 16) = u32x4{0, 0, 0, 0};
      }
      asm volatile("" ::: "memory");
      STAMP(0,3);
      u32x4 c1[4];
#pragma unroll
      for (int rr = 0; rr < 4; ++rr) c1[rr] = *(const u32x4*)(sk + (64 * rr + lane) * 16 + 16);
#pragma unroll
      for (int rr = 0; rr < 4; ++rr) {
        char* p = sk + (64 * rr + lane) * 16;
        unsigned e[8] = {c0[rr][0], c0[rr][1], c0[rr][2], c0[rr][3], c1[rr][0], c1[rr][1], c1[rr][2], c1[rr][3]}, o[7];
#pragma unroll
        for (int i = 0; i < 7; ++i) o[i] = __builtin_amdgcn_alignbit(e[i + 1], e[i], 16);
#pragma unroll
        for (int s = 0; s < 4; ++s) {
          if (s) *(u32x4*)(p + (2 * s) * 260 * 16) = u32x4{e[s], e[s + 1], e[s + 2], e[s + 3]};
          *(u32x4*)(p + (2 * s + 1) * 260 * 16) = u32x4{o[s], o[s + 1], o[s + 2], o[s + 3]};
        }
      }
      STAMP(0,4);
    } else {
      char* s0 = sm + C_U;
      __builtin_amdgcn_s_setprio(2);
      u32x2 xr[9];
      const char* src = x + (size_t)(16 * min(cg, 127) + 4 * ch + q) * 1152 + li * 8;
#pragma unroll
      for (int k = 0; k < 9; ++k) xr[k] = *(const u32x2*)(src + k * 128);
      tag = mk_tag();
      if (cg < 128) {
        asm volatile("" :: "v"(xr[8])); STAMPW(1,0,4);
#pragma unroll
        for (int k = 0; k < 9; ++k) st8(R, li * 32 + q * 8 + k * 512, O_XB + (4 * cg + ch) * 4608, xr[k]);
        float a[4] = {0, 0, 0, 0};
#pragma unroll
        for (int k = 0; k < 9; ++k)
#pragma unroll
          for (int e = 0; e < 2; ++e) { a[e] = dot2(xr[k][e], 0x3F803F80u, a[e]); a[2 + e] = dot2(xr[k][e], xr[k][e], a[2 + e]); }
        const float s = row16_sum(a[0] + a[1]), ss = row16_sum(a[2] + a[3]);
        float mean = s * (1.0f / D), mb, rinv;
        ln_scalars(mean, fmaxf(ss * (1.0f / D) - mean * mean, 0.f), mb, rinv);
        const f32x2 r2 = {rinv, rinv}, b2 = {-mb * rinv, -mb * rinv};
        char* p = s0 + (4 * ch + q) * TP + li * 8;
#pragma unroll
        for (int k = 0; k < 9; ++k)
          *(u32x2*)(p + k * 128) = u32x2{pkh(__builtin_elementwise_fma(up2(xr[k][0]), r2, b2)), pkh(__builtin_elementwise_fma(up2(xr[k][1]), r2, b2))};
        asm volatile("" ::: "memory");
        unsigned* cnt = (unsigned*)(sm + X_CNT) + 3;
        if (lane == 0) __hip_atomic_fetch_add(cnt, 1u, __ATOMIC_RELAXED, __HIP_MEMORY_SCOPE_WORKGROUP);
#pragma unroll 1
        for (int spin = 0; spin < (SPIN << 4) && __builtin_amdgcn_readfirstlane(__hip_atomic_load(cnt, __ATOMIC_RELAXED, __HIP_MEMORY_SCOPE_WORKGROUP)) < 4; ++spin) {}
        asm volatile("" ::: "memory");
        STAMPW(1,1,4);
        const char* trb = s0 + (8 * (q & 1) + (li >> 2)) * TP + (q >> 1) * 32 + (li & 3) * 8 + ch * 64;
        const unsigned uo = (16 * (q >> 1) + li) * 32 + (q & 1) * 16 + O_U + cg * 18432 + ch * 1024;
#pragma unroll
        for (int k = 0; k < 5; ++k)
          if (k < 4 || ch < 2) {
            u32x2 lo = tr_read(trb + k * 256), hi = tr_read(trb + 4 * TP + k * 256);
            st16(R, uo, k * 4096, u32x4{lo[0], lo[1], hi[0], hi[1]});
          }
        STAMPW(1,2,4);
        arrive(0, 4, O_F1 + cg * 8, 1024, NREP);
        STAMPW(1,3,4);
      }
      __builtin_amdgcn_s_setprio(0);
      if (cg < 136 && w >= 6) st16(R, (tid - 384) * 16, O_ST + cg * 2048, u32x4{SENT, SENT, SENT, SENT});
      if (w == 4) {
        wait_flags<1>(R, O_F1 + rep * 1024, 128, tag, lane);
        STAMPW(5,0,4);
        const unsigned v0 = (lane >> 1) * 18432 + (lane & 1) * 16, dv = 32 * 18432 - 1024;
#pragma unroll
        for (int i = 0; i < 4; ++i) glds4<true>(lds_addr(sm) + C_U + i * CV_U + 480, ws + O_U + (4 * cg + i) * 32, v0, v0 + dv, v0 + 2 * dv, v0 + 3 * dv);
#pragma unroll
        for (int i = 0; i < 4; ++i) if (lane < 30) *(u32x4*)(sm + C_U + i * CV_U + lane * 16) = u32x4{0, 0, 0, 0};
        wait_vm<0>();
      }
    }
    bar();
    STAMP(0,5);
    u32x4 xv[4];
    if (w < 4) {
      f32x4 acc[8] = {};
      const char* pa = sk + ((7 - (li & 7)) * 260 + 253 - (li >> 3) + q) * 16 - 4032;
      const char* pb = su + li * 32 + q * 16;
      struct CF { u32x4 a[8], b[8]; } A, B;
      auto ldc = [&](CF& f, int a0) {
#pragma unroll
        for (int m = 0; m < 8; ++m) { f.a[m] = *(const u32x4*)(pa + 4032 - 64 * a0 - 512 * m); f.b[m] = *(const u32x4*)(pb + 448 - 64 * a0 + 512 * m); }
      };
      auto mmc = [&](const CF& f) {
#pragma unroll
        for (int m = 0; m < 8; ++m)
#pragma unroll
          for (int t = m; t < 8; ++t) acc[t] = mfma_h(f.a[m], f.b[t - m], acc[t]);
#pragma unroll
        for (int i = 0; i < 16; ++i) { __builtin_amdgcn_sched_group_barrier(0x008, 2, 0); __builtin_amdgcn_sched_group_barrier(0x100, 1, 0); }
        __builtin_amdgcn_sched_barrier(0);
      };
      ldc(A, 0);
      __builtin_amdgcn_sched_barrier(0);
#pragma unroll
      for (int a0 = 0; a0 < 8; a0 += 2) { ldc(B, a0 + 1); mmc(A); if (a0 < 6) ldc(A, a0 + 2); mmc(B); }
      STAMP(0,6);
#pragma unroll
      for (int t = 0; t < 8; ++t) *(u32x2*)(sk + t * 512 + li * 32 + q * 8) = u32x2{pk2(lo2(acc[t]) * sc), pk2(hi2(acc[t]) * sc)};
    } else {
#pragma unroll
      for (int r = 0; r < 4; ++r) {
        const int T = ((tid & 255) >> 3) + 32 * r, ip = tid & 7;
        xv[r] = ld16(R, (8 * (T >> 1) + 2 * (ip >> 1) + (T & 1)) * 4608 + (ip & 1) * 16, O_XB + cg * 32);
      }
      STAMPW(2,0,4);
    }
    bar();
    if (w >= 4) {
#pragma unroll
      for (int r = 0; r < 4; ++r) {
        const int T = ((tid & 255) >> 3) + 32 * r, ip = tid & 7;
        const char* p = sm + (32 * (T >> 1) + 8 * (ip >> 1) + 4 * (T & 1) + 2 * (ip & 1)) * 2;
        const unsigned c0 = *(const unsigned*)p, c1 = *(const unsigned*)(p + CV_K), c2 = *(const unsigned*)(p + 2 * CV_K), c3 = *(const unsigned*)(p + 3 * CV_K);
        u32x4 v = {pk2(up2(xv[r][0]) + f32x2{bf_lo(c0), bf_lo(c1)}), pk2(up2(xv[r][1]) + f32x2{bf_lo(c2), bf_lo(c3)}),
                   pk2(up2(xv[r][2]) + f32x2{bf_hi(c0), bf_hi(c1)}), pk2(up2(xv[r][3]) + f32x2{bf_hi(c2), bf_hi(c3)})};
        st16(R, (T >> 3) * (18 * 8192) + (T & 7) * 1024 + ip * 16, O_HF + (cg >> 3) * 8192 + (cg & 1) * 512 + ((cg >> 1) & 3) * 128, v);
      }
      STAMPW(2,1,4);
      arrive(1, 4, O_F2 + cg * 8, F2S, NREP);
      STAMPW(2,2,4);
    }
  } else {
    const int tj = (ii - 18) * 8 + xc;
    const int kc = tj / 36, cb = tj % 36;
    for (int i = 0; i < WDELAY; ++i) __builtin_amdgcn_s_sleep(16);
    char* s0 = sm + 65536;
    const int G = max(tj - 40, 0), par = G & 1;
    {
      const int row = tid >> 4, uu = G / 9, idx = G % 9, hh = row >> 4;
      const int panel = idx < 4 ? 2 * uu : (idx < 8 ? 2 * uu + 1 : 2 * uu + hh), tile = idx < 8 ? 2 * (idx & 3) + hh : 8;
      const char* src = w_in + (size_t)(192 * min(kc, 2) + row) * 9216 + cb * 256 + li * 16;
      const char* wsrc = w_out + (size_t)(144 * panel + 16 * tile + (row & 15)) * 1152 + li * 8;
      u32x4 v[6]; u32x2 vo[9];
#pragma unroll
      for (int k = 0; k < 6; ++k) v[k] = *(const u32x4*)(src + (size_t)k * 32 * 9216);
      if (tj >= 40) {
#pragma unroll
        for (int k = 0; k < 9; ++k) vo[k] = *(const u32x2*)(wsrc + k * 128);
      }
#pragma unroll
      for (int k = 0; k < 6; ++k) *(u32x4*)(sm + (row + 32 * k) * 272 + li * 16) = v[k];
      if (tj >= 40) {
#pragma unroll
        for (int k = 0; k < 9; ++k) *(u32x2*)(s0 + row * TP + li * 8 + k * 128) = vo[k];
      }
    }
    tag = mk_tag();
    if (w == 0) {
      wait_flags<1>(R, O_F1 + rep * 1024, 128, tag, lane);
      for (int i = 0; i < SDELAY; ++i) __builtin_amdgcn_s_sleep(16);
    }
    bar();
    STAMP(4,0);
    if (tj < 108) {
      const int ct = 8 * cb + w;
      const int hf = ct < 144 ? 1 : 0, cl = ct < 144 ? ct : ct - 144, fp = cl / 9, n = cl % 9;
      const char* trb = sm + (8 * q + (li >> 2)) * 272 + w * 32 + (li & 3) * 8;
      const unsigned dst = lane * 16 + O_WI + (((fp * 2 + hf) * 18 + 6 * kc) * 9 + n) * 1024;
      float s = 0.f;
#pragma unroll
      for (int g = 0; g < 6; ++g) {
        u32x2 lo = tr_read(trb + g * 32 * 272), hi = tr_read(trb + g * 32 * 272 + 4 * 272);
        st16(R, dst, g * 9216, u32x4{lo[0], lo[1], hi[0], hi[1]});
        s = dot2(lo[0], 0x3F803F80u, s); s = dot2(lo[1], 0x3F803F80u, s); s = dot2(hi[0], 0x3F803F80u, s); s = dot2(hi[1], 0x3F803F80u, s);
      }
      s = q_sum(s);
      if (q == 0) st4(R, li * 4, O_CS + kc * F * 4 + ct * 64, asu(s));
    }
    if (tj >= 40) {
      const char* trb = s0 + (4 * q + (li >> 2)) * TP + (li & 3) * 8;
      const unsigned dst = lane * 16 + O_WO + (w * 36 + (G >> 1)) * 9216;
#pragma unroll
      for (int n = 0; n < 5; ++n) {
        const int c0 = 72 * w + 16 * n - (n == 4 ? 8 * par : 0);
        u32x2 lo = tr_read(trb + c0 * 2), hi = tr_read(trb + 16 * TP + c0 * 2);
        if (n < 4) st16(R, dst, (4 * par + n) * 1024, u32x4{lo[0], lo[1], hi[0], hi[1]});
        else if ((li >> 3) == par) st16(R, dst, 8192, u32x4{lo[0], lo[1], hi[0], hi[1]});
      }
    }
    STAMP(4,1);
    arrive(0, 8, O_F2 + (144 + tj) * 8, F2S, NREP);
    STAMP(4,2);
  }
  const int tp = (xc & 1) * 8 + (ii >> 2), fp = (xc >> 1) * 4 + (ii & 3);
  const int t64 = 4 * xc + (ii >> 3), j3 = ii & 7;
  if (w == 0) { wait_flags<1>(R, O_F2 + rep * F2S + 144 * 8, 112, tag, lane); STAMPW(6,0,0); }
  bar();
  if (w >= 4) {
    const int lw = w - 4;
    const unsigned l16 = lane * 16, base = lds_addr(sm);
    const int ph = ((ii >> 2) + ii) & 3, ph3 = ROT3 ? ii & 3 : ((ii >> 3) + ii) & 3, rot3 = ROT3 ? (9 * (ii >> 3) + 1) >> 1 : 0;
    auto k9 = [&](int t) { return (t & 3) == ph ? (t + 4 < 9 ? t + 4 : ph) : t; };
    auto k32 = [&](int t) { return (t & 3) == ph ? ((t + 4) & 31) : t; };
    auto k18 = [&](int t) { const int k = ((t & 3) == ph3 ? (t + 4 < 18 ? t + 4 : ph3) : t) + rot3; return k >= 18 ? k - 18 : k; };
    if (lw < 2) {
      constexpr int NP = 8;
      unsigned vo[8];
#pragma unroll
      for (int j = 0; j < 8; ++j) {
        int t = 64 * lw + 32 * (j >> 2) + 4 * ((j >> 1) & 1) + 16 * (j & 1) + 8 * (lane >> 5) + ((lane >> 3) & 3);
        vo[j] = t * 4096 + (((lane & 7) ^ ((lane >> 3) & 7)) * 16) + 3072 - (j & 3) * 1024;
      }
      const char* wsrc = w_sp + (size_t)tp * 128 * 4096 - 3072;
      const char* hsrc = ws + O_HF + tp * 18 * 8192 + lw * 8192;
      const char* psrc = ws + O_PF + t64 * 72 * 4096 + lw * 8192;
      const unsigned dst = base + lw * 8192;
      if (lw == 0) wait_flags<2>(R, O_F2 + rep * F2S, 144, tag, lane);
      STAMPW(5,1,4);
      bar();
#pragma unroll 1
      for (int t = 0; t < 4; ++t) ld_lin<NP>(dst + t * STG, hsrc + k9(t) * 16384, l16);
      wait_vm<3 * NP>(); bar();
#pragma unroll 1
      for (int t = 4; t < 18; ++t) { wait_vm<2 * NP>(); bar(); ld_lin<NP>(dst + (t & 3) * STG, hsrc + k9(t < 9 ? t : t - 9) * 16384, l16); }
#pragma unroll 1
      for (int t = 18; t < 50; ++t) {
        wait_vm<2 * NP>(); bar();
        const unsigned dd = dst + (t & 3) * STG; const char* sp = wsrc + k32(t - 18) * 128;
        glds4<false>(dd, sp, vo[0], vo[1], vo[2], vo[3]); glds4<false>(dd + 4096, sp, vo[4], vo[5], vo[6], vo[7]);
      }
      wait_vm<2 * NP>(); bar(); wait_vm<NP>(); bar(); wait_vm<0>(); bar();
      bar(); bar();
      wait_flags<1>(R, O_PFL + (t64 >> 1) * 2048 + ((t64 & 1) * 8 + j3) * 128, 16, tag, lane);
      STAMPW(5,2,4);
#pragma unroll 1
      for (int t = 50; t < 68; ++t) {
        if (t >= 53) { wait_vm<2 * NP>(); bar(); }
        ld_lin<NP>(dst + (t & 3) * STG, psrc + k18(t - 50) * 16384, l16);
      }
      wait_vm<2 * NP>(); bar(); wait_vm<NP>(); bar(); wait_vm<0>(); bar();
    } else {
      constexpr int NP = 9;
      const char* wi = ws + O_WI + (fp * 2) * 18 * 9216 + (lw - 2) * 9216;
      const char* zs = ws + O_ZF + fp * 64 * 9216 + (lw - 2) * 9216;
      const char* wo = ws + O_WO + j3 * 36 * 9216 + (lw - 2) * 9216;
      const unsigned dst = base + 16384 + (lw - 2) * 9216;
#pragma unroll 1
      for (int t = 0; t < 4; ++t) ld_lin<NP>(dst + t * STG, wi + k9(t) * 18432, l16);
      bar();
      wait_vm<3 * NP>(); bar();
      if (lw == 3) {
        f32x2 a = *(const f32x2*)(sm + X_LN + lane * 8) + *(const f32x2*)(sm + X_LN + 512 + lane * 8);
        a[0] = q_sum(dpp_add<0x128>(a[0])); a[1] = q_sum(dpp_add<0x128>(a[1]));
        float mean = a[0] * (1.0f / D), mb, ri;
        ln_scalars(mean, fmaxf(a[1] * (1.0f / D) - mean * mean, 0.f), mb, ri);
        if (lane < 8) st8(R, lane * 8, O_HS + tp * 1024 + fp * 64, u32x2{asu(ri) == SENT ? 0x7FC00000u : asu(ri), asu(-mb)});
      }
#pragma unroll 1
      for (int t = 4; t < 18; ++t) { wait_vm<2 * NP>(); bar(); ld_lin<NP>(dst + (t & 3) * STG, wi + (t < 9 ? k9(t) : 9 + k9(t - 9)) * 18432, l16); }
      wait_vm<2 * NP>(); bar(); wait_vm<NP>(); bar(); wait_vm<0>(); bar();
      wait_flags<1>(R, O_ZFL + fp * 2048 + tp * 128, 16, tag, lane);
      STAMPW(6,1,6);
#pragma unroll 1
      for (int t = 18; t < 53; ++t) {
        if (t >= 21) { wait_vm<2 * NP>(); bar(); }
        ld_lin<NP>(dst + (t & 3) * STG, t < 50 ? zs + k32(t - 18) * 18432 : wo + k18(t - 50) * 18432, l16);
      }
      wait_vm<0>(); bar();
      bar();
#pragma unroll 1
      for (int t = 53; t < 57; ++t) ld_lin<NP>(dst + (t & 3) * STG, wo + k18(t - 50) * 18432, l16);
      bar(); bar(); bar();
      wait_vm<3 * NP>(); bar();
#pragma unroll 1
      for (int t = 57; t < 68; ++t) { wait_vm<2 * NP>(); bar(); ld_lin<NP>(dst + (t & 3) * STG, wo + k18(t - 50) * 18432, l16); }
      wait_vm<2 * NP>(); bar(); wait_vm<NP>(); bar(); wait_vm<0>(); bar();
    }
    return;
  }
  Ctx c;
  c.sm = sm;
#pragma unroll
  for (int i = 0; i < 4; ++i) { c.am0[i] = (2 * w + (i >> 1)) * 1024 + (i & 1) * 512 + lane * 8; asm volatile("" : "+v"(c.am0[i])); }
  c.am1 = 2 * w * 2048 + li * 128 + ((q ^ (lane & 7)) * 16);
  c.an = 16384 + lane * 16;
  const int prow = 8 * (li >> 2) + (li & 3);
  if (tid < 72) {
    const unsigned o = tid < 36 ? FH * 4 + tid * 16 : (tid - 36) * 16;
    const u32x4 c0 = ld16(R, o, O_CS + fp * 576), c1 = ld16(R, o, O_CS + F * 4 + fp * 576), c2 = ld16(R, o, O_CS + F * 8 + fp * 576);
    *(f32x4*)(sm + X_CS + tid * 16) = (__builtin_bit_cast(f32x4, c0) + __builtin_bit_cast(f32x4, c1)) + __builtin_bit_cast(f32x4, c2);
  }
  bar();
  char* pvb = sm + X_PV + w * 4096;
  {
    u32x4 hv[3];
#pragma unroll
    for (int k = 0; k < 3; ++k)
      hv[k] = ld16(R, (lane >> 5) * 8192 + ((lane >> 2) & 7) * 128 + (lane & 3) * 16, O_HF + (tp * 18 * 8 + (fp >> 1)) * 1024 + (fp & 1) * 64 + min(w + 4 * k, 8) * 16384);
    if (w) hv[2] = u32x4{0, 0, 0, 0};
    f32x4 a = {0, 0, 0, 0};
#pragma unroll
    for (int k = 0; k < 3; ++k)
#pragma unroll
      for (int e = 0; e < 4; ++e) { a[e & 2] = dot2(hv[k][e], 0x3F803F80u, a[e & 2]); a[(e & 2) + 1] = dot2(hv[k][e], hv[k][e], a[(e & 2) + 1]); }
#pragma unroll
    for (int e = 0; e < 4; ++e) a[e] = dpp_add<0x124>(dpp_add<0x128>(a[e]));
    if (li < 4) *(f32x4*)(sm + X_LN + (w * 4 + q) * 64 + li * 16) = a;
  }
  f32x4 acc[2][9];
  Fr X, Y;
  bar();
  STAMP(3,0);
  ldf<0>(X, c, 0, 0);
  seg<0, 0, true, true>(acc, X, Y, c, 0, 1);
  const unsigned hs_m0 = __builtin_amdgcn_readfirstlane(lds_addr(sm) + X_HS + w * 256);
  auto ld_hs = [&]() {
    unsigned keep;
    asm volatile("s_mov_b32 %0, m0\n\ts_mov_b32 m0, %1\n\ts_nop 0\n\tglobal_load_lds_dword %3, %2 sc1\n\ts_mov_b32 m0, %0"
                 : "=&s"(keep) : "s"(hs_m0), "s"(ws), "v"(O_HS + tp * 1024 + tid * 4) : "memory");
  };
#pragma unroll 1
  for (int t = 0; t < 8; ++t) {
    if (t == 5) ld_hs();
    bar();
    seg<0, 0, true>(acc, Y, X, c, t + 1, 0);
    seg<0, 0, true>(acc, X, Y, c, t + 1, 1);
  }
  u32x4 hc[4];
  float cs[9];
  auto rd_hs = [&]() {
    wait_vm<0>();
#pragma unroll
    for (int jp = 0; jp < 4; ++jp) hc[jp] = *(const u32x4*)(sm + X_HS + w * 256 + (jp >> 1) * 128 + q * 32 + (jp & 1) * 16);
  };
  rd_hs();
#pragma unroll
  for (int n = 0; n < 9; ++n) cs[n] = *(const float*)(sm + X_CS + (16 * n + li) * 4);
  bar();
  seg<0, 0, true>(acc, Y, X, c, 9, 0);
  STAMP(3,1);
#pragma unroll 1
  for (int spin = 0; spin < SPIN; ++spin) {
    if (__all(max(max(umax4(hc[0]), umax4(hc[1])), max(umax4(hc[2]), umax4(hc[3]))) != SENT)) break;
    ld_hs();
    rd_hs();
  }
  u32x4 zb[9];
  f32x2 rs[4];
  {
    f32x2 nm[4], k1[4], k0[4], S1[4] = {}, S2[4] = {};
#pragma unroll
    for (int jp = 0; jp < 4; ++jp) {
      rs[jp] = f32x2{asf(hc[jp][0]), asf(hc[jp][2])}; nm[jp] = f32x2{asf(hc[jp][1]), asf(hc[jp][3])};
      k0[jp] = rs[jp] * GC0; k1[jp] = rs[jp] * rs[jp] * rs[jp] * GC1;
    }
#pragma unroll
    for (int n = 0; n < 9; ++n) {
#pragma unroll
      for (int jp = 0; jp < 4; ++jp) {
        f32x2 a = (jp & 1) ? hi2(acc[jp >> 1][n]) : lo2(acc[jp >> 1][n]);
        f32x2 z = gelu2(__builtin_elementwise_fma(nm[jp], f32x2{cs[n], cs[n]}, a), k1[jp], k0[jp]);
        zb[n][jp] = pk2(z);
        S1[jp] += z; S2[jp] = __builtin_elementwise_fma(z, z, S2[jp]);
      }
      asm volatile("" : "+v"(zb[n]));
    }
#pragma unroll
    for (int jp = 0; jp < 4; ++jp) { S1[jp] *= rs[jp]; S2[jp] *= rs[jp] * rs[jp]; }
    float v[16];
#pragma unroll
    for (int j = 0; j < 8; ++j) { v[2 * j] = S1[j >> 1][j & 1]; v[2 * j + 1] = S2[j >> 1][j & 1]; }
#pragma unroll
    for (int i = 0; i < 8; ++i) { bool h = li & 8; v[i] = (h ? v[i + 8] : v[i]) + dpp_get<0x128>(h ? v[i] : v[i + 8]); }
#pragma unroll
    for (int i = 0; i < 4; ++i) { bool h = li & 4; v[i] = (h ? v[i + 4] : v[i]) + dpp_get<0x141>(h ? v[i] : v[i + 4]); }
#pragma unroll
    for (int i = 0; i < 2; ++i) { bool h = li & 2; v[i] = (h ? v[i + 2] : v[i]) + dpp_get<0x4E>(h ? v[i] : v[i + 2]); }
    { bool h = li & 1; v[0] = (h ? v[1] : v[0]) + dpp_get<0xB1>(h ? v[0] : v[1]); }
    unsigned tot = asu(v[0]);
    st4(R, lane * 4, O_ST + ((tp * 4 + w) * 16 + fp) * 256, tot == SENT ? 0x7FC00000u : tot);
  }
  STAMP(3,2);
  const char* sts = ws + O_ST + (tp * 4 + w) * 4096;
  seg<0, 1, true, true>(acc, X, Y, c, 9, 1);
#pragma unroll 1
  for (int t = 9; t < TCHK; ++t) {
    if (t == TPOLL) glds4<true>(lds_addr(pvb), sts, lane * 16, lane * 16, lane * 16, lane * 16);
    bar();
    seg<0, 1, true>(acc, Y, X, c, t + 1, 0);
    seg<0, 1, true>(acc, X, Y, c, t + 1, 1);
  }
  STAMP(3,3);
  u32x4 pv[4];
#pragma unroll 1
  for (int spin = 0; spin < SPIN; ++spin) {
    wait_vm<0>();
#pragma unroll
    for (int k = 0; k < 4; ++k) pv[k] = *(const u32x4*)(pvb + k * 1024 + lane * 16);
    if (__all(max(max(umax4(pv[0]), umax4(pv[1])), max(umax4(pv[2]), umax4(pv[3]))) != SENT)) break;
    glds4<true>(lds_addr(pvb), sts, lane * 16, lane * 16, lane * 16, lane * 16);
  }
  STAMP(3,4);
  {
    float t4[4];
#pragma unroll
    for (int e = 0; e < 4; ++e) t4[e] = q_sum((asf(pv[0][e]) + asf(pv[1][e])) + (asf(pv[2][e]) + asf(pv[3][e])));
    f32x2 o0, o1;
#pragma unroll
    for (int r = 0; r < 2; ++r) {
      float mean = t4[2 * r] * (1.0f / FH), var = fmaxf(t4[2 * r + 1] * (1.0f / FH) - mean * mean, 0.f), mb, ri;
      ln_scalars(mean, var, mb, ri);
      o0[r] = ri; o1[r] = -mb * ri;
    }
    char* ln = sm + X_LN + w * 256;
    if (lane < 16) { *(f32x2*)(ln + li * 8) = o0; *(f32x2*)(ln + 128 + li * 8) = o1; }
    asm volatile("" ::: "memory");
    f32x4 ri[2], rb[2];
#pragma unroll
    for (int m = 0; m < 2; ++m) { ri[m] = *(const f32x4*)(ln + (8 * q + 4 * m) * 4); rb[m] = *(const f32x4*)(ln + 128 + (8 * q + 4 * m) * 4); }
#pragma unroll
    for (int jp = 0; jp < 4; ++jp) rs[jp] *= (jp & 1) ? hi2(ri[jp >> 1]) : lo2(ri[jp >> 1]);
    const unsigned zo = lane * 16 + O_ZF + (fp * 64 + 4 * tp + w) * 9216;
#pragma unroll
    for (int n = 0; n < 9; ++n) {
      u32x4 o4;
#pragma unroll
      for (int jp = 0; jp < 4; ++jp)
        o4[jp] = pk2(__builtin_elementwise_fma(up2(zb[n][jp]), rs[jp], (jp & 1) ? hi2(rb[jp >> 1]) : lo2(rb[jp >> 1])));
      st16(R, zo, n * 1024, o4);
    }
  }
  STAMP(3,5);
#pragma unroll 1
  for (int t = TCHK; t < 17; ++t) {
    if (t == TFLAG) wait_vm<0>();
    bar();
    if (t == TFLAG && tid < 16) st8(R, tid * 128, O_ZFL + fp * 2048 + tp * 8, tag);
    seg<0, 1, true>(acc, Y, X, c, t + 1, 0);
    seg<0, 1, true>(acc, X, Y, c, t + 1, 1);
  }
  float lrs[2], lnm[2];
  f32x4 cs1[9];
#pragma unroll
  for (int m = 0; m < 2; ++m) { const f32x2 h = *(const f32x2*)(sm + X_HS + w * 256 + m * 128 + li * 8); lrs[m] = h[0]; lnm[m] = h[1]; }
#pragma unroll
  for (int n = 0; n < 9; ++n) cs1[n] = *(const f32x4*)(sm + X_CS + (144 + 16 * n + 4 * q) * 4);
  __builtin_amdgcn_sched_barrier(0);
  seg<0, 1, false>(acc, Y, X, c, 17, 1);
  if (TFLAG == 17) arrive(4, 4, O_ZFL + fp * 2048 + tp * 8, 128, 16);
  STAMP(3,6);
  u32x2 z1[2][9];
  float bs[2];
#pragma unroll
  for (int m = 0; m < 2; ++m) bs[m] = ldbf(b_sp, 128 * tp + 32 * w + prow + 4 * m) * lrs[m];
  {
#pragma unroll
    for (int n = 0; n < 9; ++n)
#pragma unroll
      for (int m = 0; m < 2; ++m) {
        const float a1 = lrs[m] * lrs[m] * lrs[m] * GC1, a0 = lrs[m] * GC0;
        const f32x2 k1 = {a1, a1}, k0 = {a0, a0}, nm = {lnm[m], lnm[m]};
        z1[m][n][0] = pk2(gelu2(__builtin_elementwise_fma(nm, lo2(cs1[n]), lo2(acc[m][n])), k1, k0));
        z1[m][n][1] = pk2(gelu2(__builtin_elementwise_fma(nm, hi2(cs1[n]), hi2(acc[m][n])), k1, k0));
        asm volatile("" : "+v"(z1[m][n]));
      }
  }
  STAMP(3,7);
  bar();
  STAMP(3,8);
  ldf<1>(X, c, 18, 0);
  seg<1, 1, true, true>(acc, X, Y, c, 18, 1);
#pragma unroll 1
  for (int t = 18; t < 49; ++t) {
    bar();
    seg<1, 1, true>(acc, Y, X, c, t + 1, 0);
    seg<1, 1, true>(acc, X, Y, c, t + 1, 1);
  }
  seg<1, 1, false>(acc, Y, X, c, 49, 1);
  STAMP(3,9);
  {
    const unsigned pf = O_PF + (((2 * tp + (w >> 1)) * 72 + 9 * (fp >> 1)) * 4 + 2 * (w & 1)) * 1024;
    const unsigned po = lane * 16 + pf + (fp & 1) * 16384;
#pragma unroll
    for (int m = 0; m < 2; ++m) {
      const f32x2 r2 = {lrs[m], lrs[m]}, b2 = {bs[m], bs[m]};
      auto pp = [&](int n) { return u32x2{pk2(__builtin_elementwise_fma(lo2(acc[m][n]), r2, b2) * up2(z1[m][n][0])), pk2(__builtin_elementwise_fma(hi2(acc[m][n]), r2, b2) * up2(z1[m][n][1]))}; };
#pragma unroll
      for (int a = 0; a < 4; ++a) {
        u32x2 p0 = pp(2 * a), p1 = pp(2 * a + 1);
        st16(R, po, a * 4096 + m * 1024, u32x4{p0[0], p0[1], p1[0], p1[1]});
      }
      st8(R, lane * 16, pf + 8 * 4096 + m * 1024 + (fp & 1) * 8, pp(8));
    }
  }
  STAMP(3,10);
  const int th = w & 1, kq = w >> 1, T = 4 * t64 + 2 * th + kq;
  const int lane5 = fresh_lane();
#define lane lane5
#define li (lane5 & 15)
#define q (lane5 >> 4)
#define prow (8 * (li >> 2) + (li & 3))
  struct P3 { u32x4 p[2][2]; } Up, Vp;
  struct N3 { u32x4 n[9]; } Un, Vn, En;
  const int ap = (8 * kq + 2 * th) * 1024 + lane * 16, aw = 16384 + kq * 9216 + lane * 16;
  auto ldp = [&](P3& f, int t) {
#pragma unroll
    for (int g = 0; g < 2; ++g)
#pragma unroll
      for (int m = 0; m < 2; ++m) f.p[g][m] = *(const u32x4*)(sm + (t & 3) * STG + ap + (4 * g + m) * 1024);
  };
  auto ldn = [&](N3& f, int t) {
#pragma unroll
    for (int n = 0; n < 9; ++n) f.n[n] = *(const u32x4*)(sm + (t & 3) * STG + aw + n * 1024);
  };
  bar();
  ldn(Un, 50); ldn(Vn, 51); ldn(En, 52);
  arrive(2, 4, O_PFL + tp * 2048 + fp * 8, 128, 16);
  bar();
  u32x2 hv[5];
#pragma unroll
  for (int n = 0; n < 5; ++n) {
    const int dd = 72 * j3 + 16 * n + 4 * (n == 4 ? (q & 1) : q);
    hv[n] = ld8(R, (dd >> 5) * 8192 + ((dd >> 2) & 1) * 512 + (((dd >> 3) & 3) * 16 + li) * 8, O_HF + ((T >> 3) * 18 * 8 + (T & 7)) * 1024);
  }
  f32x4 a3[2][6] = {};
  auto mm3 = [&](const P3& f, const N3& e) {
#pragma unroll
    for (int g = 0; g < 2; ++g)
#pragma unroll
      for (int m = 0; m < 2; ++m) {
#pragma unroll
        for (int n = 0; n < 4; ++n) a3[m][n] = mfma_bf(e.n[4 * g + n], f.p[g][m], a3[m][n]);
        a3[m][4 + g] = mfma_bf(e.n[8], f.p[g][m], a3[m][4 + g]);
      }
#pragma unroll
    for (int i = 0; i < 13; ++i) { __builtin_amdgcn_sched_group_barrier(0x008, 1, 0); __builtin_amdgcn_sched_group_barrier(0x100, 1, 0); }
    __builtin_amdgcn_sched_barrier(0);
  };
  STAMP(3,11);
  bar();
  STAMP(3,12);
  ldp(Up, 50);
  bar(); ldp(Vp, 51); mm3(Up, Un);
  bar(); ldp(Up, 52); mm3(Vp, Vn);
  bar(); ldp(Vp, 53); ldn(Vn, 53); mm3(Up, En);
#pragma unroll 1
  for (int t = 53; t < 67; t += 2) { bar(); ldp(Up, t + 1); ldn(Un, t + 1); mm3(Vp, Vn); bar(); ldp(Vp, t + 2); ldn(Vn, t + 2); mm3(Up, Un); }
  mm3(Vp, Vn);
  STAMP(3,13);
#pragma unroll
  for (int m = 0; m < 2; ++m) a3[m][4] = lane < 32 ? a3[m][4] : a3[m][5];
  if (kq) {
#pragma unroll
    for (int n = 0; n < 5; ++n) { *(f32x4*)(sm + w * 5120 + n * 1024 + lane * 16) = a3[0][n]; asm volatile("" : "+v"(a3[1][n])); a3[0][n] = a3[1][n]; }
  } else {
#pragma unroll
    for (int n = 0; n < 5; ++n) *(f32x4*)(sm + w * 5120 + n * 1024 + lane * 16) = a3[1][n];
  }
  bar();
  char* o = out + (size_t)(32 * (T >> 1) + 4 * (T & 1) + prow) * 1152 + (72 * j3 + 4 * q) * 2;
#pragma unroll
  for (int n = 0; n < 5; ++n) {
    f32x4 v = a3[0][n] + *(const f32x4*)(sm + (w ^ 2) * 5120 + n * 1024 + lane * 16);
    if (n == 4) v = f32x4{half_sum(v[0]), half_sum(v[1]), half_sum(v[2]), half_sum(v[3])};
    if (n < 4 || q < 2) *(u32x2*)(o + n * 32) = u32x2{pk2(up2(hv[n][0]) + lo2(v)), pk2(up2(hv[n][1]) + hi2(v))};
  }
  if (bid == 0 && w == 0 && lane == 0) { u64 e = (((u64)tag[1] << 32) | tag[0]) - 0x9E3779B97F4A7C15ull + 1; st8(R, 0, O_EP, u32x2{(unsigned)e, (unsigned)(e >> 32)}); }
  STAMP(3,14);
}

#undef lane
#undef li
#undef q
#undef prow
extern "C" void kernel_launch(void* const* d_in, const int* in_sizes, int n_in, void* d_out, int out_size, void* d_ws, size_t ws_size,
                              hipStream_t stream) {
  (void)hipFuncSetAttribute((const void*)k_all, hipFuncAttributeMaxDynamicSharedMemorySize, LDS_BYTES);
  k_all<<<256, 512, LDS_BYTES, stream>>>((const char*)d_in[0], (const char*)d_in[1], (const char*)d_in[2], (const char*)d_in[3],
      (const char*)d_in[4], (const char*)d_in[5], (const char*)d_in[6], (const char*)d_in[7], (const char*)d_in[8], (const char*)d_in[10],
      (const char*)d_in[11], (const char*)d_in[12], (const char*)d_in[13], (char*)d_out, (char*)d_ws);
}
```

```cpp
#include <hip/hip_runtime.h>
#include <hip/hip_bf16.h>
#include <stdint.h>

typedef __attribute__((ext_vector_type(8))) __bf16 bf16x8;
typedef __attribute__((ext_vector_type(2))) __bf16 bf16x2;
typedef __attribute__((ext_vector_type(8))) _Float16 f16x8;
typedef __attribute__((ext_vector_type(2))) _Float16 f16x2;
typedef __attribute__((ext_vector_type(4))) float f32x4;
typedef __attribute__((ext_vector_type(2))) float f32x2;
typedef __attribute__((ext_vector_type(4))) unsigned u32x4;
typedef __attribute__((ext_vector_type(2))) unsigned u32x2;
typedef __attribute__((ext_vector_type(4))) short s16x4;
#define LDSP __attribute__((address_space(3)))
#define DEVI __device__ __forceinline__

constexpr int L = 2048, D = 576, NM = 128, F = 4608, FH = 2304;
constexpr float EPS = 1e-5f;
constexpr unsigned O_EP = 0;
constexpr int NREP = 32;
constexpr unsigned O_F1 = 256;
constexpr unsigned O_F2 = O_F1 + NREP * 1024;
constexpr unsigned F2S = 2048;
constexpr unsigned O_ZFL = O_F2 + NREP * F2S;
constexpr unsigned O_PFL = O_ZFL + 32768;
constexpr unsigned O_CS = 188416;
static_assert(O_PFL + 32768 <= O_CS, "ws");
constexpr unsigned O_ST = O_CS + F * 12;
constexpr unsigned O_HS = O_ST + 262144;
constexpr unsigned O_U = O_HS + 16384;
constexpr unsigned O_XB = O_U + L * D * 2;
constexpr unsigned O_WI = O_XB + L * D * 2;
constexpr unsigned O_WO = O_WI + D * F * 2;
constexpr unsigned O_HF = O_WO + FH * D * 2;
constexpr unsigned O_ZF = O_HF + L * D * 2;
constexpr unsigned O_PF = O_ZF + L * FH * 2;
constexpr unsigned WS_USED = O_PF + L * FH * 2;
constexpr size_t O_STAMP = 128u << 20;
#define STAMP(k,i) __builtin_amdgcn_sched_barrier(0)
#define STAMPW(k,i,W) __builtin_amdgcn_sched_barrier(0)
#define RSTAMP(k,i)
constexpr unsigned SENT = 0xFFFFFFFFu;

DEVI unsigned pk_bf16(float a, float b) { f32x2 v = {a, b}; return __builtin_bit_cast(unsigned, __builtin_convertvector(v, bf16x2)); }
DEVI unsigned pk_f16(float a, float b) { f32x2 v = {a, b}; return __builtin_bit_cast(unsigned, __builtin_convertvector(v, f16x2)); }
DEVI float bf_lo(unsigned u) { return __builtin_bit_cast(float, u << 16); }
DEVI float bf_hi(unsigned u) { return __builtin_bit_cast(float, u & 0xFFFF0000u); }
DEVI float asf(unsigned u) { return __builtin_bit_cast(float, u); }
DEVI unsigned asu(float f) { return __builtin_bit_cast(unsigned, f); }
DEVI float rbf(float a) { return bf_lo(pk_bf16(a, a)); }
DEVI float h_lo(unsigned u) { return (float)__builtin_bit_cast(f16x2, u)[0]; }
DEVI float h_hi(unsigned u) { return (float)__builtin_bit_cast(f16x2, u)[1]; }
template <int CTRL> DEVI float dpp_add(float v) {
  return v + __builtin_bit_cast(float, __builtin_amdgcn_update_dpp(0, __builtin_bit_cast(int, v), CTRL, 0xF, 0xF, true));
}
DEVI float row16_sum(float v) { v = dpp_add<0x128>(v); v = dpp_add<0x124>(v); v = dpp_add<0x122>(v); v = dpp_add<0x121>(v); return v; }
DEVI float half_sum(float v) { auto r = __builtin_amdgcn_permlane32_swap(asu(v), asu(v), false, false); return asf(r[0]) + asf(r[1]); }
DEVI float q_sum(float v) { auto r = __builtin_amdgcn_permlane16_swap(asu(v), asu(v), false, false); return half_sum(asf(r[0]) + asf(r[1])); }
DEVI unsigned umax4(u32x4 v) { return max(max(v[0], v[1]), max(v[2], v[3])); }
DEVI u32x2 tr_read(const char* p) {
  return __builtin_bit_cast(u32x2, __builtin_amdgcn_ds_read_tr16_b64_v4i16((LDSP s16x4*)p));
}
DEVI f32x4 mfma_bf(u32x4 a, u32x4 b, f32x4 c) {
  return __builtin_amdgcn_mfma_f32_16x16x32_bf16(__builtin_bit_cast(bf16x8, a), __builtin_bit_cast(bf16x8, b), c, 0, 0, 0);
}
DEVI f32x4 mfma_h(u32x4 a, u32x4 b, f32x4 c) {
  return __builtin_amdgcn_mfma_f32_16x16x32_f16(__builtin_bit_cast(f16x8, a), __builtin_bit_cast(f16x8, b), c, 0, 0, 0);
}
DEVI void ln_scalars(float mean, float var, float& mb, float& rinv) {
  mb = rbf(mean);
  float d = rbf(__builtin_amdgcn_sqrtf(rbf(rbf(var) + EPS)));
  rinv = __builtin_amdgcn_rcpf(d);
}
DEVI float gelu(float x) {
  float w = x * __builtin_fmaf(x * x, -0.10294324f, -2.3022082f);
  return x * __builtin_amdgcn_rcpf(1.0f + __builtin_amdgcn_exp2f(w));
}

typedef unsigned long long u64;
typedef __amdgpu_buffer_rsrc_t rsrc_t;
constexpr int SPIN = 1 << 16;
constexpr int CODE_LINES = 39;
template <int CTRL> DEVI float dpp_get(float v) {
  return __builtin_bit_cast(float, __builtin_amdgcn_update_dpp(0, __builtin_bit_cast(int, v), CTRL, 0xF, 0xF, true));
}
constexpr float GC1 = -0.10294324f, GC0 = -2.3022082f;
DEVI f32x2 gelu2(f32x2 x, f32x2 k1, f32x2 k0) {
  f32x2 w = x * __builtin_elementwise_fma(x * x, k1, k0);
  f32x2 d = f32x2{__builtin_amdgcn_exp2f(w[0]), __builtin_amdgcn_exp2f(w[1])} + f32x2{1.f, 1.f};
  return x * f32x2{__builtin_amdgcn_rcpf(d[0]), __builtin_amdgcn_rcpf(d[1])};
}
DEVI unsigned pk2(f32x2 v) { return __builtin_bit_cast(unsigned, __builtin_convertvector(v, bf16x2)); }
DEVI f32x2 up2(unsigned u) { return f32x2{bf_lo(u), bf_hi(u)}; }
DEVI f32x2 lo2(f32x4 v) { return __builtin_shufflevector(v, v, 0, 1); }
DEVI f32x2 hi2(f32x4 v) { return __builtin_shufflevector(v, v, 2, 3); }
DEVI float dot2(unsigned a, unsigned b, float c) {
  return __builtin_amdgcn_fdot2_f32_bf16(__builtin_bit_cast(bf16x2, a), __builtin_bit_cast(bf16x2, b), c, false);
}
DEVI void st16(rsrc_t r, unsigned voff, unsigned soff, u32x4 v) { __builtin_amdgcn_raw_buffer_store_b128(v, r, voff + soff, 0, 16); }
DEVI void st8(rsrc_t r, unsigned voff, unsigned soff, u32x2 v) { __builtin_amdgcn_raw_buffer_store_b64(v, r, voff, soff, 16); }
DEVI void st4(rsrc_t r, unsigned voff, unsigned soff, unsigned v) { __builtin_amdgcn_raw_buffer_store_b32(v, r, voff, soff, 16); }
DEVI u32x4 ld16(rsrc_t r, unsigned voff, unsigned soff) { return __builtin_amdgcn_raw_buffer_load_b128(r, voff, soff, 16); }
DEVI u32x2 ld8(rsrc_t r, unsigned voff, unsigned soff) { return __builtin_amdgcn_raw_buffer_load_b64(r, voff, soff, 16); }
template <int N> DEVI void wait_vm() { asm volatile("s_waitcnt vmcnt(%0)" ::"i"(N) : "memory"); }
DEVI void bar() { __builtin_amdgcn_sched_barrier(0); asm volatile("s_waitcnt lgkmcnt(0)\n\ts_barrier" ::: "memory"); __builtin_amdgcn_sched_barrier(0); }
DEVI int fresh_lane() { int l; asm volatile("v_mbcnt_lo_u32_b32 %0, -1, 0\n\tv_mbcnt_hi_u32_b32 %0, -1, %0" : "=v"(l)); return l; }
DEVI unsigned lds_addr(const char* p) { return (unsigned)(uintptr_t)(LDSP const char*)p; }
template <int NL> DEVI void wait_flags(rsrc_t r, unsigned off, int n, u32x2 tag, int lane) {
#pragma unroll 1
  for (int spin = 0; spin < SPIN; ++spin) {
    bool ok = true;
#pragma unroll
    for (int k = 0; k < NL; ++k) {
      int i = min(lane + 64 * k, n / 2 - 1);
      u32x4 v = ld16(r, i * 16, off);
      ok = ok && v[0] == tag[0] && v[1] == tag[1] && v[2] == tag[0] && v[3] == tag[1];
    }
    if (__all(ok)) break;
    __builtin_amdgcn_s_sleep(1);
    asm volatile("" ::: "memory");
  }
  asm volatile("" ::: "memory");
}
template <bool SC> DEVI void glds4(unsigned m0v, const char* base, unsigned v0, unsigned v1, unsigned v2, unsigned v3) {
  unsigned keep;
  if (SC)
    asm volatile("s_mov_b32 %0, m0\n\ts_mov_b32 m0, %1\n\ts_nop 2\n\t"
                 "global_load_lds_dwordx4 %3, %2 sc1\n\tglobal_load_lds_dwordx4 %4, %2 offset:1024 sc1\n\t"
                 "global_load_lds_dwordx4 %5, %2 offset:2048 sc1\n\tglobal_load_lds_dwordx4 %6, %2 offset:3072 sc1\n\ts_mov_b32 m0, %0"
                 : "=&s"(keep) : "s"(m0v), "s"(base), "v"(v0), "v"(v1), "v"(v2), "v"(v3) : "memory");
  else
    asm volatile("s_mov_b32 %0, m0\n\ts_mov_b32 m0, %1\n\ts_nop 2\n\t"
                 "global_load_lds_dwordx4 %3, %2\n\tglobal_load_lds_dwordx4 %4, %2 offset:1024\n\t"
                 "global_load_lds_dwordx4 %5, %2 offset:2048\n\tglobal_load_lds_dwordx4 %6, %2 offset:3072\n\ts_mov_b32 m0, %0"
                 : "=&s"(keep) : "s"(m0v), "s"(base), "v"(v0), "v"(v1), "v"(v2), "v"(v3) : "memory");
}
DEVI void glds1(unsigned m0v, const char* base, unsigned v0) {
  unsigned keep;
  asm volatile("s_mov_b32 %0, m0\n\ts_mov_b32 m0, %1\n\ts_nop 2\n\tglobal_load_lds_dwordx4 %3, %2 sc1\n\ts_mov_b32 m0, %0"
               : "=&s"(keep) : "s"(m0v), "s"(base), "v"(v0) : "memory");
}
template <int NP> DEVI void ld_lin(unsigned dst, const char* src, unsigned l16) {
  glds4<true>(dst, src, l16, l16, l16, l16);
  glds4<true>(dst + 4096, src + 4096, l16, l16, l16, l16);
  if (NP == 9) glds1(dst + 8192, src + 8192, l16);
}

constexpr int TP = 1168;
constexpr int CV_K = 30208, CV_C = 4160;
constexpr int C_U = 4 * CV_K, C_KD = C_U + 16 * TP, C_END = C_KD + 4 * 1536;
constexpr int KT = 528;
constexpr int STG = 34816, RING = 4 * STG;
constexpr int X_CS = RING, X_LN = X_CS + 1152, X_PV = X_LN + 1024, X_HS = X_PV + 16384, X_CNT = 158976;
constexpr int LDS_BYTES = X_CNT + 64 + 256;
static_assert(C_END <= X_CNT && (15 + 128) * 128 <= 16 * TP && 57 * KT <= CV_K && X_HS + 1024 <= X_CNT && LDS_BYTES <= 163840, "lds");
#define TPOLL 12
#define WDELAY 4
#define SDELAY 1
#define ROT3 0
#define TCHK 13
#define TFLAG 16

struct Fr { u32x4 m[2], n[9]; };
struct Ctx { const char* sm; int am0[4], am1, an; };
template <int MK> DEVI void ldf(Fr& f, const Ctx& c, int t, int kk) {
  int so = (t & 3) * STG;
  asm volatile("" : "+s"(so));
  const char* slot = c.sm + so;
  if (MK == 0) {
#pragma unroll
    for (int m = 0; m < 2; ++m) {
      const char* p = slot + c.am0[0] + kk * 8192 + m * 1024;
      u32x2 a = *(volatile const LDSP u32x2*)p, b = *(volatile const LDSP u32x2*)(p + 512);
      f.m[m] = u32x4{a[0], a[1], b[0], b[1]};
    }
  } else {
#pragma unroll
    for (int m = 0; m < 2; ++m) f.m[m] = *(const u32x4*)(slot + (c.am1 ^ (kk * 64)) + m * 2048);
  }
#pragma unroll
  for (int n = 0; n < 9; ++n) f.n[n] = *(const u32x4*)(slot + c.an + (kk * 9 + n) * 1024);
}
template <int MK, int ORI, bool LD, bool FIRST = false> DEVI void seg(f32x4 (&acc)[2][9], const Fr& cur, Fr& nxt, const Ctx& c, int t, int kk) {
  __builtin_amdgcn_s_waitcnt(0xC07F);
  __builtin_amdgcn_sched_barrier(0);
  if (LD) ldf<MK>(nxt, c, t, kk);
#pragma unroll
  for (int m = 0; m < 2; ++m)
#pragma unroll
    for (int nn = 0; nn < 9; ++nn) {
      const int n = m ? 8 - nn : nn;
      const f32x4 ci = FIRST ? f32x4{0, 0, 0, 0} : acc[m][n];
      acc[m][n] = ORI == 0 ? mfma_bf(cur.m[m], cur.n[n], ci) : mfma_bf(cur.n[n], cur.m[m], ci);
    }
#pragma unroll
  for (int i = 0; i < (MK == 0 ? 13 : 11); ++i) { __builtin_amdgcn_sched_group_barrier(0x008, 1, 0); __builtin_amdgcn_sched_group_barrier(0x100, 1, 0); }
  __builtin_amdgcn_sched_barrier(0);
}
DEVI f32x2 cmul(f32x2 a, f32x2 b, f32x2 bp) {
  return __builtin_elementwise_fma(f32x2{a[1], a[1]}, bp, f32x2{a[0], a[0]} * b);
}
DEVI f32x2 perp(f32x2 b) { return f32x2{-b[1], b[0]}; }
DEVI f32x2 cexp(float ar, float ai, float l) {
  float m = __builtin_amdgcn_exp2f(ar * l), ph = __builtin_amdgcn_fractf(ai * l);
  return f32x2{m * __builtin_amdgcn_cosf(ph), m * __builtin_amdgcn_sinf(ph)};
}
DEVI unsigned pkh(f32x2 v) { return __builtin_bit_cast(unsigned, __builtin_convertvector(v, f16x2)); }
DEVI float ldbf(const char* p, int i) { return bf_lo(*(const unsigned short*)(p + 2 * i)); }

__global__ __launch_bounds__(512, 2) void k_all(const char* __restrict__ x, const char* __restrict__ a_re, const char* __restrict__ a_im,
    const char* __restrict__ b_re, const char* __restrict__ b_im, const char* __restrict__ c_re, const char* __restrict__ c_im,
    const char* __restrict__ log_dt, const char* __restrict__ dskip, const char* __restrict__ w_in, const char* __restrict__ w_sp,
    const char* __restrict__ b_sp, const char* __restrict__ w_out, char* __restrict__ out, char* __restrict__ ws) {
  extern __shared__ __attribute__((aligned(16))) char sm[];
  const int tid = threadIdx.x, lane = tid & 63, w = __builtin_amdgcn_readfirstlane(tid >> 6), li = lane & 15, q = lane >> 4;
  const int bid = blockIdx.x, xc = bid & 7, ii = bid >> 3;
  if (w == 7) {
    unsigned long long pc;
    asm volatile("s_getpc_b64 %0" : "=s"(pc));
    const char* cp = (const char*)(pc & ~255ull) + (ii & 7) * (CODE_LINES * 128);
    unsigned keep;
    asm volatile("s_mov_b32 %0, m0\n\ts_mov_b32 m0, %1\n\ts_nop 2\n\tglobal_load_lds_dword %3, %2\n\ts_mov_b32 m0, %0"
                 : "=&s"(keep) : "s"(lds_addr(sm) + X_CNT + 64), "s"(cp), "v"(min(lane, CODE_LINES - 1) * 128) : "memory");
  }
  if (tid == 256) *(u32x4*)(sm + X_CNT) = u32x4{0, 0, 0, 0};
  asm volatile("" ::"s"(x), "s"(a_re), "s"(a_im), "s"(b_re), "s"(b_im), "s"(c_re), "s"(c_im), "s"(log_dt), "s"(dskip), "s"(w_in), "s"(w_out), "s"(ws));
  const rsrc_t R = __builtin_amdgcn_make_buffer_rsrc(ws, 0, WS_USED, 0x00020000);
  STAMP(0,0);
  const u32x2 ep = ld8(R, 0, O_EP);
  const bool is_conv = ii < 18;
  const int cg = xc * 18 + ii, rep = bid & (NREP - 1);
  auto mk_tag = [&]() {
    u64 e = (((u64)__builtin_amdgcn_readfirstlane(ep[1]) << 32) | (unsigned)__builtin_amdgcn_readfirstlane(ep[0])) + 0x9E3779B97F4A7C15ull;
    return u32x2{(unsigned)e, (unsigned)(e >> 32)};
  };
  u32x2 tag;
  auto arrive = [&](int slot, unsigned n, unsigned flag_off, unsigned stride, int nrep) {
    wait_vm<0>();
    const int lane = fresh_lane();
    unsigned old = 0;
    if (lane == 0) old = __hip_atomic_fetch_add((unsigned*)(sm + X_CNT) + slot, 1u, __ATOMIC_RELAXED, __HIP_MEMORY_SCOPE_WORKGROUP);
    if (__builtin_amdgcn_readfirstlane(old) == n - 1 && lane < nrep) st8(R, lane * stride, flag_off, tag);
  };
  if (is_conv) {
    const int ch = w & 3, dch = 4 * cg + ch;
    char* sk = sm + ch * CV_K;
    float sc = 0.f;
    if (w < 4) {
      const int pi = (dch * NM + 2 * lane) * 2;
      const unsigned v_ar = *(const unsigned*)(a_re + pi), v_ai = *(const unsigned*)(a_im + pi), v_br = *(const unsigned*)(b_re + pi),
                     v_bi = *(const unsigned*)(b_im + pi), v_cr = *(const unsigned*)(c_re + pi), v_ci = *(const unsigned*)(c_im + pi);
      const float p_dk = ldbf(dskip, dch);
      const float dt = rbf(__expf(ldbf(log_dt, dch)));
      tag = mk_tag();
      STAMP(0,1);
      const f32x2 ar = (up2(v_ar) * dt) * 1.44269504f, ai = (up2(v_ai) * dt) * 0.15915494f;
      const f32x2 br = up2(v_br) * dt, bi = up2(v_bi) * dt, cr = up2(v_cr), ci = up2(v_ci);
      const f32x2 cbr = cr * br - ci * bi, cbi = -(cr * bi + ci * br);
      float mx = fmaxf(fmaxf(fabsf(cbr[0]), fabsf(cbi[0])), fmaxf(fabsf(cbr[1]), fabsf(cbi[1])));
      mx = fmaxf(mx, dpp_get<0x128>(mx)); mx = fmaxf(mx, dpp_get<0x124>(mx)); mx = fmaxf(mx, dpp_get<0x122>(mx)); mx = fmaxf(mx, dpp_get<0x121>(mx));
      auto rl = [&](int l) { return asf(__builtin_amdgcn_readlane((int)asu(mx), l)); };
      sc = fmaxf(fmaxf(rl(0), rl(16)), fmaxf(rl(32), rl(48)));
      sc = fmaxf(sc, fabsf(p_dk) * (1.0f / 256));
      const float isc = sc > 0.f ? 1.0f / sc : 0.f;
      {
        struct C2 { f32x2 a, b; };
        char* tb = sk + lane * 8;
        auto put = [&](int row, C2 v) { *(u32x2*)(tb + row * KT) = u32x2{pkh(v.a), pkh(v.b)}; };
        auto mul = [&](C2 v, C2 u, C2 up) { return C2{cmul(v.a, u.a, up.a), cmul(v.b, u.b, up.b)}; };
        auto prp = [&](C2 v) { return C2{perp(v.a), perp(v.b)}; };
        auto ex = [&](float l) { return C2{cexp(ar[0], ai[0], l), cexp(ar[1], ai[1], l)}; };
        const C2 w1 = ex(1.f), w128 = ex(128.f);
        C2 w8 = ex(8.f); w8.a[1] = -w8.a[1]; w8.b[1] = -w8.b[1];
        const C2 w1p = prp(w1), w8p = prp(w8), w128p = prp(w128);
        C2 w64 = ex(64.f); w64.a[1] = -w64.a[1]; w64.b[1] = -w64.b[1];
        const C2 w1k = ex(1024.f), w64p = prp(w64), w1kp = prp(w1k);
        C2 y = {f32x2{cbr[0], cbi[0]} * isc, f32x2{cbr[1], cbi[1]} * isc};
        C2 E[8]; E[0] = C2{f32x2{1.f, 0.f}, f32x2{1.f, 0.f}}; E[1] = w1;
        C2 H = w128;
        put(0, E[0]); put(8, w128); put(32, w1k); put(40, mul(w1k, w1k, w1kp));
#pragma unroll
        for (int e = 1; e < 8; ++e) {
          if (e > 1) E[e] = mul(E[e - 1], w1, w1p);
          put(e, E[e]); put(8 + e, mul(E[e], w128, w128p));
        }
        const C2 e2p = prp(E[2]), e4p = prp(E[4]), e6p = prp(E[6]);
#pragma unroll
        for (int a = 0; a < 8; ++a) {
          put(41 + a, y); put(49 + a, mul(y, w64, w64p));
          if (a < 7) y = mul(y, w8, w8p);
          if (a >= 2) H = mul(H, w128, w128p);
          if (a >= 1) put(32 + a, mul(H, w1k, w1kp));
          if (a >= 4) { put(16 + 2 * a, H); put(17 + 2 * a, mul(H, E[4], e4p)); }
          else if (a >= 2) { put(8 + 4 * a, H); put(9 + 4 * a, mul(H, E[2], e2p)); put(10 + 4 * a, mul(H, E[4], e4p)); put(11 + 4 * a, mul(H, E[6], e6p)); }
        }
      }
      asm volatile("" ::: "memory");
      STAMP(0,2);
      {
        f32x4 kk[3] = {};
        const char* pa = sk + li * KT + q * 16;
        u32x4 fr[8][4];
#pragma unroll
        for (int s = 0; s < 8; ++s)
#pragma unroll
          for (int t = 0; t < 4; ++t) fr[s][t] = *(const u32x4*)(pa + (t < 3 ? 16 * t : 41) * KT + s * 64);
        __builtin_amdgcn_sched_barrier(0);
#pragma unroll
        for (int s = 0; s < 8; ++s)
#pragma unroll
          for (int t = 0; t < 3; ++t) kk[t] = mfma_h(fr[s][t], fr[s][3], kk[t]);
        if (lane == 0) kk[0][0] += p_dk * isc;
        char* kd = sm + C_KD + ch * 1536;
        *(u32x2*)(kd + (128 * (q >> 1) + 4 * (q & 1) + 8 * li) * 2) = u32x2{pkh(lo2(kk[0])), pkh(hi2(kk[0]))};
        const int i1 = q < 2 ? 256 + 64 * q + 4 * li : 384 + 64 * (q - 2) + 2 * li;
        *(unsigned*)(kd + i1 * 2) = pkh(lo2(kk[1])); *(unsigned*)(kd + (i1 + (q < 2 ? 2 : 32)) * 2) = pkh(hi2(kk[1]));
#pragma unroll
        for (int r = 0; r < 4; ++r) *(_Float16*)(kd + (512 + 16 * (4 * q + r) + li) * 2) = (_Float16)kk[2][r];
      }
      asm volatile("" ::: "memory");
      u32x4 c0[4];
      {
        const unsigned short* kd = (const unsigned short*)(sm + C_KD + ch * 1536);
#pragma unroll
        for (int rr = 0; rr < 2; ++rr) {
          const int k = 255 - 64 * rr - lane;
          const unsigned a = kd[384 + k], b = kd[385 + k], aa = a | (a << 16);
          c0[rr] = u32x4{b | (b << 16), b | (((k & 1) ? b : a) << 16), aa, aa};
        }
        { const int k = 127 - lane;
          const unsigned ab = *(const unsigned*)(kd + 256 + 2 * k), cc = kd[258 + 2 * k];
          c0[2] = u32x4{cc | (cc << 16), __builtin_amdgcn_perm(ab, ab, 0x03020302), __builtin_amdgcn_perm(ab, ab, 0x01000302), __builtin_amdgcn_perm(ab, ab, 0x01000100)}; }
        { const int k = 63 - lane;
          u32x4 v = *(const u32x4*)(kd + 8 * (k & 31));
          u32x2 u = *(const u32x2*)(kd + 128 + 4 * k); unsigned e = kd[132 + 4 * k];
          asm volatile("" : "+v"(v), "+v"(u), "+v"(e));
          c0[3] = lane < 32 ? u32x4{__builtin_amdgcn_perm(u[1], e, 0x07060100), __builtin_amdgcn_perm(u[1], u[1], 0x01000100), __builtin_amdgcn_perm(u[0], u[1], 0x07060100), __builtin_amdgcn_perm(u[0], u[0], 0x01000100)}
                            : u32x4{__builtin_amdgcn_alignbit(v[3], v[3], 16), __builtin_amdgcn_alignbit(v[2], v[2], 16), __builtin_amdgcn_alignbit(v[1], v[1], 16), __builtin_amdgcn_alignbit(v[0], v[0], 16)}; }
#pragma unroll
        for (int rr = 0; rr < 4; ++rr) *(u32x4*)(sk + (64 * rr + lane) * 16) = c0[rr];
        if (lane < 8) *(u32x4*)(sk + (lane >> 1) * CV_C + 4096 + (lane & 1) * 16) = u32x4{0, 0, 0, 0};
      }
      asm volatile("" ::: "memory");
      STAMP(0,3);
      u32x2 c1[4];
#pragma unroll
      for (int rr = 0; rr < 4; ++rr) c1[rr] = *(const u32x2*)(sk + (64 * rr + lane) * 16 + 16);
#pragma unroll
      for (int rr = 0; rr < 4; ++rr) {
        char* p = sk + (64 * rr + lane) * 16;
        unsigned e[6] = {c0[rr][0], c0[rr][1], c0[rr][2], c0[rr][3], c1[rr][0], c1[rr][1]}, o[5];
#pragma unroll
        for (int i = 0; i < 5; ++i) o[i] = __builtin_amdgcn_alignbit(e[i + 1], e[i], 16);
        *(u32x4*)(p + CV_C) = u32x4{o[0], o[1], o[2], o[3]};
        *(u32x4*)(p + 2 * CV_C) = u32x4{e[1], e[2], e[3], e[4]};
        *(u32x4*)(p + 3 * CV_C) = u32x4{o[1], o[2], o[3], o[4]};
      }
      STAMP(0,4);
    } else {
      char* s0 = sm + C_U;
      __builtin_amdgcn_s_setprio(2);
      u32x2 xr[9];
      const char* src = x + (size_t)(16 * min(cg, 127) + 4 * ch + q) * 1152 + li * 8;
#pragma unroll
      for (int k = 0; k < 9; ++k) xr[k] = *(const u32x2*)(src + k * 128);
      tag = mk_tag();
      if (cg < 128) {
        asm volatile("" :: "v"(xr[8])); STAMPW(1,0,4);
        float a[4] = {0, 0, 0, 0};
#pragma unroll
        for (int k = 0; k < 9; ++k)
#pragma unroll
          for (int e = 0; e < 2; ++e) { a[e] = dot2(xr[k][e], 0x3F803F80u, a[e]); a[2 + e] = dot2(xr[k][e], xr[k][e], a[2 + e]); }
        const float s = row16_sum(a[0] + a[1]), ss = row16_sum(a[2] + a[3]);
        float mean = s * (1.0f / D), mb, rinv;
        ln_scalars(mean, fmaxf(ss * (1.0f / D) - mean * mean, 0.f), mb, rinv);
        const f32x2 r2 = {rinv, rinv}, b2 = {-mb * rinv, -mb * rinv};
        char* p = s0 + (4 * ch + q) * TP + li * 8;
#pragma unroll
        for (int k = 0; k < 9; ++k)
          *(u32x2*)(p + k * 128) = u32x2{pkh(__builtin_elementwise_fma(up2(xr[k][0]), r2, b2)), pkh(__builtin_elementwise_fma(up2(xr[k][1]), r2, b2))};
        asm volatile("" ::: "memory");
        if (lane == 0) *(volatile LDSP u32x2*)(sm + X_CNT + 32 + ch * 8) = tag;
#pragma unroll
        for (int k = 0; k < 9; ++k) st8(R, li * 32 + q * 8 + k * 512, O_XB + (4 * cg + ch) * 4608, xr[k]);
#pragma unroll 1
        for (int spin = 0; spin < (SPIN << 4); ++spin) {
          const u32x2 v = *(volatile const LDSP u32x2*)(sm + X_CNT + 32 + (lane & 3) * 8);
          if (__all(v[0] == tag[0] && v[1] == tag[1])) break;
        }
        asm volatile("" ::: "memory");
        STAMPW(1,1,4);
        const char* trb = s0 + (8 * (q & 1) + (li >> 2)) * TP + (q >> 1) * 32 + (li & 3) * 8 + ch * 64;
        const unsigned uo = (16 * (q >> 1) + li) * 32 + (q & 1) * 16 + O_U + cg * 18432 + ch * 1024;
#pragma unroll
        for (int k = 0; k < 5; ++k)
          if (k < 4 || ch < 2) {
            u32x2 lo = tr_read(trb + k * 256), hi = tr_read(trb + 4 * TP + k * 256);
            st16(R, uo, k * 4096, u32x4{lo[0], lo[1], hi[0], hi[1]});
          }
        STAMPW(1,2,4);
        arrive(0, 4, O_F1 + cg * 8, 1024, NREP);
        STAMPW(1,3,4);
      }
      __builtin_amdgcn_s_setprio(0);
      if (cg < 136 && w >= 6) st16(R, (tid - 384) * 16, O_ST + cg * 2048, u32x4{SENT, SENT, SENT, SENT});
      if (w == 4) {
        wait_flags<1>(R, O_F1 + rep * 1024, 128, tag, lane);
        STAMPW(5,0,4);
        const int r0 = 15 + (lane >> 3);
        const unsigned va = (lane >> 3) * 18432 + (((lane & 7) ^ ((r0 >> 1) & 7)) * 16), vb = (lane >> 3) * 18432 + (((lane & 7) ^ (((r0 + 8) >> 1) & 7)) * 16), dv = 8 * 18432 - 1024;
#pragma unroll
        for (int i = 0; i < 4; ++i) glds4<true>(lds_addr(sm) + C_U + 1920 + i * 4096, ws + O_U + cg * 128 + i * 32 * 18432, va, vb + dv, va + 2 * dv, vb + 3 * dv);
#pragma unroll
        for (int i = 0; i < 2; ++i) if (lane < 60) *(u32x4*)(sm + C_U + (lane + 60 * i) * 16) = u32x4{0, 0, 0, 0};
        wait_vm<0>();
      }
    }
    bar();
    STAMP(0,5);
    if (tid >= 510) *(u32x4*)(sm + X_CNT + 32 + (tid & 1) * 16) = u32x4{~tag[0], 0, ~tag[0], 0};
    u32x4 xv[2];
#pragma unroll
    for (int r = 0; r < 2; ++r) {
      const int T = (tid >> 3) + 64 * r, ip = tid & 7;
      xv[r] = ld16(R, (8 * (T >> 1) + 2 * (ip >> 1) + (T & 1)) * 4608 + (ip & 1) * 16, O_XB + cg * 32);
    }
    if (w < 4) {
      f32x4 acc[8];
      const int sg = 7 - (li & 7);
      const char* pa = sk + (sg & 3) * CV_C + (253 - (li >> 3) + q) * 16 + (sg >> 2) * 8 - 4032;
      struct CF { u32x4 a[8], b[8]; } A, B;
      auto ldc = [&](CF& f, int a0) {
        const int r = 14 - 2 * a0 + li + (q >> 1);
        const char* pb = sm + C_U + r * 128 + (((2 * ch + (q & 1)) ^ ((r >> 1) & 7)) * 16);
#pragma unroll
        for (int m = 0; m < 8; ++m) {
          const char* p = pa + 4032 - 64 * a0 - 512 * m;
          const u32x2 x0 = *(volatile const LDSP u32x2*)p, x1 = *(volatile const LDSP u32x2*)(p + 8);
          f.a[m] = u32x4{x0[0], x0[1], x1[0], x1[1]};
          f.b[m] = *(const u32x4*)(pb + 2048 * m);
        }
      };
      auto mmc = [&](const CF& f, bool first) {
#pragma unroll
        for (int m = 0; m < 8; ++m)
#pragma unroll
          for (int t = m; t < 8; ++t) acc[t] = mfma_h(f.a[m], f.b[t - m], first && m == 0 ? f32x4{0, 0, 0, 0} : acc[t]);
#pragma unroll
        for (int i = 0; i < 24; ++i) { __builtin_amdgcn_sched_group_barrier(0x008, 1, 0); __builtin_amdgcn_sched_group_barrier(0x100, 1, 0); }
        __builtin_amdgcn_sched_barrier(0);
      };
      ldc(A, 0);
      __builtin_amdgcn_sched_barrier(0);
#pragma unroll
      for (int a0 = 0; a0 < 8; a0 += 2) { ldc(B, a0 + 1); mmc(A, a0 == 0); if (a0 < 6) ldc(A, a0 + 2); mmc(B, false); }
      STAMP(0,6);
#pragma unroll
      for (int t = 0; t < 8; ++t) *(u32x2*)(sk + t * 512 + li * 32 + q * 8) = u32x2{pk2(lo2(acc[t]) * sc), pk2(hi2(acc[t]) * sc)};
    }
    bar();
#pragma unroll
    for (int r = 0; r < 2; ++r) {
      const int T = (tid >> 3) + 64 * r, ip = tid & 7;
      const char* p = sm + (32 * (T >> 1) + 8 * (ip >> 1) + 4 * (T & 1) + 2 * (ip & 1)) * 2;
      const unsigned c0 = *(const unsigned*)p, c1 = *(const unsigned*)(p + CV_K), c2 = *(const unsigned*)(p + 2 * CV_K), c3 = *(const unsigned*)(p + 3 * CV_K);
      u32x4 v = {pk2(up2(xv[r][0]) + f32x2{bf_lo(c0), bf_lo(c1)}), pk2(up2(xv[r][1]) + f32x2{bf_lo(c2), bf_lo(c3)}),
                 pk2(up2(xv[r][2]) + f32x2{bf_hi(c0), bf_hi(c1)}), pk2(up2(xv[r][3]) + f32x2{bf_hi(c2), bf_hi(c3)})};
      st16(R, (T >> 3) * (18 * 8192) + (T & 7) * 1024 + ip * 16, O_HF + (cg >> 3) * 8192 + (cg & 1) * 512 + ((cg >> 1) & 3) * 128, v);
    }
    STAMPW(2,1,4);
    arrive(1, 8, O_F2 + cg * 8, F2S, NREP);
    STAMPW(2,2,4);
  } else {
    const int tj = (ii - 18) * 8 + xc;
    const int kc = tj / 36, cb = tj % 36;
    for (int i = 0; i < WDELAY; ++i) __builtin_amdgcn_s_sleep(16);
    char* s0 = sm + 65536;
    const int G = max(tj - 40, 0), par = G & 1;
    {
      const int row = tid >> 4, uu = G / 9, idx = G % 9, hh = row >> 4;
      const int panel = idx < 4 ? 2 * uu : (idx < 8 ? 2 * uu + 1 : 2 * uu + hh), tile = idx < 8 ? 2 * (idx & 3) + hh : 8;
      const char* src = w_in + (size_t)(192 * min(kc, 2) + row) * 9216 + cb * 256 + li * 16;
      const char* wsrc = w_out + (size_t)(144 * panel + 16 * tile + (row & 15)) * 1152 + li * 8;
      u32x4 v[6]; u32x2 vo[9];
#pragma unroll
      for (int k = 0; k < 6; ++k) v[k] = *(const u32x4*)(src + (size_t)k * 32 * 9216);
      if (tj >= 40) {
#pragma unroll
        for (int k = 0; k < 9; ++k) vo[k] = *(const u32x2*)(wsrc + k * 128);
      }
#pragma unroll
      for (int k = 0; k < 6; ++k) *(u32x4*)(sm + (row + 32 * k) * 272 + li * 16) = v[k];
      if (tj >= 40) {
#pragma unroll
        for (int k = 0; k < 9; ++k) *(u32x2*)(s0 + row * TP + li * 8 + k * 128) = vo[k];
      }
    }
    tag = mk_tag();
    if (w == 0) {
      wait_flags<1>(R, O_F1 + rep * 1024, 128, tag, lane);
      for (int i = 0; i < SDELAY; ++i) __builtin_amdgcn_s_sleep(16);
    }
    bar();
    STAMP(4,0);
    if (tj < 108) {
      const int ct = 8 * cb + w;
      const int hf = ct < 144 ? 1 : 0, cl = ct < 144 ? ct : ct - 144, fp = cl / 9, n = cl % 9;
      const char* trb = sm + (8 * q + (li >> 2)) * 272 + w * 32 + (li & 3) * 8;
      const unsigned dst = lane * 16 + O_WI + (((fp * 2 + hf) * 18 + 6 * kc) * 9 + n) * 1024;
      float s = 0.f;
#pragma unroll
      for (int g = 0; g < 6; ++g) {
        u32x2 lo = tr_read(trb + g * 32 * 272), hi = tr_read(trb + g * 32 * 272 + 4 * 272);
        st16(R, dst, g * 9216, u32x4{lo[0], lo[1], hi[0], hi[1]});
        s = dot2(lo[0], 0x3F803F80u, s); s = dot2(lo[1], 0x3F803F80u, s); s = dot2(hi[0], 0x3F803F80u, s); s = dot2(hi[1], 0x3F803F80u, s);
      }
      s = q_sum(s);
      if (q == 0) st4(R, li * 4, O_CS + kc * F * 4 + ct * 64, asu(s));
    }
    if (tj >= 40) {
      const bool mixed = G % 9 == 8;
      const char* trb = s0 + ((mixed ? 16 * (q >> 1) + 4 * (q & 1) : 4 * q) + (li >> 2)) * TP + (li & 3) * 8;
      const char* trh = trb + (mixed ? 8 : 16) * TP;
      const unsigned dst = lane * 16 + O_WO + (w * 36 + (G >> 1)) * 9216;
#pragma unroll
      for (int n = 0; n < 5; ++n) {
        const int c0 = 72 * w + 16 * n - (n == 4 ? 8 * par : 0);
        u32x2 lo = tr_read(trb + c0 * 2), hi = tr_read(trh + c0 * 2);
        if (n < 4) st16(R, dst, (4 * par + n) * 1024, u32x4{lo[0], lo[1], hi[0], hi[1]});
        else if ((li >> 3) == par) st16(R, dst, 8192, u32x4{lo[0], lo[1], hi[0], hi[1]});
      }
    }
    STAMP(4,1);
    arrive(0, 8, O_F2 + (144 + tj) * 8, F2S, NREP);
    STAMP(4,2);
  }
  const int tp = (xc & 1) * 8 + (ii >> 2), fp = (xc >> 1) * 4 + (ii & 3);
  const int t64 = 4 * xc + (ii >> 3), j3 = ii & 7;
  if (w == 0) { wait_flags<1>(R, O_F2 + rep * F2S + 144 * 8, 112, tag, lane); STAMPW(6,0,0); }
  bar();
  if (w >= 4) {
    const int lw = w - 4;
    const unsigned l16 = lane * 16, base = lds_addr(sm);
    const int ph = ((ii >> 2) + ii) & 3, ph3 = ROT3 ? ii & 3 : ((ii >> 3) + ii) & 3, rot3 = ROT3 ? (9 * (ii >> 3) + 1) >> 1 : 0;
    auto k9 = [&](int t) { return (t & 3) == ph ? (t + 4 < 9 ? t + 4 : ph) : t; };
    auto k32 = [&](int t) { return (t & 3) == ph ? ((t + 4) & 31) : t; };
    auto k18 = [&](int t) { const int k = ((t & 3) == ph3 ? (t + 4 < 18 ? t + 4 : ph3) : t) + rot3; return k >= 18 ? k - 18 : k; };
    if (lw < 2) {
      constexpr int NP = 8;
      unsigned vo[8];
#pragma unroll
      for (int j = 0; j < 8; ++j) {
        int t = 64 * lw + 32 * (j >> 2) + 4 * ((j >> 1) & 1) + 16 * (j & 1) + 8 * (lane >> 5) + ((lane >> 3) & 3);
        vo[j] = t * 4096 + (((lane & 7) ^ ((lane >> 3) & 7)) * 16) + 3072 - (j & 3) * 1024;
      }
      const char* wsrc = w_sp + (size_t)tp * 128 * 4096 - 3072;
      const char* hsrc = ws + O_HF + tp * 18 * 8192 + lw * 8192;
      const char* psrc = ws + O_PF + t64 * 72 * 4096 + lw * 8192;
      const unsigned dst = base + lw * 8192;
      if (lw == 0) wait_flags<2>(R, O_F2 + rep * F2S, 144, tag, lane);
      STAMPW(5,1,4);
      bar();
#pragma unroll 1
      for (int t = 0; t < 4; ++t) ld_lin<NP>(dst + t * STG, hsrc + k9(t) * 16384, l16);
      wait_vm<3 * NP>(); bar();
#pragma unroll 1
      for (int t = 4; t < 18; ++t) { wait_vm<2 * NP>(); bar(); ld_lin<NP>(dst + (t & 3) * STG, hsrc + k9(t < 9 ? t : t - 9) * 16384, l16); }
#pragma unroll 1
      for (int t = 18; t < 50; ++t) {
        wait_vm<2 * NP>(); bar();
        const unsigned dd = dst + (t & 3) * STG; const char* sp = wsrc + k32(t - 18) * 128;
        glds4<false>(dd, sp, vo[0], vo[1], vo[2], vo[3]); glds4<false>(dd + 4096, sp, vo[4], vo[5], vo[6], vo[7]);
      }
      wait_vm<2 * NP>(); bar(); wait_vm<NP>(); bar(); wait_vm<0>(); bar();
      bar(); bar();
      wait_flags<1>(R, O_PFL + (t64 >> 1) * 2048 + ((t64 & 1) * 8 + j3) * 128, 16, tag, lane);
      STAMPW(5,2,4);
#pragma unroll 1
      for (int t = 50; t < 68; ++t) {
        if (t >= 53) { wait_vm<2 * NP>(); bar(); }
        ld_lin<NP>(dst + (t & 3) * STG, psrc + k18(t - 50) * 16384, l16);
      }
      wait_vm<2 * NP>(); bar(); wait_vm<NP>(); bar(); wait_vm<0>(); bar();
    } else {
      constexpr int NP = 9;
      const char* wi = ws + O_WI + (fp * 2) * 18 * 9216 + (lw - 2) * 9216;
      const char* zs = ws + O_ZF + fp * 64 * 9216 + (lw - 2) * 9216;
      const char* wo = ws + O_WO + j3 * 36 * 9216 + (lw - 2) * 9216;
      const unsigned dst = base + 16384 + (lw - 2) * 9216;
#pragma unroll 1
      for (int t = 0; t < 4; ++t) ld_lin<NP>(dst + t * STG, wi + k9(t) * 18432, l16);
      bar();
      wait_vm<3 * NP>(); bar();
      if (lw == 3) {
        f32x2 a = *(const f32x2*)(sm + X_LN + lane * 8) + *(const f32x2*)(sm + X_LN + 512 + lane * 8);
        a[0] = q_sum(dpp_add<0x128>(a[0])); a[1] = q_sum(dpp_add<0x128>(a[1]));
        float mean = a[0] * (1.0f / D), mb, ri;
        ln_scalars(mean, fmaxf(a[1] * (1.0f / D) - mean * mean, 0.f), mb, ri);
        if (lane < 8) st8(R, lane * 8, O_HS + tp * 1024 + fp * 64, u32x2{asu(ri) == SENT ? 0x7FC00000u : asu(ri), asu(-mb)});
      }
#pragma unroll 1
      for (int t = 4; t < 18; ++t) { wait_vm<2 * NP>(); bar(); ld_lin<NP>(dst + (t & 3) * STG, wi + (t < 9 ? k9(t) : 9 + k9(t - 9)) * 18432, l16); }
      wait_vm<2 * NP>(); bar(); wait_vm<NP>(); bar(); wait_vm<0>(); bar();
      wait_flags<1>(R, O_ZFL + fp * 2048 + tp * 128, 16, tag, lane);
      STAMPW(6,1,6);
#pragma unroll 1
      for (int t = 18; t < 53; ++t) {
        if (t >= 21) { wait_vm<2 * NP>(); bar(); }
        ld_lin<NP>(dst + (t & 3) * STG, t < 50 ? zs + k32(t - 18) * 18432 : wo + k18(t - 50) * 18432, l16);
      }
      wait_vm<0>(); bar();
      bar();
#pragma unroll 1
      for (int t = 53; t < 57; ++t) ld_lin<NP>(dst + (t & 3) * STG, wo + k18(t - 50) * 18432, l16);
      bar(); bar(); bar();
      wait_vm<3 * NP>(); bar();
#pragma unroll 1
      for (int t = 57; t < 68; ++t) { wait_vm<2 * NP>(); bar(); ld_lin<NP>(dst + (t & 3) * STG, wo + k18(t - 50) * 18432, l16); }
      wait_vm<2 * NP>(); bar(); wait_vm<NP>(); bar(); wait_vm<0>(); bar();
    }
    return;
  }
  Ctx c;
  c.sm = sm;
#pragma unroll
  for (int i = 0; i < 4; ++i) { c.am0[i] = (2 * w + (i >> 1)) * 1024 + (i & 1) * 512 + lane * 8; asm volatile("" : "+v"(c.am0[i])); }
  c.am1 = 2 * w * 2048 + li * 128 + ((q ^ (lane & 7)) * 16);
  c.an = 16384 + lane * 16;
  const int prow = 8 * (li >> 2) + (li & 3);
  if (tid < 72) {
    const unsigned o = tid < 36 ? FH * 4 + tid * 16 : (tid - 36) * 16;
    const u32x4 c0 = ld16(R, o, O_CS + fp * 576), c1 = ld16(R, o, O_CS + F * 4 + fp * 576), c2 = ld16(R, o, O_CS + F * 8 + fp * 576);
    *(f32x4*)(sm + X_CS + tid * 16) = (__builtin_bit_cast(f32x4, c0) + __builtin_bit_cast(f32x4, c1)) + __builtin_bit_cast(f32x4, c2);
  }
  bar();
  char* pvb = sm + X_PV + w * 4096;
  {
    u32x4 hv[3];
#pragma unroll
    for (int k = 0; k < 3; ++k)
      hv[k] = ld16(R, (lane >> 5) * 8192 + ((lane >> 2) & 7) * 128 + (lane & 3) * 16, O_HF + (tp * 18 * 8 + (fp >> 1)) * 1024 + (fp & 1) * 64 + min(w + 4 * k, 8) * 16384);
    if (w) hv[2] = u32x4{0, 0, 0, 0};
    f32x4 a = {0, 0, 0, 0};
#pragma unroll
    for (int k = 0; k < 3; ++k)
#pragma unroll
      for (int e = 0; e < 4; ++e) { a[e & 2] = dot2(hv[k][e], 0x3F803F80u, a[e & 2]); a[(e & 2) + 1] = dot2(hv[k][e], hv[k][e], a[(e & 2) + 1]); }
#pragma unroll
    for (int e = 0; e < 4; ++e) a[e] = dpp_add<0x124>(dpp_add<0x128>(a[e]));
    if (li < 4) *(f32x4*)(sm + X_LN + (w * 4 + q) * 64 + li * 16) = a;
  }
  f32x4 acc[2][9];
  Fr X, Y;
  bar();
  STAMP(3,0);
  ldf<0>(X, c, 0, 0);
  seg<0, 0, true, true>(acc, X, Y, c, 0, 1);
  const unsigned hs_m0 = __builtin_amdgcn_readfirstlane(lds_addr(sm) + X_HS + w * 256);
  auto ld_hs = [&]() {
    unsigned keep;
    asm volatile("s_mov_b32 %0, m0\n\ts_mov_b32 m0, %1\n\ts_nop 2\n\tglobal_load_lds_dword %3, %2 sc1\n\ts_mov_b32 m0, %0"
                 : "=&s"(keep) : "s"(hs_m0), "s"(ws), "v"(O_HS + tp * 1024 + tid * 4) : "memory");
  };
#pragma unroll 1
  for (int t = 0; t < 8; ++t) {
    if (t == 5) ld_hs();
    bar();
    seg<0, 0, true>(acc, Y, X, c, t + 1, 0);
    seg<0, 0, true>(acc, X, Y, c, t + 1, 1);
  }
  u32x4 hc[4];
  float cs[9];
  auto rd_hs = [&]() {
    wait_vm<0>();
#pragma unroll
    for (int jp = 0; jp < 4; ++jp) hc[jp] = *(const u32x4*)(sm + X_HS + w * 256 + (jp >> 1) * 128 + q * 32 + (jp & 1) * 16);
  };
  rd_hs();
#pragma unroll
  for (int n = 0; n < 9; ++n) cs[n] = *(const float*)(sm + X_CS + (16 * n + li) * 4);
  bar();
  seg<0, 0, true>(acc, Y, X, c, 9, 0);
  STAMP(3,1);
#pragma unroll 1
  for (int spin = 0; spin < SPIN; ++spin) {
    if (__all(max(max(umax4(hc[0]), umax4(hc[1])), max(umax4(hc[2]), umax4(hc[3]))) != SENT)) break;
    ld_hs();
    rd_hs();
  }
  u32x4 zb[9];
  f32x2 rs[4];
  {
    f32x2 nm[4], k1[4], k0[4], S1[4] = {}, S2[4] = {};
#pragma unroll
    for (int jp = 0; jp < 4; ++jp) {
      rs[jp] = f32x2{asf(hc[jp][0]), asf(hc[jp][2])}; nm[jp] = f32x2{asf(hc[jp][1]), asf(hc[jp][3])};
      k0[jp] = rs[jp] * GC0; k1[jp] = rs[jp] * rs[jp] * rs[jp] * GC1;
    }
#pragma unroll
    for (int n = 0; n < 9; ++n) {
#pragma unroll
      for (int jp = 0; jp < 4; ++jp) {
        f32x2 a = (jp & 1) ? hi2(acc[jp >> 1][n]) : lo2(acc[jp >> 1][n]);
        f32x2 z = gelu2(__builtin_elementwise_fma(nm[jp], f32x2{cs[n], cs[n]}, a), k1[jp], k0[jp]);
        zb[n][jp] = pk2(z);
        S1[jp] += z; S2[jp] = __builtin_elementwise_fma(z, z, S2[jp]);
      }
      asm volatile("" : "+v"(zb[n]));
    }
#pragma unroll
    for (int jp = 0; jp < 4; ++jp) { S1[jp] *= rs[jp]; S2[jp] *= rs[jp] * rs[jp]; }
    float v[16];
#pragma unroll
    for (int j = 0; j < 8; ++j) { v[2 * j] = S1[j >> 1][j & 1]; v[2 * j + 1] = S2[j >> 1][j & 1]; }
#pragma unroll
    for (int i = 0; i < 8; ++i) { bool h = li & 8; v[i] = (h ? v[i + 8] : v[i]) + dpp_get<0x128>(h ? v[i] : v[i + 8]); }
#pragma unroll
    for (int i = 0; i < 4; ++i) { bool h = li & 4; v[i] = (h ? v[i + 4] : v[i]) + dpp_get<0x141>(h ? v[i] : v[i + 4]); }
#pragma unroll
    for (int i = 0; i < 2; ++i) { bool h = li & 2; v[i] = (h ? v[i + 2] : v[i]) + dpp_get<0x4E>(h ? v[i] : v[i + 2]); }
    { bool h = li & 1; v[0] = (h ? v[1] : v[0]) + dpp_get<0xB1>(h ? v[0] : v[1]); }
    unsigned tot = asu(v[0]);
    st4(R, lane * 4, O_ST + ((tp * 4 + w) * 16 + fp) * 256, tot == SENT ? 0x7FC00000u : tot);
  }
  STAMP(3,2);
  const char* sts = ws + O_ST + (tp * 4 + w) * 4096;
  seg<0, 1, true, true>(acc, X, Y, c, 9, 1);
#pragma unroll 1
  for (int t = 9; t < TCHK; ++t) {
    if (t == TPOLL) glds4<true>(lds_addr(pvb), sts, lane * 16, lane * 16, lane * 16, lane * 16);
    bar();
    seg<0, 1, true>(acc, Y, X, c, t + 1, 0);
    seg<0, 1, true>(acc, X, Y, c, t + 1, 1);
  }
  STAMP(3,3);
  u32x4 pv[4];
#pragma unroll 1
  for (int spin = 0; spin < SPIN; ++spin) {
    wait_vm<0>();
#pragma unroll
    for (int k = 0; k < 4; ++k) pv[k] = *(const u32x4*)(pvb + k * 1024 + lane * 16);
    if (__all(max(max(umax4(pv[0]), umax4(pv[1])), max(umax4(pv[2]), umax4(pv[3]))) != SENT)) break;
    glds4<true>(lds_addr(pvb), sts, lane * 16, lane * 16, lane * 16, lane * 16);
  }
  STAMP(3,4);
  {
    float t4[4];
#pragma unroll
    for (int e = 0; e < 4; ++e) t4[e] = q_sum((asf(pv[0][e]) + asf(pv[1][e])) + (asf(pv[2][e]) + asf(pv[3][e])));
    f32x2 o0, o1;
#pragma unroll
    for (int r = 0; r < 2; ++r) {
      float mean = t4[2 * r] * (1.0f / FH), var = fmaxf(t4[2 * r + 1] * (1.0f / FH) - mean * mean, 0.f), mb, ri;
      ln_scalars(mean, var, mb, ri);
      o0[r] = ri; o1[r] = -mb * ri;
    }
    char* ln = sm + X_LN + w * 256;
    if (lane < 16) { *(f32x2*)(ln + li * 8) = o0; *(f32x2*)(ln + 128 + li * 8) = o1; }
    asm volatile("" ::: "memory");
    f32x4 ri[2], rb[2];
#pragma unroll
    for (int m = 0; m < 2; ++m) { ri[m] = *(const f32x4*)(ln + (8 * q + 4 * m) * 4); rb[m] = *(const f32x4*)(ln + 128 + (8 * q + 4 * m) * 4); }
#pragma unroll
    for (int jp = 0; jp < 4; ++jp) rs[jp] *= (jp & 1) ? hi2(ri[jp >> 1]) : lo2(ri[jp >> 1]);
    const unsigned zo = lane * 16 + O_ZF + (fp * 64 + 4 * tp + w) * 9216;
#pragma unroll
    for (int n = 0; n < 9; ++n) {
      u32x4 o4;
#pragma unroll
      for (int jp = 0; jp < 4; ++jp)
        o4[jp] = pk2(__builtin_elementwise_fma(up2(zb[n][jp]), rs[jp], (jp & 1) ? hi2(rb[jp >> 1]) : lo2(rb[jp >> 1])));
      st16(R, zo, n * 1024, o4);
    }
  }
  STAMP(3,5);
#pragma unroll 1
  for (int t = TCHK; t < 17; ++t) {
    if (t == TFLAG) wait_vm<0>();
    bar();
    if (t == TFLAG && tid < 16) st8(R, tid * 128, O_ZFL + fp * 2048 + tp * 8, tag);
    seg<0, 1, true>(acc, Y, X, c, t + 1, 0);
    seg<0, 1, true>(acc, X, Y, c, t + 1, 1);
  }
  float lrs[2], lnm[2];
  f32x4 cs1[9];
#pragma unroll
  for (int m = 0; m < 2; ++m) { const f32x2 h = *(const f32x2*)(sm + X_HS + w * 256 + m * 128 + li * 8); lrs[m] = h[0]; lnm[m] = h[1]; }
#pragma unroll
  for (int n = 0; n < 9; ++n) cs1[n] = *(const f32x4*)(sm + X_CS + (144 + 16 * n + 4 * q) * 4);
  __builtin_amdgcn_sched_barrier(0);
  seg<0, 1, false>(acc, Y, X, c, 17, 1);
  STAMP(3,6);
  u32x2 z1[2][9];
  float bs[2];
#pragma unroll
  for (int m = 0; m < 2; ++m) bs[m] = ldbf(b_sp, 128 * tp + 32 * w + prow + 4 * m) * lrs[m];
  {
#pragma unroll
    for (int n = 0; n < 9; ++n)
#pragma unroll
      for (int m = 0; m < 2; ++m) {
        const float a1 = lrs[m] * lrs[m] * lrs[m] * GC1, a0 = lrs[m] * GC0;
        const f32x2 k1 = {a1, a1}, k0 = {a0, a0}, nm = {lnm[m], lnm[m]};
        z1[m][n][0] = pk2(gelu2(__builtin_elementwise_fma(nm, lo2(cs1[n]), lo2(acc[m][n])), k1, k0));
        z1[m][n][1] = pk2(gelu2(__builtin_elementwise_fma(nm, hi2(cs1[n]), hi2(acc[m][n])), k1, k0));
        asm volatile("" : "+v"(z1[m][n]));
      }
  }
  STAMP(3,7);
  bar();
  STAMP(3,8);
  ldf<1>(X, c, 18, 0);
  seg<1, 1, true, true>(acc, X, Y, c, 18, 1);
#pragma unroll 1
  for (int t = 18; t < 49; ++t) {
    bar();
    seg<1, 1, true>(acc, Y, X, c, t + 1, 0);
    seg<1, 1, true>(acc, X, Y, c, t + 1, 1);
  }
  seg<1, 1, false>(acc, Y, X, c, 49, 1);
  STAMP(3,9);
  {
    const unsigned pf = O_PF + (((2 * tp + (w >> 1)) * 72 + 9 * (fp >> 1)) * 4 + 2 * (w & 1)) * 1024;
    const unsigned po = lane * 16 + pf + (fp & 1) * 16384;
    u32x2 p8[2];
#pragma unroll
    for (int m = 0; m < 2; ++m) {
      const f32x2 r2 = {lrs[m], lrs[m]}, b2 = {bs[m], bs[m]};
      auto pp = [&](int n) { return u32x2{pk2(__builtin_elementwise_fma(lo2(acc[m][n]), r2, b2) * up2(z1[m][n][0])), pk2(__builtin_elementwise_fma(hi2(acc[m][n]), r2, b2) * up2(z1[m][n][1]))}; };
#pragma unroll
      for (int a = 0; a < 4; ++a) {
        u32x2 p0 = pp(2 * a), p1 = pp(2 * a + 1);
        st16(R, po, a * 4096 + m * 1024, u32x4{p0[0], p0[1], p1[0], p1[1]});
      }
      p8[m] = pp(8);
    }
    const auto s0 = __builtin_amdgcn_permlane32_swap(p8[0][0], p8[1][0], false, false), s1 = __builtin_amdgcn_permlane32_swap(p8[0][1], p8[1][1], false, false);
    st16(R, (lane >> 5) * 1024 + (lane & 31) * 16, pf + 8 * 4096 + (fp & 1) * 512, u32x4{s0[0], s1[0], s0[1], s1[1]});
  }
  STAMP(3,10);
  const int th = w & 1, kq = w >> 1, T = 4 * t64 + 2 * th + kq;
  const int lane5 = fresh_lane();
#define lane lane5
#define li (lane5 & 15)
#define q (lane5 >> 4)
#define prow (8 * (li >> 2) + (li & 3))
  struct P3 { u32x4 p[2][2]; } Up, Vp;
  struct N3 { u32x4 n[9]; } Un, Vn, En;
  const int ap = (8 * kq + 2 * th) * 1024 + lane * 16, aw = 16384 + kq * 9216 + lane * 16;
  auto ldp = [&](P3& f, int t) {
#pragma unroll
    for (int g = 0; g < 2; ++g)
#pragma unroll
      for (int m = 0; m < 2; ++m) f.p[g][m] = *(const u32x4*)(sm + (t & 3) * STG + ap + (4 * g + m) * 1024);
  };
  auto ldn = [&](N3& f, int t) {
#pragma unroll
    for (int n = 0; n < 9; ++n) f.n[n] = *(const u32x4*)(sm + (t & 3) * STG + aw + n * 1024);
  };
  bar();
  ldn(Un, 50); ldn(Vn, 51); ldn(En, 52);
  arrive(2, 4, O_PFL + tp * 2048 + fp * 8, 128, 16);
  bar();
  u32x2 hv[5];
#pragma unroll
  for (int n = 0; n < 5; ++n) {
    const int dd = 72 * j3 + 16 * n + 4 * (n == 4 ? (q & 1) : q);
    hv[n] = ld8(R, (dd >> 5) * 8192 + ((dd >> 2) & 1) * 512 + (((dd >> 3) & 3) * 16 + li) * 8, O_HF + ((T >> 3) * 18 * 8 + (T & 7)) * 1024);
  }
  f32x4 a3[2][6] = {};
  auto mm3 = [&](const P3& f, const N3& e) {
#pragma unroll
    for (int g = 0; g < 2; ++g)
#pragma unroll
      for (int m = 0; m < 2; ++m) {
#pragma unroll
        for (int n = 0; n < 4; ++n) a3[m][n] = mfma_bf(e.n[4 * g + n], f.p[g][m], a3[m][n]);
        a3[m][4 + g] = mfma_bf(e.n[8], f.p[g][m], a3[m][4 + g]);
      }
#pragma unroll
    for (int i = 0; i < 13; ++i) { __builtin_amdgcn_sched_group_barrier(0x008, 1, 0); __builtin_amdgcn_sched_group_barrier(0x100, 1, 0); }
    __builtin_amdgcn_sched_barrier(0);
  };
  STAMP(3,11);
  bar();
  STAMP(3,12);
  ldp(Up, 50);
  bar(); ldp(Vp, 51); mm3(Up, Un);
  bar(); ldp(Up, 52); mm3(Vp, Vn);
  bar(); ldp(Vp, 53); ldn(Vn, 53); mm3(Up, En);
#pragma unroll 1
  for (int t = 53; t < 67; t += 2) { bar(); ldp(Up, t + 1); ldn(Un, t + 1); mm3(Vp, Vn); bar(); ldp(Vp, t + 2); ldn(Vn, t + 2); mm3(Up, Un); }
  mm3(Vp, Vn);
  STAMP(3,13);
#pragma unroll
  for (int m = 0; m < 2; ++m) a3[m][4] = lane < 32 ? a3[m][4] : a3[m][5];
  if (kq) {
#pragma unroll
    for (int n = 0; n < 5; ++n) { *(f32x4*)(sm + w * 5120 + n * 1024 + lane * 16) = a3[0][n]; asm volatile("" : "+v"(a3[1][n])); a3[0][n] = a3[1][n]; }
  } else {
#pragma unroll
    for (int n = 0; n < 5; ++n) *(f32x4*)(sm + w * 5120 + n * 1024 + lane * 16) = a3[1][n];
  }
  bar();
  char* o = out + (size_t)(32 * (T >> 1) + 4 * (T & 1) + prow) * 1152 + (72 * j3 + 4 * q) * 2;
#pragma unroll
  for (int n = 0; n < 5; ++n) {
    f32x4 v = a3[0][n] + *(const f32x4*)(sm + (w ^ 2) * 5120 + n * 1024 + lane * 16);
    if (n == 4) v = f32x4{half_sum(v[0]), half_sum(v[1]), half_sum(v[2]), half_sum(v[3])};
    if (n < 4 || q < 2) *(u32x2*)(o + n * 32) = u32x2{pk2(up2(hv[n][0]) + lo2(v)), pk2(up2(hv[n][1]) + hi2(v))};
  }
  if (bid == 0 && w == 0 && lane == 0) { u64 e = (((u64)tag[1] << 32) | tag[0]) - 0x9E3779B97F4A7C15ull + 1; st8(R, 0, O_EP, u32x2{(unsigned)e, (unsigned)(e >> 32)}); }
  STAMP(3,14);
}

#undef lane
#undef li
#undef q
#undef prow
extern "C" void kernel_launch(void* const* d_in, const int* in_sizes, int n_in, void* d_out, int out_size, void* d_ws, size_t ws_size,
                              hipStream_t stream) {
  (void)hipFuncSetAttribute((const void*)k_all, hipFuncAttributeMaxDynamicSharedMemorySize, LDS_BYTES);
  k_all<<<256, 512, LDS_BYTES, stream>>>((const char*)d_in[0], (const char*)d_in[1], (const char*)d_in[2], (const char*)d_in[3],
      (const char*)d_in[4], (const char*)d_in[5], (const char*)d_in[6], (const char*)d_in[7], (const char*)d_in[8], (const char*)d_in[10],
      (const char*)d_in[11], (const char*)d_in[12], (const char*)d_in[13], (char*)d_out, (char*)d_ws + ((ws_size - WS_USED) & ~(size_t)0xFFFFF));
}
```

```cpp
#include <hip/hip_runtime.h>
#include <hip/hip_bf16.h>
#include <stdint.h>

typedef __attribute__((ext_vector_type(8))) __bf16 bf16x8;
typedef __attribute__((ext_vector_type(2))) __bf16 bf16x2;
typedef __attribute__((ext_vector_type(8))) _Float16 f16x8;
typedef __attribute__((ext_vector_type(2))) _Float16 f16x2;
typedef __attribute__((ext_vector_type(4))) float f32x4;
typedef __attribute__((ext_vector_type(2))) float f32x2;
typedef __attribute__((ext_vector_type(4))) unsigned u32x4;
typedef __attribute__((ext_vector_type(2))) unsigned u32x2;
typedef __attribute__((ext_vector_type(4))) short s16x4;
#define LDSP __attribute__((address_space(3)))
#define DEVI __device__ __forceinline__

constexpr int L = 2048, D = 576, NM = 128, F = 4608, FH = 2304;
constexpr float EPS = 1e-5f;
constexpr unsigned O_EP = 0;
#define PSLEEP 0
#define NREPV 64
constexpr int NREP = NREPV;
constexpr unsigned O_F2 = 256;
constexpr unsigned F2S = 2048;
constexpr unsigned O_ZFL = O_F2 + NREP * F2S;
constexpr unsigned O_PFL = O_ZFL + 32768;
constexpr unsigned O_CS = O_PFL + 32768;
static_assert(O_PFL + 32768 <= O_CS, "ws");
constexpr unsigned O_ST = O_CS + F * 12;
constexpr unsigned O_HS = O_ST + 262144;
constexpr unsigned O_LS = O_HS + 16384;
constexpr unsigned O_WI = O_LS + 16 * 16384;
constexpr unsigned O_WO = O_WI + D * F * 2;
constexpr unsigned O_HF = O_WO + FH * D * 2;
constexpr unsigned O_ZF = O_HF + L * D * 2;
constexpr unsigned O_PF = O_ZF + L * FH * 2;
constexpr unsigned WS_USED = O_PF + L * FH * 2;
constexpr size_t O_STAMP = 128u << 20;
#define STAMP(k,i) __builtin_amdgcn_sched_barrier(0)
#define STAMPW(k,i,W) __builtin_amdgcn_sched_barrier(0)
#define RSTAMP(k,i)
constexpr unsigned SENT = 0xFFFFFFFFu;

DEVI unsigned pk_bf16(float a, float b) { f32x2 v = {a, b}; return __builtin_bit_cast(unsigned, __builtin_convertvector(v, bf16x2)); }
DEVI unsigned pk_f16(float a, float b) { f32x2 v = {a, b}; return __builtin_bit_cast(unsigned, __builtin_convertvector(v, f16x2)); }
DEVI float bf_lo(unsigned u) { return __builtin_bit_cast(float, u << 16); }
DEVI float bf_hi(unsigned u) { return __builtin_bit_cast(float, u & 0xFFFF0000u); }
DEVI float asf(unsigned u) { return __builtin_bit_cast(float, u); }
DEVI unsigned asu(float f) { return __builtin_bit_cast(unsigned, f); }
DEVI float rbf(float a) { return bf_lo(pk_bf16(a, a)); }
DEVI float h_lo(unsigned u) { return (float)__builtin_bit_cast(f16x2, u)[0]; }
DEVI float h_hi(unsigned u) { return (float)__builtin_bit_cast(f16x2, u)[1]; }
template <int CTRL> DEVI float dpp_add(float v) {
  return v + __builtin_bit_cast(float, __builtin_amdgcn_update_dpp(0, __builtin_bit_cast(int, v), CTRL, 0xF, 0xF, true));
}
DEVI float row16_sum(float v) { v = dpp_add<0x128>(v); v = dpp_add<0x124>(v); v = dpp_add<0x122>(v); v = dpp_add<0x121>(v); return v; }
DEVI float half_sum(float v) { auto r = __builtin_amdgcn_permlane32_swap(asu(v), asu(v), false, false); return asf(r[0]) + asf(r[1]); }
DEVI float q_sum(float v) { auto r = __builtin_amdgcn_permlane16_swap(asu(v), asu(v), false, false); return half_sum(asf(r[0]) + asf(r[1])); }
DEVI unsigned umax4(u32x4 v) { return max(max(v[0], v[1]), max(v[2], v[3])); }
DEVI u32x2 tr_read(const char* p) {
  return __builtin_bit_cast(u32x2, __builtin_amdgcn_ds_read_tr16_b64_v4i16((LDSP s16x4*)p));
}
DEVI f32x4 mfma_bf(u32x4 a, u32x4 b, f32x4 c) {
  return __builtin_amdgcn_mfma_f32_16x16x32_bf16(__builtin_bit_cast(bf16x8, a), __builtin_bit_cast(bf16x8, b), c, 0, 0, 0);
}
DEVI f32x4 mfma_h(u32x4 a, u32x4 b, f32x4 c) {
  return __builtin_amdgcn_mfma_f32_16x16x32_f16(__builtin_bit_cast(f16x8, a), __builtin_bit_cast(f16x8, b), c, 0, 0, 0);
}
DEVI void ln_scalars(float mean, float var, float& mb, float& rinv) {
  mb = rbf(mean);
  float d = rbf(__builtin_amdgcn_sqrtf(rbf(rbf(var) + EPS)));
  rinv = __builtin_amdgcn_rcpf(d);
}
DEVI float gelu(float x) {
  float w = x * __builtin_fmaf(x * x, -0.10294324f, -2.3022082f);
  return x * __builtin_amdgcn_rcpf(1.0f + __builtin_amdgcn_exp2f(w));
}

typedef unsigned long long u64;
typedef __amdgpu_buffer_rsrc_t rsrc_t;
constexpr int SPIN = 1 << 16;
constexpr int CODE_LINES = 38;
template <int CTRL> DEVI float dpp_get(float v) {
  return __builtin_bit_cast(float, __builtin_amdgcn_update_dpp(0, __builtin_bit_cast(int, v), CTRL, 0xF, 0xF, true));
}
constexpr float GC1 = -0.10294324f, GC0 = -2.3022082f;
DEVI f32x2 gelu2(f32x2 x, f32x2 k1, f32x2 k0) {
  f32x2 w = x * __builtin_elementwise_fma(x * x, k1, k0);
  f32x2 d = f32x2{__builtin_amdgcn_exp2f(w[0]), __builtin_amdgcn_exp2f(w[1])} + f32x2{1.f, 1.f};
  return x * f32x2{__builtin_amdgcn_rcpf(d[0]), __builtin_amdgcn_rcpf(d[1])};
}
DEVI unsigned pk2(f32x2 v) { return __builtin_bit_cast(unsigned, __builtin_convertvector(v, bf16x2)); }
DEVI f32x2 up2(unsigned u) { return f32x2{bf_lo(u), bf_hi(u)}; }
DEVI f32x2 lo2(f32x4 v) { return __builtin_shufflevector(v, v, 0, 1); }
DEVI f32x2 hi2(f32x4 v) { return __builtin_shufflevector(v, v, 2, 3); }
DEVI float dot2(unsigned a, unsigned b, float c) {
  return __builtin_amdgcn_fdot2_f32_bf16(__builtin_bit_cast(bf16x2, a), __builtin_bit_cast(bf16x2, b), c, false);
}
DEVI void st16(rsrc_t r, unsigned voff, unsigned soff, u32x4 v) { __builtin_amdgcn_raw_buffer_store_b128(v, r, voff + soff, 0, 16); }
DEVI void st8(rsrc_t r, unsigned voff, unsigned soff, u32x2 v) { __builtin_amdgcn_raw_buffer_store_b64(v, r, voff, soff, 16); }
DEVI void st4(rsrc_t r, unsigned voff, unsigned soff, unsigned v) { __builtin_amdgcn_raw_buffer_store_b32(v, r, voff, soff, 16); }
DEVI u32x4 ld16(rsrc_t r, unsigned voff, unsigned soff) { return __builtin_amdgcn_raw_buffer_load_b128(r, voff, soff, 16); }
DEVI u32x2 ld8(rsrc_t r, unsigned voff, unsigned soff) { return __builtin_amdgcn_raw_buffer_load_b64(r, voff, soff, 16); }
template <int N> DEVI void wait_vm() { asm volatile("s_waitcnt vmcnt(%0)" ::"i"(N) : "memory"); }
DEVI void bar() { __builtin_amdgcn_sched_barrier(0); asm volatile("s_waitcnt lgkmcnt(0)\n\ts_barrier" ::: "memory"); __builtin_amdgcn_sched_barrier(0); }
DEVI int fresh_lane() { int l; asm volatile("v_mbcnt_lo_u32_b32 %0, -1, 0\n\tv_mbcnt_hi_u32_b32 %0, -1, %0" : "=v"(l)); return l; }
DEVI unsigned lds_addr(const char* p) { return (unsigned)(uintptr_t)(LDSP const char*)p; }
template <int NL> DEVI void wait_flags(rsrc_t r, unsigned off, int n, u32x2 tag, int lane) {
#pragma unroll 1
  for (int spin = 0; spin < SPIN; ++spin) {
    bool ok = true;
#pragma unroll
    for (int k = 0; k < NL; ++k) {
      int i = min(lane + 64 * k, n / 2 - 1);
      u32x4 v = ld16(r, i * 16, off);
      ok = ok && v[0] == tag[0] && v[1] == tag[1] && v[2] == tag[0] && v[3] == tag[1];
    }
    if (__all(ok)) break;
    __builtin_amdgcn_s_sleep(PSLEEP);
    asm volatile("" ::: "memory");
  }
  asm volatile("" ::: "memory");
}
template <bool SC> DEVI void glds4(unsigned m0v, const char* base, unsigned v0, unsigned v1, unsigned v2, unsigned v3) {
  unsigned keep;
  if (SC)
    asm volatile("s_mov_b32 %0, m0\n\ts_mov_b32 m0, %1\n\ts_nop 2\n\t"
                 "global_load_lds_dwordx4 %3, %2 sc1\n\tglobal_load_lds_dwordx4 %4, %2 offset:1024 sc1\n\t"
                 "global_load_lds_dwordx4 %5, %2 offset:2048 sc1\n\tglobal_load_lds_dwordx4 %6, %2 offset:3072 sc1\n\ts_mov_b32 m0, %0"
                 : "=&s"(keep) : "s"(m0v), "s"(base), "v"(v0), "v"(v1), "v"(v2), "v"(v3) : "memory");
  else
    asm volatile("s_mov_b32 %0, m0\n\ts_mov_b32 m0, %1\n\ts_nop 2\n\t"
                 "global_load_lds_dwordx4 %3, %2\n\tglobal_load_lds_dwordx4 %4, %2 offset:1024\n\t"
                 "global_load_lds_dwordx4 %5, %2 offset:2048\n\tglobal_load_lds_dwordx4 %6, %2 offset:3072\n\ts_mov_b32 m0, %0"
                 : "=&s"(keep) : "s"(m0v), "s"(base), "v"(v0), "v"(v1), "v"(v2), "v"(v3) : "memory");
}
DEVI void glds1(unsigned m0v, const char* base, unsigned v0) {
  unsigned keep;
  asm volatile("s_mov_b32 %0, m0\n\ts_mov_b32 m0, %1\n\ts_nop 2\n\tglobal_load_lds_dwordx4 %3, %2 sc1\n\ts_mov_b32 m0, %0"
               : "=&s"(keep) : "s"(m0v), "s"(base), "v"(v0) : "memory");
}
template <int NP> DEVI void ld_lin(unsigned dst, const char* src, unsigned l16) {
  glds4<true>(dst, src, l16, l16, l16, l16);
  glds4<true>(dst + 4096, src + 4096, l16, l16, l16, l16);
  if (NP == 9) glds1(dst + 8192, src + 8192, l16);
}

constexpr int TP = 1168;
constexpr int CV_K = 30208, CV_C = 4160, CV_X = 17408;
constexpr int C_U = 4 * CV_K, C_KD = C_U + 16 * TP, C_END = C_KD + 4 * 1536;
constexpr int KT = 528;
constexpr int STG = 34816, RING = 4 * STG;
constexpr int X_CS = RING, X_LN = X_CS + 1152, X_PV = X_LN + 1024, X_HS = X_PV + 16384, X_CNT = 158976;
constexpr int LDS_BYTES = X_CNT + 64 + 256;
static_assert(C_END <= X_CNT && (15 + 128) * 128 <= 16 * TP && 57 * KT <= CV_K && X_HS + 1024 <= X_CNT && LDS_BYTES <= 163840, "lds");
#define TPOLL 12
#define WDELAY 5
#define ROT3 0
#define GDELAY 0
#define GAUX 16
#define TCHK 13
#define TFLAG 16

struct Fr { u32x4 m[2], n[9]; };
struct Ctx { const char* sm; int am0[4], am1, an; };
template <int MK> DEVI void ldf(Fr& f, const Ctx& c, int t, int kk) {
  int so = (t & 3) * STG;
  asm volatile("" : "+s"(so));
  const char* slot = c.sm + so;
  if (MK == 0) {
#pragma unroll
    for (int m = 0; m < 2; ++m) {
      const char* p = slot + c.am0[0] + kk * 8192 + m * 1024;
      u32x2 a = *(volatile const LDSP u32x2*)p, b = *(volatile const LDSP u32x2*)(p + 512);
      f.m[m] = u32x4{a[0], a[1], b[0], b[1]};
    }
  } else {
#pragma unroll
    for (int m = 0; m < 2; ++m) f.m[m] = *(const u32x4*)(slot + (c.am1 ^ (kk * 64)) + m * 2048);
  }
#pragma unroll
  for (int n = 0; n < 9; ++n) f.n[n] = *(const u32x4*)(slot + c.an + (kk * 9 + n) * 1024);
}
template <int MK, int ORI, bool LD, bool FIRST = false> DEVI void seg(f32x4 (&acc)[2][9], const Fr& cur, Fr& nxt, const Ctx& c, int t, int kk) {
  __builtin_amdgcn_s_waitcnt(0xC07F);
  __builtin_amdgcn_sched_barrier(0);
  if (LD) ldf<MK>(nxt, c, t, kk);
#pragma unroll
  for (int m = 0; m < 2; ++m)
#pragma unroll
    for (int nn = 0; nn < 9; ++nn) {
      const int n = m ? 8 - nn : nn;
      const f32x4 ci = FIRST ? f32x4{0, 0, 0, 0} : acc[m][n];
      acc[m][n] = ORI == 0 ? mfma_bf(cur.m[m], cur.n[n], ci) : mfma_bf(cur.n[n], cur.m[m], ci);
    }
#pragma unroll
  for (int i = 0; i < (MK == 0 ? 13 : 11); ++i) { __builtin_amdgcn_sched_group_barrier(0x008, 1, 0); __builtin_amdgcn_sched_group_barrier(0x100, 1, 0); }
  __builtin_amdgcn_sched_barrier(0);
}
DEVI f32x2 cmul(f32x2 a, f32x2 b, f32x2 bp) {
  return __builtin_elementwise_fma(f32x2{a[1], a[1]}, bp, f32x2{a[0], a[0]} * b);
}
DEVI f32x2 perp(f32x2 b) { return f32x2{-b[1], b[0]}; }
DEVI f32x2 cexp(float ar, float ai, float l) {
  float m = __builtin_amdgcn_exp2f(ar * l), ph = __builtin_amdgcn_fractf(ai * l);
  return f32x2{m * __builtin_amdgcn_cosf(ph), m * __builtin_amdgcn_sinf(ph)};
}
DEVI unsigned pkh(f32x2 v) { return __builtin_bit_cast(unsigned, __builtin_convertvector(v, f16x2)); }
DEVI float ldbf(const char* p, int i) { return bf_lo(*(const unsigned short*)(p + 2 * i)); }

__global__ __launch_bounds__(512, 2) void k_all(const char* __restrict__ x, const char* __restrict__ a_re, const char* __restrict__ a_im,
    const char* __restrict__ b_re, const char* __restrict__ b_im, const char* __restrict__ c_re, const char* __restrict__ c_im,
    const char* __restrict__ log_dt, const char* __restrict__ dskip, const char* __restrict__ w_in, const char* __restrict__ w_sp,
    const char* __restrict__ b_sp, const char* __restrict__ w_out, char* __restrict__ out, char* __restrict__ ws) {
  extern __shared__ __attribute__((aligned(16))) char sm[];
  const int tid = threadIdx.x, lane = tid & 63, w = __builtin_amdgcn_readfirstlane(tid >> 6), li = lane & 15, q = lane >> 4;
  const int bid = blockIdx.x, xc = bid & 7, ii = bid >> 3;
  if (w == 7) {
    unsigned long long pc;
    asm volatile("s_getpc_b64 %0" : "=s"(pc));
    const char* cp = (const char*)(pc & ~255ull) + (ii & 7) * (CODE_LINES * 128);
    unsigned keep;
    asm volatile("s_mov_b32 %0, m0\n\ts_mov_b32 m0, %1\n\ts_nop 2\n\tglobal_load_lds_dword %3, %2\n\ts_mov_b32 m0, %0"
                 : "=&s"(keep) : "s"(lds_addr(sm) + X_CNT + 64), "s"(cp), "v"(min(lane, CODE_LINES - 1) * 128) : "memory");
  }
  if (tid == 256) *(u32x4*)(sm + X_CNT) = u32x4{0, 0, 0, 0};
  asm volatile("" ::"s"(x), "s"(a_re), "s"(a_im), "s"(b_re), "s"(b_im), "s"(c_re), "s"(c_im), "s"(log_dt), "s"(dskip), "s"(w_in), "s"(w_out), "s"(ws));
  const rsrc_t R = __builtin_amdgcn_make_buffer_rsrc(ws, 0, WS_USED, 0x00020000);
  STAMP(0,0);
  const u32x2 ep = ld8(R, 0, O_EP);
  const bool is_conv = ii < 18;
  const int cg = xc * 18 + ii, rep = bid & (NREP - 1);
  auto mk_tag = [&]() {
    u64 e = (((u64)__builtin_amdgcn_readfirstlane(ep[1]) << 32) | (unsigned)__builtin_amdgcn_readfirstlane(ep[0])) + 0x9E3779B97F4A7C15ull;
    return u32x2{(unsigned)e, (unsigned)(e >> 32)};
  };
  u32x2 tag;
  auto arrive = [&](int slot, unsigned n, unsigned flag_off, unsigned stride, int nrep) {
    wait_vm<0>();
    const int lane = fresh_lane();
    unsigned old = 0;
    if (lane == 0) old = __hip_atomic_fetch_add((unsigned*)(sm + X_CNT) + slot, 1u, __ATOMIC_RELAXED, __HIP_MEMORY_SCOPE_WORKGROUP);
    if (__builtin_amdgcn_readfirstlane(old) == n - 1 && lane < nrep) {
#pragma unroll
      for (int k = 0; k < (nrep + 63) / 64; ++k) st8(R, (lane + 64 * k) * stride, flag_off, tag);
    }
  };
  if (is_conv) {
    const int ch = w & 3, dch = 4 * cg + ch;
    char* sk = sm + ch * CV_K;
    float sc = 0.f;
    u32x2 xc8[8];
    if (w < 4) {
      const int pi = (dch * NM + 2 * lane) * 2;
      const unsigned v_ar = *(const unsigned*)(a_re + pi), v_ai = *(const unsigned*)(a_im + pi), v_br = *(const unsigned*)(b_re + pi),
                     v_bi = *(const unsigned*)(b_im + pi), v_cr = *(const unsigned*)(c_re + pi), v_ci = *(const unsigned*)(c_im + pi);
      const unsigned v_dk = *(const unsigned short*)(dskip + 2 * dch), v_dt = *(const unsigned short*)(log_dt + 2 * dch);
      bar();
      const float p_dk = bf_lo(v_dk), dt = rbf(__expf(bf_lo(v_dt)));
      tag = mk_tag();
      STAMP(0,1);
      const f32x2 ar = (up2(v_ar) * dt) * 1.44269504f, ai = (up2(v_ai) * dt) * 0.15915494f;
      const f32x2 br = up2(v_br) * dt, bi = up2(v_bi) * dt, cr = up2(v_cr), ci = up2(v_ci);
      const f32x2 cbr = cr * br - ci * bi, cbi = -(cr * bi + ci * br);
      float mx = fmaxf(fmaxf(fabsf(cbr[0]), fabsf(cbi[0])), fmaxf(fabsf(cbr[1]), fabsf(cbi[1])));
      mx = fmaxf(mx, dpp_get<0x128>(mx)); mx = fmaxf(mx, dpp_get<0x124>(mx)); mx = fmaxf(mx, dpp_get<0x122>(mx)); mx = fmaxf(mx, dpp_get<0x121>(mx));
      auto rl = [&](int l) { return asf(__builtin_amdgcn_readlane((int)asu(mx), l)); };
      sc = fmaxf(fmaxf(rl(0), rl(16)), fmaxf(rl(32), rl(48)));
      sc = fmaxf(sc, fabsf(p_dk) * (1.0f / 256));
      const float isc = sc > 0.f ? 1.0f / sc : 0.f;
      {
        struct C2 { f32x2 a, b; };
        char* tb = sk + lane * 8;
        auto put = [&](int row, C2 v) { *(u32x2*)(tb + row * KT) = u32x2{pkh(v.a), pkh(v.b)}; };
        auto mul = [&](C2 v, C2 u, C2 up) { return C2{cmul(v.a, u.a, up.a), cmul(v.b, u.b, up.b)}; };
        auto prp = [&](C2 v) { return C2{perp(v.a), perp(v.b)}; };
        auto ex = [&](float l) { return C2{cexp(ar[0], ai[0], l), cexp(ar[1], ai[1], l)}; };
        const C2 w1 = ex(1.f), w128 = ex(128.f);
        C2 w8 = ex(8.f); w8.a[1] = -w8.a[1]; w8.b[1] = -w8.b[1];
        const C2 w1p = prp(w1), w8p = prp(w8), w128p = prp(w128);
        C2 w64 = ex(64.f); w64.a[1] = -w64.a[1]; w64.b[1] = -w64.b[1];
        const C2 w1k = ex(1024.f), w64p = prp(w64), w1kp = prp(w1k);
        C2 y = {f32x2{cbr[0], cbi[0]} * isc, f32x2{cbr[1], cbi[1]} * isc};
        C2 E[8]; E[0] = C2{f32x2{1.f, 0.f}, f32x2{1.f, 0.f}}; E[1] = w1;
        C2 H = w128;
        put(0, E[0]); put(8, w128); put(32, w1k); put(40, mul(w1k, w1k, w1kp));
#pragma unroll
        for (int e = 1; e < 8; ++e) {
          if (e > 1) E[e] = mul(E[e - 1], w1, w1p);
          put(e, E[e]); put(8 + e, mul(E[e], w128, w128p));
        }
        const C2 e2p = prp(E[2]), e4p = prp(E[4]), e6p = prp(E[6]);
#pragma unroll
        for (int a = 0; a < 8; ++a) {
          put(41 + a, y); put(49 + a, mul(y, w64, w64p));
          if (a < 7) y = mul(y, w8, w8p);
          if (a >= 2) H = mul(H, w128, w128p);
          if (a >= 1) put(32 + a, mul(H, w1k, w1kp));
          if (a >= 4) { put(16 + 2 * a, H); put(17 + 2 * a, mul(H, E[4], e4p)); }
          else if (a >= 2) { put(8 + 4 * a, H); put(9 + 4 * a, mul(H, E[2], e2p)); put(10 + 4 * a, mul(H, E[4], e4p)); put(11 + 4 * a, mul(H, E[6], e6p)); }
        }
      }
      asm volatile("" ::: "memory");
      STAMP(0,2);
      {
        f32x4 kk[3] = {};
        const char* pa = sk + li * KT + q * 16;
        u32x4 fr[8][4];
#pragma unroll
        for (int s = 0; s < 8; ++s)
#pragma unroll
          for (int t = 0; t < 4; ++t) fr[s][t] = *(const u32x4*)(pa + (t < 3 ? 16 * t : 41) * KT + s * 64);
        __builtin_amdgcn_sched_barrier(0);
#pragma unroll
        for (int s = 0; s < 8; ++s)
#pragma unroll
          for (int t = 0; t < 3; ++t) kk[t] = mfma_h(fr[s][t], fr[s][3], kk[t]);
        if (lane == 0) kk[0][0] += p_dk * isc;
        char* kd = sm + C_KD + ch * 1536;
        *(u32x2*)(kd + (128 * (q >> 1) + 4 * (q & 1) + 8 * li) * 2) = u32x2{pkh(lo2(kk[0])), pkh(hi2(kk[0]))};
        const int i1 = q < 2 ? 256 + 64 * q + 4 * li : 384 + 64 * (q - 2) + 2 * li;
        *(unsigned*)(kd + i1 * 2) = pkh(lo2(kk[1])); *(unsigned*)(kd + (i1 + (q < 2 ? 2 : 32)) * 2) = pkh(hi2(kk[1]));
#pragma unroll
        for (int r = 0; r < 4; ++r) *(_Float16*)(kd + (512 + 16 * (4 * q + r) + li) * 2) = (_Float16)kk[2][r];
      }
      asm volatile("" ::: "memory");
      u32x4 c0[4];
      {
        const unsigned short* kd = (const unsigned short*)(sm + C_KD + ch * 1536);
#pragma unroll
        for (int rr = 0; rr < 2; ++rr) {
          const int k = 255 - 64 * rr - lane;
          const unsigned a = kd[384 + k], b = kd[385 + k], aa = a | (a << 16);
          c0[rr] = u32x4{b | (b << 16), b | (((k & 1) ? b : a) << 16), aa, aa};
        }
        { const int k = 127 - lane;
          const unsigned ab = *(const unsigned*)(kd + 256 + 2 * k), cc = kd[258 + 2 * k];
          c0[2] = u32x4{cc | (cc << 16), __builtin_amdgcn_perm(ab, ab, 0x03020302), __builtin_amdgcn_perm(ab, ab, 0x01000302), __builtin_amdgcn_perm(ab, ab, 0x01000100)}; }
        { const int k = 63 - lane;
          u32x4 v = *(const u32x4*)(kd + 8 * (k & 31));
          u32x2 u = *(const u32x2*)(kd + 128 + 4 * k); unsigned e = kd[132 + 4 * k];
          asm volatile("" : "+v"(v), "+v"(u), "+v"(e));
          c0[3] = lane < 32 ? u32x4{__builtin_amdgcn_perm(u[1], e, 0x07060100), __builtin_amdgcn_perm(u[1], u[1], 0x01000100), __builtin_amdgcn_perm(u[0], u[1], 0x07060100), __builtin_amdgcn_perm(u[0], u[0], 0x01000100)}
                            : u32x4{__builtin_amdgcn_alignbit(v[3], v[3], 16), __builtin_amdgcn_alignbit(v[2], v[2], 16), __builtin_amdgcn_alignbit(v[1], v[1], 16), __builtin_amdgcn_alignbit(v[0], v[0], 16)}; }
#pragma unroll
        for (int rr = 0; rr < 4; ++rr) *(u32x4*)(sk + (64 * rr + lane) * 16) = c0[rr];
        if (lane < 8) *(u32x4*)(sk + (lane >> 1) * CV_C + 4096 + (lane & 1) * 16) = u32x4{0, 0, 0, 0};
      }
      asm volatile("" ::: "memory");
      STAMP(0,3);
      u32x2 c1[4];
#pragma unroll
      for (int rr = 0; rr < 4; ++rr) c1[rr] = *(const u32x2*)(sk + (64 * rr + lane) * 16 + 16);
#pragma unroll
      for (int rr = 0; rr < 4; ++rr) {
        char* p = sk + (64 * rr + lane) * 16;
        unsigned e[6] = {c0[rr][0], c0[rr][1], c0[rr][2], c0[rr][3], c1[rr][0], c1[rr][1]}, o[5];
#pragma unroll
        for (int i = 0; i < 5; ++i) o[i] = __builtin_amdgcn_alignbit(e[i + 1], e[i], 16);
        *(u32x4*)(p + CV_C) = u32x4{o[0], o[1], o[2], o[3]};
        *(u32x4*)(p + 2 * CV_C) = u32x4{e[1], e[2], e[3], e[4]};
        *(u32x4*)(p + 3 * CV_C) = u32x4{o[1], o[2], o[3], o[4]};
      }
      STAMP(0,4);
    } else {
      const int t8 = tid - 256;
      const rsrc_t RX = __builtin_amdgcn_make_buffer_rsrc((void*)x, 0, L * D * 2, 0x00020000);
      bar();
      __builtin_amdgcn_s_sleep(GDELAY);
#pragma unroll
      for (int k = 0; k < 8; ++k) xc8[k] = __builtin_amdgcn_raw_buffer_load_b64(RX, t8 * 9216 + k * 1152, cg * 8, GAUX);
      tag = mk_tag();
      if (cg < 136 && w >= 6) st16(R, (tid - 384) * 16, O_ST + cg * 2048, u32x4{SENT, SENT, SENT, SENT});
      if (t8 < 120) *(u32x4*)(sm + C_U + t8 * 16) = u32x4{0, 0, 0, 0};
      u32x4 g[4];
#pragma unroll 1
      for (int spin = 0; spin < SPIN; ++spin) {
        bool ok = true;
#pragma unroll
        for (int k = 0; k < 4; ++k) { g[k] = ld16(R, t8 * 64 + k * 16, O_LS + (bid & 15) * 16384); ok = ok && g[k][1] == tag[0] && g[k][3] == tag[1]; }
        if (__all(ok)) break;
        asm volatile("" ::: "memory");
      }
      STAMPW(5,0,4);
      __builtin_amdgcn_s_setprio(2);
      u32x4 uo[4];
#pragma unroll
      for (int k = 0; k < 4; ++k) {
        const f32x2 r2 = {__builtin_amdgcn_rcpf(bf_hi(g[k][0])), __builtin_amdgcn_rcpf(bf_hi(g[k][2]))}, b2 = -(f32x2{bf_lo(g[k][0]), bf_lo(g[k][2])} * r2);
#pragma unroll
        for (int c = 0; c < 4; ++c) {
          const unsigned a = xc8[2 * k][c >> 1], b = xc8[2 * k + 1][c >> 1];
          uo[c][k] = pkh(__builtin_elementwise_fma((c & 1) ? f32x2{bf_hi(a), bf_hi(b)} : f32x2{bf_lo(a), bf_lo(b)}, r2, b2));
        }
      }
      const int r = 15 + (t8 >> 1);
#pragma unroll
      for (int c = 0; c < 4; ++c) *(u32x4*)(sm + C_U + r * 128 + (((2 * c + (t8 & 1)) ^ ((r >> 1) & 7)) * 16)) = uo[c];
      __builtin_amdgcn_s_setprio(0);
      STAMPW(1,0,4);
    }
    bar();
    STAMP(0,5);
    if (w < 4) {
      f32x4 acc[8];
      const int sg = 7 - (li & 7);
      const char* pa = sk + (sg & 3) * CV_C + (253 - (li >> 3) + q) * 16 + (sg >> 2) * 8 - 4032;
      struct CF { u32x4 a[8], b[8]; } A, B;
      auto ldc = [&](CF& f, int a0) {
        const int r = 14 - 2 * a0 + li + (q >> 1);
        const char* pb = sm + C_U + r * 128 + (((2 * ch + (q & 1)) ^ ((r >> 1) & 7)) * 16);
#pragma unroll
        for (int m = 0; m < 8; ++m) {
          const char* p = pa + 4032 - 64 * a0 - 512 * m;
          const u32x2 x0 = *(volatile const LDSP u32x2*)p, x1 = *(volatile const LDSP u32x2*)(p + 8);
          f.a[m] = u32x4{x0[0], x0[1], x1[0], x1[1]};
          f.b[m] = *(const u32x4*)(pb + 2048 * m);
        }
      };
      auto mmc = [&](const CF& f, bool first) {
#pragma unroll
        for (int m = 0; m < 8; ++m)
#pragma unroll
          for (int t = m; t < 8; ++t) acc[t] = mfma_h(f.a[m], f.b[t - m], first && m == 0 ? f32x4{0, 0, 0, 0} : acc[t]);
#pragma unroll
        for (int i = 0; i < 24; ++i) { __builtin_amdgcn_sched_group_barrier(0x008, 1, 0); __builtin_amdgcn_sched_group_barrier(0x100, 1, 0); }
        __builtin_amdgcn_sched_barrier(0);
      };
      ldc(A, 0);
      __builtin_amdgcn_sched_barrier(0);
#pragma unroll
      for (int a0 = 0; a0 < 8; a0 += 2) { ldc(B, a0 + 1); mmc(A, a0 == 0); if (a0 < 6) ldc(A, a0 + 2); mmc(B, false); }
      STAMP(0,6);
#pragma unroll
      for (int t = 0; t < 8; ++t) *(u32x2*)(sk + t * 512 + li * 32 + q * 8) = u32x2{pk2(lo2(acc[t]) * sc), pk2(hi2(acc[t]) * sc)};
    } else {
#pragma unroll
      for (int k = 0; k < 4; ++k) *(u32x4*)(sk + CV_X + lane * 64 + k * 16) = u32x4{xc8[2 * k][0], xc8[2 * k][1], xc8[2 * k + 1][0], xc8[2 * k + 1][1]};
    }
    bar();
#pragma unroll
    for (int r = 0; r < 2; ++r) {
      const int T = (tid >> 3) + 64 * r, ip = tid & 7;
      const int t0 = 32 * (T >> 1) + 8 * (ip >> 1) + 4 * (T & 1) + 2 * (ip & 1);
      const char* p = sm + t0 * 2;
      const u32x4 xv = *(const u32x4*)(sm + (t0 >> 9) * CV_K + CV_X + (t0 & 511) * 8);
      const unsigned c0 = *(const unsigned*)p, c1 = *(const unsigned*)(p + CV_K), c2 = *(const unsigned*)(p + 2 * CV_K), c3 = *(const unsigned*)(p + 3 * CV_K);
      u32x4 v = {pk2(up2(xv[0]) + f32x2{bf_lo(c0), bf_lo(c1)}), pk2(up2(xv[1]) + f32x2{bf_lo(c2), bf_lo(c3)}),
                 pk2(up2(xv[2]) + f32x2{bf_hi(c0), bf_hi(c1)}), pk2(up2(xv[3]) + f32x2{bf_hi(c2), bf_hi(c3)})};
      st16(R, (T >> 3) * (18 * 8192) + (T & 7) * 1024 + ip * 16, O_HF + (cg >> 3) * 8192 + (cg & 1) * 512 + ((cg >> 1) & 3) * 128, v);
    }
    STAMPW(2,1,4);
    arrive(1, 8, O_F2 + cg * 8, F2S, NREP);
    STAMPW(2,2,4);
  } else {
    const int tj = (ii - 18) * 8 + xc;
    const int kc = tj / 36, cb = tj % 36;
    if (tj < 64) {
      const int row = 32 * tj + 4 * w + q;
      const char* src = x + (size_t)row * 1152 + li * 8;
      u32x2 xr[9];
#pragma unroll
      for (int k = 0; k < 9; ++k) xr[k] = *(const u32x2*)(src + k * 128);
      tag = mk_tag();
      float a[4] = {0, 0, 0, 0};
#pragma unroll
      for (int k = 0; k < 9; ++k)
#pragma unroll
        for (int e = 0; e < 2; ++e) { a[e] = dot2(xr[k][e], 0x3F803F80u, a[e]); a[2 + e] = dot2(xr[k][e], xr[k][e], a[2 + e]); }
      const float mean = row16_sum(a[0] + a[1]) * (1.0f / D), var = fmaxf(row16_sum(a[2] + a[3]) * (1.0f / D) - mean * mean, 0.f);
      st8(R, li * 16384 + row * 8, O_LS, u32x2{pk_bf16(mean, __builtin_amdgcn_sqrtf(rbf(rbf(var) + EPS))), (q & 1) ? tag[1] : tag[0]});
      STAMP(1,1);
      for (int i = 0; i < WDELAY - 3; ++i) __builtin_amdgcn_s_sleep(16);
    } else
      for (int i = 0; i < WDELAY; ++i) __builtin_amdgcn_s_sleep(16);
    char* s0 = sm + 65536;
    const int G = max(tj - 40, 0), par = G & 1;
    {
      const int row = tid >> 4, uu = G / 9, idx = G % 9, hh = row >> 4;
      const int panel = idx < 4 ? 2 * uu : (idx < 8 ? 2 * uu + 1 : 2 * uu + hh), tile = idx < 8 ? 2 * (idx & 3) + hh : 8;
      const char* src = w_in + (size_t)(192 * min(kc, 2) + row) * 9216 + cb * 256 + li * 16;
      const char* wsrc = w_out + (size_t)(144 * panel + 16 * tile + (row & 15)) * 1152 + li * 8;
      u32x4 v[6]; u32x2 vo[9];
#pragma unroll
      for (int k = 0; k < 6; ++k) v[k] = *(const u32x4*)(src + (size_t)k * 32 * 9216);
      if (tj >= 40) {
#pragma unroll
        for (int k = 0; k < 9; ++k) vo[k] = *(const u32x2*)(wsrc + k * 128);
      }
#pragma unroll
      for (int k = 0; k < 6; ++k) *(u32x4*)(sm + (row + 32 * k) * 272 + li * 16) = v[k];
      if (tj >= 40) {
#pragma unroll
        for (int k = 0; k < 9; ++k) *(u32x2*)(s0 + row * TP + li * 8 + k * 128) = vo[k];
      }
    }
    tag = mk_tag();
    bar();
    STAMP(4,0);
    if (tj < 108) {
      const int ct = 8 * cb + w;
      const int hf = ct < 144 ? 1 : 0, cl = ct < 144 ? ct : ct - 144, fp = cl / 9, n = cl % 9;
      const char* trb = sm + (8 * q + (li >> 2)) * 272 + w * 32 + (li & 3) * 8;
      const unsigned dst = lane * 16 + O_WI + (((fp * 2 + hf) * 18 + 6 * kc) * 9 + n) * 1024;
      float s = 0.f;
#pragma unroll
      for (int g = 0; g < 6; ++g) {
        u32x2 lo = tr_read(trb + g * 32 * 272), hi = tr_read(trb + g * 32 * 272 + 4 * 272);
        st16(R, dst, g * 9216, u32x4{lo[0], lo[1], hi[0], hi[1]});
        s = dot2(lo[0], 0x3F803F80u, s); s = dot2(lo[1], 0x3F803F80u, s); s = dot2(hi[0], 0x3F803F80u, s); s = dot2(hi[1], 0x3F803F80u, s);
      }
      s = q_sum(s);
      if (q == 0) st4(R, li * 4, O_CS + kc * F * 4 + ct * 64, asu(s));
    }
    if (tj >= 40) {
      const bool mixed = G % 9 == 8;
      const char* trb = s0 + ((mixed ? 16 * (q >> 1) + 4 * (q & 1) : 4 * q) + (li >> 2)) * TP + (li & 3) * 8;
      const char* trh = trb + (mixed ? 8 : 16) * TP;
      const unsigned dst = lane * 16 + O_WO + (w * 36 + (G >> 1)) * 9216;
#pragma unroll
      for (int n = 0; n < 5; ++n) {
        const int c0 = 72 * w + 16 * n - (n == 4 ? 8 * par : 0);
        u32x2 lo = tr_read(trb + c0 * 2), hi = tr_read(trh + c0 * 2);
        if (n < 4) st16(R, dst, (4 * par + n) * 1024, u32x4{lo[0], lo[1], hi[0], hi[1]});
        else if ((li >> 3) == par) st16(R, dst, 8192, u32x4{lo[0], lo[1], hi[0], hi[1]});
      }
    }
    STAMP(4,1);
    arrive(0, 8, O_F2 + (144 + tj) * 8, F2S, NREP);
    STAMP(4,2);
  }
  const int tp = (xc & 1) * 8 + (ii >> 2), fp = (xc >> 1) * 4 + (ii & 3);
  const int t64 = 4 * xc + (ii >> 3), j3 = ii & 7;
  if (w == 0) { wait_flags<1>(R, O_F2 + rep * F2S + 144 * 8, 112, tag, lane); STAMPW(6,0,0); }
  bar();
  if (w >= 4) {
    const int lw = w - 4;
    const unsigned l16 = lane * 16, base = lds_addr(sm);
    const int ph = ((ii >> 2) + ii) & 3, ph3 = ROT3 ? ii & 3 : ((ii >> 3) + ii) & 3, rot3 = ROT3 ? (9 * (ii >> 3) + 1) >> 1 : 0;
    auto k9 = [&](int t) { return (t & 3) == ph ? (t + 4 < 9 ? t + 4 : ph) : t; };
    auto k32 = [&](int t) { return (t & 3) == ph ? ((t + 4) & 31) : t; };
    auto k18 = [&](int t) { const int k = ((t & 3) == ph3 ? (t + 4 < 18 ? t + 4 : ph3) : t) + rot3; return k >= 18 ? k - 18 : k; };
    if (lw < 2) {
      constexpr int NP = 8;
      unsigned vo[8];
#pragma unroll
      for (int j = 0; j < 8; ++j) {
        int t = 64 * lw + 32 * (j >> 2) + 4 * ((j >> 1) & 1) + 16 * (j & 1) + 8 * (lane >> 5) + ((lane >> 3) & 3);
        vo[j] = t * 4096 + (((lane & 7) ^ ((lane >> 3) & 7)) * 16) + 3072 - (j & 3) * 1024;
      }
      const char* wsrc = w_sp + (size_t)tp * 128 * 4096 - 3072;
      const char* hsrc = ws + O_HF + tp * 18 * 8192 + lw * 8192;
      const char* psrc = ws + O_PF + t64 * 72 * 4096 + lw * 8192;
      const unsigned dst = base + lw * 8192;
      if (lw == 0) wait_flags<2>(R, O_F2 + rep * F2S, 144, tag, lane);
      STAMPW(5,1,4);
      bar();
#pragma unroll 1
      for (int t = 0; t < 4; ++t) ld_lin<NP>(dst + t * STG, hsrc + k9(t) * 16384, l16);
      wait_vm<3 * NP>(); bar();
#pragma unroll 1
      for (int t = 4; t < 18; ++t) { wait_vm<2 * NP>(); bar(); ld_lin<NP>(dst + (t & 3) * STG, hsrc + k9(t < 9 ? t : t - 9) * 16384, l16); }
#pragma unroll 1
      for (int t = 18; t < 50; ++t) {
        wait_vm<2 * NP>(); bar();
        const unsigned dd = dst + (t & 3) * STG; const char* sp = wsrc + k32(t - 18) * 128;
        glds4<false>(dd, sp, vo[0], vo[1], vo[2], vo[3]); glds4<false>(dd + 4096, sp, vo[4], vo[5], vo[6], vo[7]);
      }
      wait_vm<2 * NP>(); bar(); wait_vm<NP>(); bar(); wait_vm<0>(); bar();
      bar(); bar();
      wait_flags<1>(R, O_PFL + (t64 >> 1) * 2048 + ((t64 & 1) * 8 + j3) * 128, 16, tag, lane);
      STAMPW(5,2,4);
#pragma unroll 1
      for (int t = 50; t < 68; ++t) {
        if (t >= 53) { wait_vm<2 * NP>(); bar(); }
        ld_lin<NP>(dst + (t & 3) * STG, psrc + k18(t - 50) * 16384, l16);
      }
      wait_vm<2 * NP>(); bar(); wait_vm<NP>(); bar(); wait_vm<0>(); bar();
    } else {
      constexpr int NP = 9;
      const char* wi = ws + O_WI + (fp * 2) * 18 * 9216 + (lw - 2) * 9216;
      const char* zs = ws + O_ZF + fp * 64 * 9216 + (lw - 2) * 9216;
      const char* wo = ws + O_WO + j3 * 36 * 9216 + (lw - 2) * 9216;
      const unsigned dst = base + 16384 + (lw - 2) * 9216;
#pragma unroll 1
      for (int t = 0; t < 4; ++t) ld_lin<NP>(dst + t * STG, wi + k9(t) * 18432, l16);
      bar();
      wait_vm<3 * NP>(); bar();
      if (lw == 3) {
        f32x2 a = *(const f32x2*)(sm + X_LN + lane * 8) + *(const f32x2*)(sm + X_LN + 512 + lane * 8);
        a[0] = q_sum(dpp_add<0x128>(a[0])); a[1] = q_sum(dpp_add<0x128>(a[1]));
        float mean = a[0] * (1.0f / D), mb, ri;
        ln_scalars(mean, fmaxf(a[1] * (1.0f / D) - mean * mean, 0.f), mb, ri);
        if (lane < 8) st8(R, lane * 8, O_HS + tp * 1024 + fp * 64, u32x2{asu(ri) == SENT ? 0x7FC00000u : asu(ri), asu(-mb)});
      }
#pragma unroll 1
      for (int t = 4; t < 18; ++t) { wait_vm<2 * NP>(); bar(); ld_lin<NP>(dst + (t & 3) * STG, wi + (t < 9 ? k9(t) : 9 + k9(t - 9)) * 18432, l16); }
      wait_vm<2 * NP>(); bar(); wait_vm<NP>(); bar(); wait_vm<0>(); bar();
      wait_flags<1>(R, O_ZFL + fp * 2048 + tp * 128, 16, tag, lane);
      STAMPW(6,1,6);
#pragma unroll 1
      for (int t = 18; t < 53; ++t) {
        if (t >= 21) { wait_vm<2 * NP>(); bar(); }
        ld_lin<NP>(dst + (t & 3) * STG, t < 50 ? zs + k32(t - 18) * 18432 : wo + k18(t - 50) * 18432, l16);
      }
      wait_vm<0>(); bar();
      bar();
#pragma unroll 1
      for (int t = 53; t < 57; ++t) ld_lin<NP>(dst + (t & 3) * STG, wo + k18(t - 50) * 18432, l16);
      bar(); bar(); bar();
      wait_vm<3 * NP>(); bar();
#pragma unroll 1
      for (int t = 57; t < 68; ++t) { wait_vm<2 * NP>(); bar(); ld_lin<NP>(dst + (t & 3) * STG, wo + k18(t - 50) * 18432, l16); }
      wait_vm<2 * NP>(); bar(); wait_vm<NP>(); bar(); wait_vm<0>(); bar();
    }
    return;
  }
  Ctx c;
  c.sm = sm;
#pragma unroll
  for (int i = 0; i < 4; ++i) { c.am0[i] = (2 * w + (i >> 1)) * 1024 + (i & 1) * 512 + lane * 8; asm volatile("" : "+v"(c.am0[i])); }
  c.am1 = 2 * w * 2048 + li * 128 + ((q ^ (lane & 7)) * 16);
  c.an = 16384 + lane * 16;
  const int prow = 8 * (li >> 2) + (li & 3);
  if (tid < 72) {
    const unsigned o = tid < 36 ? FH * 4 + tid * 16 : (tid - 36) * 16;
    const u32x4 c0 = ld16(R, o, O_CS + fp * 576), c1 = ld16(R, o, O_CS + F * 4 + fp * 576), c2 = ld16(R, o, O_CS + F * 8 + fp * 576);
    *(f32x4*)(sm + X_CS + tid * 16) = (__builtin_bit_cast(f32x4, c0) + __builtin_bit_cast(f32x4, c1)) + __builtin_bit_cast(f32x4, c2);
  }
  bar();
  char* pvb = sm + X_PV + w * 4096;
  {
    u32x4 hv[3];
#pragma unroll
    for (int k = 0; k < 3; ++k)
      hv[k] = ld16(R, (lane >> 5) * 8192 + ((lane >> 2) & 7) * 128 + (lane & 3) * 16, O_HF + (tp * 18 * 8 + (fp >> 1)) * 1024 + (fp & 1) * 64 + min(w + 4 * k, 8) * 16384);
    if (w) hv[2] = u32x4{0, 0, 0, 0};
    f32x4 a = {0, 0, 0, 0};
#pragma unroll
    for (int k = 0; k < 3; ++k)
#pragma unroll
      for (int e = 0; e < 4; ++e) { a[e & 2] = dot2(hv[k][e], 0x3F803F80u, a[e & 2]); a[(e & 2) + 1] = dot2(hv[k][e], hv[k][e], a[(e & 2) + 1]); }
#pragma unroll
    for (int e = 0; e < 4; ++e) a[e] = dpp_add<0x124>(dpp_add<0x128>(a[e]));
    if (li < 4) *(f32x4*)(sm + X_LN + (w * 4 + q) * 64 + li * 16) = a;
  }
  f32x4 acc[2][9];
  Fr X, Y;
  bar();
  STAMP(3,0);
  ldf<0>(X, c, 0, 0);
  seg<0, 0, true, true>(acc, X, Y, c, 0, 1);
  const unsigned hs_m0 = __builtin_amdgcn_readfirstlane(lds_addr(sm) + X_HS + w * 256);
  auto ld_hs = [&]() {
    unsigned keep;
    asm volatile("s_mov_b32 %0, m0\n\ts_mov_b32 m0, %1\n\ts_nop 2\n\tglobal_load_lds_dword %3, %2 sc1\n\ts_mov_b32 m0, %0"
                 : "=&s"(keep) : "s"(hs_m0), "s"(ws), "v"(O_HS + tp * 1024 + tid * 4) : "memory");
  };
#pragma unroll 1
  for (int t = 0; t < 8; ++t) {
    if (t == 5) ld_hs();
    bar();
    seg<0, 0, true>(acc, Y, X, c, t + 1, 0);
    seg<0, 0, true>(acc, X, Y, c, t + 1, 1);
  }
  u32x4 hc[4];
  float cs[9];
  auto rd_hs = [&]() {
    wait_vm<0>();
#pragma unroll
    for (int jp = 0; jp < 4; ++jp) hc[jp] = *(const u32x4*)(sm + X_HS + w * 256 + (jp >> 1) * 128 + q * 32 + (jp & 1) * 16);
  };
  rd_hs();
#pragma unroll
  for (int n = 0; n < 9; ++n) cs[n] = *(const float*)(sm + X_CS + (16 * n + li) * 4);
  bar();
  seg<0, 0, true>(acc, Y, X, c, 9, 0);
  STAMP(3,1);
#pragma unroll 1
  for (int spin = 0; spin < SPIN; ++spin) {
    if (__all(max(max(umax4(hc[0]), umax4(hc[1])), max(umax4(hc[2]), umax4(hc[3]))) != SENT)) break;
    ld_hs();
    rd_hs();
  }
  u32x4 zb[9];
  f32x2 rs[4];
  {
    f32x2 nm[4], k1[4], k0[4], S1[4] = {}, S2[4] = {};
#pragma unroll
    for (int jp = 0; jp < 4; ++jp) {
      rs[jp] = f32x2{asf(hc[jp][0]), asf(hc[jp][2])}; nm[jp] = f32x2{asf(hc[jp][1]), asf(hc[jp][3])};
      k0[jp] = rs[jp] * GC0; k1[jp] = rs[jp] * rs[jp] * rs[jp] * GC1;
    }
#pragma unroll
    for (int n = 0; n < 9; ++n) {
#pragma unroll
      for (int jp = 0; jp < 4; ++jp) {
        f32x2 a = (jp & 1) ? hi2(acc[jp >> 1][n]) : lo2(acc[jp >> 1][n]);
        f32x2 z = gelu2(__builtin_elementwise_fma(nm[jp], f32x2{cs[n], cs[n]}, a), k1[jp], k0[jp]);
        zb[n][jp] = pk2(z);
        S1[jp] += z; S2[jp] = __builtin_elementwise_fma(z, z, S2[jp]);
      }
      asm volatile("" : "+v"(zb[n]));
    }
#pragma unroll
    for (int jp = 0; jp < 4; ++jp) { S1[jp] *= rs[jp]; S2[jp] *= rs[jp] * rs[jp]; }
    float v[16];
#pragma unroll
    for (int j = 0; j < 8; ++j) { v[2 * j] = S1[j >> 1][j & 1]; v[2 * j + 1] = S2[j >> 1][j & 1]; }
#pragma unroll
    for (int i = 0; i < 8; ++i) { bool h = li & 8; v[i] = (h ? v[i + 8] : v[i]) + dpp_get<0x128>(h ? v[i] : v[i + 8]); }
#pragma unroll
    for (int i = 0; i < 4; ++i) { bool h = li & 4; v[i] = (h ? v[i + 4] : v[i]) + dpp_get<0x141>(h ? v[i] : v[i + 4]); }
#pragma unroll
    for (int i = 0; i < 2; ++i) { bool h = li & 2; v[i] = (h ? v[i + 2] : v[i]) + dpp_get<0x4E>(h ? v[i] : v[i + 2]); }
    { bool h = li & 1; v[0] = (h ? v[1] : v[0]) + dpp_get<0xB1>(h ? v[0] : v[1]); }
    unsigned tot = asu(v[0]);
    st4(R, lane * 4, O_ST + ((tp * 4 + w) * 16 + fp) * 256, tot == SENT ? 0x7FC00000u : tot);
  }
  STAMP(3,2);
  const char* sts = ws + O_ST + (tp * 4 + w) * 4096;
  seg<0, 1, true, true>(acc, X, Y, c, 9, 1);
#pragma unroll 1
  for (int t = 9; t < TCHK; ++t) {
    if (t == TPOLL) glds4<true>(lds_addr(pvb), sts, lane * 16, lane * 16, lane * 16, lane * 16);
    bar();
    seg<0, 1, true>(acc, Y, X, c, t + 1, 0);
    seg<0, 1, true>(acc, X, Y, c, t + 1, 1);
  }
  STAMP(3,3);
  u32x4 pv[4];
#pragma unroll 1
  for (int spin = 0; spin < SPIN; ++spin) {
    wait_vm<0>();
#pragma unroll
    for (int k = 0; k < 4; ++k) pv[k] = *(const u32x4*)(pvb + k * 1024 + lane * 16);
    if (__all(max(max(umax4(pv[0]), umax4(pv[1])), max(umax4(pv[2]), umax4(pv[3]))) != SENT)) break;
    glds4<true>(lds_addr(pvb), sts, lane * 16, lane * 16, lane * 16, lane * 16);
  }
  STAMP(3,4);
  {
    float t4[4];
#pragma unroll
    for (int e = 0; e < 4; ++e) t4[e] = q_sum((asf(pv[0][e]) + asf(pv[1][e])) + (asf(pv[2][e]) + asf(pv[3][e])));
    f32x2 o0, o1;
#pragma unroll
    for (int r = 0; r < 2; ++r) {
      float mean = t4[2 * r] * (1.0f / FH), var = fmaxf(t4[2 * r + 1] * (1.0f / FH) - mean * mean, 0.f), mb, ri;
      ln_scalars(mean, var, mb, ri);
      o0[r] = ri; o1[r] = -mb * ri;
    }
    char* ln = sm + X_LN + w * 256;
    if (lane < 16) { *(f32x2*)(ln + li * 8) = o0; *(f32x2*)(ln + 128 + li * 8) = o1; }
    asm volatile("" ::: "memory");
    f32x4 ri[2], rb[2];
#pragma unroll
    for (int m = 0; m < 2; ++m) { ri[m] = *(const f32x4*)(ln + (8 * q + 4 * m) * 4); rb[m] = *(const f32x4*)(ln + 128 + (8 * q + 4 * m) * 4); }
#pragma unroll
    for (int jp = 0; jp < 4; ++jp) rs[jp] *= (jp & 1) ? hi2(ri[jp >> 1]) : lo2(ri[jp >> 1]);
    const unsigned zo = lane * 16 + O_ZF + (fp * 64 + 4 * tp + w) * 9216;
#pragma unroll
    for (int n = 0; n < 9; ++n) {
      u32x4 o4;
#pragma unroll
      for (int jp = 0; jp < 4; ++jp)
        o4[jp] = pk2(__builtin_elementwise_fma(up2(zb[n][jp]), rs[jp], (jp & 1) ? hi2(rb[jp >> 1]) : lo2(rb[jp >> 1])));
      st16(R, zo, n * 1024, o4);
    }
  }
  STAMP(3,5);
#pragma unroll 1
  for (int t = TCHK; t < 17; ++t) {
    if (t == TFLAG) wait_vm<0>();
    bar();
    if (t == TFLAG && tid < 16) st8(R, tid * 128, O_ZFL + fp * 2048 + tp * 8, tag);
    seg<0, 1, true>(acc, Y, X, c, t + 1, 0);
    seg<0, 1, true>(acc, X, Y, c, t + 1, 1);
  }
  float lrs[2], lnm[2];
  f32x4 cs1[9];
#pragma unroll
  for (int m = 0; m < 2; ++m) { const f32x2 h = *(const f32x2*)(sm + X_HS + w * 256 + m * 128 + li * 8); lrs[m] = h[0]; lnm[m] = h[1]; }
#pragma unroll
  for (int n = 0; n < 9; ++n) cs1[n] = *(const f32x4*)(sm + X_CS + (144 + 16 * n + 4 * q) * 4);
  __builtin_amdgcn_sched_barrier(0);
  seg<0, 1, false>(acc, Y, X, c, 17, 1);
  STAMP(3,6);
  u32x2 z1[2][9];
  float bs[2];
#pragma unroll
  for (int m = 0; m < 2; ++m) bs[m] = ldbf(b_sp, 128 * tp + 32 * w + prow + 4 * m) * lrs[m];
  {
#pragma unroll
    for (int n = 0; n < 9; ++n)
#pragma unroll
      for (int m = 0; m < 2; ++m) {
        const float a1 = lrs[m] * lrs[m] * lrs[m] * GC1, a0 = lrs[m] * GC0;
        const f32x2 k1 = {a1, a1}, k0 = {a0, a0}, nm = {lnm[m], lnm[m]};
        z1[m][n][0] = pk2(gelu2(__builtin_elementwise_fma(nm, lo2(cs1[n]), lo2(acc[m][n])), k1, k0));
        z1[m][n][1] = pk2(gelu2(__builtin_elementwise_fma(nm, hi2(cs1[n]), hi2(acc[m][n])), k1, k0));
        asm volatile("" : "+v"(z1[m][n]));
      }
  }
  STAMP(3,7);
  bar();
  STAMP(3,8);
  ldf<1>(X, c, 18, 0);
  seg<1, 1, true, true>(acc, X, Y, c, 18, 1);
#pragma unroll 1
  for (int t = 18; t < 49; ++t) {
    bar();
    seg<1, 1, true>(acc, Y, X, c, t + 1, 0);
    seg<1, 1, true>(acc, X, Y, c, t + 1, 1);
  }
  seg<1, 1, false>(acc, Y, X, c, 49, 1);
  STAMP(3,9);
  {
    const unsigned pf = O_PF + (((2 * tp + (w >> 1)) * 72 + 9 * (fp >> 1)) * 4 + 2 * (w & 1)) * 1024;
    const unsigned po = lane * 16 + pf + (fp & 1) * 16384;
    u32x2 p8[2];
#pragma unroll
    for (int m = 0; m < 2; ++m) {
      const f32x2 r2 = {lrs[m], lrs[m]}, b2 = {bs[m], bs[m]};
      auto pp = [&](int n) { return u32x2{pk2(__builtin_elementwise_fma(lo2(acc[m][n]), r2, b2) * up2(z1[m][n][0])), pk2(__builtin_elementwise_fma(hi2(acc[m][n]), r2, b2) * up2(z1[m][n][1]))}; };
#pragma unroll
      for (int a = 0; a < 4; ++a) {
        u32x2 p0 = pp(2 * a), p1 = pp(2 * a + 1);
        st16(R, po, a * 4096 + m * 1024, u32x4{p0[0], p0[1], p1[0], p1[1]});
      }
      p8[m] = pp(8);
    }
    const auto s0 = __builtin_amdgcn_permlane32_swap(p8[0][0], p8[1][0], false, false), s1 = __builtin_amdgcn_permlane32_swap(p8[0][1], p8[1][1], false, false);
    st16(R, (lane >> 5) * 1024 + (lane & 31) * 16, pf + 8 * 4096 + (fp & 1) * 512, u32x4{s0[0], s1[0], s0[1], s1[1]});
  }
  STAMP(3,10);
  const int th = w & 1, kq = w >> 1, T = 4 * t64 + 2 * th + kq;
  const int lane5 = fresh_lane();
#define lane lane5
#define li (lane5 & 15)
#define q (lane5 >> 4)
#define prow (8 * (li >> 2) + (li & 3))
  struct P3 { u32x4 p[2][2]; } Up, Vp;
  struct N3 { u32x4 n[9]; } Un, Vn, En;
  const int ap = (8 * kq + 2 * th) * 1024 + lane * 16, aw = 16384 + kq * 9216 + lane * 16;
  auto ldp = [&](P3& f, int t) {
#pragma unroll
    for (int g = 0; g < 2; ++g)
#pragma unroll
      for (int m = 0; m < 2; ++m) f.p[g][m] = *(const u32x4*)(sm + (t & 3) * STG + ap + (4 * g + m) * 1024);
  };
  auto ldn = [&](N3& f, int t) {
#pragma unroll
    for (int n = 0; n < 9; ++n) f.n[n] = *(const u32x4*)(sm + (t & 3) * STG + aw + n * 1024);
  };
  bar();
  ldn(Un, 50); ldn(Vn, 51); ldn(En, 52);
  arrive(2, 4, O_PFL + tp * 2048 + fp * 8, 128, 16);
  bar();
  u32x2 hv[5];
#pragma unroll
  for (int n = 0; n < 5; ++n) {
    const int dd = 72 * j3 + 16 * n + 4 * (n == 4 ? (q & 1) : q);
    hv[n] = ld8(R, (dd >> 5) * 8192 + ((dd >> 2) & 1) * 512 + (((dd >> 3) & 3) * 16 + li) * 8, O_HF + ((T >> 3) * 18 * 8 + (T & 7)) * 1024);
  }
  f32x4 a3[2][6] = {};
  auto mm3 = [&](const P3& f, const N3& e) {
#pragma unroll
    for (int g = 0; g < 2; ++g)
#pragma unroll
      for (int m = 0; m < 2; ++m) {
#pragma unroll
        for (int n = 0; n < 4; ++n) a3[m][n] = mfma_bf(e.n[4 * g + n], f.p[g][m], a3[m][n]);
        a3[m][4 + g] = mfma_bf(e.n[8], f.p[g][m], a3[m][4 + g]);
      }
#pragma unroll
    for (int i = 0; i < 13; ++i) { __builtin_amdgcn_sched_group_barrier(0x008, 1, 0); __builtin_amdgcn_sched_group_barrier(0x100, 1, 0); }
    __builtin_amdgcn_sched_barrier(0);
  };
  STAMP(3,11);
  bar();
  STAMP(3,12);
  ldp(Up, 50);
  bar(); ldp(Vp, 51); mm3(Up, Un);
  bar(); ldp(Up, 52); mm3(Vp, Vn);
  bar(); ldp(Vp, 53); ldn(Vn, 53); mm3(Up, En);
#pragma unroll 1
  for (int t = 53; t < 67; t += 2) { bar(); ldp(Up, t + 1); ldn(Un, t + 1); mm3(Vp, Vn); bar(); ldp(Vp, t + 2); ldn(Vn, t + 2); mm3(Up, Un); }
  mm3(Vp, Vn);
  STAMP(3,13);
#pragma unroll
  for (int m = 0; m < 2; ++m) a3[m][4] = lane < 32 ? a3[m][4] : a3[m][5];
  if (kq) {
#pragma unroll
    for (int n = 0; n < 5; ++n) { *(f32x4*)(sm + w * 5120 + n * 1024 + lane * 16) = a3[0][n]; asm volatile("" : "+v"(a3[1][n])); a3[0][n] = a3[1][n]; }
  } else {
#pragma unroll
    for (int n = 0; n < 5; ++n) *(f32x4*)(sm + w * 5120 + n * 1024 + lane * 16) = a3[1][n];
  }
  bar();
  char* o = out + (size_t)(32 * (T >> 1) + 4 * (T & 1) + prow) * 1152 + (72 * j3 + 4 * q) * 2;
#pragma unroll
  for (int n = 0; n < 5; ++n) {
    f32x4 v = a3[0][n] + *(const f32x4*)(sm + (w ^ 2) * 5120 + n * 1024 + lane * 16);
    if (n == 4) v = f32x4{half_sum(v[0]), half_sum(v[1]), half_sum(v[2]), half_sum(v[3])};
    if (n < 4 || q < 2) *(u32x2*)(o + n * 32) = u32x2{pk2(up2(hv[n][0]) + lo2(v)), pk2(up2(hv[n][1]) + hi2(v))};
  }
  if (bid == 0 && w == 0 && lane == 0) { u64 e = (((u64)tag[1] << 32) | tag[0]) - 0x9E3779B97F4A7C15ull + 1; st8(R, 0, O_EP, u32x2{(unsigned)e, (unsigned)(e >> 32)}); }
  STAMP(3,14);
}

#undef lane
#undef li
#undef q
#undef prow
extern "C" void kernel_launch(void* const* d_in, const int* in_sizes, int n_in, void* d_out, int out_size, void* d_ws, size_t ws_size,
                              hipStream_t stream) {
  (void)hipFuncSetAttribute((const void*)k_all, hipFuncAttributeMaxDynamicSharedMemorySize, LDS_BYTES);
  k_all<<<256, 512, LDS_BYTES, stream>>>((const char*)d_in[0], (const char*)d_in[1], (const char*)d_in[2], (const char*)d_in[3],
      (const char*)d_in[4], (const char*)d_in[5], (const char*)d_in[6], (const char*)d_in[7], (const char*)d_in[8], (const char*)d_in[10],
      (const char*)d_in[11], (const char*)d_in[12], (const char*)d_in[13], (char*)d_out, (char*)d_ws);
}
```

```cpp
#include <hip/hip_runtime.h>
#include <hip/hip_bf16.h>
#include <stdint.h>

typedef __attribute__((ext_vector_type(8))) __bf16 bf16x8;
typedef __attribute__((ext_vector_type(2))) __bf16 bf16x2;
typedef __attribute__((ext_vector_type(8))) _Float16 f16x8;
typedef __attribute__((ext_vector_type(2))) _Float16 f16x2;
typedef __attribute__((ext_vector_type(4))) float f32x4;
typedef __attribute__((ext_vector_type(2))) float f32x2;
typedef __attribute__((ext_vector_type(4))) unsigned u32x4;
typedef __attribute__((ext_vector_type(2))) unsigned u32x2;
typedef __attribute__((ext_vector_type(4))) short s16x4;
#define LDSP __attribute__((address_space(3)))
#define DEVI __device__ __forceinline__

constexpr int L = 2048, D = 576, NM = 128, F = 4608, FH = 2304;
constexpr float EPS = 1e-5f;
constexpr unsigned O_EP = 0;
#define PSLEEP 0
#define NREPV 64
constexpr int NREP = NREPV;
constexpr unsigned O_F2 = 256;
constexpr unsigned F2S = 2048;
constexpr unsigned O_ZFL = O_F2 + NREP * F2S;
constexpr unsigned O_PFL = O_ZFL + 32768;
constexpr unsigned O_CS = O_PFL + 32768;
static_assert(O_PFL + 32768 <= O_CS, "ws");
constexpr unsigned O_ST = O_CS + F * 12;
constexpr unsigned O_HS = O_ST + 262144;
constexpr unsigned O_LS = O_HS + 16384;
constexpr unsigned O_WI = O_LS + 16 * 16384;
constexpr unsigned O_WO = O_WI + D * F * 2;
constexpr unsigned O_HF = O_WO + FH * D * 2;
constexpr unsigned O_ZF = O_HF + L * D * 2;
constexpr unsigned O_PF = O_ZF + L * FH * 2;
constexpr unsigned WS_USED = O_PF + L * FH * 2;
constexpr size_t O_STAMP = 128u << 20;
#define STAMP(k,i) __builtin_amdgcn_sched_barrier(0)
#define STAMPW(k,i,W) __builtin_amdgcn_sched_barrier(0)
#define RSTAMP(k,i)
constexpr unsigned SENT = 0xFFFFFFFFu;

DEVI unsigned pk_bf16(float a, float b) { f32x2 v = {a, b}; return __builtin_bit_cast(unsigned, __builtin_convertvector(v, bf16x2)); }
DEVI unsigned pk_f16(float a, float b) { f32x2 v = {a, b}; return __builtin_bit_cast(unsigned, __builtin_convertvector(v, f16x2)); }
DEVI float bf_lo(unsigned u) { return __builtin_bit_cast(float, u << 16); }
DEVI float bf_hi(unsigned u) { return __builtin_bit_cast(float, u & 0xFFFF0000u); }
DEVI float asf(unsigned u) { return __builtin_bit_cast(float, u); }
DEVI unsigned asu(float f) { return __builtin_bit_cast(unsigned, f); }
DEVI float rbf(float a) { return bf_lo(pk_bf16(a, a)); }
DEVI float h_lo(unsigned u) { return (float)__builtin_bit_cast(f16x2, u)[0]; }
DEVI float h_hi(unsigned u) { return (float)__builtin_bit_cast(f16x2, u)[1]; }
template <int CTRL> DEVI float dpp_add(float v) {
  return v + __builtin_bit_cast(float, __builtin_amdgcn_update_dpp(0, __builtin_bit_cast(int, v), CTRL, 0xF, 0xF, true));
}
DEVI float row16_sum(float v) { v = dpp_add<0x128>(v); v = dpp_add<0x124>(v); v = dpp_add<0x122>(v); v = dpp_add<0x121>(v); return v; }
DEVI float half_sum(float v) { auto r = __builtin_amdgcn_permlane32_swap(asu(v), asu(v), false, false); return asf(r[0]) + asf(r[1]); }
DEVI float q_sum(float v) { auto r = __builtin_amdgcn_permlane16_swap(asu(v), asu(v), false, false); return half_sum(asf(r[0]) + asf(r[1])); }
DEVI unsigned umax4(u32x4 v) { return max(max(v[0], v[1]), max(v[2], v[3])); }
DEVI u32x2 tr_read(const char* p) {
  return __builtin_bit_cast(u32x2, __builtin_amdgcn_ds_read_tr16_b64_v4i16((LDSP s16x4*)p));
}
DEVI f32x4 mfma_bf(u32x4 a, u32x4 b, f32x4 c) {
  return __builtin_amdgcn_mfma_f32_16x16x32_bf16(__builtin_bit_cast(bf16x8, a), __builtin_bit_cast(bf16x8, b), c, 0, 0, 0);
}
DEVI f32x4 mfma_h(u32x4 a, u32x4 b, f32x4 c) {
  return __builtin_amdgcn_mfma_f32_16x16x32_f16(__builtin_bit_cast(f16x8, a), __builtin_bit_cast(f16x8, b), c, 0, 0, 0);
}
DEVI void ln_scalars(float mean, float var, float& mb, float& rinv) {
  mb = rbf(mean);
  float d = rbf(__builtin_amdgcn_sqrtf(rbf(rbf(var) + EPS)));
  rinv = __builtin_amdgcn_rcpf(d);
}
DEVI float gelu(float x) {
  float w = x * __builtin_fmaf(x * x, -0.10294324f, -2.3022082f);
  return x * __builtin_amdgcn_rcpf(1.0f + __builtin_amdgcn_exp2f(w));
}

typedef unsigned long long u64;
typedef __amdgpu_buffer_rsrc_t rsrc_t;
constexpr int SPIN = 1 << 16;
constexpr int CODE_LINES = 38;
template <int CTRL> DEVI float dpp_get(float v) {
  return __builtin_bit_cast(float, __builtin_amdgcn_update_dpp(0, __builtin_bit_cast(int, v), CTRL, 0xF, 0xF, true));
}
constexpr float GC1 = -0.10294324f, GC0 = -2.3022082f;
DEVI f32x2 gelu2(f32x2 x, f32x2 k1, f32x2 k0) {
  f32x2 w = x * __builtin_elementwise_fma(x * x, k1, k0);
  f32x2 d = f32x2{__builtin_amdgcn_exp2f(w[0]), __builtin_amdgcn_exp2f(w[1])} + f32x2{1.f, 1.f};
  return x * f32x2{__builtin_amdgcn_rcpf(d[0]), __builtin_amdgcn_rcpf(d[1])};
}
DEVI unsigned pk2(f32x2 v) { return __builtin_bit_cast(unsigned, __builtin_convertvector(v, bf16x2)); }
DEVI f32x2 up2(unsigned u) { return f32x2{bf_lo(u), bf_hi(u)}; }
DEVI f32x2 lo2(f32x4 v) { return __builtin_shufflevector(v, v, 0, 1); }
DEVI f32x2 hi2(f32x4 v) { return __builtin_shufflevector(v, v, 2, 3); }
DEVI float dot2(unsigned a, unsigned b, float c) {
  return __builtin_amdgcn_fdot2_f32_bf16(__builtin_bit_cast(bf16x2, a), __builtin_bit_cast(bf16x2, b), c, false);
}
DEVI void st16(rsrc_t r, unsigned voff, unsigned soff, u32x4 v) { __builtin_amdgcn_raw_buffer_store_b128(v, r, voff + soff, 0, 16); }
DEVI void st8(rsrc_t r, unsigned voff, unsigned soff, u32x2 v) { __builtin_amdgcn_raw_buffer_store_b64(v, r, voff, soff, 16); }
DEVI void st4(rsrc_t r, unsigned voff, unsigned soff, unsigned v) { __builtin_amdgcn_raw_buffer_store_b32(v, r, voff, soff, 16); }
DEVI u32x4 ld16(rsrc_t r, unsigned voff, unsigned soff) { return __builtin_amdgcn_raw_buffer_load_b128(r, voff, soff, 16); }
DEVI u32x2 ld8(rsrc_t r, unsigned voff, unsigned soff) { return __builtin_amdgcn_raw_buffer_load_b64(r, voff, soff, 16); }
template <int N> DEVI void wait_vm() { asm volatile("s_waitcnt vmcnt(%0)" ::"i"(N) : "memory"); }
DEVI void bar() { __builtin_amdgcn_sched_barrier(0); asm volatile("s_waitcnt lgkmcnt(0)\n\ts_barrier" ::: "memory"); __builtin_amdgcn_sched_barrier(0); }
DEVI int fresh_lane() { int l; asm volatile("v_mbcnt_lo_u32_b32 %0, -1, 0\n\tv_mbcnt_hi_u32_b32 %0, -1, %0" : "=v"(l)); return l; }
DEVI unsigned lds_addr(const char* p) { return (unsigned)(uintptr_t)(LDSP const char*)p; }
template <int NL> DEVI void wait_flags(rsrc_t r, unsigned off, int n, u32x2 tag, int lane) {
#pragma unroll 1
  for (int spin = 0; spin < SPIN; ++spin) {
    bool ok = true;
#pragma unroll
    for (int k = 0; k < NL; ++k) {
      int i = min(lane + 64 * k, n / 2 - 1);
      u32x4 v = ld16(r, i * 16, off);
      ok = ok && v[0] == tag[0] && v[1] == tag[1] && v[2] == tag[0] && v[3] == tag[1];
    }
    if (__all(ok)) break;
    __builtin_amdgcn_s_sleep(PSLEEP);
    asm volatile("" ::: "memory");
  }
  asm volatile("" ::: "memory");
}
template <bool SC> DEVI void glds4(unsigned m0v, const char* base, unsigned v0, unsigned v1, unsigned v2, unsigned v3) {
  unsigned keep;
  if (SC)
    asm volatile("s_mov_b32 %0, m0\n\ts_mov_b32 m0, %1\n\ts_nop 2\n\t"
                 "global_load_lds_dwordx4 %3, %2 sc1\n\tglobal_load_lds_dwordx4 %4, %2 offset:1024 sc1\n\t"
                 "global_load_lds_dwordx4 %5, %2 offset:2048 sc1\n\tglobal_load_lds_dwordx4 %6, %2 offset:3072 sc1\n\ts_mov_b32 m0, %0"
                 : "=&s"(keep) : "s"(m0v), "s"(base), "v"(v0), "v"(v1), "v"(v2), "v"(v3) : "memory");
  else
    asm volatile("s_mov_b32 %0, m0\n\ts_mov_b32 m0, %1\n\ts_nop 2\n\t"
                 "global_load_lds_dwordx4 %3, %2\n\tglobal_load_lds_dwordx4 %4, %2 offset:1024\n\t"
                 "global_load_lds_dwordx4 %5, %2 offset:2048\n\tglobal_load_lds_dwordx4 %6, %2 offset:3072\n\ts_mov_b32 m0, %0"
                 : "=&s"(keep) : "s"(m0v), "s"(base), "v"(v0), "v"(v1), "v"(v2), "v"(v3) : "memory");
}
DEVI void glds1(unsigned m0v, const char* base, unsigned v0) {
  unsigned keep;
  asm volatile("s_mov_b32 %0, m0\n\ts_mov_b32 m0, %1\n\ts_nop 2\n\tglobal_load_lds_dwordx4 %3, %2 sc1\n\ts_mov_b32 m0, %0"
               : "=&s"(keep) : "s"(m0v), "s"(base), "v"(v0) : "memory");
}
template <int NP> DEVI void ld_lin(unsigned dst, const char* src, unsigned l16) {
  glds4<true>(dst, src, l16, l16, l16, l16);
  glds4<true>(dst + 4096, src + 4096, l16, l16, l16, l16);
  if (NP == 9) glds1(dst + 8192, src + 8192, l16);
}

constexpr int TP = 1168;
constexpr int CV_K = 30208, CV_C = 4160, CV_X = 17408;
constexpr int C_U = 4 * CV_K, C_KD = C_U + 16 * TP, KDS = 1664, FT = 520, C_END = C_KD + 4 * KDS;
constexpr int KT = 528;
constexpr int STG = 34816, RING = 4 * STG;
constexpr int X_CS = RING, X_LN = X_CS + 1152, X_PV = X_LN + 1024, X_HS = X_PV + 16384, X_CNT = 158976;
constexpr int LDS_BYTES = X_CNT + 64 + 256;
static_assert(C_END <= X_CNT && (15 + 128) * 128 <= 16 * TP && 57 * KT <= CV_K && X_HS + 1024 <= X_CNT && LDS_BYTES <= 163840, "lds");
#define TPOLL 12
#define WDELAY 5
#define ROT3 0
#define GDELAY 0
#define GAUX 16
#define ALIGNL asm volatile(".p2alignl 6, 0xBF800000")
#define TCHK 13
#define TFLAG 16

struct Fr { u32x4 m[2], n[9]; };
struct Ctx { const char* sm; int am0[4], am1, an; };
template <int MK> DEVI void ldf(Fr& f, const Ctx& c, int t, int kk) {
  int so = (t & 3) * STG;
  asm volatile("" : "+s"(so));
  const char* slot = c.sm + so;
  if (MK == 0) {
#pragma unroll
    for (int m = 0; m < 2; ++m) {
      const char* p = slot + c.am0[0] + kk * 8192 + m * 1024;
      u32x2 a = *(volatile const LDSP u32x2*)p, b = *(volatile const LDSP u32x2*)(p + 512);
      f.m[m] = u32x4{a[0], a[1], b[0], b[1]};
    }
  } else {
#pragma unroll
    for (int m = 0; m < 2; ++m) f.m[m] = *(const u32x4*)(slot + (c.am1 ^ (kk * 64)) + m * 2048);
  }
#pragma unroll
  for (int n = 0; n < 9; ++n) f.n[n] = *(const u32x4*)(slot + c.an + (kk * 9 + n) * 1024);
}
template <int MK, int ORI, bool LD, bool FIRST = false> DEVI void seg(f32x4 (&acc)[2][9], const Fr& cur, Fr& nxt, const Ctx& c, int t, int kk) {
  __builtin_amdgcn_s_waitcnt(0xC07F);
  __builtin_amdgcn_sched_barrier(0);
  if (LD) ldf<MK>(nxt, c, t, kk);
#pragma unroll
  for (int m = 0; m < 2; ++m)
#pragma unroll
    for (int nn = 0; nn < 9; ++nn) {
      const int n = m ? 8 - nn : nn;
      const f32x4 ci = FIRST ? f32x4{0, 0, 0, 0} : acc[m][n];
      acc[m][n] = ORI == 0 ? mfma_bf(cur.m[m], cur.n[n], ci) : mfma_bf(cur.n[n], cur.m[m], ci);
    }
#pragma unroll
  for (int i = 0; i < (MK == 0 ? 13 : 11); ++i) { __builtin_amdgcn_sched_group_barrier(0x008, 1, 0); __builtin_amdgcn_sched_group_barrier(0x100, 1, 0); }
  __builtin_amdgcn_sched_barrier(0);
}
DEVI f32x2 cmul(f32x2 a, f32x2 b, f32x2 bp) {
  return __builtin_elementwise_fma(f32x2{a[1], a[1]}, bp, f32x2{a[0], a[0]} * b);
}
DEVI f32x2 perp(f32x2 b) { return f32x2{-b[1], b[0]}; }
DEVI f32x2 cexp(float ar, float ai, float l) {
  float m = __builtin_amdgcn_exp2f(ar * l), ph = __builtin_amdgcn_fractf(ai * l);
  return f32x2{m * __builtin_amdgcn_cosf(ph), m * __builtin_amdgcn_sinf(ph)};
}
DEVI unsigned pkh(f32x2 v) { return __builtin_bit_cast(unsigned, __builtin_convertvector(v, f16x2)); }
DEVI float ldbf(const char* p, int i) { return bf_lo(*(const unsigned short*)(p + 2 * i)); }

__global__ __launch_bounds__(512, 2) void k_all(const char* __restrict__ x, const char* __restrict__ a_re, const char* __restrict__ a_im,
    const char* __restrict__ b_re, const char* __restrict__ b_im, const char* __restrict__ c_re, const char* __restrict__ c_im,
    const char* __restrict__ log_dt, const char* __restrict__ dskip, const char* __restrict__ w_in, const char* __restrict__ w_sp,
    const char* __restrict__ b_sp, const char* __restrict__ w_out, char* __restrict__ out, char* __restrict__ ws) {
  extern __shared__ __attribute__((aligned(16))) char sm[];
  const int tid = threadIdx.x, lane = tid & 63, w = __builtin_amdgcn_readfirstlane(tid >> 6), li = lane & 15, q = lane >> 4;
  const int bid = blockIdx.x, xc = bid & 7, ii = bid >> 3;
  if (w == 7) {
    unsigned long long pc;
    asm volatile("s_getpc_b64 %0" : "=s"(pc));
    const char* cp = (const char*)(pc & ~255ull) + (ii & 7) * (CODE_LINES * 128);
    unsigned keep;
    asm volatile("s_mov_b32 %0, m0\n\ts_mov_b32 m0, %1\n\ts_nop 2\n\tglobal_load_lds_dword %3, %2\n\ts_mov_b32 m0, %0"
                 : "=&s"(keep) : "s"(lds_addr(sm) + X_CNT + 64), "s"(cp), "v"(min(lane, CODE_LINES - 1) * 128) : "memory");
  }
  if (tid == 256) *(u32x4*)(sm + X_CNT) = u32x4{0, 0, 0, 0};
  asm volatile("" ::"s"(x), "s"(a_re), "s"(a_im), "s"(b_re), "s"(b_im), "s"(c_re), "s"(c_im), "s"(log_dt), "s"(dskip), "s"(w_in), "s"(w_out), "s"(ws));
  const rsrc_t R = __builtin_amdgcn_make_buffer_rsrc(ws, 0, WS_USED, 0x00020000);
  STAMP(0,0);
  const u32x2 ep = ld8(R, 0, O_EP);
  const bool is_conv = ii < 18;
  const int cg = xc * 18 + ii, rep = bid & (NREP - 1);
  auto mk_tag = [&]() {
    u64 e = (((u64)__builtin_amdgcn_readfirstlane(ep[1]) << 32) | (unsigned)__builtin_amdgcn_readfirstlane(ep[0])) + 0x9E3779B97F4A7C15ull;
    return u32x2{(unsigned)e, (unsigned)(e >> 32)};
  };
  u32x2 tag;
  auto arrive = [&](int slot, unsigned n, unsigned flag_off, unsigned stride, int nrep) {
    wait_vm<0>();
    const int lane = fresh_lane();
    unsigned old = 0;
    if (lane == 0) old = __hip_atomic_fetch_add((unsigned*)(sm + X_CNT) + slot, 1u, __ATOMIC_RELAXED, __HIP_MEMORY_SCOPE_WORKGROUP);
    if (__builtin_amdgcn_readfirstlane(old) == n - 1 && lane < nrep) {
#pragma unroll
      for (int k = 0; k < (nrep + 63) / 64; ++k) st8(R, (lane + 64 * k) * stride, flag_off, tag);
    }
  };
  if (is_conv) {
    const int ch = w & 3, dch = 4 * cg + ch;
    char* sk = sm + ch * CV_K;
    float sc = 0.f;
    u32x2 xc8[8];
    struct CF { u32x4 a[4], b[8]; } A, B;
    u32x4 tf[4], b9, b7, a32;
    const int sg = 7 - (li & 7);
    const char* pk = sk + (sg & 3) * CV_C + (253 - (li >> 3) + q) * 16 + (sg >> 2) * 8 - 2048;
    auto lda = [&](int ap) {
      const char* p = pk + 2048 - 64 * ap;
      const u32x2 x0 = *(volatile const LDSP u32x2*)p, x1 = *(volatile const LDSP u32x2*)(p + 8);
      return u32x4{x0[0], x0[1], x1[0], x1[1]};
    };
    if (w < 4) {
      const int pi = (dch * NM + 2 * lane) * 2;
      const unsigned v_ar = *(const unsigned*)(a_re + pi), v_ai = *(const unsigned*)(a_im + pi), v_br = *(const unsigned*)(b_re + pi),
                     v_bi = *(const unsigned*)(b_im + pi), v_cr = *(const unsigned*)(c_re + pi), v_ci = *(const unsigned*)(c_im + pi);
      const unsigned v_dk = *(const unsigned short*)(dskip + 2 * dch), v_dt = *(const unsigned short*)(log_dt + 2 * dch);
      bar();
      const float p_dk = bf_lo(v_dk), dt = rbf(__expf(bf_lo(v_dt)));
      tag = mk_tag();
      STAMP(0,1);
      const f32x2 ar = (up2(v_ar) * dt) * 1.44269504f, ai = (up2(v_ai) * dt) * 0.15915494f;
      const f32x2 br = up2(v_br) * dt, bi = up2(v_bi) * dt, cr = up2(v_cr), ci = up2(v_ci);
      const f32x2 cbr = cr * br - ci * bi, cbi = -(cr * bi + ci * br);
      float mx = fmaxf(fmaxf(fabsf(cbr[0]), fabsf(cbi[0])), fmaxf(fabsf(cbr[1]), fabsf(cbi[1])));
      mx = fmaxf(mx, dpp_get<0x128>(mx)); mx = fmaxf(mx, dpp_get<0x124>(mx)); mx = fmaxf(mx, dpp_get<0x122>(mx)); mx = fmaxf(mx, dpp_get<0x121>(mx));
      auto rl = [&](int l) { return asf(__builtin_amdgcn_readlane((int)asu(mx), l)); };
      sc = fmaxf(fmaxf(rl(0), rl(16)), fmaxf(rl(32), rl(48)));
      sc = fmaxf(sc, fabsf(p_dk) * (1.0f / 256));
      const float isc = sc > 0.f ? 1.0f / sc : 0.f;
      const char* pa = sk + li * KT + q * 16;
      u32x4 fr[8][4];
      {
        struct C2 { f32x2 a, b; };
        char* tb = sk + lane * 8;
        auto put = [&](int row, C2 v) { *(u32x2*)(tb + row * KT) = u32x2{pkh(v.a), pkh(v.b)}; };
        auto mul = [&](C2 v, C2 u, C2 up) { return C2{cmul(v.a, u.a, up.a), cmul(v.b, u.b, up.b)}; };
        auto prp = [&](C2 v) { return C2{perp(v.a), perp(v.b)}; };
        auto ex = [&](float l) { return C2{cexp(ar[0], ai[0], l), cexp(ar[1], ai[1], l)}; };
        const C2 w1 = ex(1.f), w128 = ex(128.f);
        C2 w8 = ex(8.f); w8.a[1] = -w8.a[1]; w8.b[1] = -w8.b[1];
        const C2 w1p = prp(w1), w8p = prp(w8), w128p = prp(w128);
        C2 w64 = ex(64.f); w64.a[1] = -w64.a[1]; w64.b[1] = -w64.b[1];
        const C2 w1k = ex(1024.f), w64p = prp(w64), w1kp = prp(w1k);
        C2 y = {f32x2{cbr[0], cbi[0]} * isc, f32x2{cbr[1], cbi[1]} * isc};
        C2 E[8]; E[0] = C2{f32x2{1.f, 0.f}, f32x2{1.f, 0.f}}; E[1] = w1;
        put(0, E[0]); put(8, w128);
#pragma unroll
        for (int e = 0; e < 8; ++e) {
          if (e > 1) E[e] = mul(E[e - 1], w1, w1p);
          if (e) { put(e, E[e]); put(8 + e, mul(E[e], w128, w128p)); }
          put(41 + e, y); put(49 + e, mul(y, w64, w64p));
          if (e < 7) y = mul(y, w8, w8p);
        }
        asm volatile("" ::: "memory");
#pragma unroll
        for (int s = 0; s < 8; ++s) { fr[s][0] = *(const u32x4*)(pa + s * 64); fr[s][3] = *(const u32x4*)(pa + 41 * KT + s * 64); }
        asm volatile("" ::: "memory");
        const C2 e2p = prp(E[2]), e4p = prp(E[4]), e6p = prp(E[6]);
        C2 H = w128;
        put(32, w1k); put(40, mul(w1k, w1k, w1kp));
#pragma unroll
        for (int a = 1; a < 8; ++a) {
          if (a >= 2) H = mul(H, w128, w128p);
          put(32 + a, mul(H, w1k, w1kp));
          if (a >= 4) { put(16 + 2 * a, H); put(17 + 2 * a, mul(H, E[4], e4p)); }
          else if (a >= 2) { put(8 + 4 * a, H); put(9 + 4 * a, mul(H, E[2], e2p)); put(10 + 4 * a, mul(H, E[4], e4p)); put(11 + 4 * a, mul(H, E[6], e6p)); }
        }
      }
      asm volatile("" ::: "memory");
      STAMP(0,2);
      {
        f32x4 kk[3] = {};
#pragma unroll
        for (int s = 0; s < 8; ++s) { fr[s][1] = *(const u32x4*)(pa + 16 * KT + s * 64); fr[s][2] = *(const u32x4*)(pa + 32 * KT + s * 64); }
        __builtin_amdgcn_sched_barrier(0);
#pragma unroll
        for (int s = 0; s < 8; ++s) kk[0] = mfma_h(fr[s][0], fr[s][3], kk[0]);
#pragma unroll
        for (int s = 0; s < 8; ++s) { kk[1] = mfma_h(fr[s][1], fr[s][3], kk[1]); kk[2] = mfma_h(fr[s][2], fr[s][3], kk[2]); }
        if (lane == 0) kk[0][0] += p_dk * isc;
        char* kd = sm + C_KD + ch * KDS;
        *(u32x2*)(kd + (128 * (q >> 1) + 4 * (q & 1) + 8 * li) * 2) = u32x2{pkh(lo2(kk[0])), pkh(hi2(kk[0]))};
        const int i1 = q < 2 ? 256 + 64 * q + 4 * li : 384 + 64 * (q - 2) + 2 * li;
        *(unsigned*)(kd + i1 * 2) = pkh(lo2(kk[1])); *(unsigned*)(kd + (i1 + (q < 2 ? 2 : 32)) * 2) = pkh(hi2(kk[1]));
        if (lane < 4) *(u32x4*)(kd + FT * 2 + lane * 16) = u32x4{0, 0, 0, 0};
#pragma unroll
        for (int r = 0; r < 4; ++r) *(_Float16*)(kd + (r == 0 && lane == 0 ? 512 : FT + 31 + 16 * (4 * q + r) + li) * 2) = (_Float16)kk[2][r];
      }
      asm volatile("" ::: "memory");
      {
        const unsigned short* kd = (const unsigned short*)(sm + C_KD + ch * KDS);
        u32x4 c0[3];
        unsigned ab, cc, e, k0 = kd[512]; u32x4 v; u32x2 u;
        { const int k = 127 - lane; ab = *(const unsigned*)(kd + 256 + 2 * k); cc = kd[258 + 2 * k]; }
        { const int k = 63 - lane; v = *(const u32x4*)(kd + 8 * (k & 31)); u = *(const u32x2*)(kd + 128 + 4 * k); e = kd[132 + 4 * k]; }
        asm volatile("" : "+v"(v), "+v"(u), "+v"(e), "+v"(ab), "+v"(cc), "+v"(k0));
        k0 = lane == 63 ? k0 | (k0 << 16) : 0u;
        c0[0] = u32x4{0, k0 & 0xFFFF0000u, k0, k0};
        c0[1] = u32x4{cc | (cc << 16), __builtin_amdgcn_perm(ab, ab, 0x03020302), __builtin_amdgcn_perm(ab, ab, 0x01000302), __builtin_amdgcn_perm(ab, ab, 0x01000100)};
        c0[2] = lane < 32 ? u32x4{__builtin_amdgcn_perm(u[1], e, 0x07060100), __builtin_amdgcn_perm(u[1], u[1], 0x01000100), __builtin_amdgcn_perm(u[0], u[1], 0x07060100), __builtin_amdgcn_perm(u[0], u[0], 0x01000100)}
                          : u32x4{__builtin_amdgcn_alignbit(v[3], v[3], 16), __builtin_amdgcn_alignbit(v[2], v[2], 16), __builtin_amdgcn_alignbit(v[1], v[1], 16), __builtin_amdgcn_alignbit(v[0], v[0], 16)};
        if (lane < 8) *(u32x4*)(sk + (lane >> 1) * CV_C + 4096 + (lane & 1) * 16) = u32x4{0, 0, 0, 0};
        STAMP(0,3);
#pragma unroll
        for (int rr = 0; rr < 3; ++rr) {
          char* p = sk + (64 * rr + 64 + lane) * 16;
          unsigned f[6] = {c0[rr][0], c0[rr][1], c0[rr][2], c0[rr][3], 0, 0}, o[5];
#pragma unroll
          for (int i = 0; i < 2; ++i)
            f[4 + i] = __builtin_amdgcn_update_dpp(rr < 2 ? __builtin_amdgcn_readfirstlane(c0[rr < 2 ? rr + 1 : rr][i]) : 0u, c0[rr][i], 0x130, 0xF, 0xF, false);
#pragma unroll
          for (int i = 0; i < 5; ++i) o[i] = __builtin_amdgcn_alignbit(f[i + 1], f[i], 16);
          if (rr || lane >= 56) {
            *(u32x4*)p = c0[rr];
            *(u32x4*)(p + CV_C) = u32x4{o[0], o[1], o[2], o[3]};
            *(u32x4*)(p + 2 * CV_C) = u32x4{f[1], f[2], f[3], f[4]};
            *(u32x4*)(p + 3 * CV_C) = u32x4{o[1], o[2], o[3], o[4]};
          }
        }
      }
      asm volatile("" ::: "memory");
      {
        const char* pt = sm + C_KD + ch * KDS + (FT + 26 + 2 * li - 8 * q) * 2;
        a32 = lda(32);
#pragma unroll
        for (int k = 0; k < 4; ++k) {
          A.a[k] = lda(8 * k);
#pragma unroll
          for (int e = 0; e < 4; ++e) tf[k][e] = *(const unsigned*)(pt + k * 64 + e * 4);
        }
#pragma unroll
        for (int e = 0; e < 4; ++e) {
          const int d0 = 8 * q + 2 * e - 8 - li, d1 = d0 + 1;
          b9[e] = (abs(d0) <= 4 ? 0x3C00u : 0u) | (abs(d1) <= 4 ? 0x3C000000u : 0u);
          b7[e] = (abs(d0) <= 3 ? 0x3C00u : 0u) | (abs(d1) <= 3 ? 0x3C000000u : 0u);
        }
        asm volatile("" : "+v"(b9), "+v"(b7));
      }
      STAMP(0,4);
    } else {
      const int t8 = tid - 256;
      const rsrc_t RX = __builtin_amdgcn_make_buffer_rsrc((void*)x, 0, L * D * 2, 0x00020000);
      bar();
      __builtin_amdgcn_s_sleep(GDELAY);
#pragma unroll
      for (int k = 0; k < 8; ++k) xc8[k] = __builtin_amdgcn_raw_buffer_load_b64(RX, t8 * 9216 + k * 1152, cg * 8, GAUX);
      tag = mk_tag();
      if (cg < 136 && w >= 6) st16(R, (tid - 384) * 16, O_ST + cg * 2048, u32x4{SENT, SENT, SENT, SENT});
      if (t8 < 120) *(u32x4*)(sm + C_U + t8 * 16) = u32x4{0, 0, 0, 0};
      u32x4 g[4];
#pragma unroll 1
      for (int spin = 0; spin < SPIN; ++spin) {
        bool ok = true;
#pragma unroll
        for (int k = 0; k < 4; ++k) { g[k] = ld16(R, t8 * 64 + k * 16, O_LS + (bid & 15) * 16384); ok = ok && g[k][1] == tag[0] && g[k][3] == tag[1]; }
        if (__all(ok)) break;
        asm volatile("" ::: "memory");
      }
      STAMPW(5,0,4);
      __builtin_amdgcn_s_setprio(2);
      u32x4 uo[4];
#pragma unroll
      for (int k = 0; k < 4; ++k) {
        const f32x2 r2 = {__builtin_amdgcn_rcpf(bf_hi(g[k][0])), __builtin_amdgcn_rcpf(bf_hi(g[k][2]))}, b2 = -(f32x2{bf_lo(g[k][0]), bf_lo(g[k][2])} * r2);
#pragma unroll
        for (int c = 0; c < 4; ++c) {
          const unsigned a = xc8[2 * k][c >> 1], b = xc8[2 * k + 1][c >> 1];
          uo[c][k] = pkh(__builtin_elementwise_fma((c & 1) ? f32x2{bf_hi(a), bf_hi(b)} : f32x2{bf_lo(a), bf_lo(b)}, r2, b2));
        }
      }
      const int r = 15 + (t8 >> 1);
#pragma unroll
      for (int c = 0; c < 4; ++c) *(u32x4*)(sm + C_U + r * 128 + (((2 * c + (t8 & 1)) ^ ((r >> 1) & 7)) * 16)) = uo[c];
      __builtin_amdgcn_s_setprio(0);
      STAMPW(1,0,4);
    }
    bar();
    STAMP(0,5);
    if (w < 4) {
      f32x4 acc[8];
      auto ub = [&](int b) { const int r = b >> 1; return sm + C_U + r * 128 + (((2 * ch + (b & 1)) ^ ((r >> 1) & 7)) * 16); };
      auto ldc = [&](CF& f, int a0) {
        __builtin_amdgcn_s_waitcnt(0xC07F);
        __builtin_amdgcn_sched_barrier(0);
        const char* pb = ub(28 - 4 * a0 + 2 * li + q);
#pragma unroll
        for (int m = 0; m < 8; ++m) { if (m < 4) f.a[m] = lda(a0 + 8 * m); f.b[m] = *(const u32x4*)(pb + 2048 * m); }
      };
      auto mmc = [&](const CF& f) {
#pragma unroll
        for (int m = 0; m < 4; ++m)
#pragma unroll
          for (int t = m; t < 8; ++t) acc[t] = mfma_h(f.a[m], f.b[t - m], acc[t]);
#pragma unroll
        for (int i = 0; i < 16; ++i) { __builtin_amdgcn_sched_group_barrier(0x008, 1, 0); __builtin_amdgcn_sched_group_barrier(0x100, 1, 0); }
        __builtin_amdgcn_sched_barrier(0);
      };
      f32x4 s9[4], s7[4];
      {
        const char* pb = ub(28 + 2 * li + q); const char* pe = ub(27 + 2 * li + q);
        u32x4 ue[4];
#pragma unroll
        for (int k = 0; k < 4; ++k) { A.b[k] = *(const u32x4*)(pb + 2048 * k); ue[k] = *(const u32x4*)(pe + 2048 * k); }
#pragma unroll
        for (int k = 4; k < 8; ++k) A.b[k] = *(const u32x4*)(pb + 2048 * k);
        __builtin_amdgcn_sched_barrier(0);
        const f32x4 z4 = {0, 0, 0, 0};
#pragma unroll
        for (int k = 0; k < 4; ++k) { s7[k] = mfma_h(A.b[k], b7, z4); acc[4 + k] = mfma_h(a32, A.b[k], z4); s9[k] = mfma_h(ue[k], b9, z4); acc[k] = z4; }
        __builtin_amdgcn_sched_barrier(0);
      }
#pragma unroll
      for (int a0 = 0; a0 < 8; a0 += 2) {
        ldc(B, a0 + 1); mmc(A);
        if (a0 == 0) {
          u32x4 g[4];
#pragma unroll
          for (int k = 0; k < 4; ++k)
#pragma unroll
            for (int e = 0; e < 4; ++e) g[k][e] = pkh(f32x2{s7[k][3 - e], s9[k][3 - e]});
#pragma unroll
          for (int k = 0; k < 4; ++k)
#pragma unroll
            for (int t = k; t < 4; ++t) acc[4 + t] = mfma_h(g[k], tf[t - k], acc[4 + t]);
          __builtin_amdgcn_sched_barrier(0);
        }
        if (a0 < 6) ldc(A, a0 + 2);
        mmc(B);
      }
      STAMP(0,6);
#pragma unroll
      for (int t = 0; t < 8; ++t) *(u32x2*)(sk + t * 512 + li * 32 + q * 8) = u32x2{pk2(lo2(acc[t]) * sc), pk2(hi2(acc[t]) * sc)};
    } else {
#pragma unroll
      for (int k = 0; k < 4; ++k) *(u32x4*)(sk + CV_X + lane * 64 + k * 16) = u32x4{xc8[2 * k][0], xc8[2 * k][1], xc8[2 * k + 1][0], xc8[2 * k + 1][1]};
    }
    bar();
#pragma unroll
    for (int r = 0; r < 2; ++r) {
      const int T = (tid >> 3) + 64 * r, ip = tid & 7;
      const int t0 = 32 * (T >> 1) + 8 * (ip >> 1) + 4 * (T & 1) + 2 * (ip & 1);
      const char* p = sm + t0 * 2;
      const u32x4 xv = *(const u32x4*)(sm + (t0 >> 9) * CV_K + CV_X + (t0 & 511) * 8);
      const unsigned c0 = *(const unsigned*)p, c1 = *(const unsigned*)(p + CV_K), c2 = *(const unsigned*)(p + 2 * CV_K), c3 = *(const unsigned*)(p + 3 * CV_K);
      u32x4 v = {pk2(up2(xv[0]) + f32x2{bf_lo(c0), bf_lo(c1)}), pk2(up2(xv[1]) + f32x2{bf_lo(c2), bf_lo(c3)}),
                 pk2(up2(xv[2]) + f32x2{bf_hi(c0), bf_hi(c1)}), pk2(up2(xv[3]) + f32x2{bf_hi(c2), bf_hi(c3)})};
      st16(R, (T >> 3) * (18 * 8192) + (T & 7) * 1024 + ip * 16, O_HF + (cg >> 3) * 8192 + (cg & 1) * 512 + ((cg >> 1) & 3) * 128, v);
    }
    STAMPW(2,1,4);
    arrive(1, 8, O_F2 + cg * 8, F2S, NREP);
    STAMPW(2,2,4);
  } else {
    const int tj = (ii - 18) * 8 + xc;
    const int kc = tj / 36, cb = tj % 36;
    if (tj < 64) {
      const int row = 32 * tj + 4 * w + q;
      const char* src = x + (size_t)row * 1152 + li * 8;
      u32x2 xr[9];
#pragma unroll
      for (int k = 0; k < 9; ++k) xr[k] = *(const u32x2*)(src + k * 128);
      tag = mk_tag();
      float a[4] = {0, 0, 0, 0};
#pragma unroll
      for (int k = 0; k < 9; ++k)
#pragma unroll
        for (int e = 0; e < 2; ++e) { a[e] = dot2(xr[k][e], 0x3F803F80u, a[e]); a[2 + e] = dot2(xr[k][e], xr[k][e], a[2 + e]); }
      const float mean = row16_sum(a[0] + a[1]) * (1.0f / D), var = fmaxf(row16_sum(a[2] + a[3]) * (1.0f / D) - mean * mean, 0.f);
      st8(R, li * 16384 + row * 8, O_LS, u32x2{pk_bf16(mean, __builtin_amdgcn_sqrtf(rbf(rbf(var) + EPS))), (q & 1) ? tag[1] : tag[0]});
      STAMP(1,1);
      for (int i = 0; i < WDELAY - 3; ++i) __builtin_amdgcn_s_sleep(16);
    } else
      for (int i = 0; i < WDELAY; ++i) __builtin_amdgcn_s_sleep(16);
    char* s0 = sm + 65536;
    const int G = max(tj - 40, 0), par = G & 1;
    {
      const int row = tid >> 4, uu = G / 9, idx = G % 9, hh = row >> 4;
      const int panel = idx < 4 ? 2 * uu : (idx < 8 ? 2 * uu + 1 : 2 * uu + hh), tile = idx < 8 ? 2 * (idx & 3) + hh : 8;
      const char* src = w_in + (size_t)(192 * min(kc, 2) + row) * 9216 + cb * 256 + li * 16;
      const char* wsrc = w_out + (size_t)(144 * panel + 16 * tile + (row & 15)) * 1152 + li * 8;
      u32x4 v[6]; u32x2 vo[9];
#pragma unroll
      for (int k = 0; k < 6; ++k) v[k] = *(const u32x4*)(src + (size_t)k * 32 * 9216);
      if (tj >= 40) {
#pragma unroll
        for (int k = 0; k < 9; ++k) vo[k] = *(const u32x2*)(wsrc + k * 128);
      }
#pragma unroll
      for (int k = 0; k < 6; ++k) *(u32x4*)(sm + (row + 32 * k) * 272 + li * 16) = v[k];
      if (tj >= 40) {
#pragma unroll
        for (int k = 0; k < 9; ++k) *(u32x2*)(s0 + row * TP + li * 8 + k * 128) = vo[k];
      }
    }
    tag = mk_tag();
    bar();
    STAMP(4,0);
    if (tj < 108) {
      const int ct = 8 * cb + w;
      const int hf = ct < 144 ? 1 : 0, cl = ct < 144 ? ct : ct - 144, fp = cl / 9, n = cl % 9;
      const char* trb = sm + (8 * q + (li >> 2)) * 272 + w * 32 + (li & 3) * 8;
      const unsigned dst = lane * 16 + O_WI + (((fp * 2 + hf) * 18 + 6 * kc) * 9 + n) * 1024;
      float s = 0.f;
#pragma unroll
      for (int g = 0; g < 6; ++g) {
        u32x2 lo = tr_read(trb + g * 32 * 272), hi = tr_read(trb + g * 32 * 272 + 4 * 272);
        st16(R, dst, g * 9216, u32x4{lo[0], lo[1], hi[0], hi[1]});
        s = dot2(lo[0], 0x3F803F80u, s); s = dot2(lo[1], 0x3F803F80u, s); s = dot2(hi[0], 0x3F803F80u, s); s = dot2(hi[1], 0x3F803F80u, s);
      }
      s = q_sum(s);
      if (q == 0) st4(R, li * 4, O_CS + kc * F * 4 + ct * 64, asu(s));
    }
    if (tj >= 40) {
      const bool mixed = G % 9 == 8;
      const char* trb = s0 + ((mixed ? 16 * (q >> 1) + 4 * (q & 1) : 4 * q) + (li >> 2)) * TP + (li & 3) * 8;
      const char* trh = trb + (mixed ? 8 : 16) * TP;
      const unsigned dst = lane * 16 + O_WO + (w * 36 + (G >> 1)) * 9216;
#pragma unroll
      for (int n = 0; n < 5; ++n) {
        const int c0 = 72 * w + 16 * n - (n == 4 ? 8 * par : 0);
        u32x2 lo = tr_read(trb + c0 * 2), hi = tr_read(trh + c0 * 2);
        if (n < 4) st16(R, dst, (4 * par + n) * 1024, u32x4{lo[0], lo[1], hi[0], hi[1]});
        else if ((li >> 3) == par) st16(R, dst, 8192, u32x4{lo[0], lo[1], hi[0], hi[1]});
      }
    }
    STAMP(4,1);
    arrive(0, 8, O_F2 + (144 + tj) * 8, F2S, NREP);
    STAMP(4,2);
  }
  const int tp = (xc & 1) * 8 + (ii >> 2), fp = (xc >> 1) * 4 + (ii & 3);
  const int t64 = 4 * xc + (ii >> 3), j3 = ii & 7;
  if (w == 0) { wait_flags<1>(R, O_F2 + rep * F2S + 144 * 8, 112, tag, lane); STAMPW(6,0,0); }
  bar();
  if (w >= 4) {
    const int lw = w - 4;
    const unsigned l16 = lane * 16, base = lds_addr(sm);
    const int ph = ((ii >> 2) + ii) & 3, ph3 = ROT3 ? ii & 3 : ((ii >> 3) + ii) & 3, rot3 = ROT3 ? (9 * (ii >> 3) + 1) >> 1 : 0;
    auto k9 = [&](int t) { return (t & 3) == ph ? (t + 4 < 9 ? t + 4 : ph) : t; };
    auto k32 = [&](int t) { return (t & 3) == ph ? ((t + 4) & 31) : t; };
    auto k18 = [&](int t) { const int k = ((t & 3) == ph3 ? (t + 4 < 18 ? t + 4 : ph3) : t) + rot3; return k >= 18 ? k - 18 : k; };
    if (lw < 2) {
      constexpr int NP = 8;
      unsigned vo[8];
#pragma unroll
      for (int j = 0; j < 8; ++j) {
        int t = 64 * lw + 32 * (j >> 2) + 4 * ((j >> 1) & 1) + 16 * (j & 1) + 8 * (lane >> 5) + ((lane >> 3) & 3);
        vo[j] = t * 4096 + (((lane & 7) ^ ((lane >> 3) & 7)) * 16) + 3072 - (j & 3) * 1024;
      }
      const char* wsrc = w_sp + (size_t)tp * 128 * 4096 - 3072;
      const char* hsrc = ws + O_HF + tp * 18 * 8192 + lw * 8192;
      const char* psrc = ws + O_PF + t64 * 72 * 4096 + lw * 8192;
      const unsigned dst = base + lw * 8192;
      if (lw == 0) wait_flags<2>(R, O_F2 + rep * F2S, 144, tag, lane);
      STAMPW(5,1,4);
      bar();
#pragma unroll 1
      for (int t = 0; t < 4; ++t) ld_lin<NP>(dst + t * STG, hsrc + k9(t) * 16384, l16);
      wait_vm<3 * NP>(); bar();
#pragma unroll 1
      for (int t = 4; t < 18; ++t) { wait_vm<2 * NP>(); bar(); ld_lin<NP>(dst + (t & 3) * STG, hsrc + k9(t < 9 ? t : t - 9) * 16384, l16); }
#pragma unroll 1
      for (int t = 18; t < 50; ++t) {
        wait_vm<2 * NP>(); bar();
        const unsigned dd = dst + (t & 3) * STG; const char* sp = wsrc + k32(t - 18) * 128;
        glds4<false>(dd, sp, vo[0], vo[1], vo[2], vo[3]); glds4<false>(dd + 4096, sp, vo[4], vo[5], vo[6], vo[7]);
      }
      wait_vm<2 * NP>(); bar(); wait_vm<NP>(); bar(); wait_vm<0>(); bar();
      bar(); bar();
      wait_flags<1>(R, O_PFL + (t64 >> 1) * 2048 + ((t64 & 1) * 8 + j3) * 128, 16, tag, lane);
      STAMPW(5,2,4);
#pragma unroll 1
      for (int t = 50; t < 68; ++t) {
        if (t >= 53) { wait_vm<2 * NP>(); bar(); }
        ld_lin<NP>(dst + (t & 3) * STG, psrc + k18(t - 50) * 16384, l16);
      }
      wait_vm<2 * NP>(); bar(); wait_vm<NP>(); bar(); wait_vm<0>(); bar();
    } else {
      constexpr int NP = 9;
      const char* wi = ws + O_WI + (fp * 2) * 18 * 9216 + (lw - 2) * 9216;
      const char* zs = ws + O_ZF + fp * 64 * 9216 + (lw - 2) * 9216;
      const char* wo = ws + O_WO + j3 * 36 * 9216 + (lw - 2) * 9216;
      const unsigned dst = base + 16384 + (lw - 2) * 9216;
#pragma unroll 1
      for (int t = 0; t < 4; ++t) ld_lin<NP>(dst + t * STG, wi + k9(t) * 18432, l16);
      bar();
      wait_vm<3 * NP>(); bar();
      if (lw == 3) {
        f32x2 a = *(const f32x2*)(sm + X_LN + lane * 8) + *(const f32x2*)(sm + X_LN + 512 + lane * 8);
        a[0] = q_sum(dpp_add<0x128>(a[0])); a[1] = q_sum(dpp_add<0x128>(a[1]));
        float mean = a[0] * (1.0f / D), mb, ri;
        ln_scalars(mean, fmaxf(a[1] * (1.0f / D) - mean * mean, 0.f), mb, ri);
        if (lane < 8) st8(R, lane * 8, O_HS + tp * 1024 + fp * 64, u32x2{asu(ri) == SENT ? 0x7FC00000u : asu(ri), asu(-mb)});
      }
#pragma unroll 1
      for (int t = 4; t < 18; ++t) { wait_vm<2 * NP>(); bar(); ld_lin<NP>(dst + (t & 3) * STG, wi + (t < 9 ? k9(t) : 9 + k9(t - 9)) * 18432, l16); }
      wait_vm<2 * NP>(); bar(); wait_vm<NP>(); bar(); wait_vm<0>(); bar();
      wait_flags<1>(R, O_ZFL + fp * 2048 + tp * 128, 16, tag, lane);
      STAMPW(6,1,6);
#pragma unroll 1
      for (int t = 18; t < 53; ++t) {
        if (t >= 21) { wait_vm<2 * NP>(); bar(); }
        ld_lin<NP>(dst + (t & 3) * STG, t < 50 ? zs + k32(t - 18) * 18432 : wo + k18(t - 50) * 18432, l16);
      }
      wait_vm<0>(); bar();
      bar();
#pragma unroll 1
      for (int t = 53; t < 57; ++t) ld_lin<NP>(dst + (t & 3) * STG, wo + k18(t - 50) * 18432, l16);
      bar(); bar(); bar();
      wait_vm<3 * NP>(); bar();
#pragma unroll 1
      for (int t = 57; t < 68; ++t) { wait_vm<2 * NP>(); bar(); ld_lin<NP>(dst + (t & 3) * STG, wo + k18(t - 50) * 18432, l16); }
      wait_vm<2 * NP>(); bar(); wait_vm<NP>(); bar(); wait_vm<0>(); bar();
    }
    return;
  }
  Ctx c;
  c.sm = sm;
#pragma unroll
  for (int i = 0; i < 4; ++i) { c.am0[i] = (2 * w + (i >> 1)) * 1024 + (i & 1) * 512 + lane * 8; asm volatile("" : "+v"(c.am0[i])); }
  c.am1 = 2 * w * 2048 + li * 128 + ((q ^ (lane & 7)) * 16);
  c.an = 16384 + lane * 16;
  const int prow = 8 * (li >> 2) + (li & 3);
  if (tid < 72) {
    const unsigned o = tid < 36 ? FH * 4 + tid * 16 : (tid - 36) * 16;
    const u32x4 c0 = ld16(R, o, O_CS + fp * 576), c1 = ld16(R, o, O_CS + F * 4 + fp * 576), c2 = ld16(R, o, O_CS + F * 8 + fp * 576);
    *(f32x4*)(sm + X_CS + tid * 16) = (__builtin_bit_cast(f32x4, c0) + __builtin_bit_cast(f32x4, c1)) + __builtin_bit_cast(f32x4, c2);
  }
  bar();
  char* pvb = sm + X_PV + w * 4096;
  {
    u32x4 hv[3];
#pragma unroll
    for (int k = 0; k < 3; ++k)
      hv[k] = ld16(R, (lane >> 5) * 8192 + ((lane >> 2) & 7) * 128 + (lane & 3) * 16, O_HF + (tp * 18 * 8 + (fp >> 1)) * 1024 + (fp & 1) * 64 + min(w + 4 * k, 8) * 16384);
    if (w) hv[2] = u32x4{0, 0, 0, 0};
    f32x4 a = {0, 0, 0, 0};
#pragma unroll
    for (int k = 0; k < 3; ++k)
#pragma unroll
      for (int e = 0; e < 4; ++e) { a[e & 2] = dot2(hv[k][e], 0x3F803F80u, a[e & 2]); a[(e & 2) + 1] = dot2(hv[k][e], hv[k][e], a[(e & 2) + 1]); }
#pragma unroll
    for (int e = 0; e < 4; ++e) a[e] = dpp_add<0x124>(dpp_add<0x128>(a[e]));
    if (li < 4) *(f32x4*)(sm + X_LN + (w * 4 + q) * 64 + li * 16) = a;
  }
  f32x4 acc[2][9];
  Fr X, Y;
  bar();
  STAMP(3,0);
  ldf<0>(X, c, 0, 0);
  seg<0, 0, true, true>(acc, X, Y, c, 0, 1);
  const unsigned hs_m0 = __builtin_amdgcn_readfirstlane(lds_addr(sm) + X_HS + w * 256);
  auto ld_hs = [&]() {
    unsigned keep;
    asm volatile("s_mov_b32 %0, m0\n\ts_mov_b32 m0, %1\n\ts_nop 2\n\tglobal_load_lds_dword %3, %2 sc1\n\ts_mov_b32 m0, %0"
                 : "=&s"(keep) : "s"(hs_m0), "s"(ws), "v"(O_HS + tp * 1024 + tid * 4) : "memory");
  };
  ALIGNL;
#pragma unroll 1
  for (int t = 0; t < 8; ++t) {
    if (t == 5) ld_hs();
    bar();
    seg<0, 0, true>(acc, Y, X, c, t + 1, 0);
    seg<0, 0, true>(acc, X, Y, c, t + 1, 1);
  }
  u32x4 hc[4];
  float cs[9];
  auto rd_hs = [&]() {
    wait_vm<0>();
#pragma unroll
    for (int jp = 0; jp < 4; ++jp) hc[jp] = *(const u32x4*)(sm + X_HS + w * 256 + (jp >> 1) * 128 + q * 32 + (jp & 1) * 16);
  };
  rd_hs();
#pragma unroll
  for (int n = 0; n < 9; ++n) cs[n] = *(const float*)(sm + X_CS + (16 * n + li) * 4);
  bar();
  seg<0, 0, true>(acc, Y, X, c, 9, 0);
  STAMP(3,1);
#pragma unroll 1
  for (int spin = 0; spin < SPIN; ++spin) {
    if (__all(max(max(umax4(hc[0]), umax4(hc[1])), max(umax4(hc[2]), umax4(hc[3]))) != SENT)) break;
    ld_hs();
    rd_hs();
  }
  u32x4 zb[9];
  f32x2 rs[4];
  {
    f32x2 nm[4], k1[4], k0[4], S1[4] = {}, S2[4] = {};
#pragma unroll
    for (int jp = 0; jp < 4; ++jp) {
      rs[jp] = f32x2{asf(hc[jp][0]), asf(hc[jp][2])}; nm[jp] = f32x2{asf(hc[jp][1]), asf(hc[jp][3])};
      k0[jp] = rs[jp] * GC0; k1[jp] = rs[jp] * rs[jp] * rs[jp] * GC1;
    }
#pragma unroll
    for (int n = 0; n < 9; ++n) {
#pragma unroll
      for (int jp = 0; jp < 4; ++jp) {
        f32x2 a = (jp & 1) ? hi2(acc[jp >> 1][n]) : lo2(acc[jp >> 1][n]);
        f32x2 z = gelu2(__builtin_elementwise_fma(nm[jp], f32x2{cs[n], cs[n]}, a), k1[jp], k0[jp]);
        zb[n][jp] = pk2(z);
        S1[jp] += z; S2[jp] = __builtin_elementwise_fma(z, z, S2[jp]);
      }
      asm volatile("" : "+v"(zb[n]));
    }
#pragma unroll
    for (int jp = 0; jp < 4; ++jp) { S1[jp] *= rs[jp]; S2[jp] *= rs[jp] * rs[jp]; }
    float v[16];
#pragma unroll
    for (int j = 0; j < 8; ++j) { v[2 * j] = S1[j >> 1][j & 1]; v[2 * j + 1] = S2[j >> 1][j & 1]; }
#pragma unroll
    for (int i = 0; i < 8; ++i) { bool h = li & 8; v[i] = (h ? v[i + 8] : v[i]) + dpp_get<0x128>(h ? v[i] : v[i + 8]); }
#pragma unroll
    for (int i = 0; i < 4; ++i) { bool h = li & 4; v[i] = (h ? v[i + 4] : v[i]) + dpp_get<0x141>(h ? v[i] : v[i + 4]); }
#pragma unroll
    for (int i = 0; i < 2; ++i) { bool h = li & 2; v[i] = (h ? v[i + 2] : v[i]) + dpp_get<0x4E>(h ? v[i] : v[i + 2]); }
    { bool h = li & 1; v[0] = (h ? v[1] : v[0]) + dpp_get<0xB1>(h ? v[0] : v[1]); }
    unsigned tot = asu(v[0]);
    st4(R, lane * 4, O_ST + ((tp * 4 + w) * 16 + fp) * 256, tot == SENT ? 0x7FC00000u : tot);
  }
  STAMP(3,2);
  const char* sts = ws + O_ST + (tp * 4 + w) * 4096;
  seg<0, 1, true, true>(acc, X, Y, c, 9, 1);
  ALIGNL;
#pragma unroll 1
  for (int t = 9; t < TCHK; ++t) {
    if (t == TPOLL) glds4<true>(lds_addr(pvb), sts, lane * 16, lane * 16, lane * 16, lane * 16);
    bar();
    seg<0, 1, true>(acc, Y, X, c, t + 1, 0);
    seg<0, 1, true>(acc, X, Y, c, t + 1, 1);
  }
  STAMP(3,3);
  u32x4 pv[4];
#pragma unroll 1
  for (int spin = 0; spin < SPIN; ++spin) {
    wait_vm<0>();
#pragma unroll
    for (int k = 0; k < 4; ++k) pv[k] = *(const u32x4*)(pvb + k * 1024 + lane * 16);
    if (__all(max(max(umax4(pv[0]), umax4(pv[1])), max(umax4(pv[2]), umax4(pv[3]))) != SENT)) break;
    glds4<true>(lds_addr(pvb), sts, lane * 16, lane * 16, lane * 16, lane * 16);
  }
  STAMP(3,4);
  {
    float t4[4];
#pragma unroll
    for (int e = 0; e < 4; ++e) t4[e] = q_sum((asf(pv[0][e]) + asf(pv[1][e])) + (asf(pv[2][e]) + asf(pv[3][e])));
    f32x2 o0, o1;
#pragma unroll
    for (int r = 0; r < 2; ++r) {
      float mean = t4[2 * r] * (1.0f / FH), var = fmaxf(t4[2 * r + 1] * (1.0f / FH) - mean * mean, 0.f), mb, ri;
      ln_scalars(mean, var, mb, ri);
      o0[r] = ri; o1[r] = -mb * ri;
    }
    char* ln = sm + X_LN + w * 256;
    if (lane < 16) { *(f32x2*)(ln + li * 8) = o0; *(f32x2*)(ln + 128 + li * 8) = o1; }
    asm volatile("" ::: "memory");
    f32x4 ri[2], rb[2];
#pragma unroll
    for (int m = 0; m < 2; ++m) { ri[m] = *(const f32x4*)(ln + (8 * q + 4 * m) * 4); rb[m] = *(const f32x4*)(ln + 128 + (8 * q + 4 * m) * 4); }
#pragma unroll
    for (int jp = 0; jp < 4; ++jp) rs[jp] *= (jp & 1) ? hi2(ri[jp >> 1]) : lo2(ri[jp >> 1]);
    const unsigned zo = lane * 16 + O_ZF + (fp * 64 + 4 * tp + w) * 9216;
#pragma unroll
    for (int n = 0; n < 9; ++n) {
      u32x4 o4;
#pragma unroll
      for (int jp = 0; jp < 4; ++jp)
        o4[jp] = pk2(__builtin_elementwise_fma(up2(zb[n][jp]), rs[jp], (jp & 1) ? hi2(rb[jp >> 1]) : lo2(rb[jp >> 1])));
      st16(R, zo, n * 1024, o4);
    }
  }
  STAMP(3,5);
  ALIGNL;
#pragma unroll 1
  for (int t = TCHK; t < 17; ++t) {
    if (t == TFLAG) wait_vm<0>();
    bar();
    if (t == TFLAG && tid < 16) st8(R, tid * 128, O_ZFL + fp * 2048 + tp * 8, tag);
    seg<0, 1, true>(acc, Y, X, c, t + 1, 0);
    seg<0, 1, true>(acc, X, Y, c, t + 1, 1);
  }
  float lrs[2], lnm[2];
  f32x4 cs1[9];
#pragma unroll
  for (int m = 0; m < 2; ++m) { const f32x2 h = *(const f32x2*)(sm + X_HS + w * 256 + m * 128 + li * 8); lrs[m] = h[0]; lnm[m] = h[1]; }
#pragma unroll
  for (int n = 0; n < 9; ++n) cs1[n] = *(const f32x4*)(sm + X_CS + (144 + 16 * n + 4 * q) * 4);
  __builtin_amdgcn_sched_barrier(0);
  seg<0, 1, false>(acc, Y, X, c, 17, 1);
  STAMP(3,6);
  u32x2 z1[2][9];
  float bs[2];
#pragma unroll
  for (int m = 0; m < 2; ++m) bs[m] = ldbf(b_sp, 128 * tp + 32 * w + prow + 4 * m) * lrs[m];
  {
#pragma unroll
    for (int n = 0; n < 9; ++n)
#pragma unroll
      for (int m = 0; m < 2; ++m) {
        const float a1 = lrs[m] * lrs[m] * lrs[m] * GC1, a0 = lrs[m] * GC0;
        const f32x2 k1 = {a1, a1}, k0 = {a0, a0}, nm = {lnm[m], lnm[m]};
        z1[m][n][0] = pk2(gelu2(__builtin_elementwise_fma(nm, lo2(cs1[n]), lo2(acc[m][n])), k1, k0));
        z1[m][n][1] = pk2(gelu2(__builtin_elementwise_fma(nm, hi2(cs1[n]), hi2(acc[m][n])), k1, k0));
        asm volatile("" : "+v"(z1[m][n]));
      }
  }
  STAMP(3,7);
  bar();
  STAMP(3,8);
  ldf<1>(X, c, 18, 0);
  seg<1, 1, true, true>(acc, X, Y, c, 18, 1);
  ALIGNL;
#pragma unroll 1
  for (int t = 18; t < 49; ++t) {
    bar();
    seg<1, 1, true>(acc, Y, X, c, t + 1, 0);
    seg<1, 1, true>(acc, X, Y, c, t + 1, 1);
  }
  seg<1, 1, false>(acc, Y, X, c, 49, 1);
  STAMP(3,9);
  {
    const unsigned pf = O_PF + (((2 * tp + (w >> 1)) * 72 + 9 * (fp >> 1)) * 4 + 2 * (w & 1)) * 1024;
    const unsigned po = lane * 16 + pf + (fp & 1) * 16384;
    u32x2 p8[2];
#pragma unroll
    for (int m = 0; m < 2; ++m) {
      const f32x2 r2 = {lrs[m], lrs[m]}, b2 = {bs[m], bs[m]};
      auto pp = [&](int n) { return u32x2{pk2(__builtin_elementwise_fma(lo2(acc[m][n]), r2, b2) * up2(z1[m][n][0])), pk2(__builtin_elementwise_fma(hi2(acc[m][n]), r2, b2) * up2(z1[m][n][1]))}; };
#pragma unroll
      for (int a = 0; a < 4; ++a) {
        u32x2 p0 = pp(2 * a), p1 = pp(2 * a + 1);
        st16(R, po, a * 4096 + m * 1024, u32x4{p0[0], p0[1], p1[0], p1[1]});
      }
      p8[m] = pp(8);
    }
    const auto s0 = __builtin_amdgcn_permlane32_swap(p8[0][0], p8[1][0], false, false), s1 = __builtin_amdgcn_permlane32_swap(p8[0][1], p8[1][1], false, false);
    st16(R, (lane >> 5) * 1024 + (lane & 31) * 16, pf + 8 * 4096 + (fp & 1) * 512, u32x4{s0[0], s1[0], s0[1], s1[1]});
  }
  STAMP(3,10);
  const int th = w & 1, kq = w >> 1, T = 4 * t64 + 2 * th + kq;
  const int lane5 = fresh_lane();
#define lane lane5
#define li (lane5 & 15)
#define q (lane5 >> 4)
#define prow (8 * (li >> 2) + (li & 3))
  struct P3 { u32x4 p[2][2]; } Up, Vp;
  struct N3 { u32x4 n[9]; } Un, Vn, En;
  const int ap = (8 * kq + 2 * th) * 1024 + lane * 16, aw = 16384 + kq * 9216 + lane * 16;
  auto ldp = [&](P3& f, int t) {
#pragma unroll
    for (int g = 0; g < 2; ++g)
#pragma unroll
      for (int m = 0; m < 2; ++m) f.p[g][m] = *(const u32x4*)(sm + (t & 3) * STG + ap + (4 * g + m) * 1024);
  };
  auto ldn = [&](N3& f, int t) {
#pragma unroll
    for (int n = 0; n < 9; ++n) f.n[n] = *(const u32x4*)(sm + (t & 3) * STG + aw + n * 1024);
  };
  bar();
  ldn(Un, 50); ldn(Vn, 51); ldn(En, 52);
  arrive(2, 4, O_PFL + tp * 2048 + fp * 8, 128, 16);
  bar();
  u32x2 hv[5];
#pragma unroll
  for (int n = 0; n < 5; ++n) {
    const int dd = 72 * j3 + 16 * n + 4 * (n == 4 ? (q & 1) : q);
    hv[n] = ld8(R, (dd >> 5) * 8192 + ((dd >> 2) & 1) * 512 + (((dd >> 3) & 3) * 16 + li) * 8, O_HF + ((T >> 3) * 18 * 8 + (T & 7)) * 1024);
  }
  f32x4 a3[2][6] = {};
  auto mm3 = [&](const P3& f, const N3& e) {
#pragma unroll
    for (int g = 0; g < 2; ++g)
#pragma unroll
      for (int m = 0; m < 2; ++m) {
#pragma unroll
        for (int n = 0; n < 4; ++n) a3[m][n] = mfma_bf(e.n[4 * g + n], f.p[g][m], a3[m][n]);
        a3[m][4 + g] = mfma_bf(e.n[8], f.p[g][m], a3[m][4 + g]);
      }
#pragma unroll
    for (int i = 0; i < 13; ++i) { __builtin_amdgcn_sched_group_barrier(0x008, 1, 0); __builtin_amdgcn_sched_group_barrier(0x100, 1, 0); }
    __builtin_amdgcn_sched_barrier(0);
  };
  STAMP(3,11);
  bar();
  STAMP(3,12);
  ldp(Up, 50);
  bar(); ldp(Vp, 51); mm3(Up, Un);
  bar(); ldp(Up, 52); mm3(Vp, Vn);
  bar(); ldp(Vp, 53); ldn(Vn, 53); mm3(Up, En);
  ALIGNL;
#pragma unroll 1
  for (int t = 53; t < 67; t += 2) { bar(); ldp(Up, t + 1); ldn(Un, t + 1); mm3(Vp, Vn); bar(); ldp(Vp, t + 2); ldn(Vn, t + 2); mm3(Up, Un); }
  mm3(Vp, Vn);
  STAMP(3,13);
#pragma unroll
  for (int m = 0; m < 2; ++m) a3[m][4] = lane < 32 ? a3[m][4] : a3[m][5];
  if (kq) {
#pragma unroll
    for (int n = 0; n < 5; ++n) { *(f32x4*)(sm + w * 5120 + n * 1024 + lane * 16) = a3[0][n]; asm volatile("" : "+v"(a3[1][n])); a3[0][n] = a3[1][n]; }
  } else {
#pragma unroll
    for (int n = 0; n < 5; ++n) *(f32x4*)(sm + w * 5120 + n * 1024 + lane * 16) = a3[1][n];
  }
  bar();
  char* o = out + (size_t)(32 * (T >> 1) + 4 * (T & 1) + prow) * 1152 + (72 * j3 + 4 * q) * 2;
#pragma unroll
  for (int n = 0; n < 5; ++n) {
    f32x4 v = a3[0][n] + *(const f32x4*)(sm + (w ^ 2) * 5120 + n * 1024 + lane * 16);
    if (n == 4) v = f32x4{half_sum(v[0]), half_sum(v[1]), half_sum(v[2]), half_sum(v[3])};
    if (n < 4 || q < 2) *(u32x2*)(o + n * 32) = u32x2{pk2(up2(hv[n][0]) + lo2(v)), pk2(up2(hv[n][1]) + hi2(v))};
  }
  if (bid == 0 && w == 0 && lane == 0) { u64 e = (((u64)tag[1] << 32) | tag[0]) - 0x9E3779B97F4A7C15ull + 1; st8(R, 0, O_EP, u32x2{(unsigned)e, (unsigned)(e >> 32)}); }
  STAMP(3,14);
}

#undef lane
#undef li
#undef q
#undef prow
extern "C" void kernel_launch(void* const* d_in, const int* in_sizes, int n_in, void* d_out, int out_size, void* d_ws, size_t ws_size,
                              hipStream_t stream) {
  (void)hipFuncSetAttribute((const void*)k_all, hipFuncAttributeMaxDynamicSharedMemorySize, LDS_BYTES);
  k_all<<<256, 512, LDS_BYTES, stream>>>((const char*)d_in[0], (const char*)d_in[1], (const char*)d_in[2], (const char*)d_in[3],
      (const char*)d_in[4], (const char*)d_in[5], (const char*)d_in[6], (const char*)d_in[7], (const char*)d_in[8], (const char*)d_in[10],
      (const char*)d_in[11], (const char*)d_in[12], (const char*)d_in[13], (char*)d_out, (char*)d_ws);
}
```

```cpp
#include <hip/hip_runtime.h>
#include <hip/hip_bf16.h>
#include <stdint.h>

typedef __attribute__((ext_vector_type(8))) __bf16 bf16x8;
typedef __attribute__((ext_vector_type(2))) __bf16 bf16x2;
typedef __attribute__((ext_vector_type(8))) _Float16 f16x8;
typedef __attribute__((ext_vector_type(2))) _Float16 f16x2;
typedef __attribute__((ext_vector_type(4))) float f32x4;
typedef __attribute__((ext_vector_type(2))) float f32x2;
typedef __attribute__((ext_vector_type(4))) unsigned u32x4;
typedef __attribute__((ext_vector_type(2))) unsigned u32x2;
typedef __attribute__((ext_vector_type(4))) short s16x4;
#define LDSP __attribute__((address_space(3)))
#define DEVI __device__ __forceinline__

constexpr int L = 2048, D = 576, NM = 128, F = 4608, FH = 2304;
constexpr float EPS = 1e-5f;
constexpr unsigned O_EP = 0;
constexpr int NREP = 64;
constexpr unsigned O_F2 = 256;
constexpr unsigned F2S = 2048;
constexpr unsigned O_ZFL = O_F2 + NREP * F2S;
constexpr unsigned O_PFL = O_ZFL + 32768;
constexpr unsigned O_CS = O_PFL + 32768;
constexpr unsigned O_ST = O_CS + F * 12;
constexpr unsigned O_HS = O_ST + 262144;
constexpr unsigned O_LS = O_HS + 16384;
constexpr unsigned O_WI = O_LS + 16 * 16384;
constexpr unsigned O_WO = O_WI + D * F * 2;
constexpr unsigned O_HF = O_WO + FH * D * 2;
constexpr unsigned O_ZF = O_HF + L * D * 2;
constexpr unsigned O_PF = O_ZF + L * FH * 2;
constexpr unsigned WS_USED = O_PF + L * FH * 2;
#define SB __builtin_amdgcn_sched_barrier(0)
constexpr unsigned SENT = 0xFFFFFFFFu;

DEVI unsigned pk_bf16(float a, float b) { f32x2 v = {a, b}; return __builtin_bit_cast(unsigned, __builtin_convertvector(v, bf16x2)); }
DEVI float bf_lo(unsigned u) { return __builtin_bit_cast(float, u << 16); }
DEVI float bf_hi(unsigned u) { return __builtin_bit_cast(float, u & 0xFFFF0000u); }
DEVI float asf(unsigned u) { return __builtin_bit_cast(float, u); }
DEVI unsigned asu(float f) { return __builtin_bit_cast(unsigned, f); }
DEVI float rbf(float a) { return bf_lo(pk_bf16(a, a)); }
template <int CTRL> DEVI float dpp_add(float v) {
  return v + __builtin_bit_cast(float, __builtin_amdgcn_update_dpp(0, __builtin_bit_cast(int, v), CTRL, 0xF, 0xF, true));
}
DEVI float row16_sum(float v) { v = dpp_add<0x128>(v); v = dpp_add<0x124>(v); v = dpp_add<0x122>(v); v = dpp_add<0x121>(v); return v; }
DEVI float half_sum(float v) { auto r = __builtin_amdgcn_permlane32_swap(asu(v), asu(v), false, false); return asf(r[0]) + asf(r[1]); }
DEVI float q_sum(float v) { auto r = __builtin_amdgcn_permlane16_swap(asu(v), asu(v), false, false); return half_sum(asf(r[0]) + asf(r[1])); }
DEVI unsigned umax4(u32x4 v) { return max(max(v[0], v[1]), max(v[2], v[3])); }
DEVI u32x2 tr_read(const char* p) {
  return __builtin_bit_cast(u32x2, __builtin_amdgcn_ds_read_tr16_b64_v4i16((LDSP s16x4*)p));
}
DEVI f32x4 mfma_bf(u32x4 a, u32x4 b, f32x4 c) {
  return __builtin_amdgcn_mfma_f32_16x16x32_bf16(__builtin_bit_cast(bf16x8, a), __builtin_bit_cast(bf16x8, b), c, 0, 0, 0);
}
DEVI f32x4 mfma_h(u32x4 a, u32x4 b, f32x4 c) {
  return __builtin_amdgcn_mfma_f32_16x16x32_f16(__builtin_bit_cast(f16x8, a), __builtin_bit_cast(f16x8, b), c, 0, 0, 0);
}
DEVI void ln_scalars(float mean, float var, float& mb, float& rinv) {
  mb = rbf(mean);
  float d = rbf(__builtin_amdgcn_sqrtf(rbf(rbf(var) + EPS)));
  rinv = __builtin_amdgcn_rcpf(d);
}


typedef unsigned long long u64;
typedef __amdgpu_buffer_rsrc_t rsrc_t;
constexpr int SPIN = 1 << 16;
constexpr int CODE_LINES = 38;
template <int CTRL> DEVI float dpp_get(float v) {
  return __builtin_bit_cast(float, __builtin_amdgcn_update_dpp(0, __builtin_bit_cast(int, v), CTRL, 0xF, 0xF, true));
}
constexpr float GC1 = -0.10294324f, GC0 = -2.3022082f;
DEVI f32x2 gelu2(f32x2 x, f32x2 k1, f32x2 k0) {
  f32x2 w = x * __builtin_elementwise_fma(x * x, k1, k0);
  f32x2 d = f32x2{__builtin_amdgcn_exp2f(w[0]), __builtin_amdgcn_exp2f(w[1])} + f32x2{1.f, 1.f};
  return x * f32x2{__builtin_amdgcn_rcpf(d[0]), __builtin_amdgcn_rcpf(d[1])};
}
DEVI unsigned pk2(f32x2 v) { return __builtin_bit_cast(unsigned, __builtin_convertvector(v, bf16x2)); }
DEVI f32x2 up2(unsigned u) { return f32x2{bf_lo(u), bf_hi(u)}; }
DEVI f32x2 lo2(f32x4 v) { return __builtin_shufflevector(v, v, 0, 1); }
DEVI f32x2 hi2(f32x4 v) { return __builtin_shufflevector(v, v, 2, 3); }
DEVI float dot2(unsigned a, unsigned b, float c) {
  return __builtin_amdgcn_fdot2_f32_bf16(__builtin_bit_cast(bf16x2, a), __builtin_bit_cast(bf16x2, b), c, false);
}
DEVI void st16(rsrc_t r, unsigned voff, unsigned soff, u32x4 v) { __builtin_amdgcn_raw_buffer_store_b128(v, r, voff + soff, 0, 16); }
DEVI void st8(rsrc_t r, unsigned voff, unsigned soff, u32x2 v) { __builtin_amdgcn_raw_buffer_store_b64(v, r, voff, soff, 16); }
DEVI void st4(rsrc_t r, unsigned voff, unsigned soff, unsigned v) { __builtin_amdgcn_raw_buffer_store_b32(v, r, voff, soff, 16); }
DEVI u32x4 ld16(rsrc_t r, unsigned voff, unsigned soff) { return __builtin_amdgcn_raw_buffer_load_b128(r, voff, soff, 16); }
DEVI u32x2 ld8(rsrc_t r, unsigned voff, unsigned soff) { return __builtin_amdgcn_raw_buffer_load_b64(r, voff, soff, 16); }
template <int N> DEVI void wait_vm() { asm volatile("s_waitcnt vmcnt(%0)" ::"i"(N) : "memory"); }
DEVI void bar() { __builtin_amdgcn_sched_barrier(0); asm volatile("s_waitcnt lgkmcnt(0)\n\ts_barrier" ::: "memory"); __builtin_amdgcn_sched_barrier(0); }
DEVI int fresh_lane() { int l; asm volatile("v_mbcnt_lo_u32_b32 %0, -1, 0\n\tv_mbcnt_hi_u32_b32 %0, -1, %0" : "=v"(l)); return l; }
DEVI unsigned lds_addr(const char* p) { return (unsigned)(uintptr_t)(LDSP const char*)p; }
template <int NL> DEVI void wait_flags(rsrc_t r, unsigned off, int n, u32x2 tag, int lane) {
#pragma unroll 1
  for (int spin = 0; spin < SPIN; ++spin) {
    bool ok = true;
#pragma unroll
    for (int k = 0; k < NL; ++k) {
      int i = min(lane + 64 * k, n / 2 - 1);
      u32x4 v = ld16(r, i * 16, off);
      ok = ok && v[0] == tag[0] && v[1] == tag[1] && v[2] == tag[0] && v[3] == tag[1];
    }
    if (__all(ok)) break;
    asm volatile("" ::: "memory");
  }
  asm volatile("" ::: "memory");
}
template <bool SC> DEVI void glds4(unsigned m0v, const char* base, unsigned v0, unsigned v1, unsigned v2, unsigned v3) {
  unsigned keep;
  if (SC)
    asm volatile("s_mov_b32 %0, m0\n\ts_mov_b32 m0, %1\n\ts_nop 2\n\t"
                 "global_load_lds_dwordx4 %3, %2 sc1\n\tglobal_load_lds_dwordx4 %4, %2 offset:1024 sc1\n\t"
                 "global_load_lds_dwordx4 %5, %2 offset:2048 sc1\n\tglobal_load_lds_dwordx4 %6, %2 offset:3072 sc1\n\ts_mov_b32 m0, %0"
                 : "=&s"(keep) : "s"(m0v), "s"(base), "v"(v0), "v"(v1), "v"(v2), "v"(v3) : "memory");
  else
    asm volatile("s_mov_b32 %0, m0\n\ts_mov_b32 m0, %1\n\ts_nop 2\n\t"
                 "global_load_lds_dwordx4 %3, %2\n\tglobal_load_lds_dwordx4 %4, %2 offset:1024\n\t"
                 "global_load_lds_dwordx4 %5, %2 offset:2048\n\tglobal_load_lds_dwordx4 %6, %2 offset:3072\n\ts_mov_b32 m0, %0"
                 : "=&s"(keep) : "s"(m0v), "s"(base), "v"(v0), "v"(v1), "v"(v2), "v"(v3) : "memory");
}
DEVI void glds1(unsigned m0v, const char* base, unsigned v0) {
  unsigned keep;
  asm volatile("s_mov_b32 %0, m0\n\ts_mov_b32 m0, %1\n\ts_nop 2\n\tglobal_load_lds_dwordx4 %3, %2 sc1\n\ts_mov_b32 m0, %0"
               : "=&s"(keep) : "s"(m0v), "s"(base), "v"(v0) : "memory");
}
template <int NP> DEVI void ld_lin(unsigned dst, const char* src, unsigned l16) {
  glds4<true>(dst, src, l16, l16, l16, l16);
  glds4<true>(dst + 4096, src + 4096, l16, l16, l16, l16);
  if (NP == 9) glds1(dst + 8192, src + 8192, l16);
}

constexpr int TP = 1168;
constexpr int CV_K = 30208, CV_C = 4160, CV_X = 17408;
constexpr int C_U = 4 * CV_K, C_KD = C_U + 16 * TP, KDS = 1664, FT = 520, C_END = C_KD + 4 * KDS;
constexpr int KT = 528;
constexpr int STG = 34816, RING = 4 * STG;
constexpr int X_CS = RING, X_LN = X_CS + 1152, X_PV = X_LN + 1024, X_HS = X_PV + 16384, X_CNT = 158976;
constexpr int LDS_BYTES = X_CNT + 64 + 256;
static_assert(C_END <= X_CNT && (15 + 128) * 128 <= 16 * TP && 57 * KT <= CV_K && X_HS + 1024 <= X_CNT && LDS_BYTES <= 163840, "lds");
constexpr int TPOLL = 12;
constexpr int WDELAY = 5;
#define ALIGNL asm volatile(".p2alignl 6, 0xBF800000")
constexpr int TCHK = 13;
constexpr int TFLAG = 16;

struct Fr { u32x4 m[2], n[9]; };
struct Ctx { const char* sm; int am0, am1, an; };
template <int MK> DEVI void ldf(Fr& f, const Ctx& c, int t, int kk) {
  int so = (t & 3) * STG;
  asm volatile("" : "+s"(so));
  const char* slot = c.sm + so;
  if (MK == 0) {
#pragma unroll
    for (int m = 0; m < 2; ++m) {
      const char* p = slot + c.am0 + kk * 8192 + m * 1024;
      u32x2 a = *(volatile const LDSP u32x2*)p, b = *(volatile const LDSP u32x2*)(p + 512);
      f.m[m] = u32x4{a[0], a[1], b[0], b[1]};
    }
  } else {
#pragma unroll
    for (int m = 0; m < 2; ++m) f.m[m] = *(const u32x4*)(slot + (c.am1 ^ (kk * 64)) + m * 2048);
  }
#pragma unroll
  for (int n = 0; n < 9; ++n) f.n[n] = *(const u32x4*)(slot + c.an + (kk * 9 + n) * 1024);
}
template <int MK, int ORI, bool LD, bool FIRST = false> DEVI void seg(f32x4 (&acc)[2][9], const Fr& cur, Fr& nxt, const Ctx& c, int t, int kk) {
  __builtin_amdgcn_s_waitcnt(0xC07F);
  __builtin_amdgcn_sched_barrier(0);
  if (LD) ldf<MK>(nxt, c, t, kk);
#pragma unroll
  for (int m = 0; m < 2; ++m)
#pragma unroll
    for (int nn = 0; nn < 9; ++nn) {
      const int n = m ? 8 - nn : nn;
      const f32x4 ci = FIRST ? f32x4{0, 0, 0, 0} : acc[m][n];
      acc[m][n] = ORI == 0 ? mfma_bf(cur.m[m], cur.n[n], ci) : mfma_bf(cur.n[n], cur.m[m], ci);
    }
#pragma unroll
  for (int i = 0; i < (MK == 0 ? 13 : 11); ++i) { __builtin_amdgcn_sched_group_barrier(0x008, 1, 0); __builtin_amdgcn_sched_group_barrier(0x100, 1, 0); }
  __builtin_amdgcn_sched_barrier(0);
}
DEVI f32x2 cmul(f32x2 a, f32x2 b, f32x2 bp) {
  return __builtin_elementwise_fma(f32x2{a[1], a[1]}, bp, f32x2{a[0], a[0]} * b);
}
DEVI f32x2 perp(f32x2 b) { return f32x2{-b[1], b[0]}; }
DEVI f32x2 cexp(float ar, float ai, float l) {
  float m = __builtin_amdgcn_exp2f(ar * l), ph = __builtin_amdgcn_fractf(ai * l);
  return f32x2{m * __builtin_amdgcn_cosf(ph), m * __builtin_amdgcn_sinf(ph)};
}
DEVI unsigned pkh(f32x2 v) { return __builtin_bit_cast(unsigned, __builtin_convertvector(v, f16x2)); }
DEVI float ldbf(const char* p, int i) { return bf_lo(*(const unsigned short*)(p + 2 * i)); }

__global__ __launch_bounds__(512, 2) void k_all(const char* __restrict__ x, const char* __restrict__ a_re, const char* __restrict__ a_im,
    const char* __restrict__ b_re, const char* __restrict__ b_im, const char* __restrict__ c_re, const char* __restrict__ c_im,
    const char* __restrict__ log_dt, const char* __restrict__ dskip, const char* __restrict__ w_in, const char* __restrict__ w_sp,
    const char* __restrict__ b_sp, const char* __restrict__ w_out, char* __restrict__ out, char* __restrict__ ws) {
  extern __shared__ __attribute__((aligned(16))) char sm[];
  const int tid = threadIdx.x, lane = tid & 63, w = __builtin_amdgcn_readfirstlane(tid >> 6), li = lane & 15, q = lane >> 4;
  const int bid = blockIdx.x, xc = bid & 7, ii = bid >> 3;
  if (w == 7) {
    unsigned long long pc;
    asm volatile("s_getpc_b64 %0" : "=s"(pc));
    const char* cp = (const char*)(pc & ~255ull) + (ii & 7) * (CODE_LINES * 128);
    unsigned keep;
    asm volatile("s_mov_b32 %0, m0\n\ts_mov_b32 m0, %1\n\ts_nop 2\n\tglobal_load_lds_dword %3, %2\n\ts_mov_b32 m0, %0"
                 : "=&s"(keep) : "s"(lds_addr(sm) + X_CNT + 64), "s"(cp), "v"(min(lane, CODE_LINES - 1) * 128) : "memory");
  }
  if (tid == 256) *(u32x4*)(sm + X_CNT) = u32x4{0, 0, 0, 0};
  asm volatile("" ::"s"(x), "s"(a_re), "s"(a_im), "s"(b_re), "s"(b_im), "s"(c_re), "s"(c_im), "s"(log_dt), "s"(dskip), "s"(w_in), "s"(w_out), "s"(ws));
  const rsrc_t R = __builtin_amdgcn_make_buffer_rsrc(ws, 0, WS_USED, 0x00020000);
  SB;
  const u32x2 ep = ld8(R, 0, O_EP);
  const bool is_conv = ii < 18;
  const int cg = xc * 18 + ii, rep = bid & (NREP - 1);
  auto mk_tag = [&]() {
    u64 e = (((u64)__builtin_amdgcn_readfirstlane(ep[1]) << 32) | (unsigned)__builtin_amdgcn_readfirstlane(ep[0])) + 0x9E3779B97F4A7C15ull;
    return u32x2{(unsigned)e, (unsigned)(e >> 32)};
  };
  u32x2 tag;
  auto arrive = [&](int slot, unsigned n, unsigned flag_off, unsigned stride, int nrep) {
    wait_vm<0>();
    const int lane = fresh_lane();
    unsigned old = 0;
    if (lane == 0) old = __hip_atomic_fetch_add((unsigned*)(sm + X_CNT) + slot, 1u, __ATOMIC_RELAXED, __HIP_MEMORY_SCOPE_WORKGROUP);
    if (__builtin_amdgcn_readfirstlane(old) == n - 1 && lane < nrep) {
#pragma unroll
      for (int k = 0; k < (nrep + 63) / 64; ++k) st8(R, (lane + 64 * k) * stride, flag_off, tag);
    }
  };
  if (is_conv) {
    const int ch = w & 3, dch = 4 * cg + ch;
    char* sk = sm + ch * CV_K;
    float sc = 0.f;
    u32x2 xc8[8];
    struct CF { u32x4 a[4], b[8]; } A, B;
    u32x4 tf[4], b9, b7, a32;
    const int sg = 7 - (li & 7);
    const char* pk = sk + (sg & 3) * CV_C + (253 - (li >> 3) + q) * 16 + (sg >> 2) * 8 - 2048;
    auto lda = [&](int ap) {
      const char* p = pk + 2048 - 64 * ap;
      const u32x2 x0 = *(volatile const LDSP u32x2*)p, x1 = *(volatile const LDSP u32x2*)(p + 8);
      return u32x4{x0[0], x0[1], x1[0], x1[1]};
    };
    if (w < 4) {
      const int pi = (dch * NM + 2 * lane) * 2;
      const unsigned v_ar = *(const unsigned*)(a_re + pi), v_ai = *(const unsigned*)(a_im + pi), v_br = *(const unsigned*)(b_re + pi),
                     v_bi = *(const unsigned*)(b_im + pi), v_cr = *(const unsigned*)(c_re + pi), v_ci = *(const unsigned*)(c_im + pi);
      const unsigned v_dk = *(const unsigned short*)(dskip + 2 * dch), v_dt = *(const unsigned short*)(log_dt + 2 * dch);
      bar();
      const float p_dk = bf_lo(v_dk), dt = rbf(expf(bf_lo(v_dt)));
      tag = mk_tag();
      SB;
      const f32x2 ar = (up2(v_ar) * dt) * 1.44269504f, ai = (up2(v_ai) * dt) * 0.15915494f;
      const f32x2 br = up2(v_br) * dt, bi = up2(v_bi) * dt, cr = up2(v_cr), ci = up2(v_ci);
      const f32x2 cbr = cr * br - ci * bi, cbi = -(cr * bi + ci * br);
      float mx = fmaxf(fmaxf(fabsf(cbr[0]), fabsf(cbi[0])), fmaxf(fabsf(cbr[1]), fabsf(cbi[1])));
      mx = fmaxf(mx, dpp_get<0x128>(mx)); mx = fmaxf(mx, dpp_get<0x124>(mx)); mx = fmaxf(mx, dpp_get<0x122>(mx)); mx = fmaxf(mx, dpp_get<0x121>(mx));
      auto rl = [&](int l) { return asf(__builtin_amdgcn_readlane((int)asu(mx), l)); };
      sc = fmaxf(fmaxf(rl(0), rl(16)), fmaxf(rl(32), rl(48)));
      sc = fmaxf(sc, fabsf(p_dk) * (1.0f / 256));
      const float isc = sc > 0.f ? 1.0f / sc : 0.f;
      const char* pa = sk + li * KT + q * 16;
      u32x4 fr[8][4];
      {
        struct C2 { f32x2 a, b; };
        char* tb = sk + lane * 8;
        auto put = [&](int row, C2 v) { *(u32x2*)(tb + row * KT) = u32x2{pkh(v.a), pkh(v.b)}; };
        auto mul = [&](C2 v, C2 u, C2 up) { return C2{cmul(v.a, u.a, up.a), cmul(v.b, u.b, up.b)}; };
        auto prp = [&](C2 v) { return C2{perp(v.a), perp(v.b)}; };
        auto ex = [&](float l) { return C2{cexp(ar[0], ai[0], l), cexp(ar[1], ai[1], l)}; };
        const C2 w1 = ex(1.f), w128 = ex(128.f);
        C2 w8 = ex(8.f); w8.a[1] = -w8.a[1]; w8.b[1] = -w8.b[1];
        const C2 w1p = prp(w1), w8p = prp(w8), w128p = prp(w128);
        C2 w64 = ex(64.f); w64.a[1] = -w64.a[1]; w64.b[1] = -w64.b[1];
        const C2 w1k = ex(1024.f), w64p = prp(w64), w1kp = prp(w1k);
        C2 y = {f32x2{cbr[0], cbi[0]} * isc, f32x2{cbr[1], cbi[1]} * isc};
        C2 E[8]; E[0] = C2{f32x2{1.f, 0.f}, f32x2{1.f, 0.f}}; E[1] = w1;
        put(0, E[0]); put(8, w128);
#pragma unroll
        for (int e = 0; e < 8; ++e) {
          if (e > 1) E[e] = mul(E[e - 1], w1, w1p);
          if (e) { put(e, E[e]); put(8 + e, mul(E[e], w128, w128p)); }
          put(41 + e, y); put(49 + e, mul(y, w64, w64p));
          if (e < 7) y = mul(y, w8, w8p);
        }
        asm volatile("" ::: "memory");
#pragma unroll
        for (int s = 0; s < 8; ++s) { fr[s][0] = *(const u32x4*)(pa + s * 64); fr[s][3] = *(const u32x4*)(pa + 41 * KT + s * 64); }
        asm volatile("" ::: "memory");
        const C2 e2p = prp(E[2]), e4p = prp(E[4]), e6p = prp(E[6]);
        C2 H = w128;
        put(32, w1k); put(40, mul(w1k, w1k, w1kp));
#pragma unroll
        for (int a = 1; a < 8; ++a) {
          if (a >= 2) H = mul(H, w128, w128p);
          put(32 + a, mul(H, w1k, w1kp));
          if (a >= 4) { put(16 + 2 * a, H); put(17 + 2 * a, mul(H, E[4], e4p)); }
          else if (a >= 2) { put(8 + 4 * a, H); put(9 + 4 * a, mul(H, E[2], e2p)); put(10 + 4 * a, mul(H, E[4], e4p)); put(11 + 4 * a, mul(H, E[6], e6p)); }
        }
      }
      asm volatile("" ::: "memory");
      SB;
      {
        f32x4 kk[3] = {};
#pragma unroll
        for (int s = 0; s < 8; ++s) { fr[s][1] = *(const u32x4*)(pa + 16 * KT + s * 64); fr[s][2] = *(const u32x4*)(pa + 32 * KT + s * 64); }
        __builtin_amdgcn_sched_barrier(0);
#pragma unroll
        for (int s = 0; s < 8; ++s) kk[0] = mfma_h(fr[s][0], fr[s][3], kk[0]);
#pragma unroll
        for (int s = 0; s < 8; ++s) { kk[1] = mfma_h(fr[s][1], fr[s][3], kk[1]); kk[2] = mfma_h(fr[s][2], fr[s][3], kk[2]); }
        if (lane == 0) kk[0][0] += p_dk * isc;
        char* kd = sm + C_KD + ch * KDS;
        *(u32x2*)(kd + (128 * (q >> 1) + 4 * (q & 1) + 8 * li) * 2) = u32x2{pkh(lo2(kk[0])), pkh(hi2(kk[0]))};
        const int i1 = q < 2 ? 256 + 64 * q + 4 * li : 384 + 64 * (q - 2) + 2 * li;
        *(unsigned*)(kd + i1 * 2) = pkh(lo2(kk[1])); *(unsigned*)(kd + (i1 + (q < 2 ? 2 : 32)) * 2) = pkh(hi2(kk[1]));
        if (lane < 4) *(u32x4*)(kd + FT * 2 + lane * 16) = u32x4{0, 0, 0, 0};
#pragma unroll
        for (int r = 0; r < 4; ++r) *(_Float16*)(kd + (r == 0 && lane == 0 ? 512 : FT + 31 + 16 * (4 * q + r) + li) * 2) = (_Float16)kk[2][r];
      }
      asm volatile("" ::: "memory");
      {
        const unsigned short* kd = (const unsigned short*)(sm + C_KD + ch * KDS);
        u32x4 c0[3];
        unsigned ab, cc, e, k0 = kd[512]; u32x4 v; u32x2 u;
        { const int k = 127 - lane; ab = *(const unsigned*)(kd + 256 + 2 * k); cc = kd[258 + 2 * k]; }
        { const int k = 63 - lane; v = *(const u32x4*)(kd + 8 * (k & 31)); u = *(const u32x2*)(kd + 128 + 4 * k); e = kd[132 + 4 * k]; }
        asm volatile("" : "+v"(v), "+v"(u), "+v"(e), "+v"(ab), "+v"(cc), "+v"(k0));
        k0 = lane == 63 ? k0 | (k0 << 16) : 0u;
        c0[0] = u32x4{0, k0 & 0xFFFF0000u, k0, k0};
        c0[1] = u32x4{cc | (cc << 16), __builtin_amdgcn_perm(ab, ab, 0x03020302), __builtin_amdgcn_perm(ab, ab, 0x01000302), __builtin_amdgcn_perm(ab, ab, 0x01000100)};
        c0[2] = lane < 32 ? u32x4{__builtin_amdgcn_perm(u[1], e, 0x07060100), __builtin_amdgcn_perm(u[1], u[1], 0x01000100), __builtin_amdgcn_perm(u[0], u[1], 0x07060100), __builtin_amdgcn_perm(u[0], u[0], 0x01000100)}
                          : u32x4{__builtin_amdgcn_alignbit(v[3], v[3], 16), __builtin_amdgcn_alignbit(v[2], v[2], 16), __builtin_amdgcn_alignbit(v[1], v[1], 16), __builtin_amdgcn_alignbit(v[0], v[0], 16)};
        if (lane < 8) *(u32x4*)(sk + (lane >> 1) * CV_C + 4096 + (lane & 1) * 16) = u32x4{0, 0, 0, 0};
        SB;
#pragma unroll
        for (int rr = 0; rr < 3; ++rr) {
          char* p = sk + (64 * rr + 64 + lane) * 16;
          unsigned f[6] = {c0[rr][0], c0[rr][1], c0[rr][2], c0[rr][3], 0, 0}, o[5];
#pragma unroll
          for (int i = 0; i < 2; ++i)
            f[4 + i] = __builtin_amdgcn_update_dpp(rr < 2 ? __builtin_amdgcn_readfirstlane(c0[rr < 2 ? rr + 1 : rr][i]) : 0u, c0[rr][i], 0x130, 0xF, 0xF, false);
#pragma unroll
          for (int i = 0; i < 5; ++i) o[i] = __builtin_amdgcn_alignbit(f[i + 1], f[i], 16);
          if (rr || lane >= 56) {
            *(u32x4*)p = c0[rr];
            *(u32x4*)(p + CV_C) = u32x4{o[0], o[1], o[2], o[3]};
            *(u32x4*)(p + 2 * CV_C) = u32x4{f[1], f[2], f[3], f[4]};
            *(u32x4*)(p + 3 * CV_C) = u32x4{o[1], o[2], o[3], o[4]};
          }
        }
      }
      asm volatile("" ::: "memory");
      {
        const char* pt = sm + C_KD + ch * KDS + (FT + 26 + 2 * li - 8 * q) * 2;
        a32 = lda(32);
#pragma unroll
        for (int k = 0; k < 4; ++k) {
          A.a[k] = lda(8 * k);
#pragma unroll
          for (int e = 0; e < 4; ++e) tf[k][e] = *(const unsigned*)(pt + k * 64 + e * 4);
        }
#pragma unroll
        for (int e = 0; e < 4; ++e) {
          const int d0 = 8 * q + 2 * e - 8 - li, d1 = d0 + 1;
          b9[e] = (abs(d0) <= 4 ? 0x3C00u : 0u) | (abs(d1) <= 4 ? 0x3C000000u : 0u);
          b7[e] = (abs(d0) <= 3 ? 0x3C00u : 0u) | (abs(d1) <= 3 ? 0x3C000000u : 0u);
        }
        asm volatile("" : "+v"(b9), "+v"(b7));
      }
      SB;
    } else {
      const int t8 = tid - 256;
      const rsrc_t RX = __builtin_amdgcn_make_buffer_rsrc((void*)x, 0, L * D * 2, 0x00020000);
      bar();
#pragma unroll
      for (int k = 0; k < 8; ++k) xc8[k] = __builtin_amdgcn_raw_buffer_load_b64(RX, t8 * 9216 + k * 1152, cg * 8, 16);
      tag = mk_tag();
      if (cg < 136 && w >= 6) st16(R, (tid - 384) * 16, O_ST + cg * 2048, u32x4{SENT, SENT, SENT, SENT});
      if (t8 < 120) *(u32x4*)(sm + C_U + t8 * 16) = u32x4{0, 0, 0, 0};
      u32x4 g[4];
#pragma unroll 1
      for (int spin = 0; spin < SPIN; ++spin) {
        bool ok = true;
#pragma unroll
        for (int k = 0; k < 4; ++k) { g[k] = ld16(R, t8 * 64 + k * 16, O_LS + (bid & 15) * 16384); ok = ok && g[k][1] == tag[0] && g[k][3] == tag[1]; }
        if (__all(ok)) break;
        asm volatile("" ::: "memory");
      }
      SB;
      __builtin_amdgcn_s_setprio(2);
      u32x4 uo[4];
#pragma unroll
      for (int k = 0; k < 4; ++k) {
        const f32x2 r2 = {__builtin_amdgcn_rcpf(bf_hi(g[k][0])), __builtin_amdgcn_rcpf(bf_hi(g[k][2]))}, b2 = -(f32x2{bf_lo(g[k][0]), bf_lo(g[k][2])} * r2);
#pragma unroll
        for (int c = 0; c < 4; ++c) {
          const unsigned a = xc8[2 * k][c >> 1], b = xc8[2 * k + 1][c >> 1];
          uo[c][k] = pkh(__builtin_elementwise_fma((c & 1) ? f32x2{bf_hi(a), bf_hi(b)} : f32x2{bf_lo(a), bf_lo(b)}, r2, b2));
        }
      }
      const int r = 15 + (t8 >> 1);
#pragma unroll
      for (int c = 0; c < 4; ++c) *(u32x4*)(sm + C_U + r * 128 + (((2 * c + (t8 & 1)) ^ ((r >> 1) & 7)) * 16)) = uo[c];
      __builtin_amdgcn_s_setprio(0);
      SB;
    }
    bar();
    SB;
    if (w < 4) {
      f32x4 acc[8];
      auto ub = [&](int b) { const int r = b >> 1; return sm + C_U + r * 128 + (((2 * ch + (b & 1)) ^ ((r >> 1) & 7)) * 16); };
      auto ldc = [&](CF& f, int a0) {
        __builtin_amdgcn_s_waitcnt(0xC07F);
        __builtin_amdgcn_sched_barrier(0);
        const char* pb = ub(28 - 4 * a0 + 2 * li + q);
#pragma unroll
        for (int m = 0; m < 8; ++m) { if (m < 4) f.a[m] = lda(a0 + 8 * m); f.b[m] = *(const u32x4*)(pb + 2048 * m); }
      };
      auto mmc = [&](const CF& f) {
#pragma unroll
        for (int m = 0; m < 4; ++m)
#pragma unroll
          for (int t = m; t < 8; ++t) acc[t] = mfma_h(f.a[m], f.b[t - m], acc[t]);
#pragma unroll
        for (int i = 0; i < 16; ++i) { __builtin_amdgcn_sched_group_barrier(0x008, 1, 0); __builtin_amdgcn_sched_group_barrier(0x100, 1, 0); }
        __builtin_amdgcn_sched_barrier(0);
      };
      f32x4 s9[4], s7[4];
      {
        const char* pb = ub(28 + 2 * li + q); const char* pe = ub(27 + 2 * li + q);
        u32x4 ue[4];
#pragma unroll
        for (int k = 0; k < 4; ++k) { A.b[k] = *(const u32x4*)(pb + 2048 * k); ue[k] = *(const u32x4*)(pe + 2048 * k); }
#pragma unroll
        for (int k = 4; k < 8; ++k) A.b[k] = *(const u32x4*)(pb + 2048 * k);
        __builtin_amdgcn_sched_barrier(0);
        const f32x4 z4 = {0, 0, 0, 0};
#pragma unroll
        for (int k = 0; k < 4; ++k) { s7[k] = mfma_h(A.b[k], b7, z4); acc[4 + k] = mfma_h(a32, A.b[k], z4); s9[k] = mfma_h(ue[k], b9, z4); acc[k] = z4; }
        __builtin_amdgcn_sched_barrier(0);
      }
#pragma unroll
      for (int a0 = 0; a0 < 8; a0 += 2) {
        ldc(B, a0 + 1); mmc(A);
        if (a0 == 0) {
          u32x4 g[4];
#pragma unroll
          for (int k = 0; k < 4; ++k)
#pragma unroll
            for (int e = 0; e < 4; ++e) g[k][e] = pkh(f32x2{s7[k][3 - e], s9[k][3 - e]});
#pragma unroll
          for (int k = 0; k < 4; ++k)
#pragma unroll
            for (int t = k; t < 4; ++t) acc[4 + t] = mfma_h(g[k], tf[t - k], acc[4 + t]);
          __builtin_amdgcn_sched_barrier(0);
        }
        if (a0 < 6) ldc(A, a0 + 2);
        mmc(B);
      }
      SB;
#pragma unroll
      for (int t = 0; t < 8; ++t) *(u32x2*)(sk + t * 512 + li * 32 + q * 8) = u32x2{pk2(lo2(acc[t]) * sc), pk2(hi2(acc[t]) * sc)};
    } else {
#pragma unroll
      for (int k = 0; k < 4; ++k) *(u32x4*)(sk + CV_X + lane * 64 + k * 16) = u32x4{xc8[2 * k][0], xc8[2 * k][1], xc8[2 * k + 1][0], xc8[2 * k + 1][1]};
    }
    bar();
#pragma unroll
    for (int r = 0; r < 2; ++r) {
      const int T = (tid >> 3) + 64 * r, ip = tid & 7;
      const int t0 = 32 * (T >> 1) + 8 * (ip >> 1) + 4 * (T & 1) + 2 * (ip & 1);
      const char* p = sm + t0 * 2;
      const u32x4 xv = *(const u32x4*)(sm + (t0 >> 9) * CV_K + CV_X + (t0 & 511) * 8);
      const unsigned c0 = *(const unsigned*)p, c1 = *(const unsigned*)(p + CV_K), c2 = *(const unsigned*)(p + 2 * CV_K), c3 = *(const unsigned*)(p + 3 * CV_K);
      u32x4 v = {pk2(up2(xv[0]) + f32x2{bf_lo(c0), bf_lo(c1)}), pk2(up2(xv[1]) + f32x2{bf_lo(c2), bf_lo(c3)}),
                 pk2(up2(xv[2]) + f32x2{bf_hi(c0), bf_hi(c1)}), pk2(up2(xv[3]) + f32x2{bf_hi(c2), bf_hi(c3)})};
      st16(R, (T >> 3) * (18 * 8192) + (T & 7) * 1024 + ip * 16, O_HF + (cg >> 3) * 8192 + (cg & 1) * 512 + ((cg >> 1) & 3) * 128, v);
    }
    SB;
    arrive(1, 8, O_F2 + cg * 8, F2S, NREP);
    SB;
  } else {
    const int tj = (ii - 18) * 8 + xc;
    const int kc = tj / 36, cb = tj % 36;
    if (tj < 64) {
      const int row = 32 * tj + 4 * w + q;
      const char* src = x + (size_t)row * 1152 + li * 8;
      u32x2 xr[9];
#pragma unroll
      for (int k = 0; k < 9; ++k) xr[k] = *(const u32x2*)(src + k * 128);
      tag = mk_tag();
      float a[4] = {0, 0, 0, 0};
#pragma unroll
      for (int k = 0; k < 9; ++k)
#pragma unroll
        for (int e = 0; e < 2; ++e) { a[e] = dot2(xr[k][e], 0x3F803F80u, a[e]); a[2 + e] = dot2(xr[k][e], xr[k][e], a[2 + e]); }
      const float mean = row16_sum(a[0] + a[1]) * (1.0f / D), var = fmaxf(row16_sum(a[2] + a[3]) * (1.0f / D) - mean * mean, 0.f);
      st8(R, li * 16384 + row * 8, O_LS, u32x2{pk_bf16(mean, __builtin_amdgcn_sqrtf(rbf(rbf(var) + EPS))), (q & 1) ? tag[1] : tag[0]});
      SB;
      for (int i = 0; i < WDELAY - 3; ++i) __builtin_amdgcn_s_sleep(16);
    } else
      for (int i = 0; i < WDELAY; ++i) __builtin_amdgcn_s_sleep(16);
    char* s0 = sm + 65536;
    const int G = max(tj - 40, 0), par = G & 1;
    {
      const int row = tid >> 4, uu = G / 9, idx = G % 9, hh = row >> 4;
      const int panel = idx < 4 ? 2 * uu : (idx < 8 ? 2 * uu + 1 : 2 * uu + hh), tile = idx < 8 ? 2 * (idx & 3) + hh : 8;
      const char* src = w_in + (size_t)(192 * min(kc, 2) + row) * 9216 + cb * 256 + li * 16;
      const char* wsrc = w_out + (size_t)(144 * panel + 16 * tile + (row & 15)) * 1152 + li * 8;
      u32x4 v[6]; u32x2 vo[9];
#pragma unroll
      for (int k = 0; k < 6; ++k) v[k] = *(const u32x4*)(src + (size_t)k * 32 * 9216);
      if (tj >= 40) {
#pragma unroll
        for (int k = 0; k < 9; ++k) vo[k] = *(const u32x2*)(wsrc + k * 128);
      }
#pragma unroll
      for (int k = 0; k < 6; ++k) *(u32x4*)(sm + (row + 32 * k) * 272 + li * 16) = v[k];
      if (tj >= 40) {
#pragma unroll
        for (int k = 0; k < 9; ++k) *(u32x2*)(s0 + row * TP + li * 8 + k * 128) = vo[k];
      }
    }
    tag = mk_tag();
    bar();
    SB;
    if (tj < 108) {
      const int ct = 8 * cb + w;
      const int hf = ct < 144 ? 1 : 0, cl = ct < 144 ? ct : ct - 144, fp = cl / 9, n = cl % 9;
      const char* trb = sm + (8 * q + (li >> 2)) * 272 + w * 32 + (li & 3) * 8;
      const unsigned dst = lane * 16 + O_WI + (((fp * 2 + hf) * 18 + 6 * kc) * 9 + n) * 1024;
      float s = 0.f;
#pragma unroll
      for (int g = 0; g < 6; ++g) {
        u32x2 lo = tr_read(trb + g * 32 * 272), hi = tr_read(trb + g * 32 * 272 + 4 * 272);
        st16(R, dst, g * 9216, u32x4{lo[0], lo[1], hi[0], hi[1]});
        s = dot2(lo[0], 0x3F803F80u, s); s = dot2(lo[1], 0x3F803F80u, s); s = dot2(hi[0], 0x3F803F80u, s); s = dot2(hi[1], 0x3F803F80u, s);
      }
      s = q_sum(s);
      if (q == 0) st4(R, li * 4, O_CS + kc * F * 4 + ct * 64, asu(s));
    }
    if (tj >= 40) {
      const bool mixed = G % 9 == 8;
      const char* trb = s0 + ((mixed ? 16 * (q >> 1) + 4 * (q & 1) : 4 * q) + (li >> 2)) * TP + (li & 3) * 8;
      const char* trh = trb + (mixed ? 8 : 16) * TP;
      const unsigned dst = lane * 16 + O_WO + (w * 36 + (G >> 1)) * 9216;
#pragma unroll
      for (int n = 0; n < 5; ++n) {
        const int c0 = 72 * w + 16 * n - (n == 4 ? 8 * par : 0);
        u32x2 lo = tr_read(trb + c0 * 2), hi = tr_read(trh + c0 * 2);
        if (n < 4) st16(R, dst, (4 * par + n) * 1024, u32x4{lo[0], lo[1], hi[0], hi[1]});
        else if ((li >> 3) == par) st16(R, dst, 8192, u32x4{lo[0], lo[1], hi[0], hi[1]});
      }
    }
    SB;
    arrive(0, 8, O_F2 + (144 + tj) * 8, F2S, NREP);
    SB;
  }
  const int tp = (xc & 1) * 8 + (ii >> 2), fp = (xc >> 1) * 4 + (ii & 3);
  const int t64 = 4 * xc + (ii >> 3), j3 = ii & 7;
  if (w == 0) { wait_flags<1>(R, O_F2 + rep * F2S + 144 * 8, 112, tag, lane); SB; }
  bar();
  if (w >= 4) {
    const int lw = w - 4;
    const unsigned l16 = lane * 16, base = lds_addr(sm);
    const int ph = ((ii >> 2) + ii) & 3, ph3 = ((ii >> 3) + ii) & 3;
    auto k9 = [&](int t) { return (t & 3) == ph ? (t + 4 < 9 ? t + 4 : ph) : t; };
    auto k32 = [&](int t) { return (t & 3) == ph ? ((t + 4) & 31) : t; };
    auto k18 = [&](int t) { return (t & 3) == ph3 ? (t + 4 < 18 ? t + 4 : ph3) : t; };
    if (lw < 2) {
      constexpr int NP = 8;
      unsigned vo[8];
#pragma unroll
      for (int j = 0; j < 8; ++j) {
        int t = 64 * lw + 32 * (j >> 2) + 4 * ((j >> 1) & 1) + 16 * (j & 1) + 8 * (lane >> 5) + ((lane >> 3) & 3);
        vo[j] = t * 4096 + (((lane & 7) ^ ((lane >> 3) & 7)) * 16) + 3072 - (j & 3) * 1024;
      }
      const char* wsrc = w_sp + (size_t)tp * 128 * 4096 - 3072;
      const char* hsrc = ws + O_HF + tp * 18 * 8192 + lw * 8192;
      const char* psrc = ws + O_PF + t64 * 72 * 4096 + lw * 8192;
      const unsigned dst = base + lw * 8192;
      if (lw == 0) wait_flags<2>(R, O_F2 + rep * F2S, 144, tag, lane);
      SB;
      bar();
#pragma unroll 1
      for (int t = 0; t < 4; ++t) ld_lin<NP>(dst + t * STG, hsrc + k9(t) * 16384, l16);
      wait_vm<3 * NP>(); bar();
#pragma unroll 1
      for (int t = 4; t < 18; ++t) { wait_vm<2 * NP>(); bar(); ld_lin<NP>(dst + (t & 3) * STG, hsrc + k9(t < 9 ? t : t - 9) * 16384, l16); }
#pragma unroll 1
      for (int t = 18; t < 50; ++t) {
        wait_vm<2 * NP>(); bar();
        const unsigned dd = dst + (t & 3) * STG; const char* sp = wsrc + k32(t - 18) * 128;
        glds4<false>(dd, sp, vo[0], vo[1], vo[2], vo[3]); glds4<false>(dd + 4096, sp, vo[4], vo[5], vo[6], vo[7]);
      }
      wait_vm<2 * NP>(); bar(); wait_vm<NP>(); bar(); wait_vm<0>(); bar();
      bar(); bar();
      wait_flags<1>(R, O_PFL + (t64 >> 1) * 2048 + ((t64 & 1) * 8 + j3) * 128, 16, tag, lane);
      SB;
#pragma unroll 1
      for (int t = 50; t < 68; ++t) {
        if (t >= 53) { wait_vm<2 * NP>(); bar(); }
        ld_lin<NP>(dst + (t & 3) * STG, psrc + k18(t - 50) * 16384, l16);
      }
      wait_vm<2 * NP>(); bar(); wait_vm<NP>(); bar(); wait_vm<0>(); bar();
    } else {
      constexpr int NP = 9;
      const char* wi = ws + O_WI + (fp * 2) * 18 * 9216 + (lw - 2) * 9216;
      const char* zs = ws + O_ZF + fp * 64 * 9216 + (lw - 2) * 9216;
      const char* wo = ws + O_WO + j3 * 36 * 9216 + (lw - 2) * 9216;
      const unsigned dst = base + 16384 + (lw - 2) * 9216;
#pragma unroll 1
      for (int t = 0; t < 4; ++t) ld_lin<NP>(dst + t * STG, wi + k9(t) * 18432, l16);
      bar();
      wait_vm<3 * NP>(); bar();
      if (lw == 3) {
        f32x2 a = *(const f32x2*)(sm + X_LN + lane * 8) + *(const f32x2*)(sm + X_LN + 512 + lane * 8);
        a[0] = q_sum(dpp_add<0x128>(a[0])); a[1] = q_sum(dpp_add<0x128>(a[1]));
        float mean = a[0] * (1.0f / D), mb, ri;
        ln_scalars(mean, fmaxf(a[1] * (1.0f / D) - mean * mean, 0.f), mb, ri);
        if (lane < 8) st8(R, lane * 8, O_HS + tp * 1024 + fp * 64, u32x2{asu(ri) == SENT ? 0x7FC00000u : asu(ri), asu(-mb)});
      }
#pragma unroll 1
      for (int t = 4; t < 18; ++t) { wait_vm<2 * NP>(); bar(); ld_lin<NP>(dst + (t & 3) * STG, wi + (t < 9 ? k9(t) : 9 + k9(t - 9)) * 18432, l16); }
      wait_vm<2 * NP>(); bar(); wait_vm<NP>(); bar(); wait_vm<0>(); bar();
      wait_flags<1>(R, O_ZFL + fp * 2048 + tp * 128, 16, tag, lane);
      SB;
#pragma unroll 1
      for (int t = 18; t < 53; ++t) {
        if (t >= 21) { wait_vm<2 * NP>(); bar(); }
        ld_lin<NP>(dst + (t & 3) * STG, t < 50 ? zs + k32(t - 18) * 18432 : wo + k18(t - 50) * 18432, l16);
      }
      wait_vm<0>(); bar();
      bar();
#pragma unroll 1
      for (int t = 53; t < 57; ++t) ld_lin<NP>(dst + (t & 3) * STG, wo + k18(t - 50) * 18432, l16);
      bar(); bar(); bar();
      wait_vm<3 * NP>(); bar();
#pragma unroll 1
      for (int t = 57; t < 68; ++t) { wait_vm<2 * NP>(); bar(); ld_lin<NP>(dst + (t & 3) * STG, wo + k18(t - 50) * 18432, l16); }
      wait_vm<2 * NP>(); bar(); wait_vm<NP>(); bar(); wait_vm<0>(); bar();
    }
    return;
  }
  Ctx c;
  c.sm = sm;
  c.am0 = 2 * w * 1024 + lane * 8;
  c.am1 = 2 * w * 2048 + li * 128 + ((q ^ (lane & 7)) * 16);
  c.an = 16384 + lane * 16;
  const int prow = 8 * (li >> 2) + (li & 3);
  if (tid < 72) {
    const unsigned o = tid < 36 ? FH * 4 + tid * 16 : (tid - 36) * 16;
    const u32x4 c0 = ld16(R, o, O_CS + fp * 576), c1 = ld16(R, o, O_CS + F * 4 + fp * 576), c2 = ld16(R, o, O_CS + F * 8 + fp * 576);
    *(f32x4*)(sm + X_CS + tid * 16) = (__builtin_bit_cast(f32x4, c0) + __builtin_bit_cast(f32x4, c1)) + __builtin_bit_cast(f32x4, c2);
  }
  bar();
  char* pvb = sm + X_PV + w * 4096;
  {
    u32x4 hv[3];
#pragma unroll
    for (int k = 0; k < 3; ++k)
      hv[k] = ld16(R, (lane >> 5) * 8192 + ((lane >> 2) & 7) * 128 + (lane & 3) * 16, O_HF + (tp * 18 * 8 + (fp >> 1)) * 1024 + (fp & 1) * 64 + min(w + 4 * k, 8) * 16384);
    if (w) hv[2] = u32x4{0, 0, 0, 0};
    f32x4 a = {0, 0, 0, 0};
#pragma unroll
    for (int k = 0; k < 3; ++k)
#pragma unroll
      for (int e = 0; e < 4; ++e) { a[e & 2] = dot2(hv[k][e], 0x3F803F80u, a[e & 2]); a[(e & 2) + 1] = dot2(hv[k][e], hv[k][e], a[(e & 2) + 1]); }
#pragma unroll
    for (int e = 0; e < 4; ++e) a[e] = dpp_add<0x124>(dpp_add<0x128>(a[e]));
    if (li < 4) *(f32x4*)(sm + X_LN + (w * 4 + q) * 64 + li * 16) = a;
  }
  f32x4 acc[2][9];
  Fr X, Y;
  bar();
  SB;
  ldf<0>(X, c, 0, 0);
  seg<0, 0, true, true>(acc, X, Y, c, 0, 1);
  const unsigned hs_m0 = __builtin_amdgcn_readfirstlane(lds_addr(sm) + X_HS + w * 256);
  auto ld_hs = [&]() {
    unsigned keep;
    asm volatile("s_mov_b32 %0, m0\n\ts_mov_b32 m0, %1\n\ts_nop 2\n\tglobal_load_lds_dword %3, %2 sc1\n\ts_mov_b32 m0, %0"
                 : "=&s"(keep) : "s"(hs_m0), "s"(ws), "v"(O_HS + tp * 1024 + tid * 4) : "memory");
  };
  ALIGNL;
#pragma unroll 1
  for (int t = 0; t < 8; ++t) {
    if (t == 5) ld_hs();
    bar();
    seg<0, 0, true>(acc, Y, X, c, t + 1, 0);
    seg<0, 0, true>(acc, X, Y, c, t + 1, 1);
  }
  u32x4 hc[4];
  float cs[9];
  auto rd_hs = [&]() {
    wait_vm<0>();
#pragma unroll
    for (int jp = 0; jp < 4; ++jp) hc[jp] = *(const u32x4*)(sm + X_HS + w * 256 + (jp >> 1) * 128 + q * 32 + (jp & 1) * 16);
  };
  rd_hs();
#pragma unroll
  for (int n = 0; n < 9; ++n) cs[n] = *(const float*)(sm + X_CS + (16 * n + li) * 4);
  bar();
  seg<0, 0, true>(acc, Y, X, c, 9, 0);
  SB;
#pragma unroll 1
  for (int spin = 0; spin < SPIN; ++spin) {
    if (__all(max(max(umax4(hc[0]), umax4(hc[1])), max(umax4(hc[2]), umax4(hc[3]))) != SENT)) break;
    ld_hs();
    rd_hs();
  }
  u32x4 zb[9];
  f32x2 rs[4];
  {
    f32x2 nm[4], k1[4], k0[4], S1[4] = {}, S2[4] = {};
#pragma unroll
    for (int jp = 0; jp < 4; ++jp) {
      rs[jp] = f32x2{asf(hc[jp][0]), asf(hc[jp][2])}; nm[jp] = f32x2{asf(hc[jp][1]), asf(hc[jp][3])};
      k0[jp] = rs[jp] * GC0; k1[jp] = rs[jp] * rs[jp] * rs[jp] * GC1;
    }
#pragma unroll
    for (int n = 0; n < 9; ++n) {
#pragma unroll
      for (int jp = 0; jp < 4; ++jp) {
        f32x2 a = (jp & 1) ? hi2(acc[jp >> 1][n]) : lo2(acc[jp >> 1][n]);
        f32x2 z = gelu2(__builtin_elementwise_fma(nm[jp], f32x2{cs[n], cs[n]}, a), k1[jp], k0[jp]);
        zb[n][jp] = pk2(z);
        S1[jp] += z; S2[jp] = __builtin_elementwise_fma(z, z, S2[jp]);
      }
      asm volatile("" : "+v"(zb[n]));
    }
#pragma unroll
    for (int jp = 0; jp < 4; ++jp) { S1[jp] *= rs[jp]; S2[jp] *= rs[jp] * rs[jp]; }
    float v[16];
#pragma unroll
    for (int j = 0; j < 8; ++j) { v[2 * j] = S1[j >> 1][j & 1]; v[2 * j + 1] = S2[j >> 1][j & 1]; }
#pragma unroll
    for (int i = 0; i < 8; ++i) { bool h = li & 8; v[i] = (h ? v[i + 8] : v[i]) + dpp_get<0x128>(h ? v[i] : v[i + 8]); }
#pragma unroll
    for (int i = 0; i < 4; ++i) { bool h = li & 4; v[i] = (h ? v[i + 4] : v[i]) + dpp_get<0x141>(h ? v[i] : v[i + 4]); }
#pragma unroll
    for (int i = 0; i < 2; ++i) { bool h = li & 2; v[i] = (h ? v[i + 2] : v[i]) + dpp_get<0x4E>(h ? v[i] : v[i + 2]); }
    { bool h = li & 1; v[0] = (h ? v[1] : v[0]) + dpp_get<0xB1>(h ? v[0] : v[1]); }
    unsigned tot = asu(v[0]);
    st4(R, lane * 4, O_ST + ((tp * 4 + w) * 16 + fp) * 256, tot == SENT ? 0x7FC00000u : tot);
  }
  SB;
  const char* sts = ws + O_ST + (tp * 4 + w) * 4096;
  seg<0, 1, true, true>(acc, X, Y, c, 9, 1);
  ALIGNL;
#pragma unroll 1
  for (int t = 9; t < TCHK; ++t) {
    if (t == TPOLL) glds4<true>(lds_addr(pvb), sts, lane * 16, lane * 16, lane * 16, lane * 16);
    bar();
    seg<0, 1, true>(acc, Y, X, c, t + 1, 0);
    seg<0, 1, true>(acc, X, Y, c, t + 1, 1);
  }
  SB;
  u32x4 pv[4];
#pragma unroll 1
  for (int spin = 0; spin < SPIN; ++spin) {
    wait_vm<0>();
#pragma unroll
    for (int k = 0; k < 4; ++k) pv[k] = *(const u32x4*)(pvb + k * 1024 + lane * 16);
    if (__all(max(max(umax4(pv[0]), umax4(pv[1])), max(umax4(pv[2]), umax4(pv[3]))) != SENT)) break;
    glds4<true>(lds_addr(pvb), sts, lane * 16, lane * 16, lane * 16, lane * 16);
  }
  SB;
  {
    float t4[4];
#pragma unroll
    for (int e = 0; e < 4; ++e) t4[e] = q_sum((asf(pv[0][e]) + asf(pv[1][e])) + (asf(pv[2][e]) + asf(pv[3][e])));
    f32x2 o0, o1;
#pragma unroll
    for (int r = 0; r < 2; ++r) {
      float mean = t4[2 * r] * (1.0f / FH), var = fmaxf(t4[2 * r + 1] * (1.0f / FH) - mean * mean, 0.f), mb, ri;
      ln_scalars(mean, var, mb, ri);
      o0[r] = ri; o1[r] = -mb * ri;
    }
    char* ln = sm + X_LN + w * 256;
    if (lane < 16) { *(f32x2*)(ln + li * 8) = o0; *(f32x2*)(ln + 128 + li * 8) = o1; }
    asm volatile("" ::: "memory");
    f32x4 ri[2], rb[2];
#pragma unroll
    for (int m = 0; m < 2; ++m) { ri[m] = *(const f32x4*)(ln + (8 * q + 4 * m) * 4); rb[m] = *(const f32x4*)(ln + 128 + (8 * q + 4 * m) * 4); }
#pragma unroll
    for (int jp = 0; jp < 4; ++jp) rs[jp] *= (jp & 1) ? hi2(ri[jp >> 1]) : lo2(ri[jp >> 1]);
    const unsigned zo = lane * 16 + O_ZF + (fp * 64 + 4 * tp + w) * 9216;
#pragma unroll
    for (int n = 0; n < 9; ++n) {
      u32x4 o4;
#pragma unroll
      for (int jp = 0; jp < 4; ++jp)
        o4[jp] = pk2(__builtin_elementwise_fma(up2(zb[n][jp]), rs[jp], (jp & 1) ? hi2(rb[jp >> 1]) : lo2(rb[jp >> 1])));
      st16(R, zo, n * 1024, o4);
    }
  }
  SB;
  ALIGNL;
#pragma unroll 1
  for (int t = TCHK; t < 17; ++t) {
    if (t == TFLAG) wait_vm<0>();
    bar();
    if (t == TFLAG && tid < 16) st8(R, tid * 128, O_ZFL + fp * 2048 + tp * 8, tag);
    seg<0, 1, true>(acc, Y, X, c, t + 1, 0);
    seg<0, 1, true>(acc, X, Y, c, t + 1, 1);
  }
  float lrs[2], lnm[2];
  f32x4 cs1[9];
#pragma unroll
  for (int m = 0; m < 2; ++m) { const f32x2 h = *(const f32x2*)(sm + X_HS + w * 256 + m * 128 + li * 8); lrs[m] = h[0]; lnm[m] = h[1]; }
#pragma unroll
  for (int n = 0; n < 9; ++n) cs1[n] = *(const f32x4*)(sm + X_CS + (144 + 16 * n + 4 * q) * 4);
  __builtin_amdgcn_sched_barrier(0);
  seg<0, 1, false>(acc, Y, X, c, 17, 1);
  SB;
  u32x2 z1[2][9];
  float bs[2];
#pragma unroll
  for (int m = 0; m < 2; ++m) bs[m] = ldbf(b_sp, 128 * tp + 32 * w + prow + 4 * m) * lrs[m];
  {
#pragma unroll
    for (int n = 0; n < 9; ++n)
#pragma unroll
      for (int m = 0; m < 2; ++m) {
        const float a1 = lrs[m] * lrs[m] * lrs[m] * GC1, a0 = lrs[m] * GC0;
        const f32x2 k1 = {a1, a1}, k0 = {a0, a0}, nm = {lnm[m], lnm[m]};
        z1[m][n][0] = pk2(gelu2(__builtin_elementwise_fma(nm, lo2(cs1[n]), lo2(acc[m][n])), k1, k0));
        z1[m][n][1] = pk2(gelu2(__builtin_elementwise_fma(nm, hi2(cs1[n]), hi2(acc[m][n])), k1, k0));
        asm volatile("" : "+v"(z1[m][n]));
      }
  }
  SB;
  bar();
  SB;
  ldf<1>(X, c, 18, 0);
  seg<1, 1, true, true>(acc, X, Y, c, 18, 1);
  ALIGNL;
#pragma unroll 1
  for (int t = 18; t < 49; ++t) {
    bar();
    seg<1, 1, true>(acc, Y, X, c, t + 1, 0);
    seg<1, 1, true>(acc, X, Y, c, t + 1, 1);
  }
  seg<1, 1, false>(acc, Y, X, c, 49, 1);
  SB;
  {
    const unsigned pf = O_PF + (((2 * tp + (w >> 1)) * 72 + 9 * (fp >> 1)) * 4 + 2 * (w & 1)) * 1024;
    const unsigned po = lane * 16 + pf + (fp & 1) * 16384;
    u32x2 p8[2];
#pragma unroll
    for (int m = 0; m < 2; ++m) {
      const f32x2 r2 = {lrs[m], lrs[m]}, b2 = {bs[m], bs[m]};
      auto pp = [&](int n) { return u32x2{pk2(__builtin_elementwise_fma(lo2(acc[m][n]), r2, b2) * up2(z1[m][n][0])), pk2(__builtin_elementwise_fma(hi2(acc[m][n]), r2, b2) * up2(z1[m][n][1]))}; };
#pragma unroll
      for (int a = 0; a < 4; ++a) {
        u32x2 p0 = pp(2 * a), p1 = pp(2 * a + 1);
        st16(R, po, a * 4096 + m * 1024, u32x4{p0[0], p0[1], p1[0], p1[1]});
      }
      p8[m] = pp(8);
    }
    const auto s0 = __builtin_amdgcn_permlane32_swap(p8[0][0], p8[1][0], false, false), s1 = __builtin_amdgcn_permlane32_swap(p8[0][1], p8[1][1], false, false);
    st16(R, (lane >> 5) * 1024 + (lane & 31) * 16, pf + 8 * 4096 + (fp & 1) * 512, u32x4{s0[0], s1[0], s0[1], s1[1]});
  }
  SB;
  const int th = w & 1, kq = w >> 1, T = 4 * t64 + 2 * th + kq;
  const int lane5 = fresh_lane();
#define lane lane5
#define li (lane5 & 15)
#define q (lane5 >> 4)
#define prow (8 * (li >> 2) + (li & 3))
  struct P3 { u32x4 p[2][2]; } Up, Vp;
  struct N3 { u32x4 n[9]; } Un, Vn, En;
  const int ap = (8 * kq + 2 * th) * 1024 + lane * 16, aw = 16384 + kq * 9216 + lane * 16;
  auto ldp = [&](P3& f, int t) {
#pragma unroll
    for (int g = 0; g < 2; ++g)
#pragma unroll
      for (int m = 0; m < 2; ++m) f.p[g][m] = *(const u32x4*)(sm + (t & 3) * STG + ap + (4 * g + m) * 1024);
  };
  auto ldn = [&](N3& f, int t) {
#pragma unroll
    for (int n = 0; n < 9; ++n) f.n[n] = *(const u32x4*)(sm + (t & 3) * STG + aw + n * 1024);
  };
  bar();
  ldn(Un, 50); ldn(Vn, 51); ldn(En, 52);
  arrive(2, 4, O_PFL + tp * 2048 + fp * 8, 128, 16);
  bar();
  u32x2 hv[5];
#pragma unroll
  for (int n = 0; n < 5; ++n) {
    const int dd = 72 * j3 + 16 * n + 4 * (n == 4 ? (q & 1) : q);
    hv[n] = ld8(R, (dd >> 5) * 8192 + ((dd >> 2) & 1) * 512 + (((dd >> 3) & 3) * 16 + li) * 8, O_HF + ((T >> 3) * 18 * 8 + (T & 7)) * 1024);
  }
  f32x4 a3[2][6] = {};
  auto mm3 = [&](const P3& f, const N3& e) {
#pragma unroll
    for (int g = 0; g < 2; ++g)
#pragma unroll
      for (int m = 0; m < 2; ++m) {
#pragma unroll
        for (int n = 0; n < 4; ++n) a3[m][n] = mfma_bf(e.n[4 * g + n], f.p[g][m], a3[m][n]);
        a3[m][4 + g] = mfma_bf(e.n[8], f.p[g][m], a3[m][4 + g]);
      }
#pragma unroll
    for (int i = 0; i < 13; ++i) { __builtin_amdgcn_sched_group_barrier(0x008, 1, 0); __builtin_amdgcn_sched_group_barrier(0x100, 1, 0); }
    __builtin_amdgcn_sched_barrier(0);
  };
  SB;
  bar();
  SB;
  ldp(Up, 50);
  bar(); ldp(Vp, 51); mm3(Up, Un);
  bar(); ldp(Up, 52); mm3(Vp, Vn);
  bar(); ldp(Vp, 53); ldn(Vn, 53); mm3(Up, En);
  ALIGNL;
#pragma unroll 1
  for (int t = 53; t < 67; t += 2) { bar(); ldp(Up, t + 1); ldn(Un, t + 1); mm3(Vp, Vn); bar(); ldp(Vp, t + 2); ldn(Vn, t + 2); mm3(Up, Un); }
  mm3(Vp, Vn);
  SB;
#pragma unroll
  for (int m = 0; m < 2; ++m) a3[m][4] = lane < 32 ? a3[m][4] : a3[m][5];
  if (kq) {
#pragma unroll
    for (int n = 0; n < 5; ++n) { *(f32x4*)(sm + w * 5120 + n * 1024 + lane * 16) = a3[0][n]; asm volatile("" : "+v"(a3[1][n])); a3[0][n] = a3[1][n]; }
  } else {
#pragma unroll
    for (int n = 0; n < 5; ++n) *(f32x4*)(sm + w * 5120 + n * 1024 + lane * 16) = a3[1][n];
  }
  bar();
  char* o = out + (size_t)(32 * (T >> 1) + 4 * (T & 1) + prow) * 1152 + (72 * j3 + 4 * q) * 2;
#pragma unroll
  for (int n = 0; n < 5; ++n) {
    f32x4 v = a3[0][n] + *(const f32x4*)(sm + (w ^ 2) * 5120 + n * 1024 + lane * 16);
    if (n == 4) v = f32x4{half_sum(v[0]), half_sum(v[1]), half_sum(v[2]), half_sum(v[3])};
    if (n < 4 || q < 2) *(u32x2*)(o + n * 32) = u32x2{pk2(up2(hv[n][0]) + lo2(v)), pk2(up2(hv[n][1]) + hi2(v))};
  }
  if (bid == 0 && w == 0 && lane == 0) { u64 e = (((u64)tag[1] << 32) | tag[0]) - 0x9E3779B97F4A7C15ull + 0x100000001ull;       st8(R, 0, O_EP, u32x2{(unsigned)e, (unsigned)(e >> 32)}); }
  SB;
}

#undef lane
#undef li
#undef q
#undef prow
extern "C" void kernel_launch(void* const* d_in, const int* in_sizes, int n_in, void* d_out, int out_size, void* d_ws, size_t ws_size,
                              hipStream_t stream) {
  (void)hipFuncSetAttribute((const void*)k_all, hipFuncAttributeMaxDynamicSharedMemorySize, LDS_BYTES);
  k_all<<<256, 512, LDS_BYTES, stream>>>((const char*)d_in[0], (const char*)d_in[1], (const char*)d_in[2], (const char*)d_in[3],
      (const char*)d_in[4], (const char*)d_in[5], (const char*)d_in[6], (const char*)d_in[7], (const char*)d_in[8], (const char*)d_in[10],
      (const char*)d_in[11], (const char*)d_in[12], (const char*)d_in[13], (char*)d_out, (char*)d_ws);
}
```

```cpp
#include <hip/hip_runtime.h>
#include <hip/hip_bf16.h>
#include <stdint.h>

typedef __attribute__((ext_vector_type(8))) __bf16 bf16x8;
typedef __attribute__((ext_vector_type(2))) __bf16 bf16x2;
typedef __attribute__((ext_vector_type(8))) _Float16 f16x8;
typedef __attribute__((ext_vector_type(2))) _Float16 f16x2;
typedef __attribute__((ext_vector_type(4))) float f32x4;
typedef __attribute__((ext_vector_type(2))) float f32x2;
typedef __attribute__((ext_vector_type(4))) unsigned u32x4;
typedef __attribute__((ext_vector_type(2))) unsigned u32x2;
typedef __attribute__((ext_vector_type(4))) short s16x4;
#define LDSP __attribute__((address_space(3)))
#define DEVI __device__ __forceinline__

constexpr int L = 2048, D = 576, NM = 128, F = 4608, FH = 2304;
constexpr float EPS = 1e-5f;
constexpr unsigned O_EP = 0;
constexpr int NREP = 64;
constexpr unsigned O_F2 = 256;
constexpr unsigned F2S = 2048;
constexpr unsigned O_ZFL = O_F2 + NREP * F2S;
constexpr unsigned O_PFL = O_ZFL + 32768;
constexpr unsigned O_CS = O_PFL + 32768;
constexpr unsigned O_ST = O_CS + F * 12;
constexpr unsigned O_HS = O_ST + 262144;
constexpr unsigned O_LS = O_HS + 16384;
constexpr unsigned O_WI = O_LS + 16 * 16384;
constexpr unsigned O_WO = O_WI + D * F * 2;
constexpr unsigned O_HF = O_WO + FH * D * 2;
constexpr unsigned O_ZF = O_HF + L * D * 2;
constexpr unsigned O_PF = O_ZF + L * FH * 2;
constexpr unsigned WS_USED = O_PF + L * FH * 2;
constexpr size_t O_STAMP = 128u << 20;
#define STAMP(k,i) __builtin_amdgcn_sched_barrier(0)
#define STAMPW(k,i,W) __builtin_amdgcn_sched_barrier(0)
#define RSTAMP(k,i)
constexpr unsigned SENT = 0xFFFFFFFFu;

DEVI unsigned pk_bf16(float a, float b) { f32x2 v = {a, b}; return __builtin_bit_cast(unsigned, __builtin_convertvector(v, bf16x2)); }
DEVI unsigned pk_f16(float a, float b) { f32x2 v = {a, b}; return __builtin_bit_cast(unsigned, __builtin_convertvector(v, f16x2)); }
DEVI float bf_lo(unsigned u) { return __builtin_bit_cast(float, u << 16); }
DEVI float bf_hi(unsigned u) { return __builtin_bit_cast(float, u & 0xFFFF0000u); }
DEVI float asf(unsigned u) { return __builtin_bit_cast(float, u); }
DEVI unsigned asu(float f) { return __builtin_bit_cast(unsigned, f); }
DEVI float rbf(float a) { return bf_lo(pk_bf16(a, a)); }
DEVI float h_lo(unsigned u) { return (float)__builtin_bit_cast(f16x2, u)[0]; }
DEVI float h_hi(unsigned u) { return (float)__builtin_bit_cast(f16x2, u)[1]; }
template <int CTRL> DEVI float dpp_add(float v) {
  return v + __builtin_bit_cast(float, __builtin_amdgcn_update_dpp(0, __builtin_bit_cast(int, v), CTRL, 0xF, 0xF, true));
}
DEVI float row16_sum(float v) { v = dpp_add<0x128>(v); v = dpp_add<0x124>(v); v = dpp_add<0x122>(v); v = dpp_add<0x121>(v); return v; }
DEVI float half_sum(float v) { auto r = __builtin_amdgcn_permlane32_swap(asu(v), asu(v), false, false); return asf(r[0]) + asf(r[1]); }
DEVI float q_sum(float v) { auto r = __builtin_amdgcn_permlane16_swap(asu(v), asu(v), false, false); return half_sum(asf(r[0]) + asf(r[1])); }
DEVI unsigned umax4(u32x4 v) { return max(max(v[0], v[1]), max(v[2], v[3])); }
DEVI u32x2 tr_read(const char* p) {
  return __builtin_bit_cast(u32x2, __builtin_amdgcn_ds_read_tr16_b64_v4i16((LDSP s16x4*)p));
}
DEVI f32x4 mfma_bf(u32x4 a, u32x4 b, f32x4 c) {
  return __builtin_amdgcn_mfma_f32_16x16x32_bf16(__builtin_bit_cast(bf16x8, a), __builtin_bit_cast(bf16x8, b), c, 0, 0, 0);
}
DEVI f32x4 mfma_h(u32x4 a, u32x4 b, f32x4 c) {
  return __builtin_amdgcn_mfma_f32_16x16x32_f16(__builtin_bit_cast(f16x8, a), __builtin_bit_cast(f16x8, b), c, 0, 0, 0);
}
DEVI void ln_scalars(float mean, float var, float& mb, float& rinv) {
  mb = rbf(mean);
  float d = rbf(__builtin_amdgcn_sqrtf(rbf(rbf(var) + EPS)));
  rinv = __builtin_amdgcn_rcpf(d);
}
DEVI float gelu(float x) {
  float w = x * __builtin_fmaf(x * x, -0.10294324f, -2.3022082f);
  return x * __builtin_amdgcn_rcpf(1.0f + __builtin_amdgcn_exp2f(w));
}

typedef unsigned long long u64;
typedef __amdgpu_buffer_rsrc_t rsrc_t;
constexpr int SPIN = 1 << 16;
constexpr int CODE_LINES = 38;
template <int CTRL> DEVI float dpp_get(float v) {
  return __builtin_bit_cast(float, __builtin_amdgcn_update_dpp(0, __builtin_bit_cast(int, v), CTRL, 0xF, 0xF, true));
}
constexpr float GC1 = -0.10294324f, GC0 = -2.3022082f;
DEVI f32x2 gelu2(f32x2 x, f32x2 k1, f32x2 k0) {
  f32x2 w = x * __builtin_elementwise_fma(x * x, k1, k0);
  f32x2 d = f32x2{__builtin_amdgcn_exp2f(w[0]), __builtin_amdgcn_exp2f(w[1])} + f32x2{1.f, 1.f};
  return x * f32x2{__builtin_amdgcn_rcpf(d[0]), __builtin_amdgcn_rcpf(d[1])};
}
DEVI unsigned pk2(f32x2 v) { return __builtin_bit_cast(unsigned, __builtin_convertvector(v, bf16x2)); }
DEVI f32x2 up2(unsigned u) { return f32x2{bf_lo(u), bf_hi(u)}; }
DEVI f32x2 lo2(f32x4 v) { return __builtin_shufflevector(v, v, 0, 1); }
DEVI f32x2 hi2(f32x4 v) { return __builtin_shufflevector(v, v, 2, 3); }
DEVI float dot2(unsigned a, unsigned b, float c) {
  return __builtin_amdgcn_fdot2_f32_bf16(__builtin_bit_cast(bf16x2, a), __builtin_bit_cast(bf16x2, b), c, false);
}
DEVI void st16(rsrc_t r, unsigned voff, unsigned soff, u32x4 v) { __builtin_amdgcn_raw_buffer_store_b128(v, r, voff + soff, 0, 16); }
DEVI void st8(rsrc_t r, unsigned voff, unsigned soff, u32x2 v) { __builtin_amdgcn_raw_buffer_store_b64(v, r, voff, soff, 16); }
DEVI void st4(rsrc_t r, unsigned voff, unsigned soff, unsigned v) { __builtin_amdgcn_raw_buffer_store_b32(v, r, voff, soff, 16); }
DEVI u32x4 ld16(rsrc_t r, unsigned voff, unsigned soff) { return __builtin_amdgcn_raw_buffer_load_b128(r, voff, soff, 16); }
DEVI u32x2 ld8(rsrc_t r, unsigned voff, unsigned soff) { return __builtin_amdgcn_raw_buffer_load_b64(r, voff, soff, 16); }
template <int N> DEVI void wait_vm() { asm volatile("s_waitcnt vmcnt(%0)" ::"i"(N) : "memory"); }
DEVI void bar() { __builtin_amdgcn_sched_barrier(0); asm volatile("s_waitcnt lgkmcnt(0)\n\ts_barrier" ::: "memory"); __builtin_amdgcn_sched_barrier(0); }
DEVI int fresh_lane() { int l; asm volatile("v_mbcnt_lo_u32_b32 %0, -1, 0\n\tv_mbcnt_hi_u32_b32 %0, -1, %0" : "=v"(l)); return l; }
DEVI unsigned lds_addr(const char* p) { return (unsigned)(uintptr_t)(LDSP const char*)p; }
template <int NL> DEVI void wait_flags(rsrc_t r, unsigned off, int n, u32x2 tag, int lane) {
#pragma unroll 1
  for (int spin = 0; spin < SPIN; ++spin) {
    bool ok = true;
#pragma unroll
    for (int k = 0; k < NL; ++k) {
      int i = min(lane + 64 * k, n / 2 - 1);
      u32x4 v = ld16(r, i * 16, off);
      ok = ok && v[0] == tag[0] && v[1] == tag[1] && v[2] == tag[0] && v[3] == tag[1];
    }
    if (__all(ok)) break;
    asm volatile("" ::: "memory");
  }
  asm volatile("" ::: "memory");
}
template <bool SC> DEVI void glds4(unsigned m0v, const char* base, unsigned v0, unsigned v1, unsigned v2, unsigned v3) {
  unsigned keep;
  if (SC)
    asm volatile("s_mov_b32 %0, m0\n\ts_mov_b32 m0, %1\n\ts_nop 2\n\t"
                 "global_load_lds_dwordx4 %3, %2 sc1\n\tglobal_load_lds_dwordx4 %4, %2 offset:1024 sc1\n\t"
                 "global_load_lds_dwordx4 %5, %2 offset:2048 sc1\n\tglobal_load_lds_dwordx4 %6, %2 offset:3072 sc1\n\ts_mov_b32 m0, %0"
                 : "=&s"(keep) : "s"(m0v), "s"(base), "v"(v0), "v"(v1), "v"(v2), "v"(v3) : "memory");
  else
    asm volatile("s_mov_b32 %0, m0\n\ts_mov_b32 m0, %1\n\ts_nop 2\n\t"
                 "global_load_lds_dwordx4 %3, %2\n\tglobal_load_lds_dwordx4 %4, %2 offset:1024\n\t"
                 "global_load_lds_dwordx4 %5, %2 offset:2048\n\tglobal_load_lds_dwordx4 %6, %2 offset:3072\n\ts_mov_b32 m0, %0"
                 : "=&s"(keep) : "s"(m0v), "s"(base), "v"(v0), "v"(v1), "v"(v2), "v"(v3) : "memory");
}
DEVI void glds1(unsigned m0v, const char* base, unsigned v0) {
  unsigned keep;
  asm volatile("s_mov_b32 %0, m0\n\ts_mov_b32 m0, %1\n\ts_nop 2\n\tglobal_load_lds_dwordx4 %3, %2 sc1\n\ts_mov_b32 m0, %0"
               : "=&s"(keep) : "s"(m0v), "s"(base), "v"(v0) : "memory");
}
template <int NP> DEVI void ld_lin(unsigned dst, const char* src, unsigned l16) {
  glds4<true>(dst, src, l16, l16, l16, l16);
  glds4<true>(dst + 4096, src + 4096, l16, l16, l16, l16);
  if (NP == 9) glds1(dst + 8192, src + 8192, l16);
}

constexpr int TP = 1168;
constexpr int CV_K = 30208, CV_C = 4160;
constexpr int C_U = 4 * CV_K, C_KD = C_U + 16 * TP, KDS = 1664, FT = 520, C_X = C_KD + 4 * KDS, C_END = C_X + 16384;
constexpr int KT = 528;
constexpr int STG = 34816, RING = 4 * STG;
constexpr int X_CS = RING, X_LN = X_CS + 1152, X_PV = X_LN + 1024, X_HS = X_PV + 16384, X_CNT = 162560;
constexpr int LDS_BYTES = X_CNT + 64 + 256;
static_assert(C_END <= X_CNT && (15 + 128) * 128 <= 16 * TP && 57 * KT <= CV_K && X_HS + 1024 <= X_CNT && LDS_BYTES <= 163840, "lds");
#define TPOLL 12
#define WDELAY 5
#define ALIGNL asm volatile(".p2alignl 6, 0xBF800000")
#define TCHK 13
#define TFLAG 16

struct Fr { u32x4 m[2], n[9]; };
struct Ctx { const char* sm; int am0, am1, an; };
template <int MK> DEVI void ldf(Fr& f, const Ctx& c, int t, int kk) {
  int so = (t & 3) * STG;
  asm volatile("" : "+s"(so));
  const char* slot = c.sm + so;
  if (MK == 0) {
#pragma unroll
    for (int m = 0; m < 2; ++m) {
      const char* p = slot + c.am0 + kk * 8192 + m * 1024;
      u32x2 a = *(volatile const LDSP u32x2*)p, b = *(volatile const LDSP u32x2*)(p + 512);
      f.m[m] = u32x4{a[0], a[1], b[0], b[1]};
    }
  } else {
#pragma unroll
    for (int m = 0; m < 2; ++m) f.m[m] = *(const u32x4*)(slot + (c.am1 ^ (kk * 64)) + m * 2048);
  }
#pragma unroll
  for (int n = 0; n < 9; ++n) f.n[n] = *(const u32x4*)(slot + c.an + (kk * 9 + n) * 1024);
}
template <int MK, int ORI, bool LD, bool FIRST = false> DEVI void seg(f32x4 (&acc)[2][9], const Fr& cur, Fr& nxt, const Ctx& c, int t, int kk) {
  __builtin_amdgcn_s_waitcnt(0xC07F);
  __builtin_amdgcn_sched_barrier(0);
  if (LD) ldf<MK>(nxt, c, t, kk);
#pragma unroll
  for (int m = 0; m < 2; ++m)
#pragma unroll
    for (int nn = 0; nn < 9; ++nn) {
      const int n = m ? 8 - nn : nn;
      const f32x4 ci = FIRST ? f32x4{0, 0, 0, 0} : acc[m][n];
      acc[m][n] = ORI == 0 ? mfma_bf(cur.m[m], cur.n[n], ci) : mfma_bf(cur.n[n], cur.m[m], ci);
    }
#pragma unroll
  for (int i = 0; i < (MK == 0 ? 13 : 11); ++i) { __builtin_amdgcn_sched_group_barrier(0x008, 1, 0); __builtin_amdgcn_sched_group_barrier(0x100, 1, 0); }
  __builtin_amdgcn_sched_barrier(0);
}
DEVI f32x2 cmul(f32x2 a, f32x2 b, f32x2 bp) {
  return __builtin_elementwise_fma(f32x2{a[1], a[1]}, bp, f32x2{a[0], a[0]} * b);
}
DEVI f32x2 perp(f32x2 b) { return f32x2{-b[1], b[0]}; }
DEVI f32x2 cexp(float ar, float ai, float l) {
  float m = __builtin_amdgcn_exp2f(ar * l), ph = __builtin_amdgcn_fractf(ai * l);
  return f32x2{m * __builtin_amdgcn_cosf(ph), m * __builtin_amdgcn_sinf(ph)};
}
DEVI unsigned pkh(f32x2 v) { return __builtin_bit_cast(unsigned, __builtin_convertvector(v, f16x2)); }
DEVI float ldbf(const char* p, int i) { return bf_lo(*(const unsigned short*)(p + 2 * i)); }

__global__ __launch_bounds__(512, 2) void k_all(const char* __restrict__ x, const char* __restrict__ a_re, const char* __restrict__ a_im,
    const char* __restrict__ b_re, const char* __restrict__ b_im, const char* __restrict__ c_re, const char* __restrict__ c_im,
    const char* __restrict__ log_dt, const char* __restrict__ dskip, const char* __restrict__ w_in, const char* __restrict__ w_sp,
    const char* __restrict__ b_sp, const char* __restrict__ w_out, char* __restrict__ out, char* __restrict__ ws) {
  extern __shared__ __attribute__((aligned(16))) char sm[];
  const int tid = threadIdx.x, lane = tid & 63, w = __builtin_amdgcn_readfirstlane(tid >> 6), li = lane & 15, q = lane >> 4;
  const int bid = blockIdx.x, xc = bid & 7, ii = bid >> 3;
  if (w == 7) {
    unsigned long long pc;
    asm volatile("s_getpc_b64 %0" : "=s"(pc));
    const char* cp = (const char*)(pc & ~255ull) + (ii & 7) * (CODE_LINES * 128);
    unsigned keep;
    asm volatile("s_mov_b32 %0, m0\n\ts_mov_b32 m0, %1\n\ts_nop 2\n\tglobal_load_lds_dword %3, %2\n\ts_mov_b32 m0, %0"
                 : "=&s"(keep) : "s"(lds_addr(sm) + X_CNT + 64), "s"(cp), "v"(min(lane, CODE_LINES - 1) * 128) : "memory");
  }
  if (tid == 256) *(u32x4*)(sm + X_CNT) = u32x4{0, 0, 0, 0};
  asm volatile("" ::"s"(x), "s"(a_re), "s"(a_im), "s"(b_re), "s"(b_im), "s"(c_re), "s"(c_im), "s"(log_dt), "s"(dskip), "s"(w_in), "s"(w_out), "s"(ws));
  const rsrc_t R = __builtin_amdgcn_make_buffer_rsrc(ws, 0, WS_USED, 0x00020000);
  STAMP(0,0);
  const u32x2 ep = ld8(R, 0, O_EP);
  const bool is_conv = ii < 18;
  const int cg = xc * 18 + ii, rep = bid & (NREP - 1);
  auto mk_tag = [&]() {
    u64 e = (((u64)__builtin_amdgcn_readfirstlane(ep[1]) << 32) | (unsigned)__builtin_amdgcn_readfirstlane(ep[0])) + 0x9E3779B97F4A7C15ull;
    return u32x2{(unsigned)e, (unsigned)(e >> 32)};
  };
  u32x2 tag;
  auto arrive = [&](int slot, unsigned n, unsigned flag_off, unsigned stride, int nrep) {
    wait_vm<0>();
    const int lane = fresh_lane();
    unsigned old = 0;
    if (lane == 0) old = __hip_atomic_fetch_add((unsigned*)(sm + X_CNT) + slot, 1u, __ATOMIC_RELAXED, __HIP_MEMORY_SCOPE_WORKGROUP);
    if (__builtin_amdgcn_readfirstlane(old) == n - 1 && lane < nrep) {
#pragma unroll
      for (int k = 0; k < (nrep + 63) / 64; ++k) st8(R, (lane + 64 * k) * stride, flag_off, tag);
    }
  };
  if (is_conv) {
    const int ch = w & 3, dch = 4 * cg + ch;
    char* sk = sm + ch * CV_K;
    float sc = 0.f;
    struct CF { u32x4 a[4], b[8]; } A, B;
    u32x4 tf[4], b9, b7, a32;
    const int sg = 7 - (li & 7);
    const char* pk = sk + (sg & 3) * CV_C + (253 - (li >> 3) + q) * 16 + (sg >> 2) * 8 - 2048;
    auto lda = [&](int ap) {
      const char* p = pk + 2048 - 64 * ap;
      const u32x2 x0 = *(volatile const LDSP u32x2*)p, x1 = *(volatile const LDSP u32x2*)(p + 8);
      return u32x4{x0[0], x0[1], x1[0], x1[1]};
    };
    if (w < 4) {
      const int pi = (dch * NM + 2 * lane) * 2;
      const unsigned v_ar = *(const unsigned*)(a_re + pi), v_ai = *(const unsigned*)(a_im + pi), v_br = *(const unsigned*)(b_re + pi),
                     v_bi = *(const unsigned*)(b_im + pi), v_cr = *(const unsigned*)(c_re + pi), v_ci = *(const unsigned*)(c_im + pi);
      const unsigned v_dk = *(const unsigned short*)(dskip + 2 * dch), v_dt = *(const unsigned short*)(log_dt + 2 * dch);
      bar();
      const float p_dk = bf_lo(v_dk), dt = rbf(expf(bf_lo(v_dt)));
      tag = mk_tag();
      STAMP(0,1);
      const f32x2 ar = (up2(v_ar) * dt) * 1.44269504f, ai = (up2(v_ai) * dt) * 0.15915494f;
      const f32x2 br = up2(v_br) * dt, bi = up2(v_bi) * dt, cr = up2(v_cr), ci = up2(v_ci);
      const f32x2 cbr = cr * br - ci * bi, cbi = -(cr * bi + ci * br);
      float mx = fmaxf(fmaxf(fabsf(cbr[0]), fabsf(cbi[0])), fmaxf(fabsf(cbr[1]), fabsf(cbi[1])));
      mx = fmaxf(mx, dpp_get<0x128>(mx)); mx = fmaxf(mx, dpp_get<0x124>(mx)); mx = fmaxf(mx, dpp_get<0x122>(mx)); mx = fmaxf(mx, dpp_get<0x121>(mx));
      auto rl = [&](int l) { return asf(__builtin_amdgcn_readlane((int)asu(mx), l)); };
      sc = fmaxf(fmaxf(rl(0), rl(16)), fmaxf(rl(32), rl(48)));
      sc = fmaxf(sc, fabsf(p_dk) * (1.0f / 256));
      const float isc = sc > 0.f ? 1.0f / sc : 0.f;
      const char* pa = sk + li * KT + q * 16;
      u32x4 fr[8][4];
      {
        struct C2 { f32x2 a, b; };
        char* tb = sk + lane * 8;
        auto put = [&](int row, C2 v) { *(u32x2*)(tb + row * KT) = u32x2{pkh(v.a), pkh(v.b)}; };
        auto mul = [&](C2 v, C2 u, C2 up) { return C2{cmul(v.a, u.a, up.a), cmul(v.b, u.b, up.b)}; };
        auto prp = [&](C2 v) { return C2{perp(v.a), perp(v.b)}; };
        auto ex = [&](float l) { return C2{cexp(ar[0], ai[0], l), cexp(ar[1], ai[1], l)}; };
        const C2 w1 = ex(1.f), w128 = ex(128.f);
        C2 w8 = ex(8.f); w8.a[1] = -w8.a[1]; w8.b[1] = -w8.b[1];
        const C2 w1p = prp(w1), w8p = prp(w8), w128p = prp(w128);
        C2 w64 = ex(64.f); w64.a[1] = -w64.a[1]; w64.b[1] = -w64.b[1];
        const C2 w1k = ex(1024.f), w64p = prp(w64), w1kp = prp(w1k);
        C2 y = {f32x2{cbr[0], cbi[0]} * isc, f32x2{cbr[1], cbi[1]} * isc};
        C2 E[8]; E[0] = C2{f32x2{1.f, 0.f}, f32x2{1.f, 0.f}}; E[1] = w1;
        put(0, E[0]); put(8, w128);
#pragma unroll
        for (int e = 0; e < 8; ++e) {
          if (e > 1) E[e] = mul(E[e - 1], w1, w1p);
          if (e) { put(e, E[e]); put(8 + e, mul(E[e], w128, w128p)); }
          put(41 + e, y); put(49 + e, mul(y, w64, w64p));
          if (e < 7) y = mul(y, w8, w8p);
        }
        asm volatile("" ::: "memory");
#pragma unroll
        for (int s = 0; s < 8; ++s) { fr[s][0] = *(const u32x4*)(pa + s * 64); fr[s][3] = *(const u32x4*)(pa + 41 * KT + s * 64); }
        asm volatile("" ::: "memory");
        const C2 e2p = prp(E[2]), e4p = prp(E[4]), e6p = prp(E[6]);
        C2 H = w128;
        put(32, w1k); put(40, mul(w1k, w1k, w1kp));
#pragma unroll
        for (int a = 1; a < 8; ++a) {
          if (a >= 2) H = mul(H, w128, w128p);
          put(32 + a, mul(H, w1k, w1kp));
          if (a >= 4) { put(16 + 2 * a, H); put(17 + 2 * a, mul(H, E[4], e4p)); }
          else if (a >= 2) { put(8 + 4 * a, H); put(9 + 4 * a, mul(H, E[2], e2p)); put(10 + 4 * a, mul(H, E[4], e4p)); put(11 + 4 * a, mul(H, E[6], e6p)); }
        }
      }
      asm volatile("" ::: "memory");
      STAMP(0,2);
      {
        f32x4 kk[3] = {};
#pragma unroll
        for (int s = 0; s < 8; ++s) { fr[s][1] = *(const u32x4*)(pa + 16 * KT + s * 64); fr[s][2] = *(const u32x4*)(pa + 32 * KT + s * 64); }
        __builtin_amdgcn_sched_barrier(0);
#pragma unroll
        for (int s = 0; s < 8; ++s) kk[0] = mfma_h(fr[s][0], fr[s][3], kk[0]);
#pragma unroll
        for (int s = 0; s < 8; ++s) { kk[1] = mfma_h(fr[s][1], fr[s][3], kk[1]); kk[2] = mfma_h(fr[s][2], fr[s][3], kk[2]); }
        if (lane == 0) kk[0][0] += p_dk * isc;
        char* kd = sm + C_KD + ch * KDS;
        *(u32x2*)(kd + (128 * (q >> 1) + 4 * (q & 1) + 8 * li) * 2) = u32x2{pkh(lo2(kk[0])), pkh(hi2(kk[0]))};
        const int i1 = q < 2 ? 256 + 64 * q + 4 * li : 384 + 64 * (q - 2) + 2 * li;
        *(unsigned*)(kd + i1 * 2) = pkh(lo2(kk[1])); *(unsigned*)(kd + (i1 + (q < 2 ? 2 : 32)) * 2) = pkh(hi2(kk[1]));
        if (lane < 4) *(u32x4*)(kd + FT * 2 + lane * 16) = u32x4{0, 0, 0, 0};
#pragma unroll
        for (int r = 0; r < 4; ++r) *(_Float16*)(kd + (r == 0 && lane == 0 ? 512 : FT + 31 + 16 * (4 * q + r) + li) * 2) = (_Float16)kk[2][r];
      }
      asm volatile("" ::: "memory");
      {
        const unsigned short* kd = (const unsigned short*)(sm + C_KD + ch * KDS);
        u32x4 c0[3];
        unsigned ab, cc, e, k0 = kd[512]; u32x4 v; u32x2 u;
        { const int k = 127 - lane; ab = *(const unsigned*)(kd + 256 + 2 * k); cc = kd[258 + 2 * k]; }
        { const int k = 63 - lane; v = *(const u32x4*)(kd + 8 * (k & 31)); u = *(const u32x2*)(kd + 128 + 4 * k); e = kd[132 + 4 * k]; }
        asm volatile("" : "+v"(v), "+v"(u), "+v"(e), "+v"(ab), "+v"(cc), "+v"(k0));
        k0 = lane == 63 ? k0 | (k0 << 16) : 0u;
        c0[0] = u32x4{0, k0 & 0xFFFF0000u, k0, k0};
        c0[1] = u32x4{cc | (cc << 16), __builtin_amdgcn_perm(ab, ab, 0x03020302), __builtin_amdgcn_perm(ab, ab, 0x01000302), __builtin_amdgcn_perm(ab, ab, 0x01000100)};
        c0[2] = lane < 32 ? u32x4{__builtin_amdgcn_perm(u[1], e, 0x07060100), __builtin_amdgcn_perm(u[1], u[1], 0x01000100), __builtin_amdgcn_perm(u[0], u[1], 0x07060100), __builtin_amdgcn_perm(u[0], u[0], 0x01000100)}
                          : u32x4{__builtin_amdgcn_alignbit(v[3], v[3], 16), __builtin_amdgcn_alignbit(v[2], v[2], 16), __builtin_amdgcn_alignbit(v[1], v[1], 16), __builtin_amdgcn_alignbit(v[0], v[0], 16)};
        if (lane < 8) *(u32x4*)(sk + (lane >> 1) * CV_C + 4096 + (lane & 1) * 16) = u32x4{0, 0, 0, 0};
        STAMP(0,3);
#pragma unroll
        for (int rr = 0; rr < 3; ++rr) {
          char* p = sk + (64 * rr + 64 + lane) * 16;
          unsigned f[6] = {c0[rr][0], c0[rr][1], c0[rr][2], c0[rr][3], 0, 0}, o[5];
#pragma unroll
          for (int i = 0; i < 2; ++i)
            f[4 + i] = __builtin_amdgcn_update_dpp(rr < 2 ? __builtin_amdgcn_readfirstlane(c0[rr < 2 ? rr + 1 : rr][i]) : 0u, c0[rr][i], 0x130, 0xF, 0xF, false);
#pragma unroll
          for (int i = 0; i < 5; ++i) o[i] = __builtin_amdgcn_alignbit(f[i + 1], f[i], 16);
          if (rr || lane >= 56) {
            *(u32x4*)p = c0[rr];
            *(u32x4*)(p + CV_C) = u32x4{o[0], o[1], o[2], o[3]};
            *(u32x4*)(p + 2 * CV_C) = u32x4{f[1], f[2], f[3], f[4]};
            *(u32x4*)(p + 3 * CV_C) = u32x4{o[1], o[2], o[3], o[4]};
          }
        }
      }
      asm volatile("" ::: "memory");
      {
        const char* pt = sm + C_KD + ch * KDS + (FT + 26 + 2 * li - 8 * q) * 2;
        a32 = lda(32);
#pragma unroll
        for (int k = 0; k < 4; ++k) {
          A.a[k] = lda(8 * k);
#pragma unroll
          for (int e = 0; e < 4; ++e) tf[k][e] = *(const unsigned*)(pt + k * 64 + e * 4);
        }
#pragma unroll
        for (int e = 0; e < 4; ++e) {
          const int d0 = 8 * q + 2 * e - 8 - li, d1 = d0 + 1;
          b9[e] = (abs(d0) <= 4 ? 0x3C00u : 0u) | (abs(d1) <= 4 ? 0x3C000000u : 0u);
          b7[e] = (abs(d0) <= 3 ? 0x3C00u : 0u) | (abs(d1) <= 3 ? 0x3C000000u : 0u);
        }
        asm volatile("" : "+v"(b9), "+v"(b7));
      }
      STAMP(0,4);
    } else {
      const int t8 = tid - 256;
      u32x2 xc8[8];
      const rsrc_t RX = __builtin_amdgcn_make_buffer_rsrc((void*)x, 0, L * D * 2, 0x00020000);
      bar();
#pragma unroll
      for (int k = 0; k < 8; ++k) xc8[k] = __builtin_amdgcn_raw_buffer_load_b64(RX, lane * 1152, cg * 8 + (8 * ch + k) * 64 * 1152, 16);
      tag = mk_tag();
      if (cg < 136 && w >= 6) st16(R, (tid - 384) * 16, O_ST + cg * 2048, u32x4{SENT, SENT, SENT, SENT});
      if (t8 < 120) *(u32x4*)(sm + C_U + t8 * 16) = u32x4{0, 0, 0, 0};
      u32x4 g[4];
#pragma unroll
      for (int k = 0; k < 4; ++k) g[k] = ld16(R, t8 * 64 + k * 16, O_LS + (bid & 15) * 16384);
#pragma unroll
      for (int k = 0; k < 8; ++k) *(u32x2*)(sm + C_X + ch * 4096 + k * 512 + lane * 8) = xc8[k];
      asm volatile("" ::: "memory");
#pragma unroll
      for (int k = 0; k < 4; ++k) { const u32x4 v = *(const u32x4*)(sm + C_X + t8 * 64 + k * 16); xc8[2 * k] = u32x2{v[0], v[1]}; xc8[2 * k + 1] = u32x2{v[2], v[3]}; }
#pragma unroll 1
      for (int spin = 0; spin < SPIN; ++spin) {
        bool ok = true;
#pragma unroll
        for (int k = 0; k < 4; ++k) ok = ok && g[k][1] == tag[0] && g[k][3] == tag[1];
        if (__all(ok)) break;
        asm volatile("" ::: "memory");
#pragma unroll
        for (int k = 0; k < 4; ++k) g[k] = ld16(R, t8 * 64 + k * 16, O_LS + (bid & 15) * 16384);
      }
      STAMPW(5,0,4);
      __builtin_amdgcn_s_setprio(2);
      u32x4 uo[4];
#pragma unroll
      for (int k = 0; k < 4; ++k) {
        const f32x2 r2 = {__builtin_amdgcn_rcpf(bf_hi(g[k][0])), __builtin_amdgcn_rcpf(bf_hi(g[k][2]))}, b2 = -(f32x2{bf_lo(g[k][0]), bf_lo(g[k][2])} * r2);
#pragma unroll
        for (int c = 0; c < 4; ++c) {
          const unsigned a = xc8[2 * k][c >> 1], b = xc8[2 * k + 1][c >> 1];
          uo[c][k] = pkh(__builtin_elementwise_fma((c & 1) ? f32x2{bf_hi(a), bf_hi(b)} : f32x2{bf_lo(a), bf_lo(b)}, r2, b2));
        }
      }
      const int r = 15 + (t8 >> 1);
#pragma unroll
      for (int c = 0; c < 4; ++c) *(u32x4*)(sm + C_U + r * 128 + (((2 * c + (t8 & 1)) ^ ((r >> 1) & 7)) * 16)) = uo[c];
      __builtin_amdgcn_s_setprio(0);
      STAMPW(1,0,4);
    }
    bar();
    STAMP(0,5);
    if (w < 4) {
      f32x4 acc[8];
      auto ub = [&](int b) { const int r = b >> 1; return sm + C_U + r * 128 + (((2 * ch + (b & 1)) ^ ((r >> 1) & 7)) * 16); };
      auto ldc = [&](CF& f, int a0) {
        __builtin_amdgcn_s_waitcnt(0xC07F);
        __builtin_amdgcn_sched_barrier(0);
        const char* pb = ub(28 - 4 * a0 + 2 * li + q);
#pragma unroll
        for (int m = 0; m < 8; ++m) { if (m < 4) f.a[m] = lda(a0 + 8 * m); f.b[m] = *(const u32x4*)(pb + 2048 * m); }
      };
      auto mmc = [&](const CF& f) {
#pragma unroll
        for (int m = 0; m < 4; ++m)
#pragma unroll
          for (int t = m; t < 8; ++t) acc[t] = mfma_h(f.a[m], f.b[t - m], acc[t]);
#pragma unroll
        for (int i = 0; i < 16; ++i) { __builtin_amdgcn_sched_group_barrier(0x008, 1, 0); __builtin_amdgcn_sched_group_barrier(0x100, 1, 0); }
        __builtin_amdgcn_sched_barrier(0);
      };
      f32x4 s9[4], s7[4];
      {
        const char* pb = ub(28 + 2 * li + q); const char* pe = ub(27 + 2 * li + q);
        u32x4 ue[4];
#pragma unroll
        for (int k = 0; k < 4; ++k) { A.b[k] = *(const u32x4*)(pb + 2048 * k); ue[k] = *(const u32x4*)(pe + 2048 * k); }
#pragma unroll
        for (int k = 4; k < 8; ++k) A.b[k] = *(const u32x4*)(pb + 2048 * k);
        __builtin_amdgcn_sched_barrier(0);
        const f32x4 z4 = {0, 0, 0, 0};
#pragma unroll
        for (int k = 0; k < 4; ++k) { s7[k] = mfma_h(A.b[k], b7, z4); acc[4 + k] = mfma_h(a32, A.b[k], z4); s9[k] = mfma_h(ue[k], b9, z4); acc[k] = z4; }
        __builtin_amdgcn_sched_barrier(0);
      }
#pragma unroll
      for (int a0 = 0; a0 < 8; a0 += 2) {
        ldc(B, a0 + 1); mmc(A);
        if (a0 == 0) {
          u32x4 g[4];
#pragma unroll
          for (int k = 0; k < 4; ++k)
#pragma unroll
            for (int e = 0; e < 4; ++e) g[k][e] = pkh(f32x2{s7[k][3 - e], s9[k][3 - e]});
#pragma unroll
          for (int k = 0; k < 4; ++k)
#pragma unroll
            for (int t = k; t < 4; ++t) acc[4 + t] = mfma_h(g[k], tf[t - k], acc[4 + t]);
          __builtin_amdgcn_sched_barrier(0);
        }
        if (a0 < 6) ldc(A, a0 + 2);
        mmc(B);
      }
      STAMP(0,6);
#pragma unroll
      for (int t = 0; t < 8; ++t) *(u32x2*)(sk + t * 512 + li * 32 + q * 8) = u32x2{pk2(lo2(acc[t]) * sc), pk2(hi2(acc[t]) * sc)};
    }
    bar();
#pragma unroll
    for (int r = 0; r < 2; ++r) {
      const int T = (tid >> 3) + 64 * r, ip = tid & 7;
      const int t0 = 32 * (T >> 1) + 8 * (ip >> 1) + 4 * (T & 1) + 2 * (ip & 1);
      const char* p = sm + t0 * 2;
      const u32x4 xv = *(const u32x4*)(sm + C_X + t0 * 8);
      const unsigned c0 = *(const unsigned*)p, c1 = *(const unsigned*)(p + CV_K), c2 = *(const unsigned*)(p + 2 * CV_K), c3 = *(const unsigned*)(p + 3 * CV_K);
      u32x4 v = {pk2(up2(xv[0]) + f32x2{bf_lo(c0), bf_lo(c1)}), pk2(up2(xv[1]) + f32x2{bf_lo(c2), bf_lo(c3)}),
                 pk2(up2(xv[2]) + f32x2{bf_hi(c0), bf_hi(c1)}), pk2(up2(xv[3]) + f32x2{bf_hi(c2), bf_hi(c3)})};
      st16(R, (T >> 3) * (18 * 8192) + (T & 7) * 1024 + ip * 16, O_HF + (cg >> 3) * 8192 + (cg & 1) * 512 + ((cg >> 1) & 3) * 128, v);
    }
    STAMPW(2,1,4);
    arrive(1, 8, O_F2 + cg * 8, F2S, NREP);
    STAMPW(2,2,4);
  } else {
    const int tj = (ii - 18) * 8 + xc;
    const int kc = tj / 36, cb = tj % 36;
    if (tj < 64) {
      const int row = 32 * tj + 4 * w + q;
      const char* src = x + (size_t)row * 1152 + li * 8;
      u32x2 xr[9];
#pragma unroll
      for (int k = 0; k < 9; ++k) xr[k] = *(const u32x2*)(src + k * 128);
      tag = mk_tag();
      float a[4] = {0, 0, 0, 0};
#pragma unroll
      for (int k = 0; k < 9; ++k)
#pragma unroll
        for (int e = 0; e < 2; ++e) { a[e] = dot2(xr[k][e], 0x3F803F80u, a[e]); a[2 + e] = dot2(xr[k][e], xr[k][e], a[2 + e]); }
      const float mean = row16_sum(a[0] + a[1]) * (1.0f / D), var = fmaxf(row16_sum(a[2] + a[3]) * (1.0f / D) - mean * mean, 0.f);
      st8(R, li * 16384 + row * 8, O_LS, u32x2{pk_bf16(mean, __builtin_amdgcn_sqrtf(rbf(rbf(var) + EPS))), (q & 1) ? tag[1] : tag[0]});
      STAMP(1,1);
      for (int i = 0; i < WDELAY - 3; ++i) __builtin_amdgcn_s_sleep(16);
    } else
      for (int i = 0; i < WDELAY; ++i) __builtin_amdgcn_s_sleep(16);
    char* s0 = sm + 65536;
    const int G = max(tj - 40, 0), par = G & 1;
    {
      const int row = tid >> 4, uu = G / 9, idx = G % 9, hh = row >> 4;
      const int panel = idx < 4 ? 2 * uu : (idx < 8 ? 2 * uu + 1 : 2 * uu + hh), tile = idx < 8 ? 2 * (idx & 3) + hh : 8;
      const char* src = w_in + (size_t)(192 * min(kc, 2) + row) * 9216 + cb * 256 + li * 16;
      const char* wsrc = w_out + (size_t)(144 * panel + 16 * tile + (row & 15)) * 1152 + li * 8;
      u32x4 v[6]; u32x2 vo[9];
#pragma unroll
      for (int k = 0; k < 6; ++k) v[k] = *(const u32x4*)(src + (size_t)k * 32 * 9216);
      if (tj >= 40) {
#pragma unroll
        for (int k = 0; k < 9; ++k) vo[k] = *(const u32x2*)(wsrc + k * 128);
      }
#pragma unroll
      for (int k = 0; k < 6; ++k) *(u32x4*)(sm + (row + 32 * k) * 272 + li * 16) = v[k];
      if (tj >= 40) {
#pragma unroll
        for (int k = 0; k < 9; ++k) *(u32x2*)(s0 + row * TP + li * 8 + k * 128) = vo[k];
      }
    }
    tag = mk_tag();
    bar();
    STAMP(4,0);
    if (tj < 108) {
      const int ct = 8 * cb + w;
      const int hf = ct < 144 ? 1 : 0, cl = ct < 144 ? ct : ct - 144, fp = cl / 9, n = cl % 9;
      const char* trb = sm + (8 * q + (li >> 2)) * 272 + w * 32 + (li & 3) * 8;
      const unsigned dst = lane * 16 + O_WI + (((fp * 2 + hf) * 18 + 6 * kc) * 9 + n) * 1024;
      float s = 0.f;
#pragma unroll
      for (int g = 0; g < 6; ++g) {
        u32x2 lo = tr_read(trb + g * 32 * 272), hi = tr_read(trb + g * 32 * 272 + 4 * 272);
        st16(R, dst, g * 9216, u32x4{lo[0], lo[1], hi[0], hi[1]});
        s = dot2(lo[0], 0x3F803F80u, s); s = dot2(lo[1], 0x3F803F80u, s); s = dot2(hi[0], 0x3F803F80u, s); s = dot2(hi[1], 0x3F803F80u, s);
      }
      s = q_sum(s);
      if (q == 0) st4(R, li * 4, O_CS + kc * F * 4 + ct * 64, asu(s));
    }
    if (tj >= 40) {
      const bool mixed = G % 9 == 8;
      const char* trb = s0 + ((mixed ? 16 * (q >> 1) + 4 * (q & 1) : 4 * q) + (li >> 2)) * TP + (li & 3) * 8;
      const char* trh = trb + (mixed ? 8 : 16) * TP;
      const unsigned dst = lane * 16 + O_WO + (w * 36 + (G >> 1)) * 9216;
#pragma unroll
      for (int n = 0; n < 5; ++n) {
        const int c0 = 72 * w + 16 * n - (n == 4 ? 8 * par : 0);
        u32x2 lo = tr_read(trb + c0 * 2), hi = tr_read(trh + c0 * 2);
        if (n < 4) st16(R, dst, (4 * par + n) * 1024, u32x4{lo[0], lo[1], hi[0], hi[1]});
        else if ((li >> 3) == par) st16(R, dst, 8192, u32x4{lo[0], lo[1], hi[0], hi[1]});
      }
    }
    STAMP(4,1);
    arrive(0, 8, O_F2 + (144 + tj) * 8, F2S, NREP);
    STAMP(4,2);
  }
  const int tp = (xc & 1) * 8 + (ii >> 2), fp = (xc >> 1) * 4 + (ii & 3);
  const int t64 = 4 * xc + (ii >> 3), j3 = ii & 7;
  if (w == 0) { wait_flags<1>(R, O_F2 + rep * F2S + 144 * 8, 112, tag, lane); STAMPW(6,0,0); }
  bar();
  if (w >= 4) {
    const int lw = w - 4;
    const unsigned l16 = lane * 16, base = lds_addr(sm);
    const int ph = ((ii >> 2) + ii) & 3, ph3 = ((ii >> 3) + ii) & 3;
    auto k9 = [&](int t) { return (t & 3) == ph ? (t + 4 < 9 ? t + 4 : ph) : t; };
    auto k32 = [&](int t) { return (t & 3) == ph ? ((t + 4) & 31) : t; };
    auto k18 = [&](int t) { return (t & 3) == ph3 ? (t + 4 < 18 ? t + 4 : ph3) : t; };
    if (lw < 2) {
      constexpr int NP = 8;
      unsigned vo[8];
#pragma unroll
      for (int j = 0; j < 8; ++j) {
        int t = 64 * lw + 32 * (j >> 2) + 4 * ((j >> 1) & 1) + 16 * (j & 1) + 8 * (lane >> 5) + ((lane >> 3) & 3);
        vo[j] = t * 4096 + (((lane & 7) ^ ((lane >> 3) & 7)) * 16) + 3072 - (j & 3) * 1024;
      }
      const char* wsrc = w_sp + (size_t)tp * 128 * 4096 - 3072;
      const char* hsrc = ws + O_HF + tp * 18 * 8192 + lw * 8192;
      const char* psrc = ws + O_PF + t64 * 72 * 4096 + lw * 8192;
      const unsigned dst = base + lw * 8192;
      if (lw == 0) wait_flags<2>(R, O_F2 + rep * F2S, 144, tag, lane);
      STAMPW(5,1,4);
      bar();
#pragma unroll 1
      for (int t = 0; t < 4; ++t) ld_lin<NP>(dst + t * STG, hsrc + k9(t) * 16384, l16);
      wait_vm<3 * NP>(); bar();
#pragma unroll 1
      for (int t = 4; t < 18; ++t) { wait_vm<2 * NP>(); bar(); ld_lin<NP>(dst + (t & 3) * STG, hsrc + k9(t < 9 ? t : t - 9) * 16384, l16); }
#pragma unroll 1
      for (int t = 18; t < 50; ++t) {
        wait_vm<2 * NP>(); bar();
        const unsigned dd = dst + (t & 3) * STG; const char* sp = wsrc + k32(t - 18) * 128;
        glds4<false>(dd, sp, vo[0], vo[1], vo[2], vo[3]); glds4<false>(dd + 4096, sp, vo[4], vo[5], vo[6], vo[7]);
      }
      wait_vm<2 * NP>(); bar(); wait_vm<NP>(); bar(); wait_vm<0>(); bar();
      bar(); bar();
      wait_flags<1>(R, O_PFL + (t64 >> 1) * 2048 + ((t64 & 1) * 8 + j3) * 128, 16, tag, lane);
      STAMPW(5,2,4);
#pragma unroll 1
      for (int t = 50; t < 68; ++t) {
        if (t >= 53) { wait_vm<2 * NP>(); bar(); }
        ld_lin<NP>(dst + (t & 3) * STG, psrc + k18(t - 50) * 16384, l16);
      }
      wait_vm<2 * NP>(); bar(); wait_vm<NP>(); bar(); wait_vm<0>(); bar();
    } else {
      constexpr int NP = 9;
      const char* wi = ws + O_WI + (fp * 2) * 18 * 9216 + (lw - 2) * 9216;
      const char* zs = ws + O_ZF + fp * 64 * 9216 + (lw - 2) * 9216;
      const char* wo = ws + O_WO + j3 * 36 * 9216 + (lw - 2) * 9216;
      const unsigned dst = base + 16384 + (lw - 2) * 9216;
#pragma unroll 1
      for (int t = 0; t < 4; ++t) ld_lin<NP>(dst + t * STG, wi + k9(t) * 18432, l16);
      bar();
      wait_vm<3 * NP>(); bar();
      if (lw == 3) {
        f32x2 a = *(const f32x2*)(sm + X_LN + lane * 8) + *(const f32x2*)(sm + X_LN + 512 + lane * 8);
        a[0] = q_sum(dpp_add<0x128>(a[0])); a[1] = q_sum(dpp_add<0x128>(a[1]));
        float mean = a[0] * (1.0f / D), mb, ri;
        ln_scalars(mean, fmaxf(a[1] * (1.0f / D) - mean * mean, 0.f), mb, ri);
        if (lane < 8) st8(R, lane * 8, O_HS + tp * 1024 + fp * 64, u32x2{asu(ri) == SENT ? 0x7FC00000u : asu(ri), asu(-mb)});
      }
#pragma unroll 1
      for (int t = 4; t < 18; ++t) { wait_vm<2 * NP>(); bar(); ld_lin<NP>(dst + (t & 3) * STG, wi + (t < 9 ? k9(t) : 9 + k9(t - 9)) * 18432, l16); }
      wait_vm<2 * NP>(); bar(); wait_vm<NP>(); bar(); wait_vm<0>(); bar();
      wait_flags<1>(R, O_ZFL + fp * 2048 + tp * 128, 16, tag, lane);
      STAMPW(6,1,6);
#pragma unroll 1
      for (int t = 18; t < 53; ++t) {
        if (t >= 21) { wait_vm<2 * NP>(); bar(); }
        ld_lin<NP>(dst + (t & 3) * STG, t < 50 ? zs + k32(t - 18) * 18432 : wo + k18(t - 50) * 18432, l16);
      }
      wait_vm<0>(); bar();
      bar();
#pragma unroll 1
      for (int t = 53; t < 57; ++t) ld_lin<NP>(dst + (t & 3) * STG, wo + k18(t - 50) * 18432, l16);
      bar(); bar(); bar();
      wait_vm<3 * NP>(); bar();
#pragma unroll 1
      for (int t = 57; t < 68; ++t) { wait_vm<2 * NP>(); bar(); ld_lin<NP>(dst + (t & 3) * STG, wo + k18(t - 50) * 18432, l16); }
      wait_vm<2 * NP>(); bar(); wait_vm<NP>(); bar(); wait_vm<0>(); bar();
    }
    return;
  }
  Ctx c;
  c.sm = sm;
  c.am0 = 2 * w * 1024 + lane * 8;
  c.am1 = 2 * w * 2048 + li * 128 + ((q ^ (lane & 7)) * 16);
  c.an = 16384 + lane * 16;
  const int prow = 8 * (li >> 2) + (li & 3);
  if (tid < 72) {
    const unsigned o = tid < 36 ? FH * 4 + tid * 16 : (tid - 36) * 16;
    const u32x4 c0 = ld16(R, o, O_CS + fp * 576), c1 = ld16(R, o, O_CS + F * 4 + fp * 576), c2 = ld16(R, o, O_CS + F * 8 + fp * 576);
    *(f32x4*)(sm + X_CS + tid * 16) = (__builtin_bit_cast(f32x4, c0) + __builtin_bit_cast(f32x4, c1)) + __builtin_bit_cast(f32x4, c2);
  }
  bar();
  char* pvb = sm + X_PV + w * 4096;
  {
    u32x4 hv[3];
#pragma unroll
    for (int k = 0; k < 3; ++k)
      hv[k] = ld16(R, (lane >> 5) * 8192 + ((lane >> 2) & 7) * 128 + (lane & 3) * 16, O_HF + (tp * 18 * 8 + (fp >> 1)) * 1024 + (fp & 1) * 64 + min(w + 4 * k, 8) * 16384);
    if (w) hv[2] = u32x4{0, 0, 0, 0};
    f32x4 a = {0, 0, 0, 0};
#pragma unroll
    for (int k = 0; k < 3; ++k)
#pragma unroll
      for (int e = 0; e < 4; ++e) { a[e & 2] = dot2(hv[k][e], 0x3F803F80u, a[e & 2]); a[(e & 2) + 1] = dot2(hv[k][e], hv[k][e], a[(e & 2) + 1]); }
#pragma unroll
    for (int e = 0; e < 4; ++e) a[e] = dpp_add<0x124>(dpp_add<0x128>(a[e]));
    if (li < 4) *(f32x4*)(sm + X_LN + (w * 4 + q) * 64 + li * 16) = a;
  }
  f32x4 acc[2][9];
  Fr X, Y;
  bar();
  STAMP(3,0);
  ldf<0>(X, c, 0, 0);
  seg<0, 0, true, true>(acc, X, Y, c, 0, 1);
  const unsigned hs_m0 = __builtin_amdgcn_readfirstlane(lds_addr(sm) + X_HS + w * 256);
  auto ld_hs = [&]() {
    unsigned keep;
    asm volatile("s_mov_b32 %0, m0\n\ts_mov_b32 m0, %1\n\ts_nop 2\n\tglobal_load_lds_dword %3, %2 sc1\n\ts_mov_b32 m0, %0"
                 : "=&s"(keep) : "s"(hs_m0), "s"(ws), "v"(O_HS + tp * 1024 + tid * 4) : "memory");
  };
  ALIGNL;
#pragma unroll 1
  for (int t = 0; t < 8; ++t) {
    if (t == 5) ld_hs();
    bar();
    seg<0, 0, true>(acc, Y, X, c, t + 1, 0);
    seg<0, 0, true>(acc, X, Y, c, t + 1, 1);
  }
  u32x4 hc[4];
  float cs[9];
  auto rd_hs = [&]() {
    wait_vm<0>();
#pragma unroll
    for (int jp = 0; jp < 4; ++jp) hc[jp] = *(const u32x4*)(sm + X_HS + w * 256 + (jp >> 1) * 128 + q * 32 + (jp & 1) * 16);
  };
  rd_hs();
#pragma unroll
  for (int n = 0; n < 9; ++n) cs[n] = *(const float*)(sm + X_CS + (16 * n + li) * 4);
  bar();
  seg<0, 0, true>(acc, Y, X, c, 9, 0);
  STAMP(3,1);
#pragma unroll 1
  for (int spin = 0; spin < SPIN; ++spin) {
    if (__all(max(max(umax4(hc[0]), umax4(hc[1])), max(umax4(hc[2]), umax4(hc[3]))) != SENT)) break;
    ld_hs();
    rd_hs();
  }
  u32x4 zb[9];
  f32x2 rs[4];
  {
    f32x2 nm[4], k1[4], k0[4], S1[4] = {}, S2[4] = {};
#pragma unroll
    for (int jp = 0; jp < 4; ++jp) {
      rs[jp] = f32x2{asf(hc[jp][0]), asf(hc[jp][2])}; nm[jp] = f32x2{asf(hc[jp][1]), asf(hc[jp][3])};
      k0[jp] = rs[jp] * GC0; k1[jp] = rs[jp] * rs[jp] * rs[jp] * GC1;
    }
#pragma unroll
    for (int n = 0; n < 9; ++n) {
#pragma unroll
      for (int jp = 0; jp < 4; ++jp) {
        f32x2 a = (jp & 1) ? hi2(acc[jp >> 1][n]) : lo2(acc[jp >> 1][n]);
        f32x2 z = gelu2(__builtin_elementwise_fma(nm[jp], f32x2{cs[n], cs[n]}, a), k1[jp], k0[jp]);
        zb[n][jp] = pk2(z);
        S1[jp] += z; S2[jp] = __builtin_elementwise_fma(z, z, S2[jp]);
      }
      asm volatile("" : "+v"(zb[n]));
    }
#pragma unroll
    for (int jp = 0; jp < 4; ++jp) { S1[jp] *= rs[jp]; S2[jp] *= rs[jp] * rs[jp]; }
    float v[16];
#pragma unroll
    for (int j = 0; j < 8; ++j) { v[2 * j] = S1[j >> 1][j & 1]; v[2 * j + 1] = S2[j >> 1][j & 1]; }
#pragma unroll
    for (int i = 0; i < 8; ++i) { bool h = li & 8; v[i] = (h ? v[i + 8] : v[i]) + dpp_get<0x128>(h ? v[i] : v[i + 8]); }
#pragma unroll
    for (int i = 0; i < 4; ++i) { bool h = li & 4; v[i] = (h ? v[i + 4] : v[i]) + dpp_get<0x141>(h ? v[i] : v[i + 4]); }
#pragma unroll
    for (int i = 0; i < 2; ++i) { bool h = li & 2; v[i] = (h ? v[i + 2] : v[i]) + dpp_get<0x4E>(h ? v[i] : v[i + 2]); }
    { bool h = li & 1; v[0] = (h ? v[1] : v[0]) + dpp_get<0xB1>(h ? v[0] : v[1]); }
    unsigned tot = asu(v[0]);
    st4(R, lane * 4, O_ST + ((tp * 4 + w) * 16 + fp) * 256, tot == SENT ? 0x7FC00000u : tot);
  }
  STAMP(3,2);
  const char* sts = ws + O_ST + (tp * 4 + w) * 4096;
  seg<0, 1, true, true>(acc, X, Y, c, 9, 1);
  ALIGNL;
#pragma unroll 1
  for (int t = 9; t < TCHK; ++t) {
    if (t == TPOLL) glds4<true>(lds_addr(pvb), sts, lane * 16, lane * 16, lane * 16, lane * 16);
    bar();
    seg<0, 1, true>(acc, Y, X, c, t + 1, 0);
    seg<0, 1, true>(acc, X, Y, c, t + 1, 1);
  }
  STAMP(3,3);
  u32x4 pv[4];
#pragma unroll 1
  for (int spin = 0; spin < SPIN; ++spin) {
    wait_vm<0>();
#pragma unroll
    for (int k = 0; k < 4; ++k) pv[k] = *(const u32x4*)(pvb + k * 1024 + lane * 16);
    if (__all(max(max(umax4(pv[0]), umax4(pv[1])), max(umax4(pv[2]), umax4(pv[3]))) != SENT)) break;
    glds4<true>(lds_addr(pvb), sts, lane * 16, lane * 16, lane * 16, lane * 16);
  }
  STAMP(3,4);
  {
    float t4[4];
#pragma unroll
    for (int e = 0; e < 4; ++e) t4[e] = q_sum((asf(pv[0][e]) + asf(pv[1][e])) + (asf(pv[2][e]) + asf(pv[3][e])));
    f32x2 o0, o1;
#pragma unroll
    for (int r = 0; r < 2; ++r) {
      float mean = t4[2 * r] * (1.0f / FH), var = fmaxf(t4[2 * r + 1] * (1.0f / FH) - mean * mean, 0.f), mb, ri;
      ln_scalars(mean, var, mb, ri);
      o0[r] = ri; o1[r] = -mb * ri;
    }
    char* ln = sm + X_LN + w * 256;
    if (lane < 16) { *(f32x2*)(ln + li * 8) = o0; *(f32x2*)(ln + 128 + li * 8) = o1; }
    asm volatile("" ::: "memory");
    f32x4 ri[2], rb[2];
#pragma unroll
    for (int m = 0; m < 2; ++m) { ri[m] = *(const f32x4*)(ln + (8 * q + 4 * m) * 4); rb[m] = *(const f32x4*)(ln + 128 + (8 * q + 4 * m) * 4); }
#pragma unroll
    for (int jp = 0; jp < 4; ++jp) rs[jp] *= (jp & 1) ? hi2(ri[jp >> 1]) : lo2(ri[jp >> 1]);
    const unsigned zo = lane * 16 + O_ZF + (fp * 64 + 4 * tp + w) * 9216;
#pragma unroll
    for (int n = 0; n < 9; ++n) {
      u32x4 o4;
#pragma unroll
      for (int jp = 0; jp < 4; ++jp)
        o4[jp] = pk2(__builtin_elementwise_fma(up2(zb[n][jp]), rs[jp], (jp & 1) ? hi2(rb[jp >> 1]) : lo2(rb[jp >> 1])));
      st16(R, zo, n * 1024, o4);
    }
  }
  STAMP(3,5);
  ALIGNL;
#pragma unroll 1
  for (int t = TCHK; t < 17; ++t) {
    if (t == TFLAG) wait_vm<0>();
    bar();
    if (t == TFLAG && tid < 16) st8(R, tid * 128, O_ZFL + fp * 2048 + tp * 8, tag);
    seg<0, 1, true>(acc, Y, X, c, t + 1, 0);
    seg<0, 1, true>(acc, X, Y, c, t + 1, 1);
  }
  float lrs[2], lnm[2];
  f32x4 cs1[9];
#pragma unroll
  for (int m = 0; m < 2; ++m) { const f32x2 h = *(const f32x2*)(sm + X_HS + w * 256 + m * 128 + li * 8); lrs[m] = h[0]; lnm[m] = h[1]; }
#pragma unroll
  for (int n = 0; n < 9; ++n) cs1[n] = *(const f32x4*)(sm + X_CS + (144 + 16 * n + 4 * q) * 4);
  __builtin_amdgcn_sched_barrier(0);
  seg<0, 1, false>(acc, Y, X, c, 17, 1);
  STAMP(3,6);
  u32x2 z1[2][9];
  float bs[2];
#pragma unroll
  for (int m = 0; m < 2; ++m) bs[m] = ldbf(b_sp, 128 * tp + 32 * w + prow + 4 * m) * lrs[m];
  {
#pragma unroll
    for (int n = 0; n < 9; ++n)
#pragma unroll
      for (int m = 0; m < 2; ++m) {
        const float a1 = lrs[m] * lrs[m] * lrs[m] * GC1, a0 = lrs[m] * GC0;
        const f32x2 k1 = {a1, a1}, k0 = {a0, a0}, nm = {lnm[m], lnm[m]};
        z1[m][n][0] = pk2(gelu2(__builtin_elementwise_fma(nm, lo2(cs1[n]), lo2(acc[m][n])), k1, k0));
        z1[m][n][1] = pk2(gelu2(__builtin_elementwise_fma(nm, hi2(cs1[n]), hi2(acc[m][n])), k1, k0));
        asm volatile("" : "+v"(z1[m][n]));
      }
  }
  STAMP(3,7);
  bar();
  STAMP(3,8);
  ldf<1>(X, c, 18, 0);
  seg<1, 1, true, true>(acc, X, Y, c, 18, 1);
  ALIGNL;
#pragma unroll 1
  for (int t = 18; t < 49; ++t) {
    bar();
    seg<1, 1, true>(acc, Y, X, c, t + 1, 0);
    seg<1, 1, true>(acc, X, Y, c, t + 1, 1);
  }
  seg<1, 1, false>(acc, Y, X, c, 49, 1);
  STAMP(3,9);
  {
    const unsigned pf = O_PF + (((2 * tp + (w >> 1)) * 72 + 9 * (fp >> 1)) * 4 + 2 * (w & 1)) * 1024;
    const unsigned po = lane * 16 + pf + (fp & 1) * 16384;
    u32x2 p8[2];
#pragma unroll
    for (int m = 0; m < 2; ++m) {
      const f32x2 r2 = {lrs[m], lrs[m]}, b2 = {bs[m], bs[m]};
      auto pp = [&](int n) { return u32x2{pk2(__builtin_elementwise_fma(lo2(acc[m][n]), r2, b2) * up2(z1[m][n][0])), pk2(__builtin_elementwise_fma(hi2(acc[m][n]), r2, b2) * up2(z1[m][n][1]))}; };
#pragma unroll
      for (int a = 0; a < 4; ++a) {
        u32x2 p0 = pp(2 * a), p1 = pp(2 * a + 1);
        st16(R, po, a * 4096 + m * 1024, u32x4{p0[0], p0[1], p1[0], p1[1]});
      }
      p8[m] = pp(8);
    }
    const auto s0 = __builtin_amdgcn_permlane32_swap(p8[0][0], p8[1][0], false, false), s1 = __builtin_amdgcn_permlane32_swap(p8[0][1], p8[1][1], false, false);
    st16(R, (lane >> 5) * 1024 + (lane & 31) * 16, pf + 8 * 4096 + (fp & 1) * 512, u32x4{s0[0], s1[0], s0[1], s1[1]});
  }
  STAMP(3,10);
  const int th = w & 1, kq = w >> 1, T = 4 * t64 + 2 * th + kq;
  const int lane5 = fresh_lane();
#define lane lane5
#define li (lane5 & 15)
#define q (lane5 >> 4)
#define prow (8 * (li >> 2) + (li & 3))
  struct P3 { u32x4 p[2][2]; } Up, Vp;
  struct N3 { u32x4 n[9]; } Un, Vn, En;
  const int ap = (8 * kq + 2 * th) * 1024 + lane * 16, aw = 16384 + kq * 9216 + lane * 16;
  auto ldp = [&](P3& f, int t) {
#pragma unroll
    for (int g = 0; g < 2; ++g)
#pragma unroll
      for (int m = 0; m < 2; ++m) f.p[g][m] = *(const u32x4*)(sm + (t & 3) * STG + ap + (4 * g + m) * 1024);
  };
  auto ldn = [&](N3& f, int t) {
#pragma unroll
    for (int n = 0; n < 9; ++n) f.n[n] = *(const u32x4*)(sm + (t & 3) * STG + aw + n * 1024);
  };
  bar();
  ldn(Un, 50); ldn(Vn, 51); ldn(En, 52);
  arrive(2, 4, O_PFL + tp * 2048 + fp * 8, 128, 16);
  bar();
  u32x2 hv[5];
#pragma unroll
  for (int n = 0; n < 5; ++n) {
    const int dd = 72 * j3 + 16 * n + 4 * (n == 4 ? (q & 1) : q);
    hv[n] = ld8(R, (dd >> 5) * 8192 + ((dd >> 2) & 1) * 512 + (((dd >> 3) & 3) * 16 + li) * 8, O_HF + ((T >> 3) * 18 * 8 + (T & 7)) * 1024);
  }
  f32x4 a3[2][6] = {};
  auto mm3 = [&](const P3& f, const N3& e) {
#pragma unroll
    for (int g = 0; g < 2; ++g)
#pragma unroll
      for (int m = 0; m < 2; ++m) {
#pragma unroll
        for (int n = 0; n < 4; ++n) a3[m][n] = mfma_bf(e.n[4 * g + n], f.p[g][m], a3[m][n]);
        a3[m][4 + g] = mfma_bf(e.n[8], f.p[g][m], a3[m][4 + g]);
      }
#pragma unroll
    for (int i = 0; i < 13; ++i) { __builtin_amdgcn_sched_group_barrier(0x008, 1, 0); __builtin_amdgcn_sched_group_barrier(0x100, 1, 0); }
    __builtin_amdgcn_sched_barrier(0);
  };
  STAMP(3,11);
  bar();
  STAMP(3,12);
  ldp(Up, 50);
  bar(); ldp(Vp, 51); mm3(Up, Un);
  bar(); ldp(Up, 52); mm3(Vp, Vn);
  bar(); ldp(Vp, 53); ldn(Vn, 53); mm3(Up, En);
  ALIGNL;
#pragma unroll 1
  for (int t = 53; t < 67; t += 2) { bar(); ldp(Up, t + 1); ldn(Un, t + 1); mm3(Vp, Vn); bar(); ldp(Vp, t + 2); ldn(Vn, t + 2); mm3(Up, Un); }
  mm3(Vp, Vn);
  STAMP(3,13);
#pragma unroll
  for (int m = 0; m < 2; ++m) a3[m][4] = lane < 32 ? a3[m][4] : a3[m][5];
  if (kq) {
#pragma unroll
    for (int n = 0; n < 5; ++n) { *(f32x4*)(sm + w * 5120 + n * 1024 + lane * 16) = a3[0][n]; asm volatile("" : "+v"(a3[1][n])); a3[0][n] = a3[1][n]; }
  } else {
#pragma unroll
    for (int n = 0; n < 5; ++n) *(f32x4*)(sm + w * 5120 + n * 1024 + lane * 16) = a3[1][n];
  }
  bar();
  char* o = out + (size_t)(32 * (T >> 1) + 4 * (T & 1) + prow) * 1152 + (72 * j3 + 4 * q) * 2;
#pragma unroll
  for (int n = 0; n < 5; ++n) {
    f32x4 v = a3[0][n] + *(const f32x4*)(sm + (w ^ 2) * 5120 + n * 1024 + lane * 16);
    if (n == 4) v = f32x4{half_sum(v[0]), half_sum(v[1]), half_sum(v[2]), half_sum(v[3])};
    if (n < 4 || q < 2) *(u32x2*)(o + n * 32) = u32x2{pk2(up2(hv[n][0]) + lo2(v)), pk2(up2(hv[n][1]) + hi2(v))};
  }
  if (bid == 0 && w == 0 && lane == 0) { u64 e = (((u64)tag[1] << 32) | tag[0]) - 0x9E3779B97F4A7C15ull + 0x100000001ull;       st8(R, 0, O_EP, u32x2{(unsigned)e, (unsigned)(e >> 32)}); }
  STAMP(3,14);
}

#undef lane
#undef li
#undef q
#undef prow
extern "C" void kernel_launch(void* const* d_in, const int* in_sizes, int n_in, void* d_out, int out_size, void* d_ws, size_t ws_size,
                              hipStream_t stream) {
  (void)hipFuncSetAttribute((const void*)k_all, hipFuncAttributeMaxDynamicSharedMemorySize, LDS_BYTES);
  k_all<<<256, 512, LDS_BYTES, stream>>>((const char*)d_in[0], (const char*)d_in[1], (const char*)d_in[2], (const char*)d_in[3],
      (const char*)d_in[4], (const char*)d_in[5], (const char*)d_in[6], (const char*)d_in[7], (const char*)d_in[8], (const char*)d_in[10],
      (const char*)d_in[11], (const char*)d_in[12], (const char*)d_in[13], (char*)d_out, (char*)d_ws);
}
```

```cpp
#include <hip/hip_runtime.h>
#include <hip/hip_bf16.h>
#include <stdint.h>

typedef __attribute__((ext_vector_type(8))) __bf16 bf16x8;
typedef __attribute__((ext_vector_type(2))) __bf16 bf16x2;
typedef __attribute__((ext_vector_type(8))) _Float16 f16x8;
typedef __attribute__((ext_vector_type(2))) _Float16 f16x2;
typedef __attribute__((ext_vector_type(4))) float f32x4;
typedef __attribute__((ext_vector_type(2))) float f32x2;
typedef __attribute__((ext_vector_type(4))) unsigned u32x4;
typedef __attribute__((ext_vector_type(2))) unsigned u32x2;
typedef __attribute__((ext_vector_type(4))) short s16x4;
#define LDSP __attribute__((address_space(3)))
#define DEVI __device__ __forceinline__

constexpr int L = 2048, D = 576, NM = 128, F = 4608, FH = 2304;
constexpr float EPS = 1e-5f;
constexpr unsigned O_EP = 0;
constexpr int NREP = 64;
constexpr unsigned O_F2 = 256;
constexpr unsigned F2S = 2048;
constexpr unsigned O_ZFL = O_F2 + NREP * F2S;
constexpr unsigned O_PFL = O_ZFL + 32768;
constexpr unsigned O_CS = O_PFL + 32768;
constexpr unsigned O_ST = O_CS + F * 12;
constexpr unsigned O_HS = O_ST + 262144;
constexpr unsigned O_LS = O_HS + 16384;
constexpr unsigned O_WI = O_LS + 16 * 16384;
constexpr unsigned O_WO = O_WI + D * F * 2;
constexpr unsigned O_HF = O_WO + FH * D * 2;
constexpr unsigned O_ZF = O_HF + L * D * 2;
constexpr unsigned O_PF = O_ZF + L * FH * 2;
constexpr unsigned WS_USED = O_PF + L * FH * 2;
constexpr size_t O_STAMP = 128u << 20;
#define STAMP(k,i) __builtin_amdgcn_sched_barrier(0)
#define STAMPW(k,i,W) __builtin_amdgcn_sched_barrier(0)
#define RSTAMP(k,i)
constexpr unsigned SENT = 0xFFFFFFFFu;

DEVI unsigned pk_bf16(float a, float b) { f32x2 v = {a, b}; return __builtin_bit_cast(unsigned, __builtin_convertvector(v, bf16x2)); }
DEVI unsigned pk_f16(float a, float b) { f32x2 v = {a, b}; return __builtin_bit_cast(unsigned, __builtin_convertvector(v, f16x2)); }
DEVI float bf_lo(unsigned u) { return __builtin_bit_cast(float, u << 16); }
DEVI float bf_hi(unsigned u) { return __builtin_bit_cast(float, u & 0xFFFF0000u); }
DEVI float asf(unsigned u) { return __builtin_bit_cast(float, u); }
DEVI unsigned asu(float f) { return __builtin_bit_cast(unsigned, f); }
DEVI float rbf(float a) { return bf_lo(pk_bf16(a, a)); }
DEVI float h_lo(unsigned u) { return (float)__builtin_bit_cast(f16x2, u)[0]; }
DEVI float h_hi(unsigned u) { return (float)__builtin_bit_cast(f16x2, u)[1]; }
template <int CTRL> DEVI float dpp_add(float v) {
  return v + __builtin_bit_cast(float, __builtin_amdgcn_update_dpp(0, __builtin_bit_cast(int, v), CTRL, 0xF, 0xF, true));
}
DEVI float row16_sum(float v) { v = dpp_add<0x128>(v); v = dpp_add<0x124>(v); v = dpp_add<0x122>(v); v = dpp_add<0x121>(v); return v; }
DEVI float half_sum(float v) { auto r = __builtin_amdgcn_permlane32_swap(asu(v), asu(v), false, false); return asf(r[0]) + asf(r[1]); }
DEVI float q_sum(float v) { auto r = __builtin_amdgcn_permlane16_swap(asu(v), asu(v), false, false); return half_sum(asf(r[0]) + asf(r[1])); }
DEVI unsigned umax4(u32x4 v) { return max(max(v[0], v[1]), max(v[2], v[3])); }
DEVI u32x2 tr_read(const char* p) {
  return __builtin_bit_cast(u32x2, __builtin_amdgcn_ds_read_tr16_b64_v4i16((LDSP s16x4*)p));
}
DEVI f32x4 mfma_bf(u32x4 a, u32x4 b, f32x4 c) {
  return __builtin_amdgcn_mfma_f32_16x16x32_bf16(__builtin_bit_cast(bf16x8, a), __builtin_bit_cast(bf16x8, b), c, 0, 0, 0);
}
DEVI f32x4 mfma_h(u32x4 a, u32x4 b, f32x4 c) {
  return __builtin_amdgcn_mfma_f32_16x16x32_f16(__builtin_bit_cast(f16x8, a), __builtin_bit_cast(f16x8, b), c, 0, 0, 0);
}
DEVI void ln_scalars(float mean, float var, float& mb, float& rinv) {
  mb = rbf(mean);
  float d = rbf(__builtin_amdgcn_sqrtf(rbf(rbf(var) + EPS)));
  rinv = __builtin_amdgcn_rcpf(d);
}
DEVI float gelu(float x) {
  float w = x * __builtin_fmaf(x * x, -0.10294324f, -2.3022082f);
  return x * __builtin_amdgcn_rcpf(1.0f + __builtin_amdgcn_exp2f(w));
}

typedef unsigned long long u64;
typedef __amdgpu_buffer_rsrc_t rsrc_t;
constexpr int SPIN = 1 << 16;
constexpr int CODE_LINES = 37;
template <int CTRL> DEVI float dpp_get(float v) {
  return __builtin_bit_cast(float, __builtin_amdgcn_update_dpp(0, __builtin_bit_cast(int, v), CTRL, 0xF, 0xF, true));
}
constexpr float GC1 = -0.10294324f, GC0 = -2.3022082f;
DEVI f32x2 gelu2(f32x2 x, f32x2 k1, f32x2 k0) {
  f32x2 w = x * __builtin_elementwise_fma(x * x, k1, k0);
  f32x2 d = f32x2{__builtin_amdgcn_exp2f(w[0]), __builtin_amdgcn_exp2f(w[1])} + f32x2{1.f, 1.f};
  return x * f32x2{__builtin_amdgcn_rcpf(d[0]), __builtin_amdgcn_rcpf(d[1])};
}
DEVI unsigned pk2(f32x2 v) { return __builtin_bit_cast(unsigned, __builtin_convertvector(v, bf16x2)); }
DEVI f32x2 up2(unsigned u) { return f32x2{bf_lo(u), bf_hi(u)}; }
DEVI f32x2 lo2(f32x4 v) { return __builtin_shufflevector(v, v, 0, 1); }
DEVI f32x2 hi2(f32x4 v) { return __builtin_shufflevector(v, v, 2, 3); }
DEVI float dot2(unsigned a, unsigned b, float c) {
  return __builtin_amdgcn_fdot2_f32_bf16(__builtin_bit_cast(bf16x2, a), __builtin_bit_cast(bf16x2, b), c, false);
}
DEVI void st16(rsrc_t r, unsigned voff, unsigned soff, u32x4 v) { __builtin_amdgcn_raw_buffer_store_b128(v, r, voff + soff, 0, 16); }
DEVI void st8(rsrc_t r, unsigned voff, unsigned soff, u32x2 v) { __builtin_amdgcn_raw_buffer_store_b64(v, r, voff, soff, 16); }
DEVI void st4(rsrc_t r, unsigned voff, unsigned soff, unsigned v) { __builtin_amdgcn_raw_buffer_store_b32(v, r, voff, soff, 16); }
DEVI u32x4 ld16(rsrc_t r, unsigned voff, unsigned soff) { return __builtin_amdgcn_raw_buffer_load_b128(r, voff, soff, 16); }
DEVI u32x2 ld8(rsrc_t r, unsigned voff, unsigned soff) { return __builtin_amdgcn_raw_buffer_load_b64(r, voff, soff, 16); }
template <int N> DEVI void wait_vm() { asm volatile("s_waitcnt vmcnt(%0)" ::"i"(N) : "memory"); }
DEVI void bar() { __builtin_amdgcn_sched_barrier(0); asm volatile("s_waitcnt lgkmcnt(0)\n\ts_barrier" ::: "memory"); __builtin_amdgcn_sched_barrier(0); }
DEVI int fresh_lane() { int l; asm volatile("v_mbcnt_lo_u32_b32 %0, -1, 0\n\tv_mbcnt_hi_u32_b32 %0, -1, %0" : "=v"(l)); return l; }
DEVI unsigned lds_addr(const char* p) { return (unsigned)(uintptr_t)(LDSP const char*)p; }
template <int NL> DEVI void wait_flags(rsrc_t r, unsigned off, int n, u32x2 tag, int lane) {
#pragma unroll 1
  for (int spin = 0; spin < SPIN; ++spin) {
    bool ok = true;
#pragma unroll
    for (int k = 0; k < NL; ++k) {
      int i = min(lane + 64 * k, n / 2 - 1);
      u32x4 v = ld16(r, i * 16, off);
      ok = ok && v[0] == tag[0] && v[1] == tag[1] && v[2] == tag[0] && v[3] == tag[1];
    }
    if (__all(ok)) break;
    asm volatile("" ::: "memory");
  }
  asm volatile("" ::: "memory");
}
template <bool SC> DEVI void glds4(unsigned m0v, const char* base, unsigned v0, unsigned v1, unsigned v2, unsigned v3) {
  unsigned keep;
  if (SC)
    asm volatile("s_mov_b32 %0, m0\n\ts_mov_b32 m0, %1\n\ts_nop 2\n\t"
                 "global_load_lds_dwordx4 %3, %2 sc1\n\tglobal_load_lds_dwordx4 %4, %2 offset:1024 sc1\n\t"
                 "global_load_lds_dwordx4 %5, %2 offset:2048 sc1\n\tglobal_load_lds_dwordx4 %6, %2 offset:3072 sc1\n\ts_mov_b32 m0, %0"
                 : "=&s"(keep) : "s"(m0v), "s"(base), "v"(v0), "v"(v1), "v"(v2), "v"(v3) : "memory");
  else
    asm volatile("s_mov_b32 %0, m0\n\ts_mov_b32 m0, %1\n\ts_nop 2\n\t"
                 "global_load_lds_dwordx4 %3, %2\n\tglobal_load_lds_dwordx4 %4, %2 offset:1024\n\t"
                 "global_load_lds_dwordx4 %5, %2 offset:2048\n\tglobal_load_lds_dwordx4 %6, %2 offset:3072\n\ts_mov_b32 m0, %0"
                 : "=&s"(keep) : "s"(m0v), "s"(base), "v"(v0), "v"(v1), "v"(v2), "v"(v3) : "memory");
}
DEVI void glds1(unsigned m0v, const char* base, unsigned v0) {
  unsigned keep;
  asm volatile("s_mov_b32 %0, m0\n\ts_mov_b32 m0, %1\n\ts_nop 2\n\tglobal_load_lds_dwordx4 %3, %2 sc1\n\ts_mov_b32 m0, %0"
               : "=&s"(keep) : "s"(m0v), "s"(base), "v"(v0) : "memory");
}
template <int NP> DEVI void ld_lin(unsigned dst, const char* src, unsigned l16) {
  glds4<true>(dst, src, l16, l16, l16, l16);
  glds4<true>(dst + 4096, src + 4096, l16, l16, l16, l16);
  if (NP == 9) glds1(dst + 8192, src + 8192, l16);
}

constexpr int TP = 1168;
constexpr int CV_K = 30208, CV_C = 4160;
constexpr int C_U = 4 * CV_K, C_KD = C_U + 16 * TP, KDS = 1664, FT = 520, C_X = C_KD + 4 * KDS, C_END = C_X + 16384;
constexpr int KT = 528;
constexpr int STG = 34816, RING = 4 * STG;
constexpr int X_CS = RING, X_LN = X_CS + 1152, X_PV = X_LN + 1024, X_HS = X_PV + 16384, X_CNT = 162560;
constexpr int LDS_BYTES = X_CNT + 64 + 256;
static_assert(C_END <= X_CNT && (15 + 128) * 128 <= 16 * TP && 57 * KT <= CV_K && X_HS + 1024 <= X_CNT && LDS_BYTES <= 163840, "lds");
#define TPOLL 11
#define WDELAY 5
#define ALIGNL asm volatile(".p2alignl 6, 0xBF800000")
#define TCHK 12
#define TFLAG 14

struct Fr { u32x4 m[2], n[9]; };
struct Ctx { const char* sm; int am0, am1, an; };
template <int MK> DEVI void ldf(Fr& f, const Ctx& c, int t, int kk) {
  int so = (t & 3) * STG;
  asm volatile("" : "+s"(so));
  const char* slot = c.sm + so;
  if (MK == 0) {
#pragma unroll
    for (int m = 0; m < 2; ++m) {
      const char* p = slot + c.am0 + kk * 8192 + m * 1024;
      u32x2 a = *(volatile const LDSP u32x2*)p, b = *(volatile const LDSP u32x2*)(p + 512);
      f.m[m] = u32x4{a[0], a[1], b[0], b[1]};
    }
  } else {
#pragma unroll
    for (int m = 0; m < 2; ++m) f.m[m] = *(const u32x4*)(slot + (c.am1 ^ (kk * 64)) + m * 2048);
  }
#pragma unroll
  for (int n = 0; n < 9; ++n) f.n[n] = *(const u32x4*)(slot + c.an + (kk * 9 + n) * 1024);
}
template <int MK, int ORI, bool LD, bool FIRST = false> DEVI void seg(f32x4 (&acc)[2][9], const Fr& cur, Fr& nxt, const Ctx& c, int t, int kk) {
  __builtin_amdgcn_s_waitcnt(0xC07F);
  __builtin_amdgcn_sched_barrier(0);
  if (LD) ldf<MK>(nxt, c, t, kk);
#pragma unroll
  for (int m = 0; m < 2; ++m)
#pragma unroll
    for (int nn = 0; nn < 9; ++nn) {
      const int n = m ? 8 - nn : nn;
      const f32x4 ci = FIRST ? f32x4{0, 0, 0, 0} : acc[m][n];
      acc[m][n] = ORI == 0 ? mfma_bf(cur.m[m], cur.n[n], ci) : mfma_bf(cur.n[n], cur.m[m], ci);
    }
#pragma unroll
  for (int i = 0; i < (MK == 0 ? 13 : 11); ++i) { __builtin_amdgcn_sched_group_barrier(0x008, 1, 0); __builtin_amdgcn_sched_group_barrier(0x100, 1, 0); }
  __builtin_amdgcn_sched_barrier(0);
}
DEVI f32x2 cmul(f32x2 a, f32x2 b, f32x2 bp) {
  return __builtin_elementwise_fma(f32x2{a[1], a[1]}, bp, f32x2{a[0], a[0]} * b);
}
DEVI f32x2 perp(f32x2 b) { return f32x2{-b[1], b[0]}; }
DEVI f32x2 cexp(float ar, float ai, float l) {
  float m = __builtin_amdgcn_exp2f(ar * l), ph = __builtin_amdgcn_fractf(ai * l);
  return f32x2{m * __builtin_amdgcn_cosf(ph), m * __builtin_amdgcn_sinf(ph)};
}
DEVI unsigned pkh(f32x2 v) { return __builtin_bit_cast(unsigned, __builtin_convertvector(v, f16x2)); }
DEVI float ldbf(const char* p, int i) { return bf_lo(*(const unsigned short*)(p + 2 * i)); }

__global__ __launch_bounds__(512, 2) void k_all(const char* __restrict__ x, const char* __restrict__ a_re, const char* __restrict__ a_im,
    const char* __restrict__ b_re, const char* __restrict__ b_im, const char* __restrict__ c_re, const char* __restrict__ c_im,
    const char* __restrict__ log_dt, const char* __restrict__ dskip, const char* __restrict__ w_in, const char* __restrict__ w_sp,
    const char* __restrict__ b_sp, const char* __restrict__ w_out, char* __restrict__ out, char* __restrict__ ws) {
  extern __shared__ __attribute__((aligned(16))) char sm[];
  const int tid = threadIdx.x, lane = tid & 63, w = __builtin_amdgcn_readfirstlane(tid >> 6), li = lane & 15, q = lane >> 4;
  const int bid = blockIdx.x, xc = bid & 7, ii = bid >> 3;
  if (w == 7) {
    unsigned long long pc;
    asm volatile("s_getpc_b64 %0" : "=s"(pc));
    const char* cp = (const char*)(pc & ~255ull) + (ii & 7) * (CODE_LINES * 128);
    unsigned keep;
    asm volatile("s_mov_b32 %0, m0\n\ts_mov_b32 m0, %1\n\ts_nop 2\n\tglobal_load_lds_dword %3, %2\n\ts_mov_b32 m0, %0"
                 : "=&s"(keep) : "s"(lds_addr(sm) + X_CNT + 64), "s"(cp), "v"(min(lane, CODE_LINES - 1) * 128) : "memory");
  }
  if (tid == 256) *(u32x4*)(sm + X_CNT) = u32x4{0, 0, 0, 0};
  asm volatile("" ::"s"(x), "s"(a_re), "s"(a_im), "s"(b_re), "s"(b_im), "s"(c_re), "s"(c_im), "s"(log_dt), "s"(dskip), "s"(w_in), "s"(w_out), "s"(ws));
  const rsrc_t R = __builtin_amdgcn_make_buffer_rsrc(ws, 0, WS_USED, 0x00020000);
  STAMP(0,0);
  const u32x2 ep = ld8(R, 0, O_EP);
  const bool is_conv = ii < 18;
  const int cg = xc * 18 + ii, rep = bid & (NREP - 1);
  auto mk_tag = [&]() {
    u64 e = (((u64)__builtin_amdgcn_readfirstlane(ep[1]) << 32) | (unsigned)__builtin_amdgcn_readfirstlane(ep[0])) + 0x9E3779B97F4A7C15ull;
    return u32x2{(unsigned)e, (unsigned)(e >> 32)};
  };
  u32x2 tag;
  auto arrive = [&](int slot, unsigned n, unsigned flag_off, unsigned stride, int nrep) {
    wait_vm<0>();
    const int lane = fresh_lane();
    unsigned old = 0;
    if (lane == 0) old = __hip_atomic_fetch_add((unsigned*)(sm + X_CNT) + slot, 1u, __ATOMIC_RELAXED, __HIP_MEMORY_SCOPE_WORKGROUP);
    if (__builtin_amdgcn_readfirstlane(old) == n - 1 && lane < nrep) {
#pragma unroll
      for (int k = 0; k < (nrep + 63) / 64; ++k) st8(R, (lane + 64 * k) * stride, flag_off, tag);
    }
  };
  if (is_conv) {
    const int ch = w & 3, dch = 4 * cg + ch;
    char* sk = sm + ch * CV_K;
    float sc = 0.f;
    struct CF { u32x4 a[4], b[8]; } A, B;
    u32x4 tf[4], b9, b7, a32;
    const int sg = 7 - (li & 7);
    const char* pk = sk + (sg & 3) * CV_C + (253 - (li >> 3) + q) * 16 + (sg >> 2) * 8 - 2048;
    auto lda = [&](int ap) {
      const char* p = pk + 2048 - 64 * ap;
      const u32x2 x0 = *(volatile const LDSP u32x2*)p, x1 = *(volatile const LDSP u32x2*)(p + 8);
      return u32x4{x0[0], x0[1], x1[0], x1[1]};
    };
    if (w < 4) {
      const int pi = (dch * NM + 2 * lane) * 2;
      const unsigned v_ar = *(const unsigned*)(a_re + pi), v_ai = *(const unsigned*)(a_im + pi), v_br = *(const unsigned*)(b_re + pi),
                     v_bi = *(const unsigned*)(b_im + pi), v_cr = *(const unsigned*)(c_re + pi), v_ci = *(const unsigned*)(c_im + pi);
      const unsigned v_dk = *(const unsigned short*)(dskip + 2 * dch), v_dt = *(const unsigned short*)(log_dt + 2 * dch);
      bar();
      const float p_dk = bf_lo(v_dk), dt = rbf(expf(bf_lo(v_dt)));
      tag = mk_tag();
      STAMP(0,1);
      const f32x2 ar = (up2(v_ar) * dt) * 1.44269504f, ai = (up2(v_ai) * dt) * 0.15915494f;
      const f32x2 br = up2(v_br) * dt, bi = up2(v_bi) * dt, cr = up2(v_cr), ci = up2(v_ci);
      const f32x2 cbr = cr * br - ci * bi, cbi = -(cr * bi + ci * br);
      float mx = fmaxf(fmaxf(fabsf(cbr[0]), fabsf(cbi[0])), fmaxf(fabsf(cbr[1]), fabsf(cbi[1])));
      mx = fmaxf(mx, dpp_get<0x128>(mx)); mx = fmaxf(mx, dpp_get<0x124>(mx)); mx = fmaxf(mx, dpp_get<0x122>(mx)); mx = fmaxf(mx, dpp_get<0x121>(mx));
      auto rl = [&](int l) { return asf(__builtin_amdgcn_readlane((int)asu(mx), l)); };
      sc = fmaxf(fmaxf(rl(0), rl(16)), fmaxf(rl(32), rl(48)));
      sc = fmaxf(sc, fabsf(p_dk) * (1.0f / 256));
      const float isc = sc > 0.f ? 1.0f / sc : 0.f;
      const char* pa = sk + li * KT + q * 16;
      u32x4 fr[8][4];
      {
        struct C2 { f32x2 a, b; };
        char* tb = sk + lane * 8;
        auto put = [&](int row, C2 v) { *(u32x2*)(tb + row * KT) = u32x2{pkh(v.a), pkh(v.b)}; };
        auto mul = [&](C2 v, C2 u, C2 up) { return C2{cmul(v.a, u.a, up.a), cmul(v.b, u.b, up.b)}; };
        auto prp = [&](C2 v) { return C2{perp(v.a), perp(v.b)}; };
        auto ex = [&](float l) { return C2{cexp(ar[0], ai[0], l), cexp(ar[1], ai[1], l)}; };
        const C2 w1 = ex(1.f), w128 = ex(128.f);
        C2 w8 = ex(8.f); w8.a[1] = -w8.a[1]; w8.b[1] = -w8.b[1];
        const C2 w1p = prp(w1), w8p = prp(w8), w128p = prp(w128);
        C2 w64 = ex(64.f); w64.a[1] = -w64.a[1]; w64.b[1] = -w64.b[1];
        const C2 w1k = ex(1024.f), w64p = prp(w64), w1kp = prp(w1k);
        C2 y = {f32x2{cbr[0], cbi[0]} * isc, f32x2{cbr[1], cbi[1]} * isc};
        C2 E[8]; E[0] = C2{f32x2{1.f, 0.f}, f32x2{1.f, 0.f}}; E[1] = w1;
        put(0, E[0]); put(8, w128);
#pragma unroll
        for (int e = 0; e < 8; ++e) {
          if (e > 1) E[e] = mul(E[e - 1], w1, w1p);
          if (e) { put(e, E[e]); put(8 + e, mul(E[e], w128, w128p)); }
          put(41 + e, y); put(49 + e, mul(y, w64, w64p));
          if (e < 7) y = mul(y, w8, w8p);
        }
        asm volatile("" ::: "memory");
#pragma unroll
        for (int s = 0; s < 8; ++s) { fr[s][0] = *(const u32x4*)(pa + s * 64); fr[s][3] = *(const u32x4*)(pa + 41 * KT + s * 64); }
        asm volatile("" ::: "memory");
        const C2 e2p = prp(E[2]), e4p = prp(E[4]), e6p = prp(E[6]);
        C2 H = w128;
        put(32, w1k); put(40, mul(w1k, w1k, w1kp));
#pragma unroll
        for (int a = 1; a < 8; ++a) {
          if (a >= 2) H = mul(H, w128, w128p);
          put(32 + a, mul(H, w1k, w1kp));
          if (a >= 4) { put(16 + 2 * a, H); put(17 + 2 * a, mul(H, E[4], e4p)); }
          else if (a >= 2) { put(8 + 4 * a, H); put(9 + 4 * a, mul(H, E[2], e2p)); put(10 + 4 * a, mul(H, E[4], e4p)); put(11 + 4 * a, mul(H, E[6], e6p)); }
        }
      }
      asm volatile("" ::: "memory");
      STAMP(0,2);
      {
        f32x4 kk[3] = {};
#pragma unroll
        for (int s = 0; s < 8; ++s) { fr[s][1] = *(const u32x4*)(pa + 16 * KT + s * 64); fr[s][2] = *(const u32x4*)(pa + 32 * KT + s * 64); }
        __builtin_amdgcn_sched_barrier(0);
#pragma unroll
        for (int s = 0; s < 8; ++s) kk[0] = mfma_h(fr[s][0], fr[s][3], kk[0]);
#pragma unroll
        for (int s = 0; s < 8; ++s) { kk[1] = mfma_h(fr[s][1], fr[s][3], kk[1]); kk[2] = mfma_h(fr[s][2], fr[s][3], kk[2]); }
        if (lane == 0) kk[0][0] += p_dk * isc;
        char* kd = sm + C_KD + ch * KDS;
        *(u32x2*)(kd + (128 * (q >> 1) + 4 * (q & 1) + 8 * li) * 2) = u32x2{pkh(lo2(kk[0])), pkh(hi2(kk[0]))};
        const int i1 = q < 2 ? 256 + 64 * q + 4 * li : 384 + 64 * (q - 2) + 2 * li;
        *(unsigned*)(kd + i1 * 2) = pkh(lo2(kk[1])); *(unsigned*)(kd + (i1 + (q < 2 ? 2 : 32)) * 2) = pkh(hi2(kk[1]));
        if (lane < 4) *(u32x4*)(kd + FT * 2 + lane * 16) = u32x4{0, 0, 0, 0};
#pragma unroll
        for (int r = 0; r < 4; ++r) *(_Float16*)(kd + (r == 0 && lane == 0 ? 512 : FT + 31 + 16 * (4 * q + r) + li) * 2) = (_Float16)kk[2][r];
      }
      asm volatile("" ::: "memory");
      {
        const unsigned short* kd = (const unsigned short*)(sm + C_KD + ch * KDS);
        u32x4 c0[3];
        unsigned ab, cc, e, k0 = kd[512]; u32x4 v; u32x2 u;
        { const int k = 127 - lane; ab = *(const unsigned*)(kd + 256 + 2 * k); cc = kd[258 + 2 * k]; }
        { const int k = 63 - lane; v = *(const u32x4*)(kd + 8 * (k & 31)); u = *(const u32x2*)(kd + 128 + 4 * k); e = kd[132 + 4 * k]; }
        asm volatile("" : "+v"(v), "+v"(u), "+v"(e), "+v"(ab), "+v"(cc), "+v"(k0));
        k0 = lane == 63 ? k0 | (k0 << 16) : 0u;
        c0[0] = u32x4{0, k0 & 0xFFFF0000u, k0, k0};
        c0[1] = u32x4{cc | (cc << 16), __builtin_amdgcn_perm(ab, ab, 0x03020302), __builtin_amdgcn_perm(ab, ab, 0x01000302), __builtin_amdgcn_perm(ab, ab, 0x01000100)};
        c0[2] = lane < 32 ? u32x4{__builtin_amdgcn_perm(u[1], e, 0x07060100), __builtin_amdgcn_perm(u[1], u[1], 0x01000100), __builtin_amdgcn_perm(u[0], u[1], 0x07060100), __builtin_amdgcn_perm(u[0], u[0], 0x01000100)}
                          : u32x4{__builtin_amdgcn_alignbit(v[3], v[3], 16), __builtin_amdgcn_alignbit(v[2], v[2], 16), __builtin_amdgcn_alignbit(v[1], v[1], 16), __builtin_amdgcn_alignbit(v[0], v[0], 16)};
        if (lane < 8) *(u32x4*)(sk + (lane >> 1) * CV_C + 4096 + (lane & 1) * 16) = u32x4{0, 0, 0, 0};
        STAMP(0,3);
#pragma unroll
        for (int rr = 0; rr < 3; ++rr) {
          char* p = sk + (64 * rr + 64 + lane) * 16;
          unsigned f[6] = {c0[rr][0], c0[rr][1], c0[rr][2], c0[rr][3], 0, 0}, o[5];
#pragma unroll
          for (int i = 0; i < 2; ++i)
            f[4 + i] = __builtin_amdgcn_update_dpp(rr < 2 ? __builtin_amdgcn_readfirstlane(c0[rr < 2 ? rr + 1 : rr][i]) : 0u, c0[rr][i], 0x130, 0xF, 0xF, false);
#pragma unroll
          for (int i = 0; i < 5; ++i) o[i] = __builtin_amdgcn_alignbit(f[i + 1], f[i], 16);
          if (rr || lane >= 56) {
            *(u32x4*)p = c0[rr];
            *(u32x4*)(p + CV_C) = u32x4{o[0], o[1], o[2], o[3]};
            *(u32x4*)(p + 2 * CV_C) = u32x4{f[1], f[2], f[3], f[4]};
            *(u32x4*)(p + 3 * CV_C) = u32x4{o[1], o[2], o[3], o[4]};
          }
        }
      }
      asm volatile("" ::: "memory");
      {
        const char* pt = sm + C_KD + ch * KDS + (FT + 26 + 2 * li - 8 * q) * 2;
        a32 = lda(32);
#pragma unroll
        for (int k = 0; k < 4; ++k) {
          A.a[k] = lda(8 * k);
#pragma unroll
          for (int e = 0; e < 4; ++e) tf[k][e] = *(const unsigned*)(pt + k * 64 + e * 4);
        }
#pragma unroll
        for (int e = 0; e < 4; ++e) {
          const int d0 = 8 * q + 2 * e - 8 - li, d1 = d0 + 1;
          b9[e] = (abs(d0) <= 4 ? 0x3C00u : 0u) | (abs(d1) <= 4 ? 0x3C000000u : 0u);
          b7[e] = (abs(d0) <= 3 ? 0x3C00u : 0u) | (abs(d1) <= 3 ? 0x3C000000u : 0u);
        }
        asm volatile("" : "+v"(b9), "+v"(b7));
      }
      STAMP(0,4);
    } else {
      const int t8 = tid - 256;
      u32x2 xc8[8];
      const rsrc_t RX = __builtin_amdgcn_make_buffer_rsrc((void*)x, 0, L * D * 2, 0x00020000);
      bar();
#pragma unroll
      for (int k = 0; k < 8; ++k) xc8[k] = __builtin_amdgcn_raw_buffer_load_b64(RX, lane * 1152, cg * 8 + (8 * ch + k) * 64 * 1152, 16);
      tag = mk_tag();
      if (cg < 136 && w >= 6) st16(R, (tid - 384) * 16, O_ST + cg * 2048, u32x4{SENT, SENT, SENT, SENT});
      if (t8 < 120) *(u32x4*)(sm + C_U + t8 * 16) = u32x4{0, 0, 0, 0};
      u32x4 g[4];
#pragma unroll
      for (int k = 0; k < 4; ++k) g[k] = ld16(R, t8 * 64 + k * 16, O_LS + (bid & 15) * 16384);
#pragma unroll
      for (int k = 0; k < 8; ++k) *(u32x2*)(sm + C_X + ch * 4096 + k * 512 + lane * 8) = xc8[k];
      asm volatile("" ::: "memory");
#pragma unroll
      for (int k = 0; k < 4; ++k) { const u32x4 v = *(const u32x4*)(sm + C_X + t8 * 64 + k * 16); xc8[2 * k] = u32x2{v[0], v[1]}; xc8[2 * k + 1] = u32x2{v[2], v[3]}; }
#pragma unroll 1
      for (int spin = 0; spin < SPIN; ++spin) {
        bool ok = true;
#pragma unroll
        for (int k = 0; k < 4; ++k) ok = ok && g[k][1] == tag[0] && g[k][3] == tag[1];
        if (__all(ok)) break;
        asm volatile("" ::: "memory");
#pragma unroll
        for (int k = 0; k < 4; ++k) g[k] = ld16(R, t8 * 64 + k * 16, O_LS + (bid & 15) * 16384);
      }
      STAMPW(5,0,4);
      __builtin_amdgcn_s_setprio(2);
      u32x4 uo[4];
#pragma unroll
      for (int k = 0; k < 4; ++k) {
        const f32x2 r2 = {__builtin_amdgcn_rcpf(bf_hi(g[k][0])), __builtin_amdgcn_rcpf(bf_hi(g[k][2]))}, b2 = -(f32x2{bf_lo(g[k][0]), bf_lo(g[k][2])} * r2);
#pragma unroll
        for (int c = 0; c < 4; ++c) {
          const unsigned a = xc8[2 * k][c >> 1], b = xc8[2 * k + 1][c >> 1];
          uo[c][k] = pkh(__builtin_elementwise_fma((c & 1) ? f32x2{bf_hi(a), bf_hi(b)} : f32x2{bf_lo(a), bf_lo(b)}, r2, b2));
        }
      }
      const int r = 15 + (t8 >> 1);
#pragma unroll
      for (int c = 0; c < 4; ++c) *(u32x4*)(sm + C_U + r * 128 + (((2 * c + (t8 & 1)) ^ ((r >> 1) & 7)) * 16)) = uo[c];
      __builtin_amdgcn_s_setprio(0);
      STAMPW(1,0,4);
    }
    bar();
    STAMP(0,5);
    if (w < 4) {
      f32x4 acc[8];
      auto ub = [&](int b) { const int r = b >> 1; return sm + C_U + r * 128 + (((2 * ch + (b & 1)) ^ ((r >> 1) & 7)) * 16); };
      auto ldc = [&](CF& f, int a0) {
        __builtin_amdgcn_s_waitcnt(0xC07F);
        __builtin_amdgcn_sched_barrier(0);
        const char* pb = ub(28 - 4 * a0 + 2 * li + q);
#pragma unroll
        for (int m = 0; m < 8; ++m) { if (m < 4) f.a[m] = lda(a0 + 8 * m); f.b[m] = *(const u32x4*)(pb + 2048 * m); }
      };
      auto mmc = [&](const CF& f) {
#pragma unroll
        for (int m = 0; m < 4; ++m)
#pragma unroll
          for (int t = m; t < 8; ++t) acc[t] = mfma_h(f.a[m], f.b[t - m], acc[t]);
#pragma unroll
        for (int i = 0; i < 16; ++i) { __builtin_amdgcn_sched_group_barrier(0x008, 1, 0); __builtin_amdgcn_sched_group_barrier(0x100, 1, 0); }
        __builtin_amdgcn_sched_barrier(0);
      };
      f32x4 s9[4], s7[4];
      {
        const char* pb = ub(28 + 2 * li + q); const char* pe = ub(27 + 2 * li + q);
        u32x4 ue[4];
#pragma unroll
        for (int k = 0; k < 4; ++k) { A.b[k] = *(const u32x4*)(pb + 2048 * k); ue[k] = *(const u32x4*)(pe + 2048 * k); }
#pragma unroll
        for (int k = 4; k < 8; ++k) A.b[k] = *(const u32x4*)(pb + 2048 * k);
        __builtin_amdgcn_sched_barrier(0);
        const f32x4 z4 = {0, 0, 0, 0};
#pragma unroll
        for (int k = 0; k < 4; ++k) { s7[k] = mfma_h(A.b[k], b7, z4); acc[4 + k] = mfma_h(a32, A.b[k], z4); s9[k] = mfma_h(ue[k], b9, z4); acc[k] = z4; }
        __builtin_amdgcn_sched_barrier(0);
      }
#pragma unroll
      for (int a0 = 0; a0 < 8; a0 += 2) {
        ldc(B, a0 + 1); mmc(A);
        if (a0 == 0) {
          u32x4 g[4];
#pragma unroll
          for (int k = 0; k < 4; ++k)
#pragma unroll
            for (int e = 0; e < 4; ++e) g[k][e] = pkh(f32x2{s7[k][3 - e], s9[k][3 - e]});
#pragma unroll
          for (int k = 0; k < 4; ++k)
#pragma unroll
            for (int t = k; t < 4; ++t) acc[4 + t] = mfma_h(g[k], tf[t - k], acc[4 + t]);
          __builtin_amdgcn_sched_barrier(0);
        }
        if (a0 < 6) ldc(A, a0 + 2);
        mmc(B);
      }
      STAMP(0,6);
#pragma unroll
      for (int t = 0; t < 8; ++t) *(u32x2*)(sk + t * 512 + li * 32 + q * 8) = u32x2{pk2(lo2(acc[t]) * sc), pk2(hi2(acc[t]) * sc)};
    }
    bar();
#pragma unroll
    for (int r = 0; r < 2; ++r) {
      const int T = (tid >> 3) + 64 * r, ip = tid & 7;
      const int t0 = 32 * (T >> 1) + 8 * (ip >> 1) + 4 * (T & 1) + 2 * (ip & 1);
      const char* p = sm + t0 * 2;
      const u32x4 xv = *(const u32x4*)(sm + C_X + t0 * 8);
      const unsigned c0 = *(const unsigned*)p, c1 = *(const unsigned*)(p + CV_K), c2 = *(const unsigned*)(p + 2 * CV_K), c3 = *(const unsigned*)(p + 3 * CV_K);
      u32x4 v = {pk2(up2(xv[0]) + f32x2{bf_lo(c0), bf_lo(c1)}), pk2(up2(xv[1]) + f32x2{bf_lo(c2), bf_lo(c3)}),
                 pk2(up2(xv[2]) + f32x2{bf_hi(c0), bf_hi(c1)}), pk2(up2(xv[3]) + f32x2{bf_hi(c2), bf_hi(c3)})};
      st16(R, (T >> 3) * (18 * 8192) + (T & 7) * 1024 + ip * 16, O_HF + (cg >> 3) * 8192 + (cg & 1) * 512 + ((cg >> 1) & 3) * 128, v);
    }
    STAMPW(2,1,4);
    arrive(1, 8, O_F2 + cg * 8, F2S, NREP);
    STAMPW(2,2,4);
  } else {
    const int tj = (ii - 18) * 8 + xc;
    const int kc = tj / 36, cb = tj % 36;
    if (tj < 64) {
      const int row = 32 * tj + 4 * w + q;
      const char* src = x + (size_t)row * 1152 + li * 8;
      u32x2 xr[9];
#pragma unroll
      for (int k = 0; k < 9; ++k) xr[k] = *(const u32x2*)(src + k * 128);
      tag = mk_tag();
      float a[4] = {0, 0, 0, 0};
#pragma unroll
      for (int k = 0; k < 9; ++k)
#pragma unroll
        for (int e = 0; e < 2; ++e) { a[e] = dot2(xr[k][e], 0x3F803F80u, a[e]); a[2 + e] = dot2(xr[k][e], xr[k][e], a[2 + e]); }
      const float mean = row16_sum(a[0] + a[1]) * (1.0f / D), var = fmaxf(row16_sum(a[2] + a[3]) * (1.0f / D) - mean * mean, 0.f);
      st8(R, li * 16384 + row * 8, O_LS, u32x2{pk_bf16(mean, __builtin_amdgcn_sqrtf(rbf(rbf(var) + EPS))), (q & 1) ? tag[1] : tag[0]});
      STAMP(1,1);
      for (int i = 0; i < WDELAY - 3; ++i) __builtin_amdgcn_s_sleep(16);
    } else
      for (int i = 0; i < WDELAY; ++i) __builtin_amdgcn_s_sleep(16);
    char* s0 = sm + 65536;
    const int G = max(tj - 40, 0), par = G & 1;
    {
      const int row = tid >> 4, uu = G / 9, idx = G % 9, hh = row >> 4;
      const int panel = idx < 4 ? 2 * uu : (idx < 8 ? 2 * uu + 1 : 2 * uu + hh), tile = idx < 8 ? 2 * (idx & 3) + hh : 8;
      const char* src = w_in + (size_t)(192 * min(kc, 2) + row) * 9216 + cb * 256 + li * 16;
      const char* wsrc = w_out + (size_t)(144 * panel + 16 * tile + (row & 15)) * 1152 + li * 8;
      u32x4 v[6]; u32x2 vo[9];
#pragma unroll
      for (int k = 0; k < 6; ++k) v[k] = *(const u32x4*)(src + (size_t)k * 32 * 9216);
      if (tj >= 40) {
#pragma unroll
        for (int k = 0; k < 9; ++k) vo[k] = *(const u32x2*)(wsrc + k * 128);
      }
#pragma unroll
      for (int k = 0; k < 6; ++k) *(u32x4*)(sm + (row + 32 * k) * 272 + li * 16) = v[k];
      if (tj >= 40) {
#pragma unroll
        for (int k = 0; k < 9; ++k) *(u32x2*)(s0 + row * TP + li * 8 + k * 128) = vo[k];
      }
    }
    tag = mk_tag();
    bar();
    STAMP(4,0);
    if (tj < 108) {
      const int ct = 8 * cb + w;
      const int hf = ct < 144 ? 1 : 0, cl = ct < 144 ? ct : ct - 144, fp = cl / 9, n = cl % 9;
      const char* trb = sm + (8 * q + (li >> 2)) * 272 + w * 32 + (li & 3) * 8;
      const unsigned dst = lane * 16 + O_WI + (((fp * 2 + hf) * 18 + 6 * kc) * 9 + n) * 1024;
      float s = 0.f;
#pragma unroll
      for (int g = 0; g < 6; ++g) {
        u32x2 lo = tr_read(trb + g * 32 * 272), hi = tr_read(trb + g * 32 * 272 + 4 * 272);
        st16(R, dst, g * 9216, u32x4{lo[0], lo[1], hi[0], hi[1]});
        s = dot2(lo[0], 0x3F803F80u, s); s = dot2(lo[1], 0x3F803F80u, s); s = dot2(hi[0], 0x3F803F80u, s); s = dot2(hi[1], 0x3F803F80u, s);
      }
      s = q_sum(s);
      if (q == 0) st4(R, li * 4, O_CS + kc * F * 4 + ct * 64, asu(s));
    }
    if (tj >= 40) {
      const bool mixed = G % 9 == 8;
      const char* trb = s0 + ((mixed ? 16 * (q >> 1) + 4 * (q & 1) : 4 * q) + (li >> 2)) * TP + (li & 3) * 8;
      const char* trh = trb + (mixed ? 8 : 16) * TP;
      const unsigned dst = lane * 16 + O_WO + (w * 36 + (G >> 1)) * 9216;
#pragma unroll
      for (int n = 0; n < 5; ++n) {
        const int c0 = 72 * w + 16 * n - (n == 4 ? 8 * par : 0);
        u32x2 lo = tr_read(trb + c0 * 2), hi = tr_read(trh + c0 * 2);
        if (n < 4) st16(R, dst, (4 * par + n) * 1024, u32x4{lo[0], lo[1], hi[0], hi[1]});
        else if ((li >> 3) == par) st16(R, dst, 8192, u32x4{lo[0], lo[1], hi[0], hi[1]});
      }
    }
    STAMP(4,1);
    arrive(0, 8, O_F2 + (144 + tj) * 8, F2S, NREP);
    STAMP(4,2);
  }
  const int tp = (xc & 1) * 8 + (ii >> 2), fp = (xc >> 1) * 4 + (ii & 3);
  const int t64 = 4 * xc + (ii >> 3), j3 = ii & 7;
  if (w == 0) { wait_flags<1>(R, O_F2 + rep * F2S + 144 * 8, 112, tag, lane); STAMPW(6,0,0); }
  bar();
  if (w >= 4) {
    const int lw = w - 4;
    const unsigned l16 = lane * 16, base = lds_addr(sm);
    const int ph = ((ii >> 2) + ii) & 3, ph3 = ((ii >> 3) + ii) & 3;
    auto k9 = [&](int t) { return (t & 3) == ph ? (t + 4 < 9 ? t + 4 : ph) : t; };
    auto k32 = [&](int t) { return (t & 3) == ph ? ((t + 4) & 31) : t; };
    auto k18 = [&](int t) { return (t & 3) == ph3 ? (t + 4 < 18 ? t + 4 : ph3) : t; };
    if (lw < 2) {
      constexpr int NP = 8;
      unsigned vo[8];
#pragma unroll
      for (int j = 0; j < 8; ++j) {
        int t = 64 * lw + 32 * (j >> 2) + 4 * ((j >> 1) & 1) + 16 * (j & 1) + 8 * (lane >> 5) + ((lane >> 3) & 3);
        vo[j] = t * 4096 + (((lane & 7) ^ ((lane >> 3) & 7)) * 16) + 3072 - (j & 3) * 1024;
      }
      const char* wsrc = w_sp + (size_t)tp * 128 * 4096 - 3072;
      const char* hsrc = ws + O_HF + tp * 18 * 8192 + lw * 8192;
      const char* psrc = ws + O_PF + t64 * 72 * 4096 + lw * 8192;
      const unsigned dst = base + lw * 8192;
      if (lw == 0) wait_flags<2>(R, O_F2 + rep * F2S, 144, tag, lane);
      STAMPW(5,1,4);
      bar();
#pragma unroll 1
      for (int t = 0; t < 4; ++t) ld_lin<NP>(dst + t * STG, hsrc + k9(t) * 16384, l16);
      wait_vm<3 * NP>(); bar();
#pragma unroll 1
      for (int t = 4; t < 18; ++t) { wait_vm<2 * NP>(); bar(); ld_lin<NP>(dst + (t & 3) * STG, hsrc + k9(t < 9 ? t : t - 9) * 16384, l16); }
#pragma unroll 1
      for (int t = 18; t < 50; ++t) {
        wait_vm<2 * NP>(); bar();
        const unsigned dd = dst + (t & 3) * STG; const char* sp = wsrc + k32(t - 18) * 128;
        glds4<false>(dd, sp, vo[0], vo[1], vo[2], vo[3]); glds4<false>(dd + 4096, sp, vo[4], vo[5], vo[6], vo[7]);
      }
      wait_vm<2 * NP>(); bar(); wait_vm<NP>(); bar(); wait_vm<0>(); bar();
      bar(); bar();
      wait_flags<1>(R, O_PFL + (t64 >> 1) * 2048 + ((t64 & 1) * 8 + j3) * 128, 16, tag, lane);
      STAMPW(5,2,4);
#pragma unroll 1
      for (int t = 50; t < 68; ++t) {
        if (t >= 53) { wait_vm<2 * NP>(); bar(); }
        ld_lin<NP>(dst + (t & 3) * STG, psrc + k18(t - 50) * 16384, l16);
      }
      wait_vm<2 * NP>(); bar(); wait_vm<NP>(); bar(); wait_vm<0>(); bar();
    } else {
      constexpr int NP = 9;
      const char* wi = ws + O_WI + (fp * 2) * 18 * 9216 + (lw - 2) * 9216;
      const char* zs = ws + O_ZF + fp * 64 * 9216 + (lw - 2) * 9216;
      const char* wo = ws + O_WO + j3 * 36 * 9216 + (lw - 2) * 9216;
      const unsigned dst = base + 16384 + (lw - 2) * 9216;
#pragma unroll 1
      for (int t = 0; t < 4; ++t) ld_lin<NP>(dst + t * STG, wi + k9(t) * 18432, l16);
      bar();
      wait_vm<3 * NP>(); bar();
      if (lw == 3) {
        f32x2 a = *(const f32x2*)(sm + X_LN + lane * 8) + *(const f32x2*)(sm + X_LN + 512 + lane * 8);
        a[0] = q_sum(dpp_add<0x128>(a[0])); a[1] = q_sum(dpp_add<0x128>(a[1]));
        float mean = a[0] * (1.0f / D), mb, ri;
        ln_scalars(mean, fmaxf(a[1] * (1.0f / D) - mean * mean, 0.f), mb, ri);
        if (lane < 8) st8(R, lane * 8, O_HS + tp * 1024 + fp * 64, u32x2{asu(ri) == SENT ? 0x7FC00000u : asu(ri), asu(-mb)});
      }
#pragma unroll 1
      for (int t = 4; t < 18; ++t) { wait_vm<2 * NP>(); bar(); ld_lin<NP>(dst + (t & 3) * STG, wi + (t < 9 ? k9(t) : 9 + k9(t - 9)) * 18432, l16); }
      wait_vm<2 * NP>(); bar(); wait_vm<NP>(); bar(); wait_vm<0>(); bar();
      wait_flags<1>(R, O_ZFL + fp * 2048 + tp * 128, 16, tag, lane);
      STAMPW(6,1,6);
#pragma unroll 1
      for (int t = 18; t < 53; ++t) {
        if (t >= 21) { wait_vm<2 * NP>(); bar(); }
        ld_lin<NP>(dst + (t & 3) * STG, t < 50 ? zs + k32(t - 18) * 18432 : wo + k18(t - 50) * 18432, l16);
      }
      wait_vm<0>(); bar();
      bar();
#pragma unroll 1
      for (int t = 53; t < 57; ++t) ld_lin<NP>(dst + (t & 3) * STG, wo + k18(t - 50) * 18432, l16);
      bar(); bar(); bar();
      wait_vm<3 * NP>(); bar();
#pragma unroll 1
      for (int t = 57; t < 68; ++t) { wait_vm<2 * NP>(); bar(); ld_lin<NP>(dst + (t & 3) * STG, wo + k18(t - 50) * 18432, l16); }
      wait_vm<2 * NP>(); bar(); wait_vm<NP>(); bar(); wait_vm<0>(); bar();
    }
    return;
  }
  Ctx c;
  c.sm = sm;
  c.am0 = 2 * w * 1024 + lane * 8;
  c.am1 = 2 * w * 2048 + li * 128 + ((q ^ (lane & 7)) * 16);
  c.an = 16384 + lane * 16;
  const int prow = 8 * (li >> 2) + (li & 3);
  if (tid < 72) {
    const unsigned o = tid < 36 ? FH * 4 + tid * 16 : (tid - 36) * 16;
    const u32x4 c0 = ld16(R, o, O_CS + fp * 576), c1 = ld16(R, o, O_CS + F * 4 + fp * 576), c2 = ld16(R, o, O_CS + F * 8 + fp * 576);
    *(f32x4*)(sm + X_CS + tid * 16) = (__builtin_bit_cast(f32x4, c0) + __builtin_bit_cast(f32x4, c1)) + __builtin_bit_cast(f32x4, c2);
  }
  bar();
  char* pvb = sm + X_PV + w * 4096;
  {
    u32x4 hv[3];
#pragma unroll
    for (int k = 0; k < 3; ++k)
      hv[k] = ld16(R, (lane >> 5) * 8192 + ((lane >> 2) & 7) * 128 + (lane & 3) * 16, O_HF + (tp * 18 * 8 + (fp >> 1)) * 1024 + (fp & 1) * 64 + min(w + 4 * k, 8) * 16384);
    if (w) hv[2] = u32x4{0, 0, 0, 0};
    f32x4 a = {0, 0, 0, 0};
#pragma unroll
    for (int k = 0; k < 3; ++k)
#pragma unroll
      for (int e = 0; e < 4; ++e) { a[e & 2] = dot2(hv[k][e], 0x3F803F80u, a[e & 2]); a[(e & 2) + 1] = dot2(hv[k][e], hv[k][e], a[(e & 2) + 1]); }
#pragma unroll
    for (int e = 0; e < 4; ++e) a[e] = dpp_add<0x124>(dpp_add<0x128>(a[e]));
    if (li < 4) *(f32x4*)(sm + X_LN + (w * 4 + q) * 64 + li * 16) = a;
  }
  f32x4 acc[2][9];
  Fr X, Y;
  bar();
  STAMP(3,0);
  ldf<0>(X, c, 0, 0);
  seg<0, 0, true, true>(acc, X, Y, c, 0, 1);
  const unsigned hs_m0 = __builtin_amdgcn_readfirstlane(lds_addr(sm) + X_HS + w * 256);
  auto ld_hs = [&]() {
    unsigned keep;
    asm volatile("s_mov_b32 %0, m0\n\ts_mov_b32 m0, %1\n\ts_nop 2\n\tglobal_load_lds_dword %3, %2 sc1\n\ts_mov_b32 m0, %0"
                 : "=&s"(keep) : "s"(hs_m0), "s"(ws), "v"(O_HS + tp * 1024 + tid * 4) : "memory");
  };
  ALIGNL;
#pragma unroll 1
  for (int t = 0; t < 8; ++t) {
    if (t == 5) ld_hs();
    bar();
    seg<0, 0, true>(acc, Y, X, c, t + 1, 0);
    seg<0, 0, true>(acc, X, Y, c, t + 1, 1);
  }
  u32x4 hc[4];
  float cs[9];
  auto rd_hs = [&]() {
    wait_vm<0>();
#pragma unroll
    for (int jp = 0; jp < 4; ++jp) hc[jp] = *(const u32x4*)(sm + X_HS + w * 256 + (jp >> 1) * 128 + q * 32 + (jp & 1) * 16);
  };
  rd_hs();
#pragma unroll
  for (int n = 0; n < 9; ++n) cs[n] = *(const float*)(sm + X_CS + (16 * n + li) * 4);
  bar();
  seg<0, 0, true>(acc, Y, X, c, 9, 0);
  STAMP(3,1);
#pragma unroll 1
  for (int spin = 0; spin < SPIN; ++spin) {
    if (__all(max(max(umax4(hc[0]), umax4(hc[1])), max(umax4(hc[2]), umax4(hc[3]))) != SENT)) break;
    ld_hs();
    rd_hs();
  }
  f32x2 zf[9][4];
  f32x2 rs[4];
  {
    f32x2 nm[4], k1[4], k0[4], S1[4] = {}, S2[4] = {};
#pragma unroll
    for (int jp = 0; jp < 4; ++jp) {
      rs[jp] = f32x2{asf(hc[jp][0]), asf(hc[jp][2])}; nm[jp] = f32x2{asf(hc[jp][1]), asf(hc[jp][3])};
      k0[jp] = rs[jp] * GC0; k1[jp] = rs[jp] * rs[jp] * rs[jp] * GC1;
    }
#pragma unroll
    for (int n = 0; n < 9; ++n) {
#pragma unroll
      for (int jp = 0; jp < 4; ++jp) {
        f32x2 a = (jp & 1) ? hi2(acc[jp >> 1][n]) : lo2(acc[jp >> 1][n]);
        f32x2 z = gelu2(__builtin_elementwise_fma(nm[jp], f32x2{cs[n], cs[n]}, a), k1[jp], k0[jp]);
        zf[n][jp] = z;
        S1[jp] += z; S2[jp] = __builtin_elementwise_fma(z, z, S2[jp]);
      }
      asm volatile("" : "+v"(zf[n][0]), "+v"(zf[n][1]), "+v"(zf[n][2]), "+v"(zf[n][3]));
    }
#pragma unroll
    for (int jp = 0; jp < 4; ++jp) { S1[jp] *= rs[jp]; S2[jp] *= rs[jp] * rs[jp]; }
    float v[16];
#pragma unroll
    for (int j = 0; j < 8; ++j) { v[2 * j] = S1[j >> 1][j & 1]; v[2 * j + 1] = S2[j >> 1][j & 1]; }
#pragma unroll
    for (int i = 0; i < 8; ++i) { bool h = li & 8; v[i] = (h ? v[i + 8] : v[i]) + dpp_get<0x128>(h ? v[i] : v[i + 8]); }
#pragma unroll
    for (int i = 0; i < 4; ++i) { bool h = li & 4; v[i] = (h ? v[i + 4] : v[i]) + dpp_get<0x141>(h ? v[i] : v[i + 4]); }
#pragma unroll
    for (int i = 0; i < 2; ++i) { bool h = li & 2; v[i] = (h ? v[i + 2] : v[i]) + dpp_get<0x4E>(h ? v[i] : v[i + 2]); }
    { bool h = li & 1; v[0] = (h ? v[1] : v[0]) + dpp_get<0xB1>(h ? v[0] : v[1]); }
    unsigned tot = asu(v[0]);
    st4(R, lane * 4, O_ST + ((tp * 4 + w) * 16 + fp) * 256, tot == SENT ? 0x7FC00000u : tot);
  }
  STAMP(3,2);
  const char* sts = ws + O_ST + (tp * 4 + w) * 4096;
  seg<0, 1, true, true>(acc, X, Y, c, 9, 1);
  ALIGNL;
#pragma unroll 1
  for (int t = 9; t < TCHK; ++t) {
    if (t == TPOLL) glds4<true>(lds_addr(pvb), sts, lane * 16, lane * 16, lane * 16, lane * 16);
    bar();
    seg<0, 1, true>(acc, Y, X, c, t + 1, 0);
    seg<0, 1, true>(acc, X, Y, c, t + 1, 1);
  }
  STAMP(3,3);
  u32x4 pv[4];
#pragma unroll 1
  for (int spin = 0; spin < SPIN; ++spin) {
    wait_vm<0>();
#pragma unroll
    for (int k = 0; k < 4; ++k) pv[k] = *(const u32x4*)(pvb + k * 1024 + lane * 16);
    if (__all(max(max(umax4(pv[0]), umax4(pv[1])), max(umax4(pv[2]), umax4(pv[3]))) != SENT)) break;
    glds4<true>(lds_addr(pvb), sts, lane * 16, lane * 16, lane * 16, lane * 16);
  }
  STAMP(3,4);
  {
    float t4[4];
#pragma unroll
    for (int e = 0; e < 4; ++e) t4[e] = q_sum((asf(pv[0][e]) + asf(pv[1][e])) + (asf(pv[2][e]) + asf(pv[3][e])));
    f32x2 o0, o1;
#pragma unroll
    for (int r = 0; r < 2; ++r) {
      float mean = t4[2 * r] * (1.0f / FH), var = fmaxf(t4[2 * r + 1] * (1.0f / FH) - mean * mean, 0.f), mb, ri;
      ln_scalars(mean, var, mb, ri);
      o0[r] = ri; o1[r] = -mb * ri;
    }
    char* ln = sm + X_LN + w * 256;
    if (lane < 16) { *(f32x2*)(ln + li * 8) = o0; *(f32x2*)(ln + 128 + li * 8) = o1; }
    asm volatile("" ::: "memory");
    f32x4 ri[2], rb[2];
#pragma unroll
    for (int m = 0; m < 2; ++m) { ri[m] = *(const f32x4*)(ln + (8 * q + 4 * m) * 4); rb[m] = *(const f32x4*)(ln + 128 + (8 * q + 4 * m) * 4); }
#pragma unroll
    for (int jp = 0; jp < 4; ++jp) rs[jp] *= (jp & 1) ? hi2(ri[jp >> 1]) : lo2(ri[jp >> 1]);
    const unsigned zo = lane * 16 + O_ZF + (fp * 64 + 4 * tp + w) * 9216;
#pragma unroll
    for (int n = 0; n < 9; ++n) {
      u32x4 o4;
#pragma unroll
      for (int jp = 0; jp < 4; ++jp)
        o4[jp] = pk2(__builtin_elementwise_fma(zf[n][jp], rs[jp], (jp & 1) ? hi2(rb[jp >> 1]) : lo2(rb[jp >> 1])));
      st16(R, zo, n * 1024, o4);
    }
  }
  STAMP(3,5);
  ALIGNL;
#pragma unroll 1
  for (int t = TCHK; t < 17; ++t) {
    if (t == TFLAG) wait_vm<0>();
    bar();
    if (t == TFLAG && tid < 16) st8(R, tid * 128, O_ZFL + fp * 2048 + tp * 8, tag);
    seg<0, 1, true>(acc, Y, X, c, t + 1, 0);
    seg<0, 1, true>(acc, X, Y, c, t + 1, 1);
  }
  float lrs[2], lnm[2];
  f32x4 cs1[9];
#pragma unroll
  for (int m = 0; m < 2; ++m) { const f32x2 h = *(const f32x2*)(sm + X_HS + w * 256 + m * 128 + li * 8); lrs[m] = h[0]; lnm[m] = h[1]; }
#pragma unroll
  for (int n = 0; n < 9; ++n) cs1[n] = *(const f32x4*)(sm + X_CS + (144 + 16 * n + 4 * q) * 4);
  __builtin_amdgcn_sched_barrier(0);
  seg<0, 1, false>(acc, Y, X, c, 17, 1);
  STAMP(3,6);
  f32x2 z1[2][9][2];
  float bs[2];
#pragma unroll
  for (int m = 0; m < 2; ++m) bs[m] = ldbf(b_sp, 128 * tp + 32 * w + prow + 4 * m) * lrs[m];
  {
#pragma unroll
    for (int n = 0; n < 9; ++n)
#pragma unroll
      for (int m = 0; m < 2; ++m) {
        const float a1 = lrs[m] * lrs[m] * lrs[m] * GC1, a0 = lrs[m] * GC0;
        const f32x2 k1 = {a1, a1}, k0 = {a0, a0}, nm = {lnm[m], lnm[m]};
        z1[m][n][0] = gelu2(__builtin_elementwise_fma(nm, lo2(cs1[n]), lo2(acc[m][n])), k1, k0);
        z1[m][n][1] = gelu2(__builtin_elementwise_fma(nm, hi2(cs1[n]), hi2(acc[m][n])), k1, k0);
        asm volatile("" : "+v"(z1[m][n][0]), "+v"(z1[m][n][1]));
      }
  }
  STAMP(3,7);
  bar();
  STAMP(3,8);
  ldf<1>(X, c, 18, 0);
  seg<1, 1, true, true>(acc, X, Y, c, 18, 1);
  ALIGNL;
#pragma unroll 1
  for (int t = 18; t < 49; ++t) {
    bar();
    seg<1, 1, true>(acc, Y, X, c, t + 1, 0);
    seg<1, 1, true>(acc, X, Y, c, t + 1, 1);
  }
  seg<1, 1, false>(acc, Y, X, c, 49, 1);
  STAMP(3,9);
  {
    const unsigned pf = O_PF + (((2 * tp + (w >> 1)) * 72 + 9 * (fp >> 1)) * 4 + 2 * (w & 1)) * 1024;
    const unsigned po = lane * 16 + pf + (fp & 1) * 16384;
    u32x2 p8[2];
#pragma unroll
    for (int m = 0; m < 2; ++m) {
      const f32x2 r2 = {lrs[m], lrs[m]}, b2 = {bs[m], bs[m]};
      auto pp = [&](int n) { return u32x2{pk2(__builtin_elementwise_fma(lo2(acc[m][n]), r2, b2) * z1[m][n][0]), pk2(__builtin_elementwise_fma(hi2(acc[m][n]), r2, b2) * z1[m][n][1])}; };
#pragma unroll
      for (int a = 0; a < 4; ++a) {
        u32x2 p0 = pp(2 * a), p1 = pp(2 * a + 1);
        st16(R, po, a * 4096 + m * 1024, u32x4{p0[0], p0[1], p1[0], p1[1]});
      }
      p8[m] = pp(8);
    }
    const auto s0 = __builtin_amdgcn_permlane32_swap(p8[0][0], p8[1][0], false, false), s1 = __builtin_amdgcn_permlane32_swap(p8[0][1], p8[1][1], false, false);
    st16(R, (lane >> 5) * 1024 + (lane & 31) * 16, pf + 8 * 4096 + (fp & 1) * 512, u32x4{s0[0], s1[0], s0[1], s1[1]});
  }
  STAMP(3,10);
  const int th = w & 1, kq = w >> 1, T = 4 * t64 + 2 * th + kq;
  const int lane5 = fresh_lane();
#define lane lane5
#define li (lane5 & 15)
#define q (lane5 >> 4)
#define prow (8 * (li >> 2) + (li & 3))
  struct P3 { u32x4 p[2][2]; } Up, Vp;
  struct N3 { u32x4 n[9]; } Un, Vn, En;
  const int ap = (8 * kq + 2 * th) * 1024 + lane * 16, aw = 16384 + kq * 9216 + lane * 16;
  auto ldp = [&](P3& f, int t) {
#pragma unroll
    for (int g = 0; g < 2; ++g)
#pragma unroll
      for (int m = 0; m < 2; ++m) f.p[g][m] = *(const u32x4*)(sm + (t & 3) * STG + ap + (4 * g + m) * 1024);
  };
  auto ldn = [&](N3& f, int t) {
#pragma unroll
    for (int n = 0; n < 9; ++n) f.n[n] = *(const u32x4*)(sm + (t & 3) * STG + aw + n * 1024);
  };
  bar();
  ldn(Un, 50); ldn(Vn, 51); ldn(En, 52);
  arrive(2, 4, O_PFL + tp * 2048 + fp * 8, 128, 16);
  bar();
  u32x2 hv[5];
#pragma unroll
  for (int n = 0; n < 5; ++n) {
    const int dd = 72 * j3 + 16 * n + 4 * (n == 4 ? (q & 1) : q);
    hv[n] = ld8(R, (dd >> 5) * 8192 + ((dd >> 2) & 1) * 512 + (((dd >> 3) & 3) * 16 + li) * 8, O_HF + ((T >> 3) * 18 * 8 + (T & 7)) * 1024);
  }
  f32x4 a3[2][6] = {};
  auto mm3 = [&](const P3& f, const N3& e) {
#pragma unroll
    for (int g = 0; g < 2; ++g)
#pragma unroll
      for (int m = 0; m < 2; ++m) {
#pragma unroll
        for (int n = 0; n < 4; ++n) a3[m][n] = mfma_bf(e.n[4 * g + n], f.p[g][m], a3[m][n]);
        a3[m][4 + g] = mfma_bf(e.n[8], f.p[g][m], a3[m][4 + g]);
      }
#pragma unroll
    for (int i = 0; i < 13; ++i) { __builtin_amdgcn_sched_group_barrier(0x008, 1, 0); __builtin_amdgcn_sched_group_barrier(0x100, 1, 0); }
    __builtin_amdgcn_sched_barrier(0);
  };
  STAMP(3,11);
  bar();
  STAMP(3,12);
  ldp(Up, 50);
  bar(); ldp(Vp, 51); mm3(Up, Un);
  bar(); ldp(Up, 52); mm3(Vp, Vn);
  bar(); ldp(Vp, 53); ldn(Vn, 53); mm3(Up, En);
  ALIGNL;
#pragma unroll 1
  for (int t = 53; t < 67; t += 2) { bar(); ldp(Up, t + 1); ldn(Un, t + 1); mm3(Vp, Vn); bar(); ldp(Vp, t + 2); ldn(Vn, t + 2); mm3(Up, Un); }
  mm3(Vp, Vn);
  STAMP(3,13);
#pragma unroll
  for (int m = 0; m < 2; ++m) a3[m][4] = lane < 32 ? a3[m][4] : a3[m][5];
  if (kq) {
#pragma unroll
    for (int n = 0; n < 5; ++n) { *(f32x4*)(sm + w * 5120 + n * 1024 + lane * 16) = a3[0][n]; asm volatile("" : "+v"(a3[1][n])); a3[0][n] = a3[1][n]; }
  } else {
#pragma unroll
    for (int n = 0; n < 5; ++n) *(f32x4*)(sm + w * 5120 + n * 1024 + lane * 16) = a3[1][n];
  }
  bar();
  char* o = out + (size_t)(32 * (T >> 1) + 4 * (T & 1) + prow) * 1152 + (72 * j3 + 4 * q) * 2;
#pragma unroll
  for (int n = 0; n < 5; ++n) {
    f32x4 v = a3[0][n] + *(const f32x4*)(sm + (w ^ 2) * 5120 + n * 1024 + lane * 16);
    if (n == 4) v = f32x4{half_sum(v[0]), half_sum(v[1]), half_sum(v[2]), half_sum(v[3])};
    if (n < 4 || q < 2) *(u32x2*)(o + n * 32) = u32x2{pk2(up2(hv[n][0]) + lo2(v)), pk2(up2(hv[n][1]) + hi2(v))};
  }
  if (bid == 0 && w == 0 && lane == 0) { u64 e = (((u64)tag[1] << 32) | tag[0]) - 0x9E3779B97F4A7C15ull + 0x100000001ull;       st8(R, 0, O_EP, u32x2{(unsigned)e, (unsigned)(e >> 32)}); }
  STAMP(3,14);
}

#undef lane
#undef li
#undef q
#undef prow
extern "C" void kernel_launch(void* const* d_in, const int* in_sizes, int n_in, void* d_out, int out_size, void* d_ws, size_t ws_size,
                              hipStream_t stream) {
  (void)hipFuncSetAttribute((const void*)k_all, hipFuncAttributeMaxDynamicSharedMemorySize, LDS_BYTES);
  k_all<<<256, 512, LDS_BYTES, stream>>>((const char*)d_in[0], (const char*)d_in[1], (const char*)d_in[2], (const char*)d_in[3],
      (const char*)d_in[4], (const char*)d_in[5], (const char*)d_in[6], (const char*)d_in[7], (const char*)d_in[8], (const char*)d_in[10],
      (const char*)d_in[11], (const char*)d_in[12], (const char*)d_in[13], (char*)d_out, (char*)d_ws);
}
```

```cpp
#include <hip/hip_runtime.h>
#include <hip/hip_bf16.h>
#include <stdint.h>

typedef __attribute__((ext_vector_type(8))) __bf16 bf16x8;
typedef __attribute__((ext_vector_type(2))) __bf16 bf16x2;
typedef __attribute__((ext_vector_type(8))) _Float16 f16x8;
typedef __attribute__((ext_vector_type(2))) _Float16 f16x2;
typedef __attribute__((ext_vector_type(4))) float f32x4;
typedef __attribute__((ext_vector_type(2))) float f32x2;
typedef __attribute__((ext_vector_type(4))) unsigned u32x4;
typedef __attribute__((ext_vector_type(2))) unsigned u32x2;
typedef __attribute__((ext_vector_type(4))) short s16x4;
#define LDSP __attribute__((address_space(3)))
#define DEVI __device__ __forceinline__

constexpr int L = 2048, D = 576, NM = 128, F = 4608, FH = 2304;
constexpr float EPS = 1e-5f;
constexpr unsigned O_EP = 0;
constexpr int NREP = 64;
constexpr unsigned O_F2 = 256;
constexpr unsigned F2S = 2048;
constexpr unsigned O_ZFL = O_F2 + NREP * F2S;
constexpr unsigned O_PFL = O_ZFL + 32768;
constexpr unsigned O_CS = O_PFL + 32768;
constexpr unsigned O_ST = O_CS + F * 12;
constexpr unsigned O_HS = O_ST + 262144;
constexpr unsigned O_LS = O_HS + 16384;
constexpr unsigned O_WI = O_LS + 16 * 16384;
constexpr unsigned O_WO = O_WI + D * F * 2;
constexpr unsigned O_HF = O_WO + FH * D * 2;
constexpr unsigned O_ZF = O_HF + L * D * 2;
constexpr unsigned O_PF = O_ZF + L * FH * 2;
constexpr unsigned WS_USED = O_PF + L * FH * 2;
#define SB __builtin_amdgcn_sched_barrier(0)
constexpr unsigned SENT = 0xFFFFFFFFu;

DEVI unsigned pk_bf16(float a, float b) { f32x2 v = {a, b}; return __builtin_bit_cast(unsigned, __builtin_convertvector(v, bf16x2)); }
DEVI float bf_lo(unsigned u) { return __builtin_bit_cast(float, u << 16); }
DEVI float bf_hi(unsigned u) { return __builtin_bit_cast(float, u & 0xFFFF0000u); }
DEVI float asf(unsigned u) { return __builtin_bit_cast(float, u); }
DEVI unsigned asu(float f) { return __builtin_bit_cast(unsigned, f); }
DEVI float rbf(float a) { return bf_lo(pk_bf16(a, a)); }
template <int CTRL> DEVI float dpp_add(float v) {
  return v + __builtin_bit_cast(float, __builtin_amdgcn_update_dpp(0, __builtin_bit_cast(int, v), CTRL, 0xF, 0xF, true));
}
DEVI float row16_sum(float v) { v = dpp_add<0x128>(v); v = dpp_add<0x124>(v); v = dpp_add<0x122>(v); v = dpp_add<0x121>(v); return v; }
DEVI float half_sum(float v) { auto r = __builtin_amdgcn_permlane32_swap(asu(v), asu(v), false, false); return asf(r[0]) + asf(r[1]); }
DEVI float q_sum(float v) { auto r = __builtin_amdgcn_permlane16_swap(asu(v), asu(v), false, false); return half_sum(asf(r[0]) + asf(r[1])); }
DEVI unsigned umax4(u32x4 v) { return max(max(v[0], v[1]), max(v[2], v[3])); }
DEVI u32x2 tr_read(const char* p) {
  return __builtin_bit_cast(u32x2, __builtin_amdgcn_ds_read_tr16_b64_v4i16((LDSP s16x4*)p));
}
DEVI f32x4 mfma_bf(u32x4 a, u32x4 b, f32x4 c) {
  return __builtin_amdgcn_mfma_f32_16x16x32_bf16(__builtin_bit_cast(bf16x8, a), __builtin_bit_cast(bf16x8, b), c, 0, 0, 0);
}
DEVI f32x4 mfma_h(u32x4 a, u32x4 b, f32x4 c) {
  return __builtin_amdgcn_mfma_f32_16x16x32_f16(__builtin_bit_cast(f16x8, a), __builtin_bit_cast(f16x8, b), c, 0, 0, 0);
}
DEVI void ln_scalars(float mean, float var, float& mb, float& rinv) {
  mb = rbf(mean);
  float d = rbf(__builtin_amdgcn_sqrtf(rbf(rbf(var) + EPS)));
  rinv = __builtin_amdgcn_rcpf(d);
}


typedef unsigned long long u64;
typedef __amdgpu_buffer_rsrc_t rsrc_t;
constexpr int SPIN = 1 << 16;
constexpr int CODE_LINES = 37;
template <int CTRL> DEVI float dpp_get(float v) {
  return __builtin_bit_cast(float, __builtin_amdgcn_update_dpp(0, __builtin_bit_cast(int, v), CTRL, 0xF, 0xF, true));
}
constexpr float GC1 = -0.10294324f, GC0 = -2.3022082f;
DEVI f32x2 gelu2(f32x2 x, f32x2 k1, f32x2 k0) {
  f32x2 w = x * __builtin_elementwise_fma(x * x, k1, k0);
  f32x2 d = f32x2{__builtin_amdgcn_exp2f(w[0]), __builtin_amdgcn_exp2f(w[1])} + f32x2{1.f, 1.f};
  return x * f32x2{__builtin_amdgcn_rcpf(d[0]), __builtin_amdgcn_rcpf(d[1])};
}
DEVI unsigned pk2(f32x2 v) { return __builtin_bit_cast(unsigned, __builtin_convertvector(v, bf16x2)); }
DEVI f32x2 up2(unsigned u) { return f32x2{bf_lo(u), bf_hi(u)}; }
DEVI f32x2 lo2(f32x4 v) { return __builtin_shufflevector(v, v, 0, 1); }
DEVI f32x2 hi2(f32x4 v) { return __builtin_shufflevector(v, v, 2, 3); }
DEVI float dot2(unsigned a, unsigned b, float c) {
  return __builtin_amdgcn_fdot2_f32_bf16(__builtin_bit_cast(bf16x2, a), __builtin_bit_cast(bf16x2, b), c, false);
}
DEVI void st16(rsrc_t r, unsigned voff, unsigned soff, u32x4 v) { __builtin_amdgcn_raw_buffer_store_b128(v, r, voff + soff, 0, 16); }
DEVI void st8(rsrc_t r, unsigned voff, unsigned soff, u32x2 v) { __builtin_amdgcn_raw_buffer_store_b64(v, r, voff, soff, 16); }
DEVI void st4(rsrc_t r, unsigned voff, unsigned soff, unsigned v) { __builtin_amdgcn_raw_buffer_store_b32(v, r, voff, soff, 16); }
DEVI u32x4 ld16(rsrc_t r, unsigned voff, unsigned soff) { return __builtin_amdgcn_raw_buffer_load_b128(r, voff, soff, 16); }
DEVI u32x2 ld8(rsrc_t r, unsigned voff, unsigned soff) { return __builtin_amdgcn_raw_buffer_load_b64(r, voff, soff, 16); }
template <int N> DEVI void wait_vm() { asm volatile("s_waitcnt vmcnt(%0)" ::"i"(N) : "memory"); }
DEVI void bar() { __builtin_amdgcn_sched_barrier(0); asm volatile("s_waitcnt lgkmcnt(0)\n\ts_barrier" ::: "memory"); __builtin_amdgcn_sched_barrier(0); }
DEVI int fresh_lane() { int l; asm volatile("v_mbcnt_lo_u32_b32 %0, -1, 0\n\tv_mbcnt_hi_u32_b32 %0, -1, %0" : "=v"(l)); return l; }
DEVI unsigned lds_addr(const char* p) { return (unsigned)(uintptr_t)(LDSP const char*)p; }
template <int NL> DEVI void wait_flags(rsrc_t r, unsigned off, int n, u32x2 tag, int lane) {
#pragma unroll 1
  for (int spin = 0; spin < SPIN; ++spin) {
    bool ok = true;
#pragma unroll
    for (int k = 0; k < NL; ++k) {
      int i = min(lane + 64 * k, n / 2 - 1);
      u32x4 v = ld16(r, i * 16, off);
      ok = ok && v[0] == tag[0] && v[1] == tag[1] && v[2] == tag[0] && v[3] == tag[1];
    }
    if (__all(ok)) break;
    asm volatile("" ::: "memory");
  }
  asm volatile("" ::: "memory");
}
template <bool SC> DEVI void glds4(unsigned m0v, const char* base, unsigned v0, unsigned v1, unsigned v2, unsigned v3) {
  unsigned keep;
  if (SC)
    asm volatile("s_mov_b32 %0, m0\n\ts_mov_b32 m0, %1\n\ts_nop 2\n\t"
                 "global_load_lds_dwordx4 %3, %2 sc1\n\tglobal_load_lds_dwordx4 %4, %2 offset:1024 sc1\n\t"
                 "global_load_lds_dwordx4 %5, %2 offset:2048 sc1\n\tglobal_load_lds_dwordx4 %6, %2 offset:3072 sc1\n\ts_mov_b32 m0, %0"
                 : "=&s"(keep) : "s"(m0v), "s"(base), "v"(v0), "v"(v1), "v"(v2), "v"(v3) : "memory");
  else
    asm volatile("s_mov_b32 %0, m0\n\ts_mov_b32 m0, %1\n\ts_nop 2\n\t"
                 "global_load_lds_dwordx4 %3, %2\n\tglobal_load_lds_dwordx4 %4, %2 offset:1024\n\t"
                 "global_load_lds_dwordx4 %5, %2 offset:2048\n\tglobal_load_lds_dwordx4 %6, %2 offset:3072\n\ts_mov_b32 m0, %0"
                 : "=&s"(keep) : "s"(m0v), "s"(base), "v"(v0), "v"(v1), "v"(v2), "v"(v3) : "memory");
}
DEVI void glds1(unsigned m0v, const char* base, unsigned v0) {
  unsigned keep;
  asm volatile("s_mov_b32 %0, m0\n\ts_mov_b32 m0, %1\n\ts_nop 2\n\tglobal_load_lds_dwordx4 %3, %2 sc1\n\ts_mov_b32 m0, %0"
               : "=&s"(keep) : "s"(m0v), "s"(base), "v"(v0) : "memory");
}
template <int NP> DEVI void ld_lin(unsigned dst, const char* src, unsigned l16) {
  glds4<true>(dst, src, l16, l16, l16, l16);
  glds4<true>(dst + 4096, src + 4096, l16, l16, l16, l16);
  if (NP == 9) glds1(dst + 8192, src + 8192, l16);
}

constexpr int TP = 1168;
constexpr int CV_K = 30208, CV_C = 4160;
constexpr int C_U = 4 * CV_K, C_KD = C_U + 16 * TP, KDS = 1664, FT = 520, C_X = C_KD + 4 * KDS, C_END = C_X + 16384;
constexpr int KT = 528;
constexpr int STG = 34816, RING = 4 * STG;
constexpr int X_CS = RING, X_LN = X_CS + 1152, X_PV = X_LN + 1024, X_HS = X_PV + 16384, X_CNT = 162560;
constexpr int LDS_BYTES = X_CNT + 64 + 256;
static_assert(C_END <= X_CNT && (15 + 128) * 128 <= 16 * TP && 57 * KT <= CV_K && X_HS + 1024 <= X_CNT && LDS_BYTES <= 163840, "lds");
constexpr int TPOLL = 11;
constexpr int WDELAY = 5;
#define ALIGNL asm volatile(".p2alignl 6, 0xBF800000")
constexpr int TCHK = 12;
constexpr int TFLAG = 14;

struct Fr { u32x4 m[2], n[9]; };
struct Ctx { const char* sm; int am0, am1, an; };
template <int MK> DEVI void ldf(Fr& f, const Ctx& c, int t, int kk) {
  int so = (t & 3) * STG;
  asm volatile("" : "+s"(so));
  const char* slot = c.sm + so;
  if (MK == 0) {
#pragma unroll
    for (int m = 0; m < 2; ++m) {
      const char* p = slot + c.am0 + kk * 8192 + m * 1024;
      u32x2 a = *(volatile const LDSP u32x2*)p, b = *(volatile const LDSP u32x2*)(p + 512);
      f.m[m] = u32x4{a[0], a[1], b[0], b[1]};
    }
  } else {
#pragma unroll
    for (int m = 0; m < 2; ++m) f.m[m] = *(const u32x4*)(slot + (c.am1 ^ (kk * 64)) + m * 2048);
  }
#pragma unroll
  for (int n = 0; n < 9; ++n) f.n[n] = *(const u32x4*)(slot + c.an + (kk * 9 + n) * 1024);
}
template <int MK, int ORI, bool LD, bool FIRST = false> DEVI void seg(f32x4 (&acc)[2][9], const Fr& cur, Fr& nxt, const Ctx& c, int t, int kk) {
  __builtin_amdgcn_s_waitcnt(0xC07F);
  __builtin_amdgcn_sched_barrier(0);
  if (LD) ldf<MK>(nxt, c, t, kk);
#pragma unroll
  for (int m = 0; m < 2; ++m)
#pragma unroll
    for (int nn = 0; nn < 9; ++nn) {
      const int n = m ? 8 - nn : nn;
      const f32x4 ci = FIRST ? f32x4{0, 0, 0, 0} : acc[m][n];
      acc[m][n] = ORI == 0 ? mfma_bf(cur.m[m], cur.n[n], ci) : mfma_bf(cur.n[n], cur.m[m], ci);
    }
#pragma unroll
  for (int i = 0; i < (MK == 0 ? 13 : 11); ++i) { __builtin_amdgcn_sched_group_barrier(0x008, 1, 0); __builtin_amdgcn_sched_group_barrier(0x100, 1, 0); }
  __builtin_amdgcn_sched_barrier(0);
}
DEVI f32x2 cmul(f32x2 a, f32x2 b, f32x2 bp) {
  return __builtin_elementwise_fma(f32x2{a[1], a[1]}, bp, f32x2{a[0], a[0]} * b);
}
DEVI f32x2 perp(f32x2 b) { return f32x2{-b[1], b[0]}; }
DEVI f32x2 cexp(float ar, float ai, float l) {
  float m = __builtin_amdgcn_exp2f(ar * l), ph = __builtin_amdgcn_fractf(ai * l);
  return f32x2{m * __builtin_amdgcn_cosf(ph), m * __builtin_amdgcn_sinf(ph)};
}
DEVI unsigned pkh(f32x2 v) { return __builtin_bit_cast(unsigned, __builtin_convertvector(v, f16x2)); }
DEVI float ldbf(const char* p, int i) { return bf_lo(*(const unsigned short*)(p + 2 * i)); }

__global__ __launch_bounds__(512, 2) void k_all(const char* __restrict__ x, const char* __restrict__ a_re, const char* __restrict__ a_im,
    const char* __restrict__ b_re, const char* __restrict__ b_im, const char* __restrict__ c_re, const char* __restrict__ c_im,
    const char* __restrict__ log_dt, const char* __restrict__ dskip, const char* __restrict__ w_in, const char* __restrict__ w_sp,
    const char* __restrict__ b_sp, const char* __restrict__ w_out, char* __restrict__ out, char* __restrict__ ws) {
  extern __shared__ __attribute__((aligned(16))) char sm[];
  const int tid = threadIdx.x, lane = tid & 63, w = __builtin_amdgcn_readfirstlane(tid >> 6), li = lane & 15, q = lane >> 4;
  const int bid = blockIdx.x, xc = bid & 7, ii = bid >> 3;
  if (w == 7) {
    unsigned long long pc;
    asm volatile("s_getpc_b64 %0" : "=s"(pc));
    const char* cp = (const char*)(pc & ~255ull) + (ii & 7) * (CODE_LINES * 128);
    unsigned keep;
    asm volatile("s_mov_b32 %0, m0\n\ts_mov_b32 m0, %1\n\ts_nop 2\n\tglobal_load_lds_dword %3, %2\n\ts_mov_b32 m0, %0"
                 : "=&s"(keep) : "s"(lds_addr(sm) + X_CNT + 64), "s"(cp), "v"(min(lane, CODE_LINES - 1) * 128) : "memory");
  }
  if (tid == 256) *(u32x4*)(sm + X_CNT) = u32x4{0, 0, 0, 0};
  asm volatile("" ::"s"(x), "s"(a_re), "s"(a_im), "s"(b_re), "s"(b_im), "s"(c_re), "s"(c_im), "s"(log_dt), "s"(dskip), "s"(w_in), "s"(w_out), "s"(ws));
  const rsrc_t R = __builtin_amdgcn_make_buffer_rsrc(ws, 0, WS_USED, 0x00020000);
  SB;
  const u32x2 ep = ld8(R, 0, O_EP);
  const bool is_conv = ii < 18;
  const int cg = xc * 18 + ii, rep = bid & (NREP - 1);
  auto mk_tag = [&]() {
    u64 e = (((u64)__builtin_amdgcn_readfirstlane(ep[1]) << 32) | (unsigned)__builtin_amdgcn_readfirstlane(ep[0])) + 0x9E3779B97F4A7C15ull;
    return u32x2{(unsigned)e, (unsigned)(e >> 32)};
  };
  u32x2 tag;
  auto arrive = [&](int slot, unsigned n, unsigned flag_off, unsigned stride, int nrep) {
    wait_vm<0>();
    const int lane = fresh_lane();
    unsigned old = 0;
    if (lane == 0) old = __hip_atomic_fetch_add((unsigned*)(sm + X_CNT) + slot, 1u, __ATOMIC_RELAXED, __HIP_MEMORY_SCOPE_WORKGROUP);
    if (__builtin_amdgcn_readfirstlane(old) == n - 1 && lane < nrep) {
#pragma unroll
      for (int k = 0; k < (nrep + 63) / 64; ++k) st8(R, (lane + 64 * k) * stride, flag_off, tag);
    }
  };
  if (is_conv) {
    const int ch = w & 3, dch = 4 * cg + ch;
    char* sk = sm + ch * CV_K;
    float sc = 0.f;
    struct CF { u32x4 a[4], b[8]; } A, B;
    u32x4 tf[4], b9, b7, a32;
    const int sg = 7 - (li & 7);
    const char* pk = sk + (sg & 3) * CV_C + (253 - (li >> 3) + q) * 16 + (sg >> 2) * 8 - 2048;
    auto lda = [&](int ap) {
      const char* p = pk + 2048 - 64 * ap;
      const u32x2 x0 = *(volatile const LDSP u32x2*)p, x1 = *(volatile const LDSP u32x2*)(p + 8);
      return u32x4{x0[0], x0[1], x1[0], x1[1]};
    };
    if (w < 4) {
      const int pi = (dch * NM + 2 * lane) * 2;
      const unsigned v_ar = *(const unsigned*)(a_re + pi), v_ai = *(const unsigned*)(a_im + pi), v_br = *(const unsigned*)(b_re + pi),
                     v_bi = *(const unsigned*)(b_im + pi), v_cr = *(const unsigned*)(c_re + pi), v_ci = *(const unsigned*)(c_im + pi);
      const unsigned v_dk = *(const unsigned short*)(dskip + 2 * dch), v_dt = *(const unsigned short*)(log_dt + 2 * dch);
      bar();
      const float p_dk = bf_lo(v_dk), dt = rbf(expf(bf_lo(v_dt)));
      tag = mk_tag();
      SB;
      const f32x2 ar = (up2(v_ar) * dt) * 1.44269504f, ai = (up2(v_ai) * dt) * 0.15915494f;
      const f32x2 br = up2(v_br) * dt, bi = up2(v_bi) * dt, cr = up2(v_cr), ci = up2(v_ci);
      const f32x2 cbr = cr * br - ci * bi, cbi = -(cr * bi + ci * br);
      float mx = fmaxf(fmaxf(fabsf(cbr[0]), fabsf(cbi[0])), fmaxf(fabsf(cbr[1]), fabsf(cbi[1])));
      mx = fmaxf(mx, dpp_get<0x128>(mx)); mx = fmaxf(mx, dpp_get<0x124>(mx)); mx = fmaxf(mx, dpp_get<0x122>(mx)); mx = fmaxf(mx, dpp_get<0x121>(mx));
      auto rl = [&](int l) { return asf(__builtin_amdgcn_readlane((int)asu(mx), l)); };
      sc = fmaxf(fmaxf(rl(0), rl(16)), fmaxf(rl(32), rl(48)));
      sc = fmaxf(sc, fabsf(p_dk) * (1.0f / 256));
      const float isc = sc > 0.f ? 1.0f / sc : 0.f;
      const char* pa = sk + li * KT + q * 16;
      u32x4 fr[8][4];
      {
        struct C2 { f32x2 a, b; };
        char* tb = sk + lane * 8;
        auto put = [&](int row, C2 v) { *(u32x2*)(tb + row * KT) = u32x2{pkh(v.a), pkh(v.b)}; };
        auto mul = [&](C2 v, C2 u, C2 up) { return C2{cmul(v.a, u.a, up.a), cmul(v.b, u.b, up.b)}; };
        auto prp = [&](C2 v) { return C2{perp(v.a), perp(v.b)}; };
        auto ex = [&](float l) { return C2{cexp(ar[0], ai[0], l), cexp(ar[1], ai[1], l)}; };
        const C2 w1 = ex(1.f), w128 = ex(128.f);
        C2 w8 = ex(8.f); w8.a[1] = -w8.a[1]; w8.b[1] = -w8.b[1];
        const C2 w1p = prp(w1), w8p = prp(w8), w128p = prp(w128);
        C2 w64 = ex(64.f); w64.a[1] = -w64.a[1]; w64.b[1] = -w64.b[1];
        const C2 w1k = ex(1024.f), w64p = prp(w64), w1kp = prp(w1k);
        C2 y = {f32x2{cbr[0], cbi[0]} * isc, f32x2{cbr[1], cbi[1]} * isc};
        C2 E[8]; E[0] = C2{f32x2{1.f, 0.f}, f32x2{1.f, 0.f}}; E[1] = w1;
        put(0, E[0]); put(8, w128);
#pragma unroll
        for (int e = 0; e < 8; ++e) {
          if (e > 1) E[e] = mul(E[e - 1], w1, w1p);
          if (e) { put(e, E[e]); put(8 + e, mul(E[e], w128, w128p)); }
          put(41 + e, y); put(49 + e, mul(y, w64, w64p));
          if (e < 7) y = mul(y, w8, w8p);
        }
        asm volatile("" ::: "memory");
#pragma unroll
        for (int s = 0; s < 8; ++s) { fr[s][0] = *(const u32x4*)(pa + s * 64); fr[s][3] = *(const u32x4*)(pa + 41 * KT + s * 64); }
        asm volatile("" ::: "memory");
        const C2 e2p = prp(E[2]), e4p = prp(E[4]), e6p = prp(E[6]);
        C2 H = w128;
        put(32, w1k); put(40, mul(w1k, w1k, w1kp));
#pragma unroll
        for (int a = 1; a < 8; ++a) {
          if (a >= 2) H = mul(H, w128, w128p);
          put(32 + a, mul(H, w1k, w1kp));
          if (a >= 4) { put(16 + 2 * a, H); put(17 + 2 * a, mul(H, E[4], e4p)); }
          else if (a >= 2) { put(8 + 4 * a, H); put(9 + 4 * a, mul(H, E[2], e2p)); put(10 + 4 * a, mul(H, E[4], e4p)); put(11 + 4 * a, mul(H, E[6], e6p)); }
        }
      }
      asm volatile("" ::: "memory");
      SB;
      {
        f32x4 kk[3] = {};
#pragma unroll
        for (int s = 0; s < 8; ++s) { fr[s][1] = *(const u32x4*)(pa + 16 * KT + s * 64); fr[s][2] = *(const u32x4*)(pa + 32 * KT + s * 64); }
        __builtin_amdgcn_sched_barrier(0);
#pragma unroll
        for (int s = 0; s < 8; ++s) kk[0] = mfma_h(fr[s][0], fr[s][3], kk[0]);
#pragma unroll
        for (int s = 0; s < 8; ++s) { kk[1] = mfma_h(fr[s][1], fr[s][3], kk[1]); kk[2] = mfma_h(fr[s][2], fr[s][3], kk[2]); }
        if (lane == 0) kk[0][0] += p_dk * isc;
        char* kd = sm + C_KD + ch * KDS;
        *(u32x2*)(kd + (128 * (q >> 1) + 4 * (q & 1) + 8 * li) * 2) = u32x2{pkh(lo2(kk[0])), pkh(hi2(kk[0]))};
        const int i1 = q < 2 ? 256 + 64 * q + 4 * li : 384 + 64 * (q - 2) + 2 * li;
        *(unsigned*)(kd + i1 * 2) = pkh(lo2(kk[1])); *(unsigned*)(kd + (i1 + (q < 2 ? 2 : 32)) * 2) = pkh(hi2(kk[1]));
        if (lane < 4) *(u32x4*)(kd + FT * 2 + lane * 16) = u32x4{0, 0, 0, 0};
#pragma unroll
        for (int r = 0; r < 4; ++r) *(_Float16*)(kd + (r == 0 && lane == 0 ? 512 : FT + 31 + 16 * (4 * q + r) + li) * 2) = (_Float16)kk[2][r];
      }
      asm volatile("" ::: "memory");
      {
        const unsigned short* kd = (const unsigned short*)(sm + C_KD + ch * KDS);
        u32x4 c0[3];
        unsigned ab, cc, e, k0 = kd[512]; u32x4 v; u32x2 u;
        { const int k = 127 - lane; ab = *(const unsigned*)(kd + 256 + 2 * k); cc = kd[258 + 2 * k]; }
        { const int k = 63 - lane; v = *(const u32x4*)(kd + 8 * (k & 31)); u = *(const u32x2*)(kd + 128 + 4 * k); e = kd[132 + 4 * k]; }
        asm volatile("" : "+v"(v), "+v"(u), "+v"(e), "+v"(ab), "+v"(cc), "+v"(k0));
        k0 = lane == 63 ? k0 | (k0 << 16) : 0u;
        c0[0] = u32x4{0, k0 & 0xFFFF0000u, k0, k0};
        c0[1] = u32x4{cc | (cc << 16), __builtin_amdgcn_perm(ab, ab, 0x03020302), __builtin_amdgcn_perm(ab, ab, 0x01000302), __builtin_amdgcn_perm(ab, ab, 0x01000100)};
        c0[2] = lane < 32 ? u32x4{__builtin_amdgcn_perm(u[1], e, 0x07060100), __builtin_amdgcn_perm(u[1], u[1], 0x01000100), __builtin_amdgcn_perm(u[0], u[1], 0x07060100), __builtin_amdgcn_perm(u[0], u[0], 0x01000100)}
                          : u32x4{__builtin_amdgcn_alignbit(v[3], v[3], 16), __builtin_amdgcn_alignbit(v[2], v[2], 16), __builtin_amdgcn_alignbit(v[1], v[1], 16), __builtin_amdgcn_alignbit(v[0], v[0], 16)};
        if (lane < 8) *(u32x4*)(sk + (lane >> 1) * CV_C + 4096 + (lane & 1) * 16) = u32x4{0, 0, 0, 0};
        SB;
#pragma unroll
        for (int rr = 0; rr < 3; ++rr) {
          char* p = sk + (64 * rr + 64 + lane) * 16;
          unsigned f[6] = {c0[rr][0], c0[rr][1], c0[rr][2], c0[rr][3], 0, 0}, o[5];
#pragma unroll
          for (int i = 0; i < 2; ++i)
            f[4 + i] = __builtin_amdgcn_update_dpp(rr < 2 ? __builtin_amdgcn_readfirstlane(c0[rr < 2 ? rr + 1 : rr][i]) : 0u, c0[rr][i], 0x130, 0xF, 0xF, false);
#pragma unroll
          for (int i = 0; i < 5; ++i) o[i] = __builtin_amdgcn_alignbit(f[i + 1], f[i], 16);
          if (rr || lane >= 56) {
            *(u32x4*)p = c0[rr];
            *(u32x4*)(p + CV_C) = u32x4{o[0], o[1], o[2], o[3]};
            *(u32x4*)(p + 2 * CV_C) = u32x4{f[1], f[2], f[3], f[4]};
            *(u32x4*)(p + 3 * CV_C) = u32x4{o[1], o[2], o[3], o[4]};
          }
        }
      }
      asm volatile("" ::: "memory");
      {
        const char* pt = sm + C_KD + ch * KDS + (FT + 26 + 2 * li - 8 * q) * 2;
        a32 = lda(32);
#pragma unroll
        for (int k = 0; k < 4; ++k) {
          A.a[k] = lda(8 * k);
#pragma unroll
          for (int e = 0; e < 4; ++e) tf[k][e] = *(const unsigned*)(pt + k * 64 + e * 4);
        }
#pragma unroll
        for (int e = 0; e < 4; ++e) {
          const int d0 = 8 * q + 2 * e - 8 - li, d1 = d0 + 1;
          b9[e] = (abs(d0) <= 4 ? 0x3C00u : 0u) | (abs(d1) <= 4 ? 0x3C000000u : 0u);
          b7[e] = (abs(d0) <= 3 ? 0x3C00u : 0u) | (abs(d1) <= 3 ? 0x3C000000u : 0u);
        }
        asm volatile("" : "+v"(b9), "+v"(b7));
      }
      SB;
    } else {
      const int t8 = tid - 256;
      u32x2 xc8[8];
      const rsrc_t RX = __builtin_amdgcn_make_buffer_rsrc((void*)x, 0, L * D * 2, 0x00020000);
      bar();
#pragma unroll
      for (int k = 0; k < 8; ++k) xc8[k] = __builtin_amdgcn_raw_buffer_load_b64(RX, lane * 1152, cg * 8 + (8 * ch + k) * 64 * 1152, 16);
      tag = mk_tag();
      if (cg < 136 && w >= 6) st16(R, (tid - 384) * 16, O_ST + cg * 2048, u32x4{SENT, SENT, SENT, SENT});
      if (t8 < 120) *(u32x4*)(sm + C_U + t8 * 16) = u32x4{0, 0, 0, 0};
      u32x4 g[4];
#pragma unroll
      for (int k = 0; k < 4; ++k) g[k] = ld16(R, t8 * 64 + k * 16, O_LS + (bid & 15) * 16384);
#pragma unroll
      for (int k = 0; k < 8; ++k) *(u32x2*)(sm + C_X + ch * 4096 + k * 512 + lane * 8) = xc8[k];
      asm volatile("" ::: "memory");
#pragma unroll
      for (int k = 0; k < 4; ++k) { const u32x4 v = *(const u32x4*)(sm + C_X + t8 * 64 + k * 16); xc8[2 * k] = u32x2{v[0], v[1]}; xc8[2 * k + 1] = u32x2{v[2], v[3]}; }
#pragma unroll 1
      for (int spin = 0; spin < SPIN; ++spin) {
        bool ok = true;
#pragma unroll
        for (int k = 0; k < 4; ++k) ok = ok && g[k][1] == tag[0] && g[k][3] == tag[1];
        if (__all(ok)) break;
        asm volatile("" ::: "memory");
#pragma unroll
        for (int k = 0; k < 4; ++k) g[k] = ld16(R, t8 * 64 + k * 16, O_LS + (bid & 15) * 16384);
      }
      SB;
      __builtin_amdgcn_s_setprio(2);
      u32x4 uo[4];
#pragma unroll
      for (int k = 0; k < 4; ++k) {
        const f32x2 r2 = {__builtin_amdgcn_rcpf(bf_hi(g[k][0])), __builtin_amdgcn_rcpf(bf_hi(g[k][2]))}, b2 = -(f32x2{bf_lo(g[k][0]), bf_lo(g[k][2])} * r2);
#pragma unroll
        for (int c = 0; c < 4; ++c) {
          const unsigned a = xc8[2 * k][c >> 1], b = xc8[2 * k + 1][c >> 1];
          uo[c][k] = pkh(__builtin_elementwise_fma((c & 1) ? f32x2{bf_hi(a), bf_hi(b)} : f32x2{bf_lo(a), bf_lo(b)}, r2, b2));
        }
      }
      const int r = 15 + (t8 >> 1);
#pragma unroll
      for (int c = 0; c < 4; ++c) *(u32x4*)(sm + C_U + r * 128 + (((2 * c + (t8 & 1)) ^ ((r >> 1) & 7)) * 16)) = uo[c];
      __builtin_amdgcn_s_setprio(0);
      SB;
    }
    bar();
    SB;
    if (w < 4) {
      f32x4 acc[8];
      auto ub = [&](int b) { const int r = b >> 1; return sm + C_U + r * 128 + (((2 * ch + (b & 1)) ^ ((r >> 1) & 7)) * 16); };
      auto ldc = [&](CF& f, int a0) {
        __builtin_amdgcn_s_waitcnt(0xC07F);
        __builtin_amdgcn_sched_barrier(0);
        const char* pb = ub(28 - 4 * a0 + 2 * li + q);
#pragma unroll
        for (int m = 0; m < 8; ++m) { if (m < 4) f.a[m] = lda(a0 + 8 * m); f.b[m] = *(const u32x4*)(pb + 2048 * m); }
      };
      auto mmc = [&](const CF& f) {
#pragma unroll
        for (int m = 0; m < 4; ++m)
#pragma unroll
          for (int t = m; t < 8; ++t) acc[t] = mfma_h(f.a[m], f.b[t - m], acc[t]);
#pragma unroll
        for (int i = 0; i < 16; ++i) { __builtin_amdgcn_sched_group_barrier(0x008, 1, 0); __builtin_amdgcn_sched_group_barrier(0x100, 1, 0); }
        __builtin_amdgcn_sched_barrier(0);
      };
      f32x4 s9[4], s7[4];
      {
        const char* pb = ub(28 + 2 * li + q); const char* pe = ub(27 + 2 * li + q);
        u32x4 ue[4];
#pragma unroll
        for (int k = 0; k < 4; ++k) { A.b[k] = *(const u32x4*)(pb + 2048 * k); ue[k] = *(const u32x4*)(pe + 2048 * k); }
#pragma unroll
        for (int k = 4; k < 8; ++k) A.b[k] = *(const u32x4*)(pb + 2048 * k);
        __builtin_amdgcn_sched_barrier(0);
        const f32x4 z4 = {0, 0, 0, 0};
#pragma unroll
        for (int k = 0; k < 4; ++k) { s7[k] = mfma_h(A.b[k], b7, z4); acc[4 + k] = mfma_h(a32, A.b[k], z4); s9[k] = mfma_h(ue[k], b9, z4); acc[k] = z4; }
        __builtin_amdgcn_sched_barrier(0);
      }
#pragma unroll
      for (int a0 = 0; a0 < 8; a0 += 2) {
        ldc(B, a0 + 1); mmc(A);
        if (a0 == 0) {
          u32x4 g[4];
#pragma unroll
          for (int k = 0; k < 4; ++k)
#pragma unroll
            for (int e = 0; e < 4; ++e) g[k][e] = pkh(f32x2{s7[k][3 - e], s9[k][3 - e]});
#pragma unroll
          for (int k = 0; k < 4; ++k)
#pragma unroll
            for (int t = k; t < 4; ++t) acc[4 + t] = mfma_h(g[k], tf[t - k], acc[4 + t]);
          __builtin_amdgcn_sched_barrier(0);
        }
        if (a0 < 6) ldc(A, a0 + 2);
        mmc(B);
      }
      SB;
#pragma unroll
      for (int t = 0; t < 8; ++t) *(u32x2*)(sk + t * 512 + li * 32 + q * 8) = u32x2{pk2(lo2(acc[t]) * sc), pk2(hi2(acc[t]) * sc)};
    }
    bar();
#pragma unroll
    for (int r = 0; r < 2; ++r) {
      const int T = (tid >> 3) + 64 * r, ip = tid & 7;
      const int t0 = 32 * (T >> 1) + 8 * (ip >> 1) + 4 * (T & 1) + 2 * (ip & 1);
      const char* p = sm + t0 * 2;
      const u32x4 xv = *(const u32x4*)(sm + C_X + t0 * 8);
      const unsigned c0 = *(const unsigned*)p, c1 = *(const unsigned*)(p + CV_K), c2 = *(const unsigned*)(p + 2 * CV_K), c3 = *(const unsigned*)(p + 3 * CV_K);
      u32x4 v = {pk2(up2(xv[0]) + f32x2{bf_lo(c0), bf_lo(c1)}), pk2(up2(xv[1]) + f32x2{bf_lo(c2), bf_lo(c3)}),
                 pk2(up2(xv[2]) + f32x2{bf_hi(c0), bf_hi(c1)}), pk2(up2(xv[3]) + f32x2{bf_hi(c2), bf_hi(c3)})};
      st16(R, (T >> 3) * (18 * 8192) + (T & 7) * 1024 + ip * 16, O_HF + (cg >> 3) * 8192 + (cg & 1) * 512 + ((cg >> 1) & 3) * 128, v);
    }
    SB;
    arrive(1, 8, O_F2 + cg * 8, F2S, NREP);
    SB;
  } else {
    const int tj = (ii - 18) * 8 + xc;
    const int kc = tj / 36, cb = tj % 36;
    if (tj < 64) {
      const int row = 32 * tj + 4 * w + q;
      const char* src = x + (size_t)row * 1152 + li * 8;
      u32x2 xr[9];
#pragma unroll
      for (int k = 0; k < 9; ++k) xr[k] = *(const u32x2*)(src + k * 128);
      tag = mk_tag();
      float a[4] = {0, 0, 0, 0};
#pragma unroll
      for (int k = 0; k < 9; ++k)
#pragma unroll
        for (int e = 0; e < 2; ++e) { a[e] = dot2(xr[k][e], 0x3F803F80u, a[e]); a[2 + e] = dot2(xr[k][e], xr[k][e], a[2 + e]); }
      const float mean = row16_sum(a[0] + a[1]) * (1.0f / D), var = fmaxf(row16_sum(a[2] + a[3]) * (1.0f / D) - mean * mean, 0.f);
      st8(R, li * 16384 + row * 8, O_LS, u32x2{pk_bf16(mean, __builtin_amdgcn_sqrtf(rbf(rbf(var) + EPS))), (q & 1) ? tag[1] : tag[0]});
      SB;
      for (int i = 0; i < WDELAY - 3; ++i) __builtin_amdgcn_s_sleep(16);
    } else
      for (int i = 0; i < WDELAY; ++i) __builtin_amdgcn_s_sleep(16);
    char* s0 = sm + 65536;
    const int G = max(tj - 40, 0), par = G & 1;
    {
      const int row = tid >> 4, uu = G / 9, idx = G % 9, hh = row >> 4;
      const int panel = idx < 4 ? 2 * uu : (idx < 8 ? 2 * uu + 1 : 2 * uu + hh), tile = idx < 8 ? 2 * (idx & 3) + hh : 8;
      const char* src = w_in + (size_t)(192 * min(kc, 2) + row) * 9216 + cb * 256 + li * 16;
      const char* wsrc = w_out + (size_t)(144 * panel + 16 * tile + (row & 15)) * 1152 + li * 8;
      u32x4 v[6]; u32x2 vo[9];
#pragma unroll
      for (int k = 0; k < 6; ++k) v[k] = *(const u32x4*)(src + (size_t)k * 32 * 9216);
      if (tj >= 40) {
#pragma unroll
        for (int k = 0; k < 9; ++k) vo[k] = *(const u32x2*)(wsrc + k * 128);
      }
#pragma unroll
      for (int k = 0; k < 6; ++k) *(u32x4*)(sm + (row + 32 * k) * 272 + li * 16) = v[k];
      if (tj >= 40) {
#pragma unroll
        for (int k = 0; k < 9; ++k) *(u32x2*)(s0 + row * TP + li * 8 + k * 128) = vo[k];
      }
    }
    tag = mk_tag();
    bar();
    SB;
    if (tj < 108) {
      const int ct = 8 * cb + w;
      const int hf = ct < 144 ? 1 : 0, cl = ct < 144 ? ct : ct - 144, fp = cl / 9, n = cl % 9;
      const char* trb = sm + (8 * q + (li >> 2)) * 272 + w * 32 + (li & 3) * 8;
      const unsigned dst = lane * 16 + O_WI + (((fp * 2 + hf) * 18 + 6 * kc) * 9 + n) * 1024;
      float s = 0.f;
#pragma unroll
      for (int g = 0; g < 6; ++g) {
        u32x2 lo = tr_read(trb + g * 32 * 272), hi = tr_read(trb + g * 32 * 272 + 4 * 272);
        st16(R, dst, g * 9216, u32x4{lo[0], lo[1], hi[0], hi[1]});
        s = dot2(lo[0], 0x3F803F80u, s); s = dot2(lo[1], 0x3F803F80u, s); s = dot2(hi[0], 0x3F803F80u, s); s = dot2(hi[1], 0x3F803F80u, s);
      }
      s = q_sum(s);
      if (q == 0) st4(R, li * 4, O_CS + kc * F * 4 + ct * 64, asu(s));
    }
    if (tj >= 40) {
      const bool mixed = G % 9 == 8;
      const char* trb = s0 + ((mixed ? 16 * (q >> 1) + 4 * (q & 1) : 4 * q) + (li >> 2)) * TP + (li & 3) * 8;
      const char* trh = trb + (mixed ? 8 : 16) * TP;
      const unsigned dst = lane * 16 + O_WO + (w * 36 + (G >> 1)) * 9216;
#pragma unroll
      for (int n = 0; n < 5; ++n) {
        const int c0 = 72 * w + 16 * n - (n == 4 ? 8 * par : 0);
        u32x2 lo = tr_read(trb + c0 * 2), hi = tr_read(trh + c0 * 2);
        if (n < 4) st16(R, dst, (4 * par + n) * 1024, u32x4{lo[0], lo[1], hi[0], hi[1]});
        else if ((li >> 3) == par) st16(R, dst, 8192, u32x4{lo[0], lo[1], hi[0], hi[1]});
      }
    }
    SB;
    arrive(0, 8, O_F2 + (144 + tj) * 8, F2S, NREP);
    SB;
  }
  const int tp = (xc & 1) * 8 + (ii >> 2), fp = (xc >> 1) * 4 + (ii & 3);
  const int t64 = 4 * xc + (ii >> 3), j3 = ii & 7;
  if (w == 0) { wait_flags<1>(R, O_F2 + rep * F2S + 144 * 8, 112, tag, lane); SB; }
  bar();
  if (w >= 4) {
    const int lw = w - 4;
    const unsigned l16 = lane * 16, base = lds_addr(sm);
    const int ph = ((ii >> 2) + ii) & 3, ph3 = ((ii >> 3) + ii) & 3;
    auto k9 = [&](int t) { return (t & 3) == ph ? (t + 4 < 9 ? t + 4 : ph) : t; };
    auto k32 = [&](int t) { return (t & 3) == ph ? ((t + 4) & 31) : t; };
    auto k18 = [&](int t) { return (t & 3) == ph3 ? (t + 4 < 18 ? t + 4 : ph3) : t; };
    if (lw < 2) {
      constexpr int NP = 8;
      unsigned vo[8];
#pragma unroll
      for (int j = 0; j < 8; ++j) {
        int t = 64 * lw + 32 * (j >> 2) + 4 * ((j >> 1) & 1) + 16 * (j & 1) + 8 * (lane >> 5) + ((lane >> 3) & 3);
        vo[j] = t * 4096 + (((lane & 7) ^ ((lane >> 3) & 7)) * 16) + 3072 - (j & 3) * 1024;
      }
      const char* wsrc = w_sp + (size_t)tp * 128 * 4096 - 3072;
      const char* hsrc = ws + O_HF + tp * 18 * 8192 + lw * 8192;
      const char* psrc = ws + O_PF + t64 * 72 * 4096 + lw * 8192;
      const unsigned dst = base + lw * 8192;
      if (lw == 0) wait_flags<2>(R, O_F2 + rep * F2S, 144, tag, lane);
      SB;
      bar();
#pragma unroll 1
      for (int t = 0; t < 4; ++t) ld_lin<NP>(dst + t * STG, hsrc + k9(t) * 16384, l16);
      wait_vm<3 * NP>(); bar();
#pragma unroll 1
      for (int t = 4; t < 18; ++t) { wait_vm<2 * NP>(); bar(); ld_lin<NP>(dst + (t & 3) * STG, hsrc + k9(t < 9 ? t : t - 9) * 16384, l16); }
#pragma unroll 1
      for (int t = 18; t < 50; ++t) {
        wait_vm<2 * NP>(); bar();
        const unsigned dd = dst + (t & 3) * STG; const char* sp = wsrc + k32(t - 18) * 128;
        glds4<false>(dd, sp, vo[0], vo[1], vo[2], vo[3]); glds4<false>(dd + 4096, sp, vo[4], vo[5], vo[6], vo[7]);
      }
      wait_vm<2 * NP>(); bar(); wait_vm<NP>(); bar(); wait_vm<0>(); bar();
      bar(); bar();
      wait_flags<1>(R, O_PFL + (t64 >> 1) * 2048 + ((t64 & 1) * 8 + j3) * 128, 16, tag, lane);
      SB;
#pragma unroll 1
      for (int t = 50; t < 68; ++t) {
        if (t >= 53) { wait_vm<2 * NP>(); bar(); }
        ld_lin<NP>(dst + (t & 3) * STG, psrc + k18(t - 50) * 16384, l16);
      }
      wait_vm<2 * NP>(); bar(); wait_vm<NP>(); bar(); wait_vm<0>(); bar();
    } else {
      constexpr int NP = 9;
      const char* wi = ws + O_WI + (fp * 2) * 18 * 9216 + (lw - 2) * 9216;
      const char* zs = ws + O_ZF + fp * 64 * 9216 + (lw - 2) * 9216;
      const char* wo = ws + O_WO + j3 * 36 * 9216 + (lw - 2) * 9216;
      const unsigned dst = base + 16384 + (lw - 2) * 9216;
#pragma unroll 1
      for (int t = 0; t < 4; ++t) ld_lin<NP>(dst + t * STG, wi + k9(t) * 18432, l16);
      bar();
      wait_vm<3 * NP>(); bar();
      if (lw == 3) {
        f32x2 a = *(const f32x2*)(sm + X_LN + lane * 8) + *(const f32x2*)(sm + X_LN + 512 + lane * 8);
        a[0] = q_sum(dpp_add<0x128>(a[0])); a[1] = q_sum(dpp_add<0x128>(a[1]));
        float mean = a[0] * (1.0f / D), mb, ri;
        ln_scalars(mean, fmaxf(a[1] * (1.0f / D) - mean * mean, 0.f), mb, ri);
        if (lane < 8) st8(R, lane * 8, O_HS + tp * 1024 + fp * 64, u32x2{asu(ri) == SENT ? 0x7FC00000u : asu(ri), asu(-mb)});
      }
#pragma unroll 1
      for (int t = 4; t < 18; ++t) { wait_vm<2 * NP>(); bar(); ld_lin<NP>(dst + (t & 3) * STG, wi + (t < 9 ? k9(t) : 9 + k9(t - 9)) * 18432, l16); }
      wait_vm<2 * NP>(); bar(); wait_vm<NP>(); bar(); wait_vm<0>(); bar();
      wait_flags<1>(R, O_ZFL + fp * 2048 + tp * 128, 16, tag, lane);
      SB;
#pragma unroll 1
      for (int t = 18; t < 53; ++t) {
        if (t >= 21) { wait_vm<2 * NP>(); bar(); }
        ld_lin<NP>(dst + (t & 3) * STG, t < 50 ? zs + k32(t - 18) * 18432 : wo + k18(t - 50) * 18432, l16);
      }
      wait_vm<0>(); bar();
      bar();
#pragma unroll 1
      for (int t = 53; t < 57; ++t) ld_lin<NP>(dst + (t & 3) * STG, wo + k18(t - 50) * 18432, l16);
      bar(); bar(); bar();
      wait_vm<3 * NP>(); bar();
#pragma unroll 1
      for (int t = 57; t < 68; ++t) { wait_vm<2 * NP>(); bar(); ld_lin<NP>(dst + (t & 3) * STG, wo + k18(t - 50) * 18432, l16); }
      wait_vm<2 * NP>(); bar(); wait_vm<NP>(); bar(); wait_vm<0>(); bar();
    }
    return;
  }
  Ctx c;
  c.sm = sm;
  c.am0 = 2 * w * 1024 + lane * 8;
  c.am1 = 2 * w * 2048 + li * 128 + ((q ^ (lane & 7)) * 16);
  c.an = 16384 + lane * 16;
  const int prow = 8 * (li >> 2) + (li & 3);
  if (tid < 72) {
    const unsigned o = tid < 36 ? FH * 4 + tid * 16 : (tid - 36) * 16;
    const u32x4 c0 = ld16(R, o, O_CS + fp * 576), c1 = ld16(R, o, O_CS + F * 4 + fp * 576), c2 = ld16(R, o, O_CS + F * 8 + fp * 576);
    *(f32x4*)(sm + X_CS + tid * 16) = (__builtin_bit_cast(f32x4, c0) + __builtin_bit_cast(f32x4, c1)) + __builtin_bit_cast(f32x4, c2);
  }
  bar();
  char* pvb = sm + X_PV + w * 4096;
  {
    u32x4 hv[3];
#pragma unroll
    for (int k = 0; k < 3; ++k)
      hv[k] = ld16(R, (lane >> 5) * 8192 + ((lane >> 2) & 7) * 128 + (lane & 3) * 16, O_HF + (tp * 18 * 8 + (fp >> 1)) * 1024 + (fp & 1) * 64 + min(w + 4 * k, 8) * 16384);
    if (w) hv[2] = u32x4{0, 0, 0, 0};
    f32x4 a = {0, 0, 0, 0};
#pragma unroll
    for (int k = 0; k < 3; ++k)
#pragma unroll
      for (int e = 0; e < 4; ++e) { a[e & 2] = dot2(hv[k][e], 0x3F803F80u, a[e & 2]); a[(e & 2) + 1] = dot2(hv[k][e], hv[k][e], a[(e & 2) + 1]); }
#pragma unroll
    for (int e = 0; e < 4; ++e) a[e] = dpp_add<0x124>(dpp_add<0x128>(a[e]));
    if (li < 4) *(f32x4*)(sm + X_LN + (w * 4 + q) * 64 + li * 16) = a;
  }
  f32x4 acc[2][9];
  Fr X, Y;
  bar();
  SB;
  ldf<0>(X, c, 0, 0);
  seg<0, 0, true, true>(acc, X, Y, c, 0, 1);
  const unsigned hs_m0 = __builtin_amdgcn_readfirstlane(lds_addr(sm) + X_HS + w * 256);
  auto ld_hs = [&]() {
    unsigned keep;
    asm volatile("s_mov_b32 %0, m0\n\ts_mov_b32 m0, %1\n\ts_nop 2\n\tglobal_load_lds_dword %3, %2 sc1\n\ts_mov_b32 m0, %0"
                 : "=&s"(keep) : "s"(hs_m0), "s"(ws), "v"(O_HS + tp * 1024 + tid * 4) : "memory");
  };
  ALIGNL;
#pragma unroll 1
  for (int t = 0; t < 8; ++t) {
    if (t == 5) ld_hs();
    bar();
    seg<0, 0, true>(acc, Y, X, c, t + 1, 0);
    seg<0, 0, true>(acc, X, Y, c, t + 1, 1);
  }
  u32x4 hc[4];
  float cs[9];
  auto rd_hs = [&]() {
    wait_vm<0>();
#pragma unroll
    for (int jp = 0; jp < 4; ++jp) hc[jp] = *(const u32x4*)(sm + X_HS + w * 256 + (jp >> 1) * 128 + q * 32 + (jp & 1) * 16);
  };
  rd_hs();
#pragma unroll
  for (int n = 0; n < 9; ++n) cs[n] = *(const float*)(sm + X_CS + (16 * n + li) * 4);
  bar();
  seg<0, 0, true>(acc, Y, X, c, 9, 0);
  SB;
#pragma unroll 1
  for (int spin = 0; spin < SPIN; ++spin) {
    if (__all(max(max(umax4(hc[0]), umax4(hc[1])), max(umax4(hc[2]), umax4(hc[3]))) != SENT)) break;
    ld_hs();
    rd_hs();
  }
  f32x2 zf[9][4];
  f32x2 rs[4];
  {
    f32x2 nm[4], k1[4], k0[4], S1[4] = {}, S2[4] = {};
#pragma unroll
    for (int jp = 0; jp < 4; ++jp) {
      rs[jp] = f32x2{asf(hc[jp][0]), asf(hc[jp][2])}; nm[jp] = f32x2{asf(hc[jp][1]), asf(hc[jp][3])};
      k0[jp] = rs[jp] * GC0; k1[jp] = rs[jp] * rs[jp] * rs[jp] * GC1;
    }
#pragma unroll
    for (int n = 0; n < 9; ++n) {
#pragma unroll
      for (int jp = 0; jp < 4; ++jp) {
        f32x2 a = (jp & 1) ? hi2(acc[jp >> 1][n]) : lo2(acc[jp >> 1][n]);
        f32x2 z = gelu2(__builtin_elementwise_fma(nm[jp], f32x2{cs[n], cs[n]}, a), k1[jp], k0[jp]);
        zf[n][jp] = z;
        S1[jp] += z; S2[jp] = __builtin_elementwise_fma(z, z, S2[jp]);
      }
      asm volatile("" : "+v"(zf[n][0]), "+v"(zf[n][1]), "+v"(zf[n][2]), "+v"(zf[n][3]));
    }
#pragma unroll
    for (int jp = 0; jp < 4; ++jp) { S1[jp] *= rs[jp]; S2[jp] *= rs[jp] * rs[jp]; }
    float v[16];
#pragma unroll
    for (int j = 0; j < 8; ++j) { v[2 * j] = S1[j >> 1][j & 1]; v[2 * j + 1] = S2[j >> 1][j & 1]; }
#pragma unroll
    for (int i = 0; i < 8; ++i) { bool h = li & 8; v[i] = (h ? v[i + 8] : v[i]) + dpp_get<0x128>(h ? v[i] : v[i + 8]); }
#pragma unroll
    for (int i = 0; i < 4; ++i) { bool h = li & 4; v[i] = (h ? v[i + 4] : v[i]) + dpp_get<0x141>(h ? v[i] : v[i + 4]); }
#pragma unroll
    for (int i = 0; i < 2; ++i) { bool h = li & 2; v[i] = (h ? v[i + 2] : v[i]) + dpp_get<0x4E>(h ? v[i] : v[i + 2]); }
    { bool h = li & 1; v[0] = (h ? v[1] : v[0]) + dpp_get<0xB1>(h ? v[0] : v[1]); }
    unsigned tot = asu(v[0]);
    st4(R, lane * 4, O_ST + ((tp * 4 + w) * 16 + fp) * 256, tot == SENT ? 0x7FC00000u : tot);
  }
  SB;
  const char* sts = ws + O_ST + (tp * 4 + w) * 4096;
  seg<0, 1, true, true>(acc, X, Y, c, 9, 1);
  ALIGNL;
#pragma unroll 1
  for (int t = 9; t < TCHK; ++t) {
    if (t == TPOLL) glds4<true>(lds_addr(pvb), sts, lane * 16, lane * 16, lane * 16, lane * 16);
    bar();
    seg<0, 1, true>(acc, Y, X, c, t + 1, 0);
    seg<0, 1, true>(acc, X, Y, c, t + 1, 1);
  }
  SB;
  u32x4 pv[4];
#pragma unroll 1
  for (int spin = 0; spin < SPIN; ++spin) {
    wait_vm<0>();
#pragma unroll
    for (int k = 0; k < 4; ++k) pv[k] = *(const u32x4*)(pvb + k * 1024 + lane * 16);
    if (__all(max(max(umax4(pv[0]), umax4(pv[1])), max(umax4(pv[2]), umax4(pv[3]))) != SENT)) break;
    glds4<true>(lds_addr(pvb), sts, lane * 16, lane * 16, lane * 16, lane * 16);
  }
  SB;
  {
    float t4[4];
#pragma unroll
    for (int e = 0; e < 4; ++e) t4[e] = q_sum((asf(pv[0][e]) + asf(pv[1][e])) + (asf(pv[2][e]) + asf(pv[3][e])));
    f32x2 o0, o1;
#pragma unroll
    for (int r = 0; r < 2; ++r) {
      float mean = t4[2 * r] * (1.0f / FH), var = fmaxf(t4[2 * r + 1] * (1.0f / FH) - mean * mean, 0.f), mb, ri;
      ln_scalars(mean, var, mb, ri);
      o0[r] = ri; o1[r] = -mb * ri;
    }
    char* ln = sm + X_LN + w * 256;
    if (lane < 16) { *(f32x2*)(ln + li * 8) = o0; *(f32x2*)(ln + 128 + li * 8) = o1; }
    asm volatile("" ::: "memory");
    f32x4 ri[2], rb[2];
#pragma unroll
    for (int m = 0; m < 2; ++m) { ri[m] = *(const f32x4*)(ln + (8 * q + 4 * m) * 4); rb[m] = *(const f32x4*)(ln + 128 + (8 * q + 4 * m) * 4); }
#pragma unroll
    for (int jp = 0; jp < 4; ++jp) rs[jp] *= (jp & 1) ? hi2(ri[jp >> 1]) : lo2(ri[jp >> 1]);
    const unsigned zo = lane * 16 + O_ZF + (fp * 64 + 4 * tp + w) * 9216;
#pragma unroll
    for (int n = 0; n < 9; ++n) {
      u32x4 o4;
#pragma unroll
      for (int jp = 0; jp < 4; ++jp)
        o4[jp] = pk2(__builtin_elementwise_fma(zf[n][jp], rs[jp], (jp & 1) ? hi2(rb[jp >> 1]) : lo2(rb[jp >> 1])));
      st16(R, zo, n * 1024, o4);
    }
  }
  SB;
  ALIGNL;
#pragma unroll 1
  for (int t = TCHK; t < 17; ++t) {
    if (t == TFLAG) wait_vm<0>();
    bar();
    if (t == TFLAG && tid < 16) st8(R, tid * 128, O_ZFL + fp * 2048 + tp * 8, tag);
    seg<0, 1, true>(acc, Y, X, c, t + 1, 0);
    seg<0, 1, true>(acc, X, Y, c, t + 1, 1);
  }
  float lrs[2], lnm[2];
  f32x4 cs1[9];
#pragma unroll
  for (int m = 0; m < 2; ++m) { const f32x2 h = *(const f32x2*)(sm + X_HS + w * 256 + m * 128 + li * 8); lrs[m] = h[0]; lnm[m] = h[1]; }
#pragma unroll
  for (int n = 0; n < 9; ++n) cs1[n] = *(const f32x4*)(sm + X_CS + (144 + 16 * n + 4 * q) * 4);
  __builtin_amdgcn_sched_barrier(0);
  seg<0, 1, false>(acc, Y, X, c, 17, 1);
  SB;
  f32x2 z1[2][9][2];
  float bs[2];
#pragma unroll
  for (int m = 0; m < 2; ++m) bs[m] = ldbf(b_sp, 128 * tp + 32 * w + prow + 4 * m) * lrs[m];
  {
#pragma unroll
    for (int n = 0; n < 9; ++n)
#pragma unroll
      for (int m = 0; m < 2; ++m) {
        const float a1 = lrs[m] * lrs[m] * lrs[m] * GC1, a0 = lrs[m] * GC0;
        const f32x2 k1 = {a1, a1}, k0 = {a0, a0}, nm = {lnm[m], lnm[m]};
        z1[m][n][0] = gelu2(__builtin_elementwise_fma(nm, lo2(cs1[n]), lo2(acc[m][n])), k1, k0);
        z1[m][n][1] = gelu2(__builtin_elementwise_fma(nm, hi2(cs1[n]), hi2(acc[m][n])), k1, k0);
        asm volatile("" : "+v"(z1[m][n][0]), "+v"(z1[m][n][1]));
      }
  }
  SB;
  bar();
  SB;
  ldf<1>(X, c, 18, 0);
  seg<1, 1, true, true>(acc, X, Y, c, 18, 1);
  ALIGNL;
#pragma unroll 1
  for (int t = 18; t < 49; ++t) {
    bar();
    seg<1, 1, true>(acc, Y, X, c, t + 1, 0);
    seg<1, 1, true>(acc, X, Y, c, t + 1, 1);
  }
  seg<1, 1, false>(acc, Y, X, c, 49, 1);
  SB;
  {
    const unsigned pf = O_PF + (((2 * tp + (w >> 1)) * 72 + 9 * (fp >> 1)) * 4 + 2 * (w & 1)) * 1024;
    const unsigned po = lane * 16 + pf + (fp & 1) * 16384;
    u32x2 p8[2];
#pragma unroll
    for (int m = 0; m < 2; ++m) {
      const f32x2 r2 = {lrs[m], lrs[m]}, b2 = {bs[m], bs[m]};
      auto pp = [&](int n) { return u32x2{pk2(__builtin_elementwise_fma(lo2(acc[m][n]), r2, b2) * z1[m][n][0]), pk2(__builtin_elementwise_fma(hi2(acc[m][n]), r2, b2) * z1[m][n][1])}; };
#pragma unroll
      for (int a = 0; a < 4; ++a) {
        u32x2 p0 = pp(2 * a), p1 = pp(2 * a + 1);
        st16(R, po, a * 4096 + m * 1024, u32x4{p0[0], p0[1], p1[0], p1[1]});
      }
      p8[m] = pp(8);
    }
    const auto s0 = __builtin_amdgcn_permlane32_swap(p8[0][0], p8[1][0], false, false), s1 = __builtin_amdgcn_permlane32_swap(p8[0][1], p8[1][1], false, false);
    st16(R, (lane >> 5) * 1024 + (lane & 31) * 16, pf + 8 * 4096 + (fp & 1) * 512, u32x4{s0[0], s1[0], s0[1], s1[1]});
  }
  SB;
  const int th = w & 1, kq = w >> 1, T = 4 * t64 + 2 * th + kq;
  const int lane5 = fresh_lane();
#define lane lane5
#define li (lane5 & 15)
#define q (lane5 >> 4)
#define prow (8 * (li >> 2) + (li & 3))
  struct P3 { u32x4 p[2][2]; } Up, Vp;
  struct N3 { u32x4 n[9]; } Un, Vn, En;
  const int ap = (8 * kq + 2 * th) * 1024 + lane * 16, aw = 16384 + kq * 9216 + lane * 16;
  auto ldp = [&](P3& f, int t) {
#pragma unroll
    for (int g = 0; g < 2; ++g)
#pragma unroll
      for (int m = 0; m < 2; ++m) f.p[g][m] = *(const u32x4*)(sm + (t & 3) * STG + ap + (4 * g + m) * 1024);
  };
  auto ldn = [&](N3& f, int t) {
#pragma unroll
    for (int n = 0; n < 9; ++n) f.n[n] = *(const u32x4*)(sm + (t & 3) * STG + aw + n * 1024);
  };
  bar();
  ldn(Un, 50); ldn(Vn, 51); ldn(En, 52);
  arrive(2, 4, O_PFL + tp * 2048 + fp * 8, 128, 16);
  bar();
  u32x2 hv[5];
#pragma unroll
  for (int n = 0; n < 5; ++n) {
    const int dd = 72 * j3 + 16 * n + 4 * (n == 4 ? (q & 1) : q);
    hv[n] = ld8(R, (dd >> 5) * 8192 + ((dd >> 2) & 1) * 512 + (((dd >> 3) & 3) * 16 + li) * 8, O_HF + ((T >> 3) * 18 * 8 + (T & 7)) * 1024);
  }
  f32x4 a3[2][6] = {};
  auto mm3 = [&](const P3& f, const N3& e) {
#pragma unroll
    for (int g = 0; g < 2; ++g)
#pragma unroll
      for (int m = 0; m < 2; ++m) {
#pragma unroll
        for (int n = 0; n < 4; ++n) a3[m][n] = mfma_bf(e.n[4 * g + n], f.p[g][m], a3[m][n]);
        a3[m][4 + g] = mfma_bf(e.n[8], f.p[g][m], a3[m][4 + g]);
      }
#pragma unroll
    for (int i = 0; i < 13; ++i) { __builtin_amdgcn_sched_group_barrier(0x008, 1, 0); __builtin_amdgcn_sched_group_barrier(0x100, 1, 0); }
    __builtin_amdgcn_sched_barrier(0);
  };
  SB;
  bar();
  SB;
  ldp(Up, 50);
  bar(); ldp(Vp, 51); mm3(Up, Un);
  bar(); ldp(Up, 52); mm3(Vp, Vn);
  bar(); ldp(Vp, 53); ldn(Vn, 53); mm3(Up, En);
  ALIGNL;
#pragma unroll 1
  for (int t = 53; t < 67; t += 2) { bar(); ldp(Up, t + 1); ldn(Un, t + 1); mm3(Vp, Vn); bar(); ldp(Vp, t + 2); ldn(Vn, t + 2); mm3(Up, Un); }
  mm3(Vp, Vn);
  SB;
#pragma unroll
  for (int m = 0; m < 2; ++m) a3[m][4] = lane < 32 ? a3[m][4] : a3[m][5];
  if (kq) {
#pragma unroll
    for (int n = 0; n < 5; ++n) { *(f32x4*)(sm + w * 5120 + n * 1024 + lane * 16) = a3[0][n]; asm volatile("" : "+v"(a3[1][n])); a3[0][n] = a3[1][n]; }
  } else {
#pragma unroll
    for (int n = 0; n < 5; ++n) *(f32x4*)(sm + w * 5120 + n * 1024 + lane * 16) = a3[1][n];
  }
  bar();
  char* o = out + (size_t)(32 * (T >> 1) + 4 * (T & 1) + prow) * 1152 + (72 * j3 + 4 * q) * 2;
#pragma unroll
  for (int n = 0; n < 5; ++n) {
    f32x4 v = a3[0][n] + *(const f32x4*)(sm + (w ^ 2) * 5120 + n * 1024 + lane * 16);
    if (n == 4) v = f32x4{half_sum(v[0]), half_sum(v[1]), half_sum(v[2]), half_sum(v[3])};
    if (n < 4 || q < 2) *(u32x2*)(o + n * 32) = u32x2{pk2(up2(hv[n][0]) + lo2(v)), pk2(up2(hv[n][1]) + hi2(v))};
  }
  if (bid == 0 && w == 0 && lane == 0) { u64 e = (((u64)tag[1] << 32) | tag[0]) - 0x9E3779B97F4A7C15ull + 0x100000001ull;       st8(R, 0, O_EP, u32x2{(unsigned)e, (unsigned)(e >> 32)}); }
  SB;
}

#undef lane
#undef li
#undef q
#undef prow
extern "C" void kernel_launch(void* const* d_in, const int* in_sizes, int n_in, void* d_out, int out_size, void* d_ws, size_t ws_size,
                              hipStream_t stream) {
  (void)hipFuncSetAttribute((const void*)k_all, hipFuncAttributeMaxDynamicSharedMemorySize, LDS_BYTES);
  k_all<<<256, 512, LDS_BYTES, stream>>>((const char*)d_in[0], (const char*)d_in[1], (const char*)d_in[2], (const char*)d_in[3],
      (const char*)d_in[4], (const char*)d_in[5], (const char*)d_in[6], (const char*)d_in[7], (const char*)d_in[8], (const char*)d_in[10],
      (const char*)d_in[11], (const char*)d_in[12], (const char*)d_in[13], (char*)d_out, (char*)d_ws);
}
```
